```python
import torch

def _softmax(x, axis=-1):
    return torch.softmax(x, dim=axis)

def _gelu(x):
    return torch.nn.functional.gelu(x, approximate='tanh')

def _silu(x):
    return torch.nn.functional.silu(x)

def _sigmoid(x):
    return torch.sigmoid(x)

def _layernorm(x, axis=-1, eps=1e-05):
    m = x.mean(dim=axis, keepdim=True)
    v = x.var(dim=axis, keepdim=True, unbiased=False)
    return (x - m) / torch.sqrt(v + eps)

def _scatter_add(tgt, idx, src, axis=0):
    return tgt.clone().index_add_(axis, idx, src)

def _segment_sum(src, seg, n):
    out = torch.zeros((n,) + src.shape[1:], dtype=src.dtype)
    return out.index_add_(0, seg, src)

def _pad(x, pw):
    flat = tuple((v for pair in reversed(pw) for v in pair))
    return torch.nn.functional.pad(x, flat)
kernel_name = 'gated_mix__memory_aug_recurrent_mem_tr__spectral_gfnet__prompt'

def setup_inputs():

    _w = lambda a: (lambda t: t.to(torch.bfloat16) if t.is_floating_point() else t)(torch.as_tensor(a))

    def _A_setup():
        B, S, Dm = (1, 2048, 1024)
        H, Dh = (32, 128)
        M, seg = (8, 32)
        seg = min(seg, S)
        S = S // seg * seg
        return {'x': _w((lambda *s: torch.randn(*s, dtype=torch.bfloat16) * 0.05)(B, S, Dm)), 'mem0': _w((lambda *s: torch.randn(*s, dtype=torch.bfloat16) * 0.4)(B, M, Dm)), 'Wq': _w((lambda *s: torch.randn(*s, dtype=torch.bfloat16) * 6.4)(Dm, H, Dh) * Dm ** (-0.5)), 'Wk': _w((lambda *s: torch.randn(*s, dtype=torch.bfloat16) * 6.4)(Dm, H, Dh) * Dm ** (-0.5)), 'Wv': _w((lambda *s: torch.randn(*s, dtype=torch.bfloat16) * 0.4)(Dm, H, Dh) * Dm ** (-0.5)), 'Wo': _w((lambda *s: torch.randn(*s, dtype=torch.bfloat16) * 0.4)(H, Dh, Dm) * (H * Dh) ** (-0.5)), 'seg_len': seg}
    _A_in = _A_setup()

    def _B_setup():
        B, S, D = (1, 2048, 1024)
        Df = D // 2 + 1
        return {'x': _w((lambda *s: torch.randn(*s, dtype=torch.bfloat16) * 0.05)(B, S, D)), 'w_r': _w((lambda *s: torch.randn(*s, dtype=torch.bfloat16) * 3.2)(S, Df) * 0.02), 'w_i': _w((lambda *s: torch.randn(*s, dtype=torch.bfloat16) * 3.2)(S, Df) * 0.02)}
    _B_in = _B_setup()
    return {'A_x': _A_in['x'], 'A_mem0': _A_in['mem0'], 'A_Wq': _A_in['Wq'], 'A_Wk': _A_in['Wk'], 'A_Wv': _A_in['Wv'], 'A_Wo': _A_in['Wo'], 'A_seg_len': _A_in['seg_len'], 'B_x': _B_in['x'], 'B_w_r': _B_in['w_r'], 'B_w_i': _B_in['w_i'], 'gate_w': _w((lambda *s: torch.randn(*s, dtype=torch.bfloat16) * 0.05)(1024) * 16.0)}

def reference(A_x, A_mem0, A_Wq, A_Wk, A_Wv, A_Wo, A_seg_len, B_x, B_w_r, B_w_i, gate_w):

    def _A_ref(x, mem0, Wq, Wk, Wv, Wo, seg_len):
        B, S, Dm = x.shape
        H, Dh = (Wq.shape[1], Wq.shape[2])
        M = mem0.shape[1]
        scale = Dh ** (-0.5)
        n_seg = S // seg_len
        L = M + seg_len
        idx = torch.arange(L)
        mask = (idx[:, None] < M) | (idx[:, None] >= idx[None, :])
        mem = mem0
        outs = []
        for s in range(n_seg):
            xs = x[:, s * seg_len:(s + 1) * seg_len, :].clone()
            h = torch.concatenate([mem, xs], axis=1)
            q = torch.einsum('btd,dhe->bhte', h, Wq)
            k = torch.einsum('btd,dhe->bhte', h, Wk)
            v = torch.einsum('btd,dhe->bhte', h, Wv)
            scores = torch.einsum('bhqe,bhke->bhqk', q, k) * scale
            scores = torch.where(mask[None, None], scores, -1000000000.0)
            p = _softmax(scores, axis=-1)
            o = torch.einsum('bhqk,bhke->bhqe', p, v)
            o = torch.einsum('bhte,hed->btd', o, Wo)
            mem = o[:, :M, :].clone()
            outs.append(o[:, M:, :])
        return torch.concatenate(outs, axis=1)

    def _B_ref(x, w_r, w_i):
        B, S, D = x.shape
        X = (lambda x, axes=(-2, -1): torch.fft.rfft2(x.float(), dim=axes))(x, axes=(-2, -1))
        W = w_r + 1j * w_i
        X = X * W
        y = (lambda x, s, axes=(-2, -1): torch.fft.irfft2(x, s=s, dim=axes).to(torch.bfloat16))(X, (S, D), axes=(-2, -1))
        return y
    x = A_x
    g = _sigmoid((x * gate_w).sum(-1, keepdims=True))
    a = _A_ref(x, A_mem0, A_Wq, A_Wk, A_Wv, A_Wo, A_seg_len)
    b = _B_ref(x, B_w_r, B_w_i)
    return g * a + (1 - g) * b
```

```cpp
#include <hip/hip_runtime.h>
#include <hip/hip_bf16.h>
#include <stdint.h>
#include <type_traits>

typedef short bf16x8 __attribute__((ext_vector_type(8)));
typedef short s16x4 __attribute__((ext_vector_type(4)));
typedef float f32x4 __attribute__((ext_vector_type(4)));
typedef unsigned u32x4 __attribute__((ext_vector_type(4)));
typedef unsigned u32x2 __attribute__((ext_vector_type(2)));
typedef unsigned short u16;

#define DEVI __device__ __forceinline__

DEVI float bf2f(u16 v) { return __uint_as_float(((unsigned)v) << 16); }
DEVI u16 f2bf(float f) { return __builtin_bit_cast(u16, (__bf16)f); }
DEVI float rbf(float f) { return bf2f(f2bf(f)); }
DEVI unsigned pack2(u16 a, u16 b) { return (unsigned)a | ((unsigned)b << 16); }
DEVI f32x4 mfma16(bf16x8 a, bf16x8 b, f32x4 c) { return __builtin_amdgcn_mfma_f32_16x16x32_bf16(a, b, c, 0, 0, 0); }

constexpr int S_ = 2048, D_ = 1024, H_ = 32, DH_ = 128, MEM_ = 8, SEG_ = 32, NSEG_ = 64, LK_ = 40;
constexpr int HD_ = H_ * DH_;
#define FSV 56
constexpr int NL_ = NSEG_ - 1;
constexpr int FS_ = FSV;
constexpr int XS_ = 8;
constexpr int FB_ = 513;
constexpr float SCALE_ = 0.08838834764831845f;

constexpr unsigned POISON = 0xffffffffu;
constexpr unsigned SET_ = 4u << 20, BLK_ = 32768u, NB3 = 0, NB2 = 32, NB1 = 48, NBLK = 52;
constexpr unsigned OFF_CKV = 24u << 20;
constexpr unsigned OFF_CO = OFF_CKV + 16384u;
constexpr unsigned OFF_CPX = OFF_CO + 65536u;
constexpr unsigned OFF_CF = OFF_CPX + 32768u;
constexpr unsigned OFF_REC = OFF_CF + 1024u;
constexpr unsigned OFF_EP = OFF_REC + 5120u;
constexpr unsigned COMM_BYTES = OFF_EP + 1024u;
struct NearMap {
  unsigned k;
  __device__ static unsigned pg(unsigned c) { return ((c & 1u) << 14) | ((c & 6u) << 11); }
#define BLAY 0
  __device__ static constexpr unsigned blk(unsigned n) { return BLAY ? ((n & 3u) << 15) | ((n & 28u) << 17) | ((n >> 5) << 17) : n * BLK_; }
  __device__ unsigned operator()(unsigned set, unsigned g, unsigned n) const { return set * SET_ + blk(n) + pg(k ^ g ^ (blk(n) >> 19)); }
};
DEVI unsigned far8a(unsigned u, unsigned hb) { return u * 16384u + (((u >> 7) ^ hb) & 1u) * 8192u; }
DEVI unsigned far8(unsigned u, unsigned hb) { return u * 16384u + (((u >> 7) ^ (u < 128u ? 0u : hb)) & 1u) * 8192u; }
#define NPROBE 8
DEVI unsigned long long clk_now() {
  unsigned long long t;
  asm volatile("s_waitcnt vmcnt(0) lgkmcnt(0)\n s_memtime %0\n s_waitcnt lgkmcnt(0)" : "=s"(t) :: "memory");
  return t;
}
constexpr unsigned OFF_G = COMM_BYTES;
constexpr unsigned OFF_Q = 28u << 20, OFF_K = 60u << 20, OFF_VX = 92u << 20, OFF_OT = 124u << 20;
constexpr unsigned OFF_T = 128u << 20;
constexpr unsigned OFF_PX = 144u << 20;
constexpr unsigned OFF_BF = 184u << 20;
constexpr unsigned OFF_XF = 192u << 20;
constexpr unsigned WS_USED = 200u << 20;
static_assert(FS_ >= NSEG_ - 8 && 32 + NL_ - FS_ <= 40 && XS_ == 8, "tails");
constexpr unsigned HALF_ = BLK_;
DEVI unsigned tile8(int s, int h, unsigned hb) { return (unsigned)(s * 8 + (h & 7)) * (2u * BLK_) + NearMap::pg((s < 2 ? 0u : hb >> 1) ^ (unsigned)(h >> 3) ^ (unsigned)s); }
DEVI unsigned ooff(int s, int h, unsigned hb) { return (s < FS_ ? OFF_Q : OFF_OT - (NSEG_ - 8) * H_ * 16384u) + tile8(s, h, hb); }
DEVI unsigned bfoff(int blk, int tok, unsigned hb) { return OFF_BF + far8a(blk * 4 + (tok >> 9), hb) + (unsigned)(tok & 511) * 16u; }
DEVI unsigned kmoff(int s, int h, unsigned hb) { return NearMap{hb >> 1}(3u, h >> 3, 64 + s * 8 + (h & 7)); }
DEVI unsigned cooff(int h, unsigned hb) { return OFF_CO + far8a(h >> 3, hb ^ 1u) + (unsigned)(h & 7) * 1024u; }

constexpr int SMEM_BYTES = 163840;
__shared__ __attribute__((aligned(16))) char lds_[SMEM_BYTES];
#define LDS3 ((__attribute__((address_space(3))) char*)lds_)

typedef __amdgpu_buffer_rsrc_t rsrc_t;
DEVI u32x4 ld_sc1(rsrc_t r, unsigned off) { return __builtin_amdgcn_raw_buffer_load_b128(r, off, 0, 16); }
DEVI u32x2 ld_sc1_64(rsrc_t r, unsigned off) { return __builtin_amdgcn_raw_buffer_load_b64(r, off, 0, 16); }
DEVI unsigned ld_sc1_32(rsrc_t r, unsigned off) { return __builtin_amdgcn_raw_buffer_load_b32(r, off, 0, 16); }
DEVI void st_sc1(rsrc_t r, unsigned off, u32x4 v) { __builtin_amdgcn_raw_buffer_store_b128(v, r, off, 0, 16); }
DEVI void st_sc1_64(rsrc_t r, unsigned off, u32x2 v) { __builtin_amdgcn_raw_buffer_store_b64(v, r, off, 0, 16); }
DEVI void st_sc1_32(rsrc_t r, unsigned off, unsigned v) { __builtin_amdgcn_raw_buffer_store_b32(v, r, off, 0, 16); }
DEVI void st_sc1_16(rsrc_t r, unsigned off, u16 v) { __builtin_amdgcn_raw_buffer_store_b16(v, r, off, 0, 16); }
DEVI u32x4 ld_sc1s(rsrc_t r, unsigned off, unsigned so) { return __builtin_amdgcn_raw_buffer_load_b128(r, off, so, 16); }
DEVI void st_sc1s(rsrc_t r, unsigned off, unsigned so, u32x4 v) { __builtin_amdgcn_raw_buffer_store_b128(v, r, off, so, 16); }
DEVI void st_sc1s_64(rsrc_t r, unsigned off, unsigned so, u32x2 v) { __builtin_amdgcn_raw_buffer_store_b64(v, r, off, so, 16); }
DEVI void st_sc1s_32(rsrc_t r, unsigned off, unsigned so, unsigned v) { __builtin_amdgcn_raw_buffer_store_b32(v, r, off, so, 16); }
#define HAUX 16
DEVI void hst(rsrc_t r, unsigned off, u32x4 v) { __builtin_amdgcn_raw_buffer_store_b128(v, r, off, 0, HAUX); }
DEVI void hst64(rsrc_t r, unsigned off, u32x2 v) { __builtin_amdgcn_raw_buffer_store_b64(v, r, off, 0, HAUX); }
DEVI void drain_vm() { asm volatile("s_waitcnt vmcnt(0)" ::: "memory"); }
DEVI void cnt_add(char* ws, unsigned off, unsigned v = 1u) { __hip_atomic_fetch_add((unsigned*)(ws + off), v, __ATOMIC_RELAXED, __HIP_MEMORY_SCOPE_AGENT); }

constexpr int HSPIN_CAP = 1 << 18;
DEVI void wg_wait_s(rsrc_t r, unsigned off, unsigned n) {
  if (threadIdx.x == 0) {
#pragma nounroll
    for (int it = 0; it < HSPIN_CAP; ++it) {
      if (ld_sc1_32(r, off) >= n) break;
      __builtin_amdgcn_s_sleep(8);
    }
  }
  asm volatile("" ::: "memory");
  __syncthreads();
}
DEVI void wg_signal(char* ws, unsigned off) {
  drain_vm();
  __syncthreads();
  if (threadIdx.x == 0) cnt_add(ws, off);
}

DEVI u32x2 launch_no(const char* ws) {
  u32x2 e;
  asm volatile("s_load_dwordx2 %0, %1, 0x0\n s_waitcnt lgkmcnt(0)" : "=s"(e) : "s"(ws + OFF_EP) : "memory");
  return u32x2{e[0] + 1u, e[1] + 0x9e3779b1u};
}
DEVI void rec_put(rsrc_t rw, int i, u32x2 no) {
  drain_vm();
  __syncthreads();
  if (threadIdx.x == 0) st_sc1_64(rw, OFF_REC + (unsigned)i * 16u, no);
}
DEVI u32x4 rec_wait(rsrc_t rw, u32x2 no, unsigned mask, unsigned lo = 0u, unsigned own = 0u) {
  u32x4 r;
#pragma nounroll
  for (int it = 0; it < HSPIN_CAP; ++it) {
    asm volatile("" ::: "memory");
    r = ld_sc1(rw, OFF_REC + (own && __lane_id() <= own ? __lane_id() : (__lane_id() & mask) + lo) * 16u);
    if (__all(r[0] == no[0] && r[1] == no[1])) break;
    __builtin_amdgcn_s_sleep(8);
  }
  asm volatile("" ::: "memory");
  return r;
}
DEVI unsigned near_kind(rsrc_t rw, u32x2 no) {
  const unsigned lat = rec_wait(rw, no, 63u)[2], ls2 = (lat & 0xffffu) + (lat >> 16), l = __lane_id() & 7u;
  unsigned cost = 0u;
#pragma unroll
  for (int i = 0; i < 4; ++i) cost += __shfl(ls2, 32 + 8 * i + (l ^ i));
  cost = cost * 8u + l;
#pragma unroll
  for (int i = 1; i < 8; i *= 2) cost = min(cost, (unsigned)__shfl_xor(cost, i));
  return (__builtin_amdgcn_readfirstlane(cost) & 7u) ^ 4u;
}
DEVI unsigned far_bit(unsigned kind) { return ((kind >> 2) ^ 1u) | (kind << 1); }
DEVI void probe(char* ws, rsrc_t rw, int i) {
  const NearMap nm{0u};
  if (threadIdx.x == 0) {
    char* p0 = ws + nm(4u, i & 7, 40u + (i >> 3));
    unsigned mn[2] = {0xffffu, 0xffffu};
#pragma unroll 1
    for (int k = 0; k < NPROBE; ++k) {
#pragma unroll
      for (int hf = 0; hf < 2; ++hf) {
        const unsigned long long t0 = clk_now();
        const unsigned v = __hip_atomic_fetch_add((unsigned*)(p0 + hf * 2048), 1u, __ATOMIC_RELAXED, __HIP_MEMORY_SCOPE_AGENT);
        asm volatile("" :: "v"(v));
        const unsigned long long t1 = clk_now();
        mn[hf] = min(mn[hf], (unsigned)(t1 - t0));
      }
    }
    st_sc1_32(rw, OFF_REC + (unsigned)(32 + i) * 16u + 8u, mn[0] | (mn[1] << 16));
  }
}

template <int N>
DEVI void wait_vm() { asm volatile("s_waitcnt vmcnt(%0)" :: "n"(N) : "memory"); }
#define BPR 2
#define ISG 3
#define ESG 4
#define NTW 0
#define RN 1
#define NTO 0
#define OAUX 0
#define NTX 2
#define SCHED() __builtin_amdgcn_sched_barrier(0)
DEVI u32x4 rsrc4(const void* p, unsigned bytes) { return u32x4{(unsigned)(size_t)p, (unsigned)((size_t)p >> 32) & 0xffffu, bytes, 0x00020000u}; }
DEVI void bar() { asm volatile("s_waitcnt lgkmcnt(0)\n s_barrier" ::: "memory"); }
DEVI u32x2 tr_quad(const char* p) {
  return __builtin_bit_cast(u32x2, __builtin_amdgcn_ds_read_tr16_b64_v4i16((__attribute__((address_space(3))) s16x4*)(p)));
}
DEVI unsigned cvt2(float a, float b) {
  typedef float f32x2_ __attribute__((ext_vector_type(2)));
  typedef __bf16 bf16x2_ __attribute__((ext_vector_type(2)));
  return __builtin_bit_cast(unsigned, __builtin_convertvector(f32x2_{a, b}, bf16x2_));
}
DEVI unsigned cvt2x(float a, float b) { return min(cvt2(a, b), POISON - 1u); }
DEVI f32x4 mfu(u32x4 a, u32x4 b, f32x4 c) { return mfma16(__builtin_bit_cast(bf16x8, a), __builtin_bit_cast(bf16x8, b), c); }
#define PTR(i)
#define PTB(i)
template <bool swap>
DEVI unsigned proj_reg(char* lds, char* ws, rsrc_t rw, const u16* __restrict__ W, int h, int mat, int hw) {
  constexpr int nseg = NSEG_;
  const int rot = mat ? 0 : XS_;
  auto sgm = [&](int r) { return (r & 32) | ((r + ((r & 32) ? 0 : rot)) & 31); };
  int t = threadIdx.x;
  asm volatile("" : "+v"(t));
  const int l = t & 63, w = __builtin_amdgcn_readfirstlane(t >> 6), g = l >> 4, lo = l & 15, wn = w & 3, wk = w >> 2;
  const u32x4 rs = rsrc4(ws, WS_USED);
  const unsigned vo = (unsigned)l * 16u;
  auto wswz = [](int row) { return ((row & 3) | (((row >> 3) & 1) << 2)) << 1; };
  u32x4 b[16][2];
  const unsigned go = (unsigned)((512 * wk + 16 * wn + g) * HD_ + h * DH_) * 2u;
  const unsigned gv[2] = {go + (unsigned)((lo ^ wswz(g)) * 16), go + (unsigned)((lo ^ wswz(8 + g)) * 16)};
  auto ask = [&](int p) {
#pragma unroll
    for (int i = 0; i < 4; ++i)
      __builtin_amdgcn_global_load_lds((const unsigned*)((const char*)W + gv[i >> 1] + (size_t)(64 * p + 4 * i) * HD_ * 2), (unsigned*)(lds + (p + 4) % 5 * 32768 + w * 4096 + i * 1024 + l * 16), 16, 0, NTW);
    SCHED();
  };
#pragma unroll
  for (int p = 0; p < 5; ++p) ask(p);
  const u32x2 no = launch_no(ws);
  const char* bp = lds + (64 * wk + 8 * g + (lo >> 2)) * 256 + (l & 1) * 8;
  const int fq2 = wswz(8 * g + (lo >> 2));
  auto take = [&](int p) {
#pragma unroll
    for (int ks = 0; ks < 2; ++ks)
#pragma unroll
      for (int ct = 0; ct < 2; ++ct) {
        const char* p0 = bp + (p + 4) % 5 * 32768 + ks * 8192 + (((4 * wn + 2 * ct + ((l & 3) >> 1)) ^ fq2) * 16);
        const u32x2 x0 = tr_quad(p0), x1 = tr_quad(p0 + 1024);
        b[2 * p + ks][ct] = u32x4{x0[0], x0[1], x1[0], x1[1]};
      }
    SCHED();
  };
  unsigned hb = 0u;
  auto dma = [&](int i, int pc, auto&& mid) {
    auto* dst = (__attribute__((address_space(3))) void*)(LDS3 + ((i + 2) & 3) * 32768 + pc * 4096);
    const unsigned so = OFF_XF + far8(sgm(i >> 1) * 8 + (i & 1) * 4 + (pc >> 1), hb) + (unsigned)(pc & 1) * 4096u;
    const unsigned vd = i < 2 * nseg ? vo : 0x80000000u;
    __builtin_amdgcn_raw_ptr_buffer_load_lds(rw, dst, 16, vd, so, 0, 16);
    mid(0);
    __builtin_amdgcn_raw_ptr_buffer_load_lds(rw, dst, 16, vd, so, 1024, 16);
    mid(1);
    __builtin_amdgcn_raw_ptr_buffer_load_lds(rw, dst, 16, vd, so, 2048, 16);
    mid(2);
    __builtin_amdgcn_raw_ptr_buffer_load_lds(rw, dst, 16, vd, so, 3072, 16);
    mid(3);
  };
  auto none = [](int) {};
  u32x4 f[1];
#pragma unroll
  for (int n = 0; n < 8; ++n) {
    asm volatile("s_waitcnt vmcnt(%0) lgkmcnt(0)\n s_barrier" :: "n"(4 * (n == 0 ? 4 : (n < 5 ? 3 : 7 - n)) + (n > RN && n < RN + 4 ? 1 : 0) + (n > RN + 4 ? 8 : 0)) : "memory");
    if (n == RN) asm volatile("buffer_load_dwordx4 %0, %1, %2, %3 offen sc1" : "=&v"(f[0]) : "v"(vo & 511u), "s"(rs), "s"(OFF_REC) : "memory");
    if (n >= 1 && n < 4) ask(n + 4);
    if (n == RN + 4) {
      asm volatile("" : "+v"(f[0]));
      if (!__all(f[0][0] == no[0] && f[0][1] == no[1])) rec_wait(rw, no, 31u);
      dma(0, w, none); dma(1, w, none);
      SCHED();
    }
    if (n == 7) PTR(17);
    take(n);
  }
  bar();
  const u32x4 zero4 = u32x4{0u, 0u, 0u, 0u};
  if (wk) {
    dma(2, wn, none);
    st_sc1(rw, 0x80000000u, zero4);
    if (!swap) st_sc1(rw, 0x80000010u, zero4);
    wait_vm<swap ? 5 : 6>();
  } else wait_vm<0>();
  __builtin_amdgcn_s_waitcnt(0xc07f);
  __builtin_amdgcn_s_barrier();
  PTR(19);
  const unsigned lb = (unsigned)(size_t)LDS3 + (unsigned)(l * 16);
  const unsigned ab = lb + (unsigned)(wk * 16384), sc = lb + 131072u + (unsigned)(wn * 2048);
  u32x4 a[8];
  auto rd = [&](int j, unsigned ad) { asm volatile("ds_read_b128 %0, %1 offset:%2" : "=v"(a[j & 7]) : "v"(ad), "n"((j & 15) * 1024) : "memory"); };
  if (wk == 0) {
#pragma unroll
    for (int j = 0; j < 8; ++j) rd(j, ab + 65536u);
    asm volatile("s_waitcnt lgkmcnt(0)" : "+v"(a[0]), "+v"(a[1]), "+v"(a[2]), "+v"(a[3]), "+v"(a[4]), "+v"(a[5]), "+v"(a[6]), "+v"(a[7]));
#pragma nounroll
    for (int k = 0; k < 2 * nseg; ++k) {
      const unsigned a0 = ab + (unsigned)(((k + 2) & 3) * 32768), a1 = ab + (unsigned)(((k + 3) & 3) * 32768);
      asm volatile("" :: "v"(a0), "v"(a1));
      wait_vm<8>();
      __builtin_amdgcn_s_setprio(1);
      __builtin_amdgcn_s_barrier();
      f32x4 c[2];
      SCHED();
#pragma unroll
      for (int j = 0; j < 16; ++j) {
#pragma unroll
        for (int ct = 0; ct < 2; ++ct) {
          const f32x4 z = j ? c[ct] : f32x4{0.f, 0.f, 0.f, 0.f};
          c[ct] = swap ? mfu(b[j][ct], a[j & 7], z) : mfu(a[j & 7], b[j][ct], z);
        }
        SCHED();
        rd(j + 8, j < 8 ? a0 : a1);
        if (j & 1) asm volatile("s_waitcnt lgkmcnt(5)" : "+v"(a[(j + 2) & 7]), "+v"(a[(j + 3) & 7]));
        SCHED();
      }
      __builtin_amdgcn_s_setprio(2);
      dma(k + 2, 4 + wn, none);
      SCHED();
      asm volatile("s_nop 7\n ds_write_b128 %0, %1\n ds_write_b128 %0, %2 offset:1024\n s_waitcnt lgkmcnt(0)" :: "v"(sc + (unsigned)((k & 1) * 8192)), "v"(c[0]), "v"(c[1]) : "memory");
      if (__builtin_expect(k == 8, 0)) hb = far_bit(near_kind(rw, no));
    }
    asm volatile("" : "+v"(a[0]), "+v"(a[1]), "+v"(a[2]), "+v"(a[3]), "+v"(a[4]), "+v"(a[5]), "+v"(a[6]), "+v"(a[7]));
    wait_vm<8>();
    __builtin_amdgcn_s_barrier();
  } else {
    const unsigned oo = (mat == 0 ? OFF_Q : (mat == 1 ? OFF_K : OFF_VX)) + (swap ? wn * 1024u : (wn >> 1) * HALF_ + (wn & 1) * 2048u);
    const unsigned v0 = l == 0 ? 0u : 0x80000000u;
    auto signal = [&](int r, unsigned va) {
      __builtin_amdgcn_raw_ptr_buffer_atomic_add_i32(swap ? (mat ? 1 : 65536) : 256, rw, va, OFF_CKV + (unsigned)(h * 64 + sgm(r)) * 4u, 0);
    };
    f32x4 c[2];
    u32x2 v[2];
    c[0] = c[1] = f32x4{0.f, 0.f, 0.f, 0.f};
    v[0] = v[1] = u32x2{0u, 0u};
    auto put = [&](int r, int tt, unsigned vs) {
      const unsigned so = oo + tile8(sgm(r), h, hb);
      const u32x2 p0 = u32x2{cvt2x(c[0][0], c[0][1]), cvt2x(c[0][2], c[0][3])}, p1 = u32x2{cvt2x(c[1][0], c[1][1]), cvt2x(c[1][2], c[1][3])};
      if (swap) st_sc1s(rw, vs, so + (unsigned)tt * HALF_, u32x4{p0[0], p0[1], p1[0], p1[1]});
      else if (tt == 0) { v[0] = p0; v[1] = p1; }
      else {
        st_sc1s(rw, vs, so, u32x4{v[0][0], v[0][1], p0[0], p0[1]});
        st_sc1s(rw, vs, so + 1024u, u32x4{v[1][0], v[1][1], p1[0], p1[1]});
      }
    };
    __builtin_amdgcn_s_setprio(3);
    __builtin_amdgcn_s_barrier();
    rec_wait(rw, no, 3u, 64u + 4u * h);
    dma(3, wn, none);
    if (swap) st_sc1(rw, 0x80000000u, zero4);
#pragma nounroll
    for (int r = 0; r < nseg; ++r) {
#pragma unroll
      for (int tt = 0; tt < 2; ++tt) {
        const int i = 2 * r + tt;
        const unsigned a0 = ab + (unsigned)(((i + 2) & 3) * 32768);
        asm volatile("" :: "v"(a0));
        if (tt == 0) wait_vm<swap ? 5 : 4>(); else wait_vm<swap ? 6 : 7>();
        __builtin_amdgcn_s_barrier();
        if (tt == 1 && __builtin_expect(r < 2 && mat, 0)) __builtin_amdgcn_raw_ptr_buffer_atomic_add_i32(1 << 24, rw, v0, OFF_CKV + (unsigned)(h * 64 + r) * 4u, 0);
        if (__builtin_expect(i == (rot ? 1 : 4), 0)) hb = far_bit(near_kind(rw, no));
        if (tt == 0 && __builtin_expect(r == 14 - rot, 0)) {
#pragma nounroll
          for (int it = 0; it < HSPIN_CAP; ++it) {
            if (__builtin_amdgcn_readfirstlane(ld_sc1_32(rw, OFF_CF + 384u)) >= 32u) break;
            __builtin_amdgcn_s_sleep(16);
          }
          asm volatile("" ::: "memory");
        }
        if (tt == 1 && __builtin_expect(r > ISG && r <= ESG, 0)) { drain_vm(); signal(r - 1, v0); }
        f32x4 n[2];
        asm volatile("ds_read_b128 %0, %2\n ds_read_b128 %1, %2 offset:1024" : "=&v"(n[0]), "=&v"(n[1]) : "v"(sc + (unsigned)(tt * 8192)) : "memory");
        dma(i + 4, wn, [&](int q) { rd(q, a0); });
        if (tt == 0) put(r - 1, 1, r > 0 ? vo : 0x80000000u); else put(r, 0, vo);
        rd(4, a0);
        if (tt == 0) signal(r - 2, r >= ESG + 2 ? v0 : 0x80000000u);
        rd(5, a0); rd(6, a0); rd(7, a0);
        if (tt == 0 || __builtin_expect(r >= BPR, 1)) __builtin_amdgcn_s_setprio(0);
        SCHED();
        asm volatile("s_waitcnt lgkmcnt(5)" : "+v"(n[0]), "+v"(n[1]), "+v"(a[0]), "+v"(a[1]), "+v"(a[2]));
        c[0] = n[0]; c[1] = n[1];
#pragma unroll
        for (int j = 0; j < 16; ++j) {
#pragma unroll
          for (int ct = 0; ct < 2; ++ct) c[ct] = swap ? mfu(b[j][ct], a[j & 7], c[ct]) : mfu(a[j & 7], b[j][ct], c[ct]);
          SCHED();
          if (j < 8) rd(j + 8, a0);
          if ((j & 1) && j < 14) asm volatile("s_waitcnt lgkmcnt(%2)" : "+v"(a[(j + 2) & 7]), "+v"(a[(j + 3) & 7]) : "n"(j < 8 ? 5 : (j < 13 ? 12 - j : 0)));
          SCHED();
        }
        __builtin_amdgcn_s_setprio(3);
      }
      if (__builtin_expect(r < ISG, 0)) {
        put(r, 1, vo);
        if (r < 2) PTB(r ? 20 : 8);
        if (r >= 2) {
          drain_vm();
          if (r == 2) { signal(0, v0); signal(1, v0); }
          signal(r, v0);
        }
      }
    }
    put(nseg - 1, 1, vo);
    drain_vm();
    signal(nseg - 2, v0); signal(nseg - 1, v0);
  }
  __builtin_amdgcn_s_setprio(0);
  drain_vm();
  __syncthreads();
  return hb;
}

constexpr int SPIN_CAP = 1 << 19;
#define LOCK 0
#define DLYS 9, 7, 9, 8, 7, 7, 33, 7, 320, 120, 12
enum { DLY1, DLY2, DLYV, DLY3, XDL, QDL, DLYL, DLYW, XT1, XTL, DLYD, KG3, KG1, DLYG, KG2, KGV, DLYR = QDL };
#define S3 0
#define NOMIN 0
#define TID 0
#define EARLY 1
#define L2 1
#define L3 1
#define PRE3 LOCK
#define RSE 0

#define WB1 1
#define WB2 3
#define ALG 1
#define PBA 6
#define PBN 6
#define ESL 2
#define LSL 0
#define KSL 8
constexpr int PQK_OFF = 0;
constexpr int W3_OFF = 98304;
constexpr int KV_OFF = 98304;
constexpr int PS_OFF = 131072;
constexpr int PZ_OFF = PS_OFF + 4096;
constexpr int UM_OFF = PZ_OFF + 1024;
constexpr int KC_OFF = UM_OFF + 3072;
constexpr int XV_OFF = KC_OFF + 1024;
constexpr int OB_OFF = XV_OFF + 2048;
constexpr int XK_OFF = OB_OFF + 2048;
constexpr float CS = SCALE_ * 1.4426950408889634f;
typedef __bf16 bfv8 __attribute__((ext_vector_type(8)));
typedef __bf16 bfv16 __attribute__((ext_vector_type(16)));
typedef float f32x2 __attribute__((ext_vector_type(2)));
typedef unsigned u32x8 __attribute__((ext_vector_type(8)));
DEVI f32x4 smf(u32x4 a, u32x8 b, f32x4 c, int idx) {
  return __builtin_amdgcn_smfmac_f32_16x16x64_bf16(__builtin_bit_cast(bfv8, a), __builtin_bit_cast(bfv16, b), c, idx, 0, 0);
}
DEVI float fold8(float a) { return a + __uint_as_float(__builtin_amdgcn_update_dpp(0u, __float_as_uint(a), 0x128, 0xf, 0xf, true)); }
DEVI f32x4 zero4() { f32x4 z = f32x4{0.f, 0.f, 0.f, 0.f}; asm volatile("" : "+v"(z)); return z; }
DEVI u32x4 cvt8(f32x4 a, f32x4 b) { return u32x4{cvt2(a[0], a[1]), cvt2(a[2], a[3]), cvt2(b[0], b[1]), cvt2(b[2], b[3])}; }

DEVI float vmax(unsigned a, unsigned b) { float r; asm("v_max_f32 %0, %1, %2" : "=v"(r) : "v"(a), "v"(b)); return r; }
DEVI float rows_max(float x) {
  const unsigned u = __float_as_uint(x);
  auto a = __builtin_amdgcn_permlane16_swap(u, u, false, false);
  const unsigned u2 = __float_as_uint(vmax(a[0], a[1]));
  auto b = __builtin_amdgcn_permlane32_swap(u2, u2, false, false);
  return vmax(b[0], b[1]);
}
DEVI float rows_sum(float x) {
  const unsigned u = __float_as_uint(x);
  auto a = __builtin_amdgcn_permlane16_swap(u, u, false, false);
  x = __uint_as_float(a[0]) + __uint_as_float(a[1]);
  const unsigned u2 = __float_as_uint(x);
  auto b = __builtin_amdgcn_permlane32_swap(u2, u2, false, false);
  return __uint_as_float(b[0]) + __uint_as_float(b[1]);
}

DEVI void req4(u32x4 r, unsigned vo, unsigned so, u32x4* v) {
  asm volatile("buffer_load_dwordx4 %0, %4, %5, %6 offen sc1\n buffer_load_dwordx4 %1, %4, %5, %6 offen offset:1024 sc1\n"
               "buffer_load_dwordx4 %2, %4, %5, %6 offen offset:2048 sc1\n buffer_load_dwordx4 %3, %4, %5, %6 offen offset:3072 sc1"
               : "=&v"(v[0]), "=&v"(v[1]), "=&v"(v[2]), "=&v"(v[3]) : "v"(vo), "s"(r), "s"(so) : "memory");
}
#define ILV 4
DEVI void req4i(u32x4 r, unsigned vo, unsigned so, u32x4* v) {
  asm volatile("buffer_load_dwordx4 %0, %5, %6, %4 offen sc1\n s_xor_b32 %4, %4, 0x400\n s_nop %7\n buffer_load_dwordx4 %1, %5, %6, %4 offen sc1\n s_xor_b32 %4, %4, 0xc00\n s_nop %7\n"
               "buffer_load_dwordx4 %2, %5, %6, %4 offen sc1\n s_xor_b32 %4, %4, 0x400\n s_nop %7\n buffer_load_dwordx4 %3, %5, %6, %4 offen sc1"
               : "=&v"(v[0]), "=&v"(v[1]), "=&v"(v[2]), "=&v"(v[3]), "+s"(so) : "v"(vo), "s"(r), "n"(ILV ? ILV - 1 : 0) : "memory", "scc");
}
DEVI u32x4 rs_tid(u32x4 r) { return u32x4{r[0], r[1] | (16u << 16), r[2] >> 4, 1u << 23}; }
DEVI void req4t(u32x4 r, unsigned so, u32x4* v) {
  asm volatile("buffer_load_dwordx4 %0, off, %4, %5 sc1\n buffer_load_dwordx4 %1, off, %4, %5 offset:1024 sc1\n"
               "buffer_load_dwordx4 %2, off, %4, %5 offset:2048 sc1\n buffer_load_dwordx4 %3, off, %4, %5 offset:3072 sc1"
               : "=&v"(v[0]), "=&v"(v[1]), "=&v"(v[2]), "=&v"(v[3]) : "s"(r), "s"(so) : "memory");
}
DEVI void req2t(u32x4 r, unsigned so, u32x4* v) {
  asm volatile("buffer_load_dwordx4 %0, off, %2, %3 sc1\n buffer_load_dwordx4 %1, off, %2, %3 offset:1024 sc1" : "=&v"(v[0]), "=&v"(v[1]) : "s"(r), "s"(so) : "memory");
}
DEVI void req2(u32x4 r, unsigned vo, unsigned so, u32x4* v) {
  asm volatile("buffer_load_dwordx4 %0, %2, %3, %4 offen sc1\n buffer_load_dwordx4 %1, %2, %3, %4 offen offset:1024 sc1"
               : "=&v"(v[0]), "=&v"(v[1]) : "v"(vo), "s"(r), "s"(so) : "memory");
}
template <int N>
DEVI void got(u32x4& v) { asm volatile("s_waitcnt vmcnt(%1)" : "+v"(v) : "n"(N)); }
#define A8 ".p2align 3\n"
#define A4 ".p2align 3\n s_nop 0\n"
#define STR_(x) #x
#define STR(x) STR_(x)
#define MF "v_mfma_f32_16x16x32_bf16 "
#define CV "v_cvt_pk_bf16_f32 "
#define SM "v_smfmac_f32_16x16x64_bf16 "
template <int N>
DEVI void got_s4(u32x4& a, f32x4* c, u32x8 b0, u32x8 b1, u32x8 b2, u32x8 b3, int idx) {
  asm volatile(".if %10 == 3 + " STR(LOCK) "\n" A4 ".endif\n s_waitcnt vmcnt(%10)\n" SM "%1, %0, %5, %9\n" SM "%2, %0, %6, %9\n" SM "%3, %0, %7, %9\n" SM "%4, %0, %8, %9"
               : "+v"(a), "+v"(c[0]), "+v"(c[1]), "+v"(c[2]), "+v"(c[3]) : "v"(b0), "v"(b1), "v"(b2), "v"(b3), "v"(idx), "n"(N));
}
template <int N>
DEVI void got_s2(u32x4& a, f32x4* c, u32x8 b0, u32x8 b1, int idx) {
  asm volatile(".if %6 == 1\n" A4 ".endif\n s_waitcnt vmcnt(%6)\n" SM "%1, %0, %3, %5\n" SM "%2, %0, %4, %5" : "+v"(a), "+v"(c[0]), "+v"(c[1]) : "v"(b0), "v"(b1), "v"(idx), "n"(N));
}
template <int N>
DEVI void got_x(u32x4& x, f32x4& sa, f32x4& sb, f32x4& sm, u32x4 ka, u32x4 kb) {
  asm volatile("s_waitcnt vmcnt(%6)\n" MF "%1, %4, %0, %1\n" MF "%2, %5, %0, %2\n" MF "%3, %0, %0, %3" : "+v"(x), "+v"(sa), "+v"(sb), "+v"(sm) : "v"(ka), "v"(kb), "n"(N));
}
template <int N>
DEVI void got_m(u32x4& b, f32x4& d, u32x4 a) { asm volatile("s_waitcnt vmcnt(%3)\n" MF "%1, %2, %0, %1" : "+v"(b), "+v"(d) : "v"(a), "n"(N)); }
DEVI unsigned umax3(unsigned a, unsigned b, unsigned c) { return max(max(a, b), c); }
DEVI unsigned umax5(u32x4 v, unsigned m) { return umax3(v[2], v[3], umax3(v[0], v[1], m)); }
DEVI bool bad(unsigned m) { return __builtin_expect(__builtin_amdgcn_ballot_w64(m == POISON) != 0ull, 0); }
DEVI bool over(unsigned m, unsigned thr) { return __builtin_expect(__builtin_amdgcn_ballot_w64(m > thr) != 0ull, 0); }
DEVI bool bad4(const u32x4* v) { return bad(umax5(v[3], umax5(v[2], umax5(v[1], umax5(v[0], 0u))))); }
DEVI unsigned tag5(u32x4 v, unsigned t, unsigned m) {
  return __builtin_amdgcn_bitop3_b32(v[0], v[1], t, 0x7e) | __builtin_amdgcn_bitop3_b32(v[2], v[3], t, 0x7e) | m;
}
DEVI unsigned rt_now() {
  unsigned long long t;
  asm volatile("s_memrealtime %0\n s_waitcnt lgkmcnt(0)" : "=s"(t) :: "memory");
  return (unsigned)t;
}
#define DLYARG
#define DLYA
#define DLYI(mask_) unsigned dly[16] = {DLYS}; _Pragma("unroll") for (int i = 0; i < 16; ++i) if (((mask_) >> i) & 1) asm volatile("" : "+s"(dly[i]));
#define PAL 6
DEVI void pause(unsigned n) { asm volatile(".p2align %1\n 1: s_sub_u32 %0, %0, 1\n s_cbranch_scc0 1b" : "+s"(n) : "n"(PAL) : "scc", "memory"); }
DEVI unsigned mt_now() {
  unsigned long long t;
  asm volatile("s_memtime %0\n s_waitcnt lgkmcnt(0)" : "=s"(t) :: "memory");
  return (unsigned)t;
}
DEVI unsigned gt_now() {
  unsigned long long b, c, d;
  asm volatile("s_memrealtime %0\n s_nop 6\n s_memrealtime %1\n s_nop 6\n s_memrealtime %2\n s_waitcnt lgkmcnt(0)" : "=&s"(b), "=&s"(c), "=&s"(d) :: "memory");
  return ((unsigned)b + (unsigned)c + (unsigned)d) * 8u;
}
DEVI void clk_pair(unsigned& t, unsigned& gt) {
  unsigned long long a;
  asm volatile("s_memtime %0" : "=s"(a) :: "memory");
  gt = gt_now();
  asm volatile("" : "+s"(a));
  t = (unsigned)a;
}
DEVI unsigned local_of(unsigned tgt, int rq, unsigned t, unsigned g) {
  int d = (int)(tgt - g);
  asm("s_max_i32 %0, %0, 0xffffc000\n s_min_i32 %0, %0, 0x4000" : "+s"(d) :: "scc");
  return t + (unsigned)(d * rq >> 16);
}
DEVI int until(unsigned tgt, int dmin, unsigned now) {
  const int left = (int)(tgt - now);
  int d;
  asm volatile("s_max_i32 %0, %1, %3\n s_min_i32 %0, %0, 4000\n"
               ".p2align %2\n 1: s_sub_u32 %0, %0, 28\n s_cbranch_scc0 1b\n s_cmp_lt_i32 %0, -20\n s_cbranch_scc1 2f\n s_cmp_lt_i32 %0, -12\n s_cbranch_scc1 2f\n"
               "s_cmp_lt_i32 %0, -4\n s_cbranch_scc1 2f\n s_nop 0\n s_branch 2f\n 2:" : "=&s"(d) : "s"(left), "n"(PAL), "s"(dmin) : "scc", "memory");
  return left;
}
DEVI int until(unsigned tgt, int dmin) { return until(tgt, dmin, mt_now()); }
constexpr int TG_OFF = KC_OFF + 16, GP_OFF = XK_OFF + 3072;
#define KTHR 200
#define KFOL 0
#define KPEN 600
#define KLATE 2050

#define TRD
#define TR(i)
#define TRW()
#define TRE(b)
#define TRARGD
#define TRARG
#define SLP(i)
#define TRT()

#define T0(i)
#define TP(i)
#define XM 0
#define TRX(n) do { if (XM == (n)) TR(7); } while (0)
#define TRY(n) do { if (XM == (n)) TR(4); } while (0)
DEVI unsigned kv_want(int s) { return s < XS_ ? 0x404u : 0x40404u; }
DEVI void kv_wait(u32x4 rs, int s, int h, int& budget) {
  const unsigned want = s > 1 ? kv_want(s) : 8u << 24;
  while (true) {
    unsigned c;
    asm volatile("buffer_load_dword %0, off, %1, %2 sc1\n s_waitcnt vmcnt(0)" : "=v"(c) : "s"(rs), "s"(OFF_CKV + (unsigned)(h * 64 + s) * 4u) : "memory");
    if ((__builtin_amdgcn_readfirstlane(c) & want) == want || --budget <= 0) break;
    __builtin_amdgcn_s_sleep(4);
  }
}

DEVI unsigned kv_src(int s, int h, int w, unsigned hb) { return ((w >> 1) ? OFF_VX : OFF_K) + tile8(s, h, hb) + (w & 1) * HALF_; }
DEVI unsigned kv_dst(int s, int w) { return (unsigned)(size_t)LDS3 + KV_OFF + (s & 1) * 16384 + w * 4096; }
DEVI void kv_dma(u32x4 rs, unsigned dvo, unsigned so, unsigned dst) {
  asm volatile("s_mov_b32 m0, %3\n s_nop 0\n buffer_load_dwordx4 %0, %1, %2 offen sc1 lds\n buffer_load_dwordx4 %0, %1, %2 offen offset:1024 sc1 lds\n"
               "buffer_load_dwordx4 %0, %1, %2 offen offset:2048 sc1 lds\n buffer_load_dwordx4 %0, %1, %2 offen offset:3072 sc1 lds"
               :: "v"(dvo), "s"(rs), "s"(so), "s"(dst) : "memory");
}

DEVI unsigned ovoff(unsigned lv) { return (lv & 128u) * 8u + (lv & 0x370u); }
DEVI unsigned osoff(int s, int h, int jj, unsigned hb) { return ooff(s, h, hb) + (jj >> 1) * HALF_ + (jj & 1) * 128u; }
DEVI void ostore(u32x4 rs, const u32x4* oc, unsigned ov, unsigned os) {
  asm volatile("buffer_store_dwordx4 %0, %2, %3, %4 offen sc1\n buffer_store_dwordx4 %1, %2, %3, %4 offen offset:2048 sc1\n s_nop 1" :: "v"(oc[0]), "v"(oc[1]), "v"(ov), "s"(rs), "s"(os) : "memory");
}
DEVI void kv_first(char* lds, u32x4 rs, int sg, int h, int w, unsigned lv, int& budget) {
  u32x4 t[4];
  do {
    req4(rs, lv ^ (unsigned)(w >> 1) * (((lv >> 3) & 32u) + ((lv >> 2) & 128u)), kv_src(sg, h, w, 0u), t);
    asm volatile("s_waitcnt vmcnt(0)" : "+v"(t[0]), "+v"(t[1]), "+v"(t[2]), "+v"(t[3]));
  } while (bad4(t) && --budget > 0);
#pragma unroll
  for (int i = 0; i < 4; ++i) *(u32x4*)(lds + KV_OFF + sg * 16384 + w * 4096 + i * 1024 + lv) = t[i];
}
DEVI float negmax6(const float* e) {
  float mx, t0, t1;
  asm("v_max3_f32 %0, %3, %4, %5\n v_max3_f32 %1, %6, %7, %8\n v_max_f32 %0, %0, %1\n s_nop 1\n"
      "v_max_f32_dpp %1, %0, %0 row_ror:8 row_mask:0xf bank_mask:0xf\n v_max_f32_dpp %2, %0, %0 row_ror:8 row_mask:0xf bank_mask:0xf\n s_nop 1\n"
      "v_permlane16_swap_b32 %1, %2\n v_max_f32 %0, %1, %2\n v_max_f32 %1, %1, %2\n s_nop 1\n"
      "v_permlane32_swap_b32 %0, %1\n v_max_f32 %0, %0, %1\n v_mul_f32 %0, %9, %0"
      : "=&v"(mx), "=&v"(t0), "=&v"(t1) : "v"(e[0]), "v"(e[1]), "v"(e[2]), "v"(e[3]), "v"(e[4]), "v"(e[5]), "s"(-CS));
  return mx;
}

DEVI void recur_p1(char* lds, rsrc_t rw, const u32x4 rs, const u16* __restrict__ mem0, const u16* __restrict__ Wq, const u16* __restrict__ Wk,
                   const u16* __restrict__ Wv, u16* __restrict__ out, const char* ws, int wg TRARGD DLYARG) {
  DLYI((LOCK ? 0x7849 : 0x49) | (RSE ? 0x20 : 0));
  const int l = threadIdx.x & 63, w = __builtin_amdgcn_readfirstlane(threadIdx.x >> 6) & 3, g = l >> 4, lo = l & 15;
  const int h = wg >> 2, jj = wg & 3;
  int budget = SPIN_CAP;
  unsigned te = 0u, ge = 0u, gpo = 0u, t1g = 0u, t3g = 0u;
  int kq = 0, rq = 0;
  if (LOCK && w == 1) clk_pair(te, ge);
  unsigned mz = 0u;
  if (ALG) asm volatile("" : "+s"(mz));
  const int vq = 2 * (~jj & 1) + (jj >> 1);
  u32x8 w1[6][4];
  u32x4 a[4];
  {
    char* wl = lds + w * 24576;
    const char* g1 = (const char*)((jj & 2) ? Wk : Wq) + (size_t)((256 * w + (l >> 3)) * HD_ + h * DH_ + 64 * (jj & 1) + ((l & 7) ^ (2 * g)) * 8) * 2u;
    const char* g2 = (const char*)Wv + (size_t)((256 * w + (l >> 2)) * HD_ + h * DH_ + 32 * vq + (l & 3) * 8) * 2u;
    const unsigned mo = (unsigned)((2 * (lo >> 2) + (lo & 1)) * D_ + 256 * w + 16 * g + 4 * (lo & 2)) * 2u;
    const int kx = ILV ? w : 0;
    asm volatile("buffer_load_dwordx4 %0, %4, %5, %6 offen\n buffer_load_dwordx4 %1, %4, %5, %7 offen\n"
                 "buffer_load_dwordx4 %2, %4, %5, %8 offen\n buffer_load_dwordx4 %3, %4, %5, %9 offen"
                 : "=&v"(a[0]), "=&v"(a[1]), "=&v"(a[2]), "=&v"(a[3]) : "v"(mo), "s"(rsrc4(mem0, MEM_ * D_ * 2u)), "s"(kx * 128), "s"((kx ^ 1) * 128), "s"((kx ^ 2) * 128), "s"((kx ^ 3) * 128) : "memory");
    auto ask = [&](int n) {
#pragma unroll
      for (int i = 0; i < 4; ++i)
        __builtin_amdgcn_global_load_lds((const unsigned*)(n % 3 < 2 ? g1 + (size_t)(64 * ((n / 3) ^ kx) + 32 * (n % 3) + 8 * i) * HD_ * 2 : g2 + (size_t)(64 * ((n / 3) ^ kx) + 16 * i) * HD_ * 2),
                                         (unsigned*)(wl + (n % 6) * 4096 + i * 1024 + l * 16), 16, 0, NTW);
      SCHED();
    };
    const int row = 16 * (g >> 1) + 4 * (g & 1) + (lo >> 2);
    const char* b1 = wl + row * 128 + (lo & 3) * 8;
    const char* b2 = wl + row * 64 + (lo & 3) * 8;
    auto use = [&](int n) {
#pragma unroll
      for (int i = 0; i < 8; ++i) {
        const int u = n % 3 < 2 ? i >> 1 : 4 + (i >> 2), q = n % 3 < 2 ? 2 * (n % 3) + (i & 1) : i & 3;
        const u32x2 v = tr_quad(n % 3 < 2 ? b1 + (n % 6) * 4096 + ((u ^ ((row >> 1) & 3)) * 32) + (q & 1) * 1024
                                          : b2 + (n % 6) * 4096 + (u & 1) * 32 + (32 * (q >> 1) + 8 * (q & 1)) * 64);
        w1[u][n / 3][(q & 1) + 4 * (q >> 1)] = v[0];
        w1[u][n / 3][(q & 1) + 4 * (q >> 1) + 2] = v[1];
      }
      __builtin_amdgcn_s_waitcnt(0xc07f);
      asm volatile("" ::: "memory");
      SCHED();
    };
#pragma unroll
    for (int n = 0; n < 6; ++n) ask(n);
#pragma unroll
    for (int n = 0; n < 12; ++n) {
      if (n == WB1 || n == WB2) __builtin_amdgcn_s_barrier();
      asm volatile("s_waitcnt vmcnt(%0)" :: "n"(n < 7 ? 20 : 44 - 4 * n) : "memory");
      if (n == 0) asm volatile("" : "+v"(a[0]), "+v"(a[1]), "+v"(a[2]), "+v"(a[3]));
      if (n == 0 || n == 11) TP(n == 11);
      use(n);
      if (n < 6) ask(n + 6);
    }
    TP(2);
    if (LOCK && w == 1) *(u32x4*)(lds + TG_OFF) = u32x4{te, ge, 61000u, te};
    bar();
  }
  const int idx = (lo & 2) ? 0xeeee : 0x4444;
  const unsigned vo = (unsigned)l * 16u;
  const unsigned pq0 = (unsigned)(size_t)LDS3 + PQK_OFF + (w * 960 + 2 * g * 40 + 8 * (lo >> 2) + (lo & 3)) * 4;
  const unsigned r1w = NearMap::blk(NB1 + jj) + h * 128u;
  unsigned mvo = 0u, r1r = 0u, r3r = 0u, r2b = 0u, hb = 0u;
  unsigned tg = 0u;
  int known = 0;
  unsigned qs = 0u;
  unsigned sc = 0u, sn = SET_;
  u32x4 rt = rs_tid(rs);
  if (TID) asm volatile("" : "+s"(rt));
  u32x4 zv = u32x4{0u, 0u, 0u, 0u};
  if (EARLY) asm volatile("" : "+v"(zv));
  unsigned anv = 0x80000000u;
  auto announce = [&](unsigned t, int db) {
    db += (int)dly[KG1];
    const u32x4 an = u32x4{t + (unsigned)(db * kq >> 16), t + (unsigned)((db + (int)dly[KG2]) * kq >> 16), t + (unsigned)((db + (int)dly[KG3]) * kq >> 16), 0u};
    asm volatile("buffer_store_dwordx4 %0, %1, %2, %3 offen sc1\n s_nop 1" :: "v"(an), "v"(anv), "s"(rs), "s"(4u * SET_ + NearMap::blk(NB1) + (unsigned)(wg >> 3) * 128u) : "memory");
  };
  TRD;
  for (int s = 0; s < NSEG_; ++s) {
    f32x4 acc[6];

    TR(0);
    unsigned m, thr, pq1 = pq0;
    asm volatile("" : "+v"(pq1));
    pq1 += 1280u;
    asm volatile("" : "+v"(pq1));
    unsigned kl;
    const int late = 0;
    asm volatile("ds_read_b32 %0, %1" : "=v"(kl) : "v"((unsigned)(size_t)LDS3 + KC_OFF + 8u) : "memory");
    if (s > 0) pause(dly[DLY1]);
    {
      unsigned so0 = r1r + sc, th0 = s > 0 ? POISON - 1u : POISON, so;
      thr = th0;
      asm volatile("s_cmp_eq_u32 %3, -1\n s_cbranch_scc1 8f\n 7: s_mov_b32 %1, %2\n"
                   "buffer_load_dwordx4 %5, %15, %16, %1 offen sc1\n s_xor_b32 %1, %1, 0x400\n s_nop %18\n buffer_load_dwordx4 %6, %15, %16, %1 offen sc1\n s_xor_b32 %1, %1, 0xc00\n s_nop %18\n"
                   "buffer_load_dwordx4 %7, %15, %16, %1 offen sc1\n s_xor_b32 %1, %1, 0x400\n s_nop %18\n buffer_load_dwordx4 %8, %15, %16, %1 offen sc1\n"
                   "s_add_i32 %0, %0, -1\n s_cmp_gt_i32 %0, 0\n s_cselect_b32 %4, %3, -1\n 8:"
                   MF "%9, %17, %17, 0\n" MF "%10, %17, %17, 0\n" MF "%11, %17, %17, 0\n" MF "%12, %17, %17, 0\n" MF "%13, %17, %17, 0\n" MF "%14, %17, %17, 0\n s_nop 7"
                   : "+s"(budget), "=&s"(so), "+s"(so0), "+s"(th0), "+s"(thr), "+v"(a[0]), "+v"(a[1]), "+v"(a[2]), "+v"(a[3]),
                     "=&v"(acc[0]), "=&v"(acc[1]), "=&v"(acc[2]), "=&v"(acc[3]), "=&v"(acc[4]), "=&v"(acc[5]) : "v"(vo), "s"(TID ? rt : rs), "v"(zv), "n"(ILV - 1) : "memory", "scc");
#define PIECE(i, al, chk, ...) \
      asm volatile(al "s_waitcnt vmcnt(%16)\n" SM "%1, %0, %6, %10\n" SM "%2, %0, %7, %10\n" SM "%3, %0, %8, %10\n" SM "%4, %0, %9, %10\n" chk \
                   : "+v"(a[i]), "+v"(acc[0]), "+v"(acc[1]), "+v"(acc[2]), "+v"(acc[3]), "+v"(m) \
                   : "v"(w1[0][i]), "v"(w1[1][i]), "v"(w1[2][i]), "v"(w1[3][i]), "v"(idx), "v"(a[i][0]), "v"(a[i][1]), "v"(a[i][2]), "v"(a[i][3]), "s"(thr), "n"(3 - i) __VA_ARGS__ : "vcc")
#define CK0 "v_max3_u32 %5, %11, %12, %13\n v_max_u32 %5, %5, %14\n"
#define CK1 "v_max3_u32 %5, %11, %12, %5\n v_max3_u32 %5, %13, %14, %5\n"
#define CKB "v_cmp_lt_u32 vcc, %15, %5\n s_cbranch_vccnz 7b\n"
      asm volatile("" : "=v"(m));
      SCHED();
      PIECE(0, A4, CK0 CKB);
      SCHED();
      PIECE(1, "", CK0);
      SCHED();
      PIECE(2, "", CK1);
      SCHED();
      PIECE(3, "", CK1 CKB, , "s"(so0), "s"(th0), "s"(budget), "s"(so), "v"(vo), "v"(zv), "s"(TID ? rt : rs), "v"(acc[4]), "v"(acc[5]));
    }
    TR(1);
    SCHED();
#define LO2(c) f32x2{c[0], c[1]}
#define HI2(c) f32x2{c[2], c[3]}
    asm volatile("s_nop 0\n v_pk_add_f32 %2, %2, %3\n v_pk_add_f32 %6, %6, %7\n ds_write2_b32 %0, %4, %5 offset1:40\n v_pk_add_f32 %10, %10, %11\n"
                 "ds_write2_b32 %0, %8, %9 offset0:4 offset1:44\n v_pk_add_f32 %14, %14, %15\n ds_write2_b32 %1, %12, %13 offset1:40\n ds_write2_b32 %1, %16, %17 offset0:4 offset1:44"
                 :: "v"(pq0), "v"(pq1), "v"(LO2(acc[0])), "v"(HI2(acc[0])), "v"(acc[0][0]), "v"(acc[0][1]), "v"(LO2(acc[1])), "v"(HI2(acc[1])), "v"(acc[1][0]), "v"(acc[1][1]),
                    "v"(LO2(acc[2])), "v"(HI2(acc[2])), "v"(acc[2][0]), "v"(acc[2][1]), "v"(LO2(acc[3])), "v"(HI2(acc[3])), "v"(acc[3][0]), "v"(acc[3][1]) : "memory");
    SCHED();
#pragma unroll
    for (int i = 0; i < 8; ++i) {
      acc[4 + (i & 1)] = smf(a[i >> 1], w1[4 + (i & 1)][i >> 1], acc[4 + (i & 1)], idx);
      SCHED();
      if (i == 1) { TR(2); bar(); SCHED(); }
    }
    asm volatile("v_add_u32 %0, 0x500, %0\n s_nop 2\n s_nop 2\n v_pk_add_f32 %1, %1, %2\n v_pk_add_f32 %5, %5, %6\n ds_write2_b32 %0, %3, %4 offset1:40\n ds_write2_b32 %0, %7, %8 offset0:4 offset1:44"
                 :: "v"(pq1), "v"(LO2(acc[4])), "v"(HI2(acc[4])), "v"(acc[4][0]), "v"(acc[4][1]), "v"(LO2(acc[5])), "v"(HI2(acc[5])), "v"(acc[5][0]), "v"(acc[5][1]) : "memory");
    bar();
    asm volatile("s_cmp_eq_u32 %6, 0\n s_cbranch_scc1 1f\n v_and_b32 %4, 0x370, %7\n"
                 "buffer_load_dwordx4 %0, %4, %5, %6 offen sc1\n buffer_load_dwordx4 %1, %4, %5, %6 offen offset:1024 sc1\n"
                 "buffer_load_dwordx4 %2, %4, %5, %6 offen offset:2048 sc1\n buffer_load_dwordx4 %3, %4, %5, %6 offen offset:3072 sc1\n 1:"
                 : "=&v"(a[0]), "=&v"(a[1]), "=&v"(a[2]), "=&v"(a[3]), "=&v"(m) : "s"(rs), "s"(qs), "v"(vo) : "memory", "scc");
    TR(3);
    SLP(0);
    const bool last = s == NSEG_ - 1;
    asm volatile("" : "+v"(kl));
    if (__builtin_expect(__builtin_amdgcn_readfirstlane(kl) != 0x404u && s > 0 && !last, 0)) {
      unsigned lv = vo;
      asm volatile("" : "+v"(lv));
      __builtin_amdgcn_s_sleep(LSL);
      kv_wait(rs, s + 1, h, budget);
      kv_dma(rs, lv ^ (unsigned)(w >> 1) * (((lv >> 3) & 32u) + ((lv >> 2) & 128u)), kv_src(s + 1, h, w, hb), kv_dst(s + 1, w));
      drain_vm();
    }
    auto reset = [&]() {
      u32x2 p2 = u32x2{POISON, POISON};
      const unsigned sp = 3u * SET_ - sc - sn;
      unsigned lv = vo;
      asm volatile("" : "+v"(lv));
      unsigned po = w ? (lv >> 1) + (w < 3 ? (lv >> 8) * 128u : 0u) : mvo;
      const unsigned ps = sp + (w ? r2b + (w == 3 ? BLK_ + vq * 512u : (2 * (jj & 1) + w - 1) * 1024u + (jj >> 1) * 128u) : r1w);
      asm volatile("s_waitcnt lgkmcnt(0)" : "+v"(po), "+v"(p2) : "s"(ps) : "memory");
      __builtin_amdgcn_s_barrier();
      st_sc1s_64(rw, po, ps, p2);
    };
    unsigned tv_, ov, os, pm;
    u32x4 oc[2], sel;
    auto none3 = [&]() { asm volatile("" : "=v"(tv_), "=v"(sel), "=v"(oc[0]), "=v"(oc[1]), "=v"(ov)); asm volatile("" : "=s"(pm), "=s"(os)); };
    auto prep3 = [&]() {
      tv_ = vo;
      asm volatile("" : "+v"(tv_));
      oc[0] = *(const u32x4*)(lds + OB_OFF + tv_); oc[1] = *(const u32x4*)(lds + OB_OFF + 1024 + tv_);
      ov = ovoff(tv_) | (s > XS_ && !last ? 0u : 0x80000000u); os = osoff(max(s - 1, 0), h, jj, hb);
      const unsigned sel1 = 0x3f80u << (tv_ & 16u);
      sel = u32x4{(tv_ & 96u) == 0u ? sel1 : 0u, (tv_ & 96u) == 32u ? sel1 : 0u, (tv_ & 96u) == 64u ? sel1 : 0u, (tv_ & 96u) == 96u ? sel1 : 0u};
      tv_ = (tv_ >> 7) ^ tg;
      pm = r1w + sn;
      asm volatile("" : "+v"(tv_), "+v"(sel), "+v"(oc[0]), "+v"(oc[1]), "+v"(ov));
      asm volatile("" : "+s"(pm), "+s"(os));
    };
    if (qs) {
      float e[6];
      unsigned lv = vo;
      asm volatile("" : "+v"(lv));
      f32x4 sa, sb;
      u32x4 km[4];
      asm volatile("ds_read_b128 %2, %10\n ds_read_b128 %3, %10 offset:1024\n ds_read_b128 %4, %10 offset:2048\n ds_read_b128 %5, %10 offset:3072\n s_waitcnt vmcnt(0) lgkmcnt(0)\n"
                   MF "%0, %2, %6, 0\n ds_read_b128 %2, %10 offset:4096\n" MF "%0, %3, %7, %0\n ds_read_b128 %3, %10 offset:5120\n"
                   MF "%0, %4, %8, %0\n ds_read_b128 %4, %10 offset:6144\n" MF "%0, %5, %9, %0\n ds_read_b128 %5, %10 offset:7168\n s_waitcnt lgkmcnt(3)\n"
                   MF "%1, %2, %6, 0\n s_waitcnt lgkmcnt(2)\n" MF "%1, %3, %7, %1\n s_waitcnt lgkmcnt(1)\n" MF "%1, %4, %8, %1\n s_waitcnt lgkmcnt(0)\n" MF "%1, %5, %9, %1\n s_nop 7"
                   : "=&v"(sa), "=&v"(sb), "=&v"(km[0]), "=&v"(km[1]), "=&v"(km[2]), "=&v"(km[3]), "+v"(a[0]), "+v"(a[1]), "+v"(a[2]), "+v"(a[3])
                   : "v"((unsigned)(size_t)LDS3 + KV_OFF + (s & 1) * 16384 + lv) : "memory");
      const int qi = 8 * jj + (int)((lv >> 4) & 7u) - 4 * (int)(lv >> 8) - (int)((lv >> 3) & 16u);
#pragma unroll
      for (int r = 0; r < 4; ++r) e[r] = (lv & 128u) ? sb[r] : sa[r];
      asm volatile("" : "+v"(e[0]), "+v"(e[1]), "+v"(e[2]), "+v"(e[3]));
#pragma unroll
      for (int r = 0; r < 4; ++r) e[r] = r <= qi ? e[r] : rbf(-1000000000.0f) / SCALE_;
      asm volatile("" : "+v"(e[0]), "+v"(e[1]), "+v"(e[2]), "+v"(e[3]));
      e[4] = e[5] = e[0];
      float m1 = negmax6(e);
      asm volatile("" : "+v"(m1));
      TRY(7);
      const unsigned ls = (unsigned)(size_t)LDS3, pb = ls + PS_OFF + 1024u;
      unsigned ka = ls + XK_OFF + (lv >> 8) * 128u + (lv & 112u), fa = ls + XK_OFF + 2048u, fv;
      unsigned w1a = pb + (lv >> 8) * 128u + (lv & 112u) + ((lv >> 4) & 8u), w2a = pb + 512u + (lv & 256u) + (lv & 112u) * 2u + ((lv >> 5) & 4u);
      asm volatile("" : "+v"(ka), "+v"(fa), "+v"(w1a), "+v"(w2a));
      reset();
      TR(5);
      __builtin_amdgcn_s_sleep(KSL);
      do {
        asm volatile("ds_read_b32 %4, %6\n ds_read_b128 %0, %5\n ds_read_b128 %1, %5 offset:1024\n ds_read_b128 %2, %5 offset:512\n ds_read_b128 %3, %5 offset:1536\n s_waitcnt lgkmcnt(0)"
                     : "=&v"(km[0]), "=&v"(km[1]), "=&v"(km[2]), "=&v"(km[3]), "=&v"(fv) : "v"(ka), "v"(fa) : "memory");
      } while (__builtin_amdgcn_readfirstlane(fv) != (unsigned)s);
      const f32x4 sm = mfu(km[3], a[3], mfu(km[2], a[2], mfu(km[1], a[1], mfu(km[0], a[0], f32x4{0.f, 0.f, 0.f, 0.f}))));
      const unsigned m2 = __float_as_uint(fminf(fminf(sm[0] * -CS, sm[1] * -CS), fminf(fminf(sm[2] * -CS, sm[3] * -CS), m1)));
      e[4] = (lv & 128u) ? sm[2] : sm[0]; e[5] = (lv & 128u) ? sm[3] : sm[1];
      const auto m3 = __builtin_amdgcn_permlane16_swap(m2, m2, false, false);
      float mx;
      asm("v_min_f32 %0, %1, %2" : "=v"(mx) : "v"(m3[0]), "v"(m3[1]));
#pragma unroll
      for (int i = 0; i < 6; ++i) e[i] = __builtin_amdgcn_exp2f(__builtin_fmaf(e[i], CS, mx));
      asm volatile("ds_write_b64 %0, %1\n ds_write2_b32 %2, %3, %3 offset1:6" :: "v"(w1a), "v"(u32x2{cvt2(e[0], e[1]), cvt2(e[2], e[3])}), "v"(w2a), "v"(cvt2(e[4], e[5])) : "memory");
      TRY(6);
      none3();
    } else {
    if (__builtin_expect(s == 0, 0)) {
      const u32x2 no = launch_no(ws);
      unsigned lv = vo;
      asm volatile("" : "+v"(lv));
      kv_wait(rs, 1, h, budget);
      __builtin_amdgcn_s_sleep(ESL);
      kv_first(lds, rs, 1, h, w, lv, budget);
      if (w >= 2) rec_wait(rw, no, 63u, 64u + 64u * w);
      const NearMap nm{(unsigned)__builtin_amdgcn_readfirstlane(*(const unsigned*)(lds + KC_OFF + 4))};
      mvo = (lv & 32u) * 2u + ((lv >> 1) & 32u) + (lv & 16u) + ((lv >> 5) & 8u) + nm(0u, ((lv >> 8) & 2u) + ((lv >> 7) & 1u), NB1) - NearMap::blk(NB1);
      r1r = nm(0u, h >> 3, NB1 + w) + (ILV ? w * 1024u : 0u); r3r = nm(0u, h >> 3, NB3 + jj * 8 + (h & 7)); r2b = nm(0u, h >> 3, NB2 + (h & 7) * 2);
      hb = far_bit(nm.k);
      gpo = nm(4u, h >> 3, NB1);
      if (w == 1 && ((wg ^ (wg >> 3)) & 7) == 0 && lv < 64u) anv = nm(0u, lv >> 4, NB1) - NearMap::blk(NB1);
    }
    if (w == 1) *(unsigned*)(lds + KC_OFF) = known > s + 2 ? 0x404u : 0u;
    if (LOCK && w == 1) {
      unsigned t1, g1;
      clk_pair(t1, g1);
      const int err = (int)(g1 - ge) - ((int)(t1 - te) * kq >> 16);
      const int dk = (unsigned)(err + 400) < 800u ? err * 3 : 0;
      kq += dk; rq -= dk * 57 >> 6;
      if (__builtin_expect(s == 0, 0)) {
        const float r = (float)err * __builtin_amdgcn_rcpf((float)(int)(t1 - te));
        kq = __builtin_amdgcn_readfirstlane((int)(r * 65536.f)); rq = __builtin_amdgcn_readfirstlane((int)(__builtin_amdgcn_rcpf(r) * 65536.f));
      }
      te = t1; ge = g1;
      if (s == 0) { t1g = t3g = g1; *(unsigned*)(lds + TG_OFF) = t1; }
      *(unsigned*)(lds + TG_OFF + 12) = local_of(t3g, rq, t1, g1);
      pause(dly[DLYG]);
      asm volatile("s_mov_b32 m0, %3\n s_nop 0\n buffer_load_dwordx4 %0, %1, %2 offen sc1 lds" :: "v"((vo & 255u) * 8u), "s"(rs), "s"(gpo), "s"((unsigned)(size_t)LDS3 + GP_OFF) : "memory");
    }
    if (PRE3 && w == 0) prep3(); else none3();
    reset();
    TR(5);
    if (w == 3) {
      unsigned now = 1u;
      if (__builtin_expect(jj == 0 && s >= FS_, 0)) now = rt_now() | 1u;
      if (l == 0 && s > 1) st_sc1_32(rw, cooff(h, hb) + (unsigned)((s - 2) * 16 + jj * 4), s < XS_ + 2 ? 2u : now);
      if (__builtin_expect(s < XS_, 0)) {
        unsigned lv = vo;
        asm volatile("" : "+v"(lv));
        const float* p = (const float*)(lds + PQK_OFF + 2560 + ((lv >> 8) & 1u) * 640u + ((lv >> 1) & 96u) + (lv >> 9) * 16u + ((lv >> 2) & 12u));
        float v[4];
#pragma unroll
        for (int r = 0; r < 4; ++r) v[r] = (p[r * 40] + p[960 + r * 40]) + (p[1920 + r * 40] + p[2880 + r * 40]);
        st_sc1s_64(rw, (lv & 511u) + (lv >> 9) * 8u, kmoff(s, h, hb) + 2048u + vq * 512u, u32x2{cvt2(v[0], v[1]), cvt2(v[2], v[3])});
      }
      if (__builtin_expect(s == FS_ - 1, 0)) {
        while (__builtin_amdgcn_readfirstlane(ld_sc1_32(rw, OFF_CF + 256u)) < 32u && --budget > 0) __builtin_amdgcn_s_sleep(8);
      }
    } else if (w == 1) {
      if (__builtin_expect(known < min(s + 4, NSEG_), 0)) {
        unsigned lv = vo;
        asm volatile("" : "+v"(lv));
        const unsigned c = ld_sc1_32(rw, OFF_CKV + (unsigned)h * 256u + (lv >> 2)), want = lv < XS_ * 16u ? 0x404u : 0x40404u;
        const unsigned long long miss = __builtin_amdgcn_ballot_w64((c & want) != want);
        known = miss ? __builtin_ctzll(miss) : NSEG_;
        *(unsigned*)(lds + KC_OFF + 8) = known > s + 2 ? 0x404u : 0u;
      }
      if (LOCK && __builtin_expect(s > 0, 1)) {
        u32x4 gp;
        unsigned lv = vo;
        asm volatile("" : "+v"(lv));
#define MX(r) "v_max_i32_dpp %1, %1, %1 row_ror:" #r " row_mask:0xf bank_mask:0xf\n v_max_i32_dpp %2, %2, %2 row_ror:" #r " row_mask:0xf bank_mask:0xf\n v_max_i32_dpp %3, %3, %3 row_ror:" #r " row_mask:0xf bank_mask:0xf\n"
        asm volatile("s_waitcnt vmcnt(1)\n ds_read_b128 %0, %1\n s_waitcnt lgkmcnt(0)" : "=&v"(gp) : "v"((unsigned)(size_t)LDS3 + GP_OFF + lv) : "memory");
        asm volatile("v_subrev_u32 %1, %4, %1\n v_subrev_u32 %2, %4, %2\n v_subrev_u32 %3, %4, %3\n" MX(8) MX(4) MX(2) MX(1) "s_nop 0"
                     : "+v"(gp) : "v"(gp[0]), "v"(gp[1]), "v"(gp[2]), "s"(ge) : "memory");
        t1g = ge + __builtin_amdgcn_readfirstlane(gp[0]); t3g = ge + __builtin_amdgcn_readfirstlane(gp[2]);
        unsigned t1, g1;
        clk_pair(t1, g1);
        typedef unsigned u32x3 __attribute__((ext_vector_type(3)));
        *(u32x3*)(lds + TG_OFF) = u32x3{local_of(t1g, rq, t1, g1), ge + gp[1], (unsigned)rq};
      }
    }
    }
    int lf3 = 0;
    float gv;
    u32x2 bw;
    asm volatile("" : "=v"(gv), "=v"(bw));
    if (__builtin_expect(last, 0)) {
      unsigned lv = vo;
      asm volatile("" : "+v"(lv));
      const unsigned tk = (NSEG_ - 1) * SEG_ + 8 * w + 4 * (h & 1);
      gv = __uint_as_float(__builtin_amdgcn_raw_buffer_load_b32(rw, (lv >> 2) & 12u, OFF_G + tk * 4u, 16));
      bw = __builtin_amdgcn_raw_buffer_load_b64(rw, (lv & 64u) * 1024u + (lv & 48u) + ((lv >> 5) & 8u), bfoff(jj * 32 + (h & ~1), tk, hb), 16);
      sc = w ? (2 + w) * SET_ : 0u;
      pause(LOCK && L3 ? dly[DLYL] : dly[DLYL] - dly[DLY3]);
      asm volatile("" : "+v"(gv), "+v"(bw));
    }
    if (w == 0 || last) {
      prep3();
      if (S3) __builtin_amdgcn_s_sleep(S3);
      pause(dly[DLY3]);
      __builtin_amdgcn_s_setprio(3);
      f32x4 d;
      do {
        thr = --budget > 0 ? POISON - 1u : POISON;
        asm volatile("" : "+s"(thr));
        TRY(4);
        if (TID) req4t(rt, r3r + sc, a); else req4(rs, vo, r3r + sc, a);
        ostore(rs, oc, ov, os);
        asm volatile("v_bfrev_b32 %0, 1" : "+v"(ov));
        d = zero4();
        asm volatile("s_nop 0");
        got_m<5>(a[0], d, sel); m = tag5(a[0], tv_, ~1u); asm volatile("" : "+v"(m)); SCHED();
        got_m<4>(a[1], d, sel); m = tag5(a[1], tv_, m); asm volatile("" : "+v"(m)); SCHED();
        got_m<3>(a[2], d, sel); m = tag5(a[2], tv_, m); asm volatile("" : "+v"(m)); SCHED();
        got_m<2>(a[3], d, sel); m = tag5(a[3], tv_, m);
      } while (over(m, thr));
      TR(6);
      T0(3);
      u32x2 c;
#define RO8 " row_ror:8 row_mask:0xf bank_mask:0xf\n"
      asm volatile("s_nop 2\n v_add_f32_dpp %1, %1, %1" RO8 "v_add_f32_dpp %2, %2, %2" RO8 "v_add_f32_dpp %3, %3, %3" RO8 "v_add_f32_dpp %4, %4, %4" RO8
                   "v_cvt_pk_bf16_f32 %1, %1, %2\n v_cvt_pk_bf16_f32 %2, %3, %4\n .if " STR(NOMIN) " == 0\n v_min_u32 %1, -2, %1\n v_min_u32 %2, -2, %2\n .endif\n"
                   "buffer_store_dwordx2 %5, %6, %7, %8 offen sc1\n code_to: v_mov_b64 %0, %5"
                   : "=&v"(c) : "v"(d[0]), "v"(d[1]), "v"(d[2]), "v"(d[3]), "v"(LO2(d)), "v"(mvo), "s"(rs), "s"(pm) : "memory");
      if (__builtin_expect(last, 0)) {
        const float omg = rbf(1.0f - gv);
        u32x2 r;
#pragma unroll
        for (int i = 0; i < 2; ++i)
          r[i] = cvt2(rbf(gv * __uint_as_float(c[i] << 16)) + rbf(omg * __uint_as_float(bw[i] << 16)),
                      rbf(gv * __uint_as_float(c[i] & 0xffff0000u)) + rbf(omg * __uint_as_float(bw[i] & 0xffff0000u)));
        unsigned lv = vo;
        asm volatile("" : "+v"(lv));
        __builtin_amdgcn_raw_buffer_store_b64(r, __builtin_amdgcn_make_buffer_rsrc(out, 0, S_ * D_ * 2, 0x00020000), (lv & 48u) * (D_ / 8) + ((lv >> 2) & 16u) + ((lv >> 5) & 8u),
                                              (unsigned)(((NSEG_ - 1) * SEG_ + 8 * w + 4 * (h & 1)) * (D_ * 2) + (jj * 32 + (h & ~1)) * 16), OAUX);
        break;
      }
      __builtin_amdgcn_s_setprio(0);
      TR(7);
      SLP(1);
    }
    if (w == 2) qs = s + 1 >= XS_ && s + 1 < NSEG_ - 1 ? OFF_Q + tile8(s + 1, h, hb) + (jj >> 1) * HALF_ + (jj & 1) * 128u : 0u;
    asm volatile("" : "+s"(qs));
    bar();
    TRW();
    asm volatile("" : "=v"(a[0]), "=v"(a[1]), "=v"(a[2]), "=v"(a[3]));
    tg ^= sc >> 23;
    sc = sn; sn = sn == 2u * SET_ ? 0u : sn + SET_;
  }
  TRE(4 + w);
}

DEVI void recur_at(char* lds, rsrc_t rw, const u32x4 rs, const u16* __restrict__ Wo, const char* ws, int wg TRARGD DLYARG) {
  DLYI(LOCK ? 0x8486 : 0x487);
  const int l = threadIdx.x & 63, w = __builtin_amdgcn_readfirstlane(threadIdx.x >> 6), g = l >> 4, lo = l & 15;
  const int h = wg >> 2, jj = wg & 3;
  int budget = SPIN_CAP;
  unsigned mz = 0u;
  if (ALG) asm volatile("" : "+s"(mz));
  u32x8 w3[6][2];
  const int er = (lo >> 2 & 1) + 4 * (lo >> 3);
  const unsigned vo = (unsigned)l * 16u;
  const unsigned dvo = vo ^ (unsigned)((w >> 1) * ((g & 1) * 32 + (g & 2) * 64));
  u32x2 no;
  {
    u32x4 p4 = u32x4{POISON, POISON, POISON, POISON};
    asm volatile("" : "+v"(p4));
#pragma unroll
    for (int i = 0; i < 2; ++i) st_sc1s(rw, vo + i * 1024u, ((w & 1) ? OFF_VX : OFF_K) + tile8(w >> 1, h, 0u) + (jj >> 1) * HALF_ + (jj & 1) * 2048u, p4);
    st_sc1s(rw, w < 2 ? vo : 0x80000000u, NearMap{0u}(0u, h >> 3, NB2 + (h & 7) * 2 + (w & 1)) + jj * 1024u, p4);
    const u16* gw = Wo + (size_t)(h * DH_ + (l >> 3)) * D_ + jj * 256 + 64 * w + ((l & 7) ^ (l >> 5)) * 8;
#pragma unroll
    for (int i = 0; i < 16; ++i)
      __builtin_amdgcn_global_load_lds((const unsigned*)(gw + (size_t)i * 8 * D_), (unsigned*)(lds + W3_OFF + w * 16384 + i * 1024 + l * 16), 16, 0, NTO);
    no = launch_no(ws);
    drain_vm();
    asm volatile("" :: "v"(p4));
    TP(0);
    __builtin_amdgcn_s_barrier();
    asm volatile("" ::: "memory");
    if (threadIdx.x == 0) st_sc1_64(rw, OFF_REC + (64u + wg) * 16u, no);
#pragma unroll
    for (int T = 0; T < 6; ++T) {
      const int Tc = T < 4 ? T : (T & 1) + (w & 2);
      const char* base = lds + W3_OFF + (T < 4 ? w * 16384 : 0) + (16 * (g & 1) + 8 * (g >> 1) + er) * 128 + (((lo & 3) * 2 + (T & 1)) ^ (lo >> 3)) * 16 + (Tc >> 1) * 8;
#pragma unroll
      for (int q = 0; q < 8; ++q) {
        const u32x2 v = tr_quad(base + (32 * (q >> 1) + 2 * (q & 1)) * 128);
        w3[T][q >> 2][2 * (q & 3)] = v[0];
        w3[T][q >> 2][2 * (q & 3) + 1] = v[1];
      }
      asm volatile("" : "+v"(w3[T][0]), "+v"(w3[T][1]));
    }
    bar();
    TP(1);
    *(u32x4*)(lds + PS_OFF + threadIdx.x * 16) = u32x4{0u, 0u, 0u, 0u};
    *(unsigned*)(lds + PZ_OFF + threadIdx.x * 4) = 0u;
    rec_wait(rw, no, 3u, 64u + 4u * h, 31u);
    if (w == 1) {
      const NearMap nm{near_kind(rw, no)};
      *(unsigned*)(lds + KC_OFF + 4) = nm.k;
      const unsigned pv = (l & 8) ? 0u : POISON;
      u32x4 p3 = u32x4{pv, pv, pv, pv};
      asm volatile("" : "+v"(p3));
#pragma unroll
      for (int i = 0; i < 12; ++i) st_sc1s(rw, vo + (i & 3) * 1024u, nm(i >> 2, h >> 3, NB3 + jj * 8 + (h & 7)), p3);
#pragma unroll
      for (int i = 0; i < 2; ++i) st_sc1s(rw, vo, nm(1u, h >> 3, NB2 + (h & 7) * 2 + i) + jj * 1024u, p4);
      st_sc1s(rw, l < 32 ? vo : 0x80000000u, nm(1u, h >> 3, NB1 + jj) + (h & 7) * 512u, p4);
      drain_vm();
      asm volatile("" :: "v"(p3), "v"(p4));
      if (l == 0) st_sc1_64(rw, OFF_REC + (192u + wg) * 16u, no);
    } else kv_wait(rs, 0, h, budget);
    TP(2);
    kv_first(lds, rs, 0, h, w, vo, budget);
    TP(3);
    bar();
    TP(4);
  }
  const int idx = lo < 8 ? 0x4444 : 0xeeee;
  const int xg = 2 * (w & 1) + (l >> 5), xm = w >> 1;
  const f32x2* pq = (const f32x2*)((const float*)(lds + PQK_OFF) + xm * 320 + ((l >> 2) & 7) * 40 + 8 * xg + 2 * (l & 3));
  const unsigned xvo = (unsigned)((2 * (jj & 1) + xm) * 1024 + xg * 256 + (jj >> 1) * 128 + (l & 31) * 4), kvo = (unsigned)(xg * 128 + (l & 31) * 4);
  unsigned r2b;
  unsigned sc = 0u, sn = SET_;
  unsigned tg = h & 1;
  const unsigned dlyd = LOCK ? dly[DLYD] * 28 : dly[DLY1] + dly[DLYD];
  const unsigned dlyv = (w == 0 ? dly[DLY2] : dly[DLYV] + (w == 1 ? dly[DLYW] : 0u)) * (LOCK && L2 ? 28 : 1), kgv = !LOCK || w == 0 ? 0u : dly[KGV] + (w == 1 ? dly[DLYW] * 28 : 0u);
  unsigned t2 = LOCK ? mt_now() : 0u;
  unsigned xo = vo, xt = POISON - 1u;
  f32x4 ma = zero4(), mb = zero4();
  const int vr = (lo & 3) * 16 + ((8 * (g >> 1) + er) ^ ((lo & 1) * 2 + (lo & 2) * 4));
  unsigned vb = KV_OFF + 8192 + (g & 1) * 1024 + vr * 16, kb = KV_OFF + l * 16;
  const unsigned ls = (unsigned)(size_t)LDS3;
  const unsigned pw1 = ls + PS_OFF + w * 1024 + (g * 8 + (lo & 7)) * 16 + (lo & 8);
  const unsigned pw2 = ls + (g >= 2 ? PS_OFF + w * 1024 + 512 : UM_OFF + 2048) + ((g & 1) * 8 + (lo & 7)) * 32 + (lo >> 3) * 4;
  unsigned pr0 = ls + PS_OFF + (g * 8 + (lo & 7)) * 16, pr3 = ls + PS_OFF + 512 + ((g & 1) * 8 + (lo & 7)) * 32 + (lo >> 3) * 16;
  unsigned pr1 = lo < 8 ? pr0 : ls + PZ_OFF, pr2 = lo < 8 ? ls + PZ_OFF : pr0;
  const unsigned oa1 = lo < 8 ? pr0 + 1024u : pr1, oa2 = lo < 8 ? pr2 : pr0 + 1024u, oa3 = g < 2 ? pr3 + 1024u : ls + PZ_OFF;
  const unsigned oam = ls + XV_OFF + (lo & 3) * 128 + ((l >> 2) & 7) * 16;
  const unsigned uma = ls + UM_OFF + max(w - 1, 0) * 1024 + vo;
  unsigned long long w0m;
  asm("s_cmp_eq_u32 %1, 0\n s_cselect_b64 %0, -1, 0" : "=s"(w0m) : "s"(w) : "scc");
  unsigned long long xwm;
  asm("s_cmp_eq_u32 %1, 1\n s_cselect_b64 %0, -1, 0" : "=s"(xwm) : "s"(w) : "scc");
  xwm |= w0m & 0xff00ff00ff00ff00ull;
  const unsigned xw = ls + (w ? XV_OFF + vo : XK_OFF + g * 128 + (lo & 7) * 16);
  const float ninf = g < 2 ? -INFINITY : 0.f;
  u32x4 one = u32x4{0x3f803f80u, 0x3f803f80u, 0x3f803f80u, 0x3f803f80u}, half = u32x4{0x3f003f00u, 0x3f003f00u, 0x3f003f00u, 0x3f003f00u};
  f32x4 nin4 = f32x4{ninf, ninf, ninf, ninf};
  asm volatile("" : "+v"(one), "+v"(half), "+v"(nin4));
  u32x4 kx[8], us[4];
  auto lo4 = [](u32x8 b) { return u32x4{b[0], b[1], b[2], b[3]}; };
  auto hi4 = [](u32x8 b) { return u32x4{b[4], b[5], b[6], b[7]}; };
  auto useg = [&]() {
    u32x4 va[2][4];
#pragma unroll
    for (int i = 0; i < 8; ++i) {
      const u32x2 x0 = tr_quad(lds + vb + (i & 3) * 2048 + (i >> 2) * 8), x1 = tr_quad(lds + (vb ^ 32u) + (i & 3) * 2048 + (i >> 2) * 8);
      va[i >> 2][i & 3] = u32x4{x0[0], x0[1], x1[0], x1[1]};
    }
#pragma unroll
    for (int i = 0; i < 8; ++i) kx[i] = *(const u32x4*)(lds + kb + i * 1024);
    vb ^= 16384u; kb ^= 16384u;
#pragma unroll
    for (int T = 0; T < 4; ++T) {
      f32x4 d[2];
#pragma unroll
      for (int ks = 0; ks < 4; ++ks)
#pragma unroll
        for (int n = 0; n < 2; ++n) d[n] = mfu(va[n][ks], (ks & 1) ? hi4(w3[T][ks >> 1]) : lo4(w3[T][ks >> 1]), ks ? d[n] : f32x4{0.f, 0.f, 0.f, 0.f});
      us[T] = cvt8(d[0], d[1]);
    }
    asm volatile("" : "+v"(us[0]), "+v"(us[1]), "+v"(us[2]), "+v"(us[3]), "+v"(kx[0]), "+v"(kx[1]), "+v"(kx[2]), "+v"(kx[3]), "+v"(kx[4]), "+v"(kx[5]), "+v"(kx[6]), "+v"(kx[7]));
  };
  u32x4 rt = rs_tid(rs);
  if (TID) asm volatile("" : "+s"(rt));
  unsigned c;
  auto pub = [&]() {
    SCHED();
    if (PBA) asm volatile(".p2align %0\n .rept %1\n s_nop 0\n .endr" :: "n"(PBA), "n"(PBN));
    bar();
    const f32x2 v = (pq[0] + pq[480]) + (pq[960] + pq[1440]);
    c = NOMIN ? cvt2(v[0], v[1]) : cvt2x(v[0], v[1]);
    asm volatile("buffer_store_dword %0, %1, %2, %3 offen sc1" :: "v"(c), "v"(xvo), "s"(rs), "s"(r2b + sc) : "memory");
    SCHED();
    __builtin_amdgcn_s_barrier();
    asm volatile("" ::: "memory");
    if (xm) {
      const f32x2 v = (pq[160] + pq[640]) + (pq[1120] + pq[1600]);
      asm volatile("buffer_store_dword %0, %1, %2, %3 offen sc1" :: "v"(NOMIN ? cvt2(v[0], v[1]) : cvt2x(v[0], v[1])), "v"(kvo), "s"(rs), "s"(r2b + BLK_ + (2 * (~jj & 1) + (jj >> 1)) * 512u + sc) : "memory");
    }
    SCHED();
  };
  const NearMap nm{(unsigned)__builtin_amdgcn_readfirstlane(*(const unsigned*)(lds + KC_OFF + 4))};
  const unsigned hb = far_bit(nm.k);
  r2b = NearMap{0u}(0u, h >> 3, NB2 + (h & 7) * 2);
  const unsigned r3o = nm(0u, w, NB3 + jj * 8 + 2 * g + ((lo >> 2) & 1)) + h * 128u + (unsigned)((lo >> 3) * 4 + (lo & 3)) * 16u;
  asm volatile("" ::: "memory");
  useg();
  if (w == 1) kb = KV_OFF + 8192 + (l ^ ((g & 1) * 2 + (g & 2) * 4)) * 16;
  TP(5);
  pub();
  TP(7);
  TRD;
  for (int s = 0;;) {
    const bool last = s == NSEG_ - 1;
    const unsigned r2c = r2b + sc;
    unsigned m;
    f32x4 d[6];
    u32x4 x[4];
    asm volatile("" : "=v"(d[0]), "=v"(d[1]), "=v"(d[2]), "=v"(d[3]), "=v"(x[0]), "=v"(x[1]), "=v"(x[2]), "=v"(x[3]));
    unsigned dox = w == 0 || last ? 2u : 1u, lim = last ? 5u * SET_ : sc;
    asm volatile("" : "+s"(dox), "+s"(lim));
    int lf2 = 0;
    if (LOCK && L2) lf2 = until(t2, dlyv + (s > 1 ? 0u : 84u)) - (int)dlyv; else pause(dlyv);
    if (w != 0) {
      u32x4* vv = x;
      unsigned thr;
      TR(0);
      do {
        thr = --budget > 0 ? POISON - 1u : POISON;
        asm volatile("" : "+s"(thr));
        if (TID) req2t(rt, r2c + BLK_, vv); else req2(rs, vo, r2c + BLK_, vv);
#pragma unroll
        for (int T = 0; T < 6; ++T) d[T] = zero4();
        SCHED();
        asm volatile("s_nop 0");
        got_s2<1>(vv[0], d + 4, w3[4][0], w3[5][0], idx);
        m = umax5(vv[0], mz); SCHED();
        got_s2<0>(vv[1], d + 4, w3[4][1], w3[5][1], idx);
      } while (over(umax5(vv[1], m), thr));
      TR(3);
      asm volatile(A8);
      SCHED();
#pragma unroll
      for (int T = 0; T < 4; ++T) d[T] = smf(vv[0], w3[T][0], d[T], idx);
      SCHED();
      asm volatile("v_cvt_pk_bf16_f32 %2, %2, %3\n v_cvt_pk_bf16_f32 %3, %4, %5\n v_cvt_pk_bf16_f32 %4, %6, %7\n v_cvt_pk_bf16_f32 %5, %8, %9\n ds_write_b128 %0, %1"
                   :: "v"(uma), "v"(d[4]), "v"(d[4][0]), "v"(d[4][1]), "v"(d[4][2]), "v"(d[4][3]), "v"(d[5][0]), "v"(d[5][1]), "v"(d[5][2]), "v"(d[5][3]), "v"(d[3]) : "memory");
      SCHED();
#pragma unroll
      for (int T = 0; T < 4; ++T) d[T] = smf(vv[1], w3[T][1], d[T], idx);
      SCHED();
    }
    if (dox == 2u) {
      f32x4 sa, sb, sm;
      TR(0);
      do {
        req4(rs, xo, r2c, x);
        if (--budget <= 0) xt = POISON;
        SCHED();
        asm volatile(A4 "s_waitcnt vmcnt(3)\n" MF "%1, %4, %0, %6\n" MF "%2, %5, %0, %7\n" MF "%3, %0, %0, %8"
                     : "+v"(x[0]), "=&v"(sa), "=&v"(sb), "=&v"(sm) : "v"(kx[0]), "v"(kx[4]), "v"(ma), "v"(mb), "v"(nin4));
        m = umax5(x[0], mz); SCHED();
        got_x<2>(x[1], sa, sb, sm, kx[1], kx[5]); m = umax5(x[1], m); SCHED();
        got_x<1>(x[2], sa, sb, sm, kx[2], kx[6]); m = umax5(x[2], m); SCHED();
        got_x<0>(x[3], sa, sb, sm, kx[3], kx[7]);
      } while (over(umax5(x[3], m), xt));
      TR(3);
#define SH8 " row_shr:8 row_mask:0xf bank_mask:0xc\n"
      asm volatile("s_nop 2\n" A8 "v_mov_b32_dpp %0, %4" SH8 "v_mov_b32_dpp %1, %5" SH8 "v_mov_b32_dpp %2, %6" SH8 "v_mov_b32_dpp %8, %10" SH8 "v_mov_b32_dpp %9, %11" SH8 "v_mov_b32_dpp %3, %7" SH8
                   "v_max3_f32 %4, %0, %1, %2\n v_max3_f32 %4, %4, %8, %9\n v_max_f32 %4, %4, %3\n s_nop 1\n"
                   "v_max_f32_dpp %5, %4, %4" RO8 "v_max_f32_dpp %6, %4, %4" RO8 "s_nop 1\n"
                   "v_permlane16_swap_b32 %5, %6\n v_max_f32 %4, %5, %6\n v_max_f32 %5, %5, %6\n s_nop 1\n"
                   "v_permlane32_swap_b32 %4, %5\n v_max_f32 %4, %4, %5\n v_mul_f32 %4, %14, %4\n"
                   "v_fma_f32 %0, %0, %14, -%4\n v_fma_f32 %1, %1, %14, -%4\n v_fma_f32 %2, %2, %14, -%4\n v_exp_f32 %0, %0\n v_exp_f32 %1, %1\n v_fma_f32 %3, %3, %14, -%4\n"
                   "v_fma_f32 %8, %8, %14, -%4\n v_exp_f32 %2, %2\n v_exp_f32 %3, %3\n v_fma_f32 %9, %9, %14, -%4\n v_cvt_pk_bf16_f32 %0, %0, %1\n v_exp_f32 %8, %8\n"
                   "v_exp_f32 %9, %9\n v_cvt_pk_bf16_f32 %1, %2, %3\n ds_write_b64 %12, %15\n v_cvt_pk_bf16_f32 %8, %8, %9\n ds_write2_b32 %13, %8, %8 offset1:6"
                   :: "v"(sa[0]), "v"(sa[1]), "v"(sa[2]), "v"(sa[3]), "v"(sb[0]), "v"(sb[1]), "v"(sb[2]), "v"(sb[3]), "v"(sm[0]), "v"(sm[1]), "v"(sm[2]), "v"(sm[3]),
                      "v"(pw1), "v"(pw2), "s"(CS), "v"(LO2(sa)) : "memory");
    }
    TR(4);
    T0(8);
    SCHED();
    bar();
    TR(5);
    u32x4 p0, p1, p2, p3;
#define RDP "ds_read_b128 %0, %4\n ds_read_b128 %1, %5\n ds_read_b128 %2, %6\n ds_read_b128 %3, %7\n"
    asm volatile(RDP CV "%8, %8, %9\n" CV "%9, %10, %11\n" CV "%10, %12, %13\n" CV "%11, %14, %15\n" CV "%16, %16, %17\n" CV "%17, %18, %19\n" CV "%18, %20, %21\n" CV "%19, %22, %23\n"
                 "s_mov_b64 exec, %27\n ds_read_b128 %24, %26\n ds_read_b128 %25, %26 offset:1024\n s_mov_b64 exec, -1"
                 : "=&v"(p0), "=&v"(p3), "=&v"(p1), "=&v"(p2) : "v"(pr0), "v"(pr3), "v"(pr1), "v"(pr2),
                   "v"(d[0][0]), "v"(d[0][1]), "v"(d[0][2]), "v"(d[0][3]), "v"(d[1][0]), "v"(d[1][1]), "v"(d[1][2]), "v"(d[1][3]),
                   "v"(d[2][0]), "v"(d[2][1]), "v"(d[2][2]), "v"(d[2][3]), "v"(d[3][0]), "v"(d[3][1]), "v"(d[3][2]), "v"(d[3][3]), "v"(d[0]), "v"(d[2]), "v"(uma), "s"(w0m) : "memory");
    while (true) {
      f32x4 su, c0, c1;
      asm volatile("s_waitcnt lgkmcnt(4)\n" A8 MF "%0, %8, %3, 0\n" MF "%0, %9, %6, %0\n s_nop 0\n s_waitcnt lgkmcnt(2)\n" MF "%1, %10, %4, 0\n" MF "%1, %11, %5, %1\n" MF "%2, %12, %4, 0\n"
                   "s_nop 0\n s_waitcnt lgkmcnt(0)\n" MF "%1, %7, %6, %1\n" MF "%2, %13, %5, %2\n"
                   : "=&v"(su), "=&v"(c0), "=&v"(c1) : "v"(p0), "v"(p1), "v"(p2), "v"(p3), "v"(d[0]), "v"(one), "v"(half), "v"(us[0]), "v"(us[1]), "v"(us[2]), "v"(us[3]) : "memory");
      asm volatile("v_rcp_f32 %0, %0\n" A8 MF "%1, %2, %3, %1\n s_nop 2\n s_nop 1\n"
                   "v_pk_mul_f32 %5, %5, %22 op_sel_hi:[1,0]\n v_pk_mul_f32 %6, %6, %22 op_sel_hi:[1,0]\n v_cvt_pk_bf16_f32 %9, %9, %10\n v_cvt_pk_bf16_f32 %10, %11, %12\n"
                   "v_pk_mul_f32 %7, %7, %22 op_sel_hi:[1,0]\n v_pk_mul_f32 %8, %8, %22 op_sel_hi:[1,0]\n v_and_or_b32 %9, %9, -2, %17\n v_cvt_pk_bf16_f32 %11, %13, %14\n"
                   "v_cvt_pk_bf16_f32 %12, %15, %16\n v_and_or_b32 %10, %10, -2, %17\n v_and_or_b32 %11, %11, -2, %17\n v_and_or_b32 %12, %12, -2, %17\n"
                   "buffer_store_dwordx4 %4, %18, %19, %20 offen sc1\n s_nop 1"
                   :: "v"(su[0]), "v"(c1), "v"(d[2]), "v"(p3), "v"(c0), "v"(LO2(c0)), "v"(HI2(c0)), "v"(LO2(c1)), "v"(HI2(c1)),
                      "v"(c0[0]), "v"(c0[1]), "v"(c0[2]), "v"(c0[3]), "v"(c1[0]), "v"(c1[1]), "v"(c1[2]), "v"(c1[3]), "s"(tg), "v"(r3o), "s"(rs), "s"(sc), "n"(0), "v"(LO2(su)) : "memory");
      SCHED();
      if (__builtin_expect(sc == lim, 1)) break;
      sc = max(sc, 2u * SET_) + SET_;
      pr0 += 1024u; pr3 += 1024u;
      pr1 = lo < 8 ? pr0 : ls + PZ_OFF; pr2 = lo < 8 ? ls + PZ_OFF : pr0;
      asm volatile(RDP "s_waitcnt lgkmcnt(0)" : "=&v"(p0), "=&v"(p3), "=&v"(p1), "=&v"(p2) : "v"(pr0), "v"(pr3), "v"(pr1), "v"(pr2) : "memory");
    }
    TR(6);
    T0(9);
    SLP(0);
    asm volatile("" : "=v"(kx[0]), "=v"(kx[1]), "=v"(kx[2]), "=v"(kx[3]), "=v"(kx[4]), "=v"(kx[5]), "=v"(kx[6]), "=v"(kx[7]));
    if (last) break;
    asm volatile("s_mov_b64 exec, %5\n ds_write_b128 %4, %0\n ds_write_b128 %4, %1 offset:1024\n s_and_b64 exec, exec, %6\n"
                 "ds_write_b128 %4, %2 offset:512\n ds_write_b128 %4, %3 offset:1536\n ds_write_b32 %7, %8\n s_mov_b64 exec, -1"
                 :: "v"(x[0]), "v"(x[1]), "v"(x[2]), "v"(x[3]), "v"(xw), "s"(xwm), "s"(w0m), "v"(ls + XK_OFF + 2048u), "v"(s) : "memory", "scc");
    if (__builtin_expect(s < XS_, 0))
      if (jj & 2) st_sc1s_32(rw, kvo, kmoff(s, h, hb) + (2 * (jj & 1) + xm) * 512u, c);
    const unsigned kc = *(const unsigned*)(lds + KC_OFF);
    useg();
    TRX(0);
    const bool have = __builtin_amdgcn_readfirstlane(kc) == 0x404u;
    unsigned ks = kv_src(s + 2, h, w, hb), kd = kv_dst(s, w), kv = have && s < NSEG_ - 2 ? dvo : 0x80000000u;
    asm volatile("" : "+s"(ks), "+s"(kd), "+v"(kv));
    bar();
#define KVE 2
    if (w == KVE) kv_dma(rs, kv, ks, kd);
    r2b = nm(0u, h >> 3, NB2 + (h & 7) * 2);
    tg ^= sc >> 23;
    sc = sn; sn = sn == 2u * SET_ ? 0u : sn + SET_;
    TRW();
    if (w == 1 && s >= XS_) {
      u32x4 p1, p2, p3;
      u32x2 vm[8];
      asm volatile("ds_read_b128 %0, %11\n ds_read_b128 %1, %12\n ds_read_b128 %2, %13\n"
                   "ds_read_b64_tr_b16 %3, %14\n ds_read_b64_tr_b16 %4, %14 offset:512\n ds_read_b64_tr_b16 %5, %14 offset:8\n ds_read_b64_tr_b16 %6, %14 offset:520\n"
                   "ds_read_b64_tr_b16 %7, %14 offset:1024\n ds_read_b64_tr_b16 %8, %14 offset:1536\n ds_read_b64_tr_b16 %9, %14 offset:1032\n ds_read_b64_tr_b16 %10, %14 offset:1544\n"
                   "s_waitcnt lgkmcnt(0)"
                   : "=&v"(p1), "=&v"(p2), "=&v"(p3), "=&v"(vm[0]), "=&v"(vm[1]), "=&v"(vm[2]), "=&v"(vm[3]), "=&v"(vm[4]), "=&v"(vm[5]), "=&v"(vm[6]), "=&v"(vm[7])
                   : "v"(oa1), "v"(oa2), "v"(oa3), "v"(oam) : "memory");
      const f32x4 su = mfu(one, p3, mfu(one, p2, mfu(one, p1, f32x4{0.f, 0.f, 0.f, 0.f})));
      f32x4 o[4];
#pragma unroll
      for (int i = 0; i < 4; ++i) o[i] = mfu(kx[4 * (i >> 1) + (i & 1)], p1, f32x4{0.f, 0.f, 0.f, 0.f});
#pragma unroll
      for (int i = 0; i < 4; ++i) o[i] = mfu(kx[4 * (i >> 1) + 2 + (i & 1)], p2, o[i]);
#pragma unroll
      for (int i = 0; i < 4; ++i) o[i] = mfu(u32x4{vm[2 * i][0], vm[2 * i][1], vm[2 * i + 1][0], vm[2 * i + 1][1]}, p3, o[i]);
      SCHED();
      const float ir = __builtin_amdgcn_rcpf(su[0]);
      u32x4 oc[2];
#pragma unroll
      for (int i = 0; i < 8; ++i) oc[i >> 2][i & 3] = cvt2x(o[i >> 1][2 * (i & 1)] * ir, o[i >> 1][2 * (i & 1) + 1] * ir);
      *(u32x4*)(lds + OB_OFF + vo) = oc[0];
      *(u32x4*)(lds + OB_OFF + 1024 + vo) = oc[1];
      if (__builtin_expect(s == NL_ - 1, 0)) {
        unsigned lv = vo;
        asm volatile("" : "+v"(lv));
        ostore(rs, oc, ovoff(lv), osoff(s, h, jj, hb));
      }
      TRX(6);
    }
    if (w != KVE) {
      pause(w == 1 && s >= XS_ ? max((int)dlyd - 19, 0) : dlyd);
      kv_dma(rs, kv, ks, kd);
    }
    ++s;
    if (__builtin_expect(!have && s < NSEG_ - 1, 0))
      if (__builtin_amdgcn_readfirstlane(*(const unsigned*)(lds + KC_OFF + 8)) == 0x404u) kv_dma(rs, dvo, ks, kd);
    if (__builtin_expect(s == NSEG_ - 1, 0)) {
      static_assert((NSEG_ - 1) % 3 == 0, "set 0");
      asm volatile("" : "+v"(xo));
      const int qi = 8 * w + (int)((xo >> 4) & 7u) - 4 * (int)(xo >> 8);
#pragma unroll
      for (int r = 0; r < 4; ++r) {
        ma[r] = r <= qi ? 0.f : rbf(-1000000000.0f) / SCALE_;
        mb[r] = 16 + r <= qi ? 0.f : rbf(-1000000000.0f) / SCALE_;
      }
      if (w == 1) {
#pragma unroll
        for (int i = 0; i < 8; ++i) kx[i] = *(const u32x4*)(lds + KV_OFF + 16384 + xo + i * 1024);
        asm volatile("" : "+v"(kx[0]), "+v"(kx[1]), "+v"(kx[2]), "+v"(kx[3]), "+v"(kx[4]), "+v"(kx[5]), "+v"(kx[6]), "+v"(kx[7]));
      }
      xt = (xo & 128u) ? xt : POISON;
      xo += (xo & 128u) ? 0u : OFF_Q + tile8(NSEG_ - 1, h, hb) + (w >> 1) * HALF_ + (w & 1) * 128u - r2b;
    }
    pub();
    TR(2);
  }
  asm volatile("" : "+s"(no));
  if (wg == 0 && w == 0 && vo == 0u) st_sc1_64(rw, OFF_EP, no);
  TRE(w);
}

DEVI void recur_role(char* lds, char* ws, rsrc_t rw, const u16* __restrict__ mem0, const u16* __restrict__ Wq, const u16* __restrict__ Wk,
                     const u16* __restrict__ Wv, const u16* __restrict__ Wo, u16* __restrict__ out, int wg TRARGD) {
  const u32x4 rs = u32x4{(unsigned)(size_t)ws, (unsigned)((size_t)ws >> 32) & 0xffffu, WS_USED, 0x00020000u};
  if (__builtin_amdgcn_readfirstlane(threadIdx.x) >= 256) recur_p1(lds, rw, rs, mem0, Wq, Wk, Wv, out, ws, wg TRARG DLYA);
  else recur_at(lds, rw, rs, Wo, ws, wg TRARG DLYA);
}

#define HTR(i)
#define XSLP 16
#define PD1 8
#define ODS 3
#define OB0 17
#define OB1 20
#define OB2 14
#define OPR 2
#define OLIM 16
#define ON0 4
#define CPF 1
#define OFA 11
#define FSLP 16
template <int SLP>
DEVI unsigned poll_flags(rsrc_t rw, unsigned off, unsigned f) {
#pragma nounroll
  for (int it = 0; it < HSPIN_CAP; ++it) {
    if (__all(f != 0u)) break;
    __builtin_amdgcn_s_sleep(SLP);
    f = ld_sc1_32(rw, off);
  }
  return f;
}
DEVI void after(unsigned f, int d) {
  const unsigned t = __builtin_amdgcn_readfirstlane(f) + (unsigned)d;
#pragma nounroll
  for (int k = 0; k < 4 && t != 1u + (unsigned)d; ++k) {
    const int r = min((int)(t - rt_now()), 4096);
    if (r < 4) break;
#pragma nounroll
    for (int i = r * 9 >> 5; i > 0; --i) __builtin_amdgcn_s_sleep(1);
    if (r < 40) break;
  }
}
DEVI void xattn_wave(rsrc_t rw, int s, int hq, unsigned hb) {
  int tx = threadIdx.x;
  asm volatile("" : "+v"(tx));
  const int l = tx & 63, g = l >> 4, lo = l & 15, h = hq * 8 + __builtin_amdgcn_readfirstlane(tx >> 6);
#pragma nounroll
  for (int it = 0; it < HSPIN_CAP; ++it) {
    const unsigned f = ld_sc1_32(rw, l < 32 ? OFF_CKV + (unsigned)(h * 64 + s) * 4u : cooff(h, hb) + (unsigned)(s * 16 + (l & 3) * 4));
    if (__all((f & 0xffffffu) == (l < 32 ? 0x40404u : 2u))) break;
    __builtin_amdgcn_s_sleep(XSLP);
  }
  asm volatile("" ::: "memory");
  const unsigned lv = (unsigned)l * 16u, so = tile8(s, h, hb);
  u32x4 bq[4][2], kx[8], vx[8];
#pragma unroll
  for (int ks = 0; ks < 4; ++ks) {
#pragma unroll
    for (int n = 0; n < 2; ++n) bq[ks][n] = ld_sc1s(rw, lv + ks * 1024u, OFF_Q + so + n * HALF_);
    kx[ks] = ld_sc1s(rw, lv + ks * 1024u, OFF_K + so);
    kx[4 + ks] = ld_sc1s(rw, lv + ks * 1024u, OFF_K + so + HALF_);
  }
#pragma unroll
  for (int te = 0; te < 8; ++te) vx[te] = ld_sc1s(rw, lv + (te & 3) * 1024u, OFF_VX + so + (te >> 2) * HALF_);
  constexpr float L2E = 1.4426950408889634f;
  float m1[2], s1[2];
  f32x4 os[8][2];
  auto seg_part = [&]() {
    f32x4 st[2][2];
#pragma unroll
    for (int i = 0; i < 4; ++i) st[i >> 1][i & 1] = f32x4{0.f, 0.f, 0.f, 0.f};
#pragma unroll
    for (int ks = 0; ks < 4; ++ks) {
      st[0][0] = mfu(kx[ks], bq[ks][0], st[0][0]);
      st[0][1] = mfu(kx[ks], bq[ks][1], st[0][1]);
      st[1][1] = mfu(kx[4 + ks], bq[ks][1], st[1][1]);
    }
#pragma unroll
    for (int n = 0; n < 2; ++n) {
      float sv[8], mx = -3.0e38f, sum = 0.f;
#pragma unroll
      for (int i = 0; i < 8; ++i) {
        sv[i] = 16 * (i >> 2) + 4 * g + (i & 3) <= 16 * n + lo ? rbf(rbf(st[i >> 2][n][i & 3]) * SCALE_) : rbf(-1000000000.0f);
        mx = fmaxf(mx, sv[i]);
      }
      mx = rows_max(mx);
#pragma unroll
      for (int i = 0; i < 8; ++i) { sv[i] = __builtin_amdgcn_exp2f((sv[i] - mx) * L2E); sum += sv[i]; }
      m1[n] = mx;
      s1[n] = rows_sum(sum);
      const u32x4 pb = u32x4{cvt2(sv[0], sv[1]), cvt2(sv[2], sv[3]), cvt2(sv[4], sv[5]), cvt2(sv[6], sv[7])};
#pragma unroll
      for (int te = 0; te < 8; ++te) os[te][n] = mfu(vx[te], pb, f32x4{0.f, 0.f, 0.f, 0.f});
    }
  };
  u32x4 km[4], vm[4];
#pragma unroll
  for (int i = 0; i < 4; ++i) km[i] = ld_sc1(rw, kmoff(s, h, hb) + (unsigned)(i * 512 + g * 128 + (lo & 7) * 16));
#pragma unroll
  for (int i = 0; i < 4; ++i) vm[i] = ld_sc1(rw, kmoff(s, h, hb) + 2048u + (unsigned)(i * 512 + (l & 31) * 16));
  seg_part();
  f32x4 sm[2];
#pragma unroll
  for (int ks = 0; ks < 4; ++ks)
#pragma unroll
    for (int n = 0; n < 2; ++n) sm[n] = mfu(km[ks], bq[ks][n], ks ? sm[n] : f32x4{0.f, 0.f, 0.f, 0.f});
  s16x4 pm[2];
  float cs[2];
#pragma unroll
  for (int n = 0; n < 2; ++n) {
    float sv[4], mx = m1[n], sum = 0.f;
#pragma unroll
    for (int r = 0; r < 4; ++r) {
      sv[r] = g < 2 ? rbf(rbf(sm[n][r]) * SCALE_) : -INFINITY;
      mx = fmaxf(mx, sv[r]);
    }
    mx = rows_max(mx);
#pragma unroll
    for (int r = 0; r < 4; ++r) { sv[r] = __builtin_amdgcn_exp2f((sv[r] - mx) * L2E); sum += sv[r]; }
    const float cr = __builtin_amdgcn_exp2f((m1[n] - mx) * L2E);
    const float inv = __builtin_amdgcn_rcpf(__builtin_fmaf(s1[n], cr, rows_sum(sum)));
    cs[n] = cr * inv;
    pm[n] = __builtin_bit_cast(s16x4, u32x2{cvt2(sv[0] * inv, sv[1] * inv), cvt2(sv[2] * inv, sv[3] * inv)});
  }
#pragma unroll
  for (int n = 0; n < 2; ++n)
#pragma unroll
    for (int a = 0; a < 4; ++a) {
      f32x4 o[2];
#pragma unroll
      for (int i = 0; i < 2; ++i)
        o[i] = __builtin_amdgcn_mfma_f32_16x16x16bf16_1k(__builtin_bit_cast(s16x4, u32x2{vm[a][2 * i], vm[a][2 * i + 1]}), pm[n], os[2 * a + i][n] * cs[n], 0, 0, 0);
      st_sc1(rw, ooff(s, h, hb) + n * HALF_ + (unsigned)(a * 1024 + l * 16),
             u32x4{cvt2x(o[0][0], o[0][1]), cvt2x(o[0][2], o[0][3]), cvt2x(o[1][0], o[1][1]), cvt2x(o[1][2], o[1][3])});
    }
  drain_vm();
  if (l < 4) st_sc1_32(rw, cooff(h, hb) + (unsigned)(s * 16 + l * 4), 1u);
}

typedef f32x2 cf;
DEVI cf cmul(cf a, cf b) { return __builtin_elementwise_fma(cf{a.y, a.y}, cf{-b.y, b.x}, cf{a.x, a.x} * b); }
DEVI cf mni(cf a) { return cf{a.y, -a.x}; }
DEVI cf wexp(float r) {
  float sn, cs;
  sincospif(r, &sn, &cs);
  return cf{cs, -sn};
}
DEVI void dft4(cf& a0, cf& a1, cf& a2, cf& a3) {
  const cf t0 = a0 + a2, t1 = a0 - a2, t2 = a1 + a3, t3 = mni(a1 - a3);
  a0 = t0 + t2; a1 = t1 + t3; a2 = t0 - t2; a3 = t1 - t3;
}
constexpr float RH_ = 0.70710678118654752f, C8_ = 0.92387953251128674f, S8_ = 0.38268343236508977f;
DEVI cf w8_1(cf a) { return (a + mni(a)) * RH_; }
DEVI cf w8_3(cf a) { return (mni(a) - a) * RH_; }
template <int ST>
DEVI void dft8(cf* v) {
  dft4(v[0], v[2 * ST], v[4 * ST], v[6 * ST]);
  dft4(v[ST], v[3 * ST], v[5 * ST], v[7 * ST]);
  const cf e[4] = {v[0], v[2 * ST], v[4 * ST], v[6 * ST]}, o[4] = {v[ST], w8_1(v[3 * ST]), mni(v[5 * ST]), w8_3(v[7 * ST])};
#pragma unroll
  for (int c = 0; c < 4; ++c) { v[c * ST] = e[c] + o[c]; v[(c + 4) * ST] = e[c] - o[c]; }
}
DEVI void dft16(cf* v) {
#pragma unroll
  for (int b = 0; b < 4; ++b) dft4(v[b], v[4 + b], v[8 + b], v[12 + b]);
  v[5] = cmul(v[5], cf{C8_, -S8_}); v[6] = w8_1(v[6]); v[7] = cmul(v[7], cf{S8_, -C8_});
  v[9] = w8_1(v[9]); v[10] = mni(v[10]); v[11] = w8_3(v[11]);
  v[13] = cmul(v[13], cf{S8_, -C8_}); v[14] = w8_3(v[14]); v[15] = cmul(v[15], cf{-C8_, S8_});
  cf y[16];
#pragma unroll
  for (int c = 0; c < 4; ++c) {
    dft4(v[4 * c], v[4 * c + 1], v[4 * c + 2], v[4 * c + 3]);
#pragma unroll
    for (int d = 0; d < 4; ++d) y[c + 4 * d] = v[4 * c + d];
  }
#pragma unroll
  for (int i = 0; i < 16; ++i) v[i] = y[i];
}
template <int N, int ST>
DEVI void twiddle(cf* v, const cf* b) {
  cf w[8];
  w[1] = b[0]; w[2] = b[1]; w[4] = b[2];
  w[3] = cmul(w[1], w[2]); w[5] = cmul(w[1], w[4]); w[6] = cmul(w[2], w[4]); w[7] = cmul(w[3], w[4]);
#pragma unroll
  for (int i = 1; i < 8; ++i) v[i * ST] = cmul(v[i * ST], w[i]);
  if (N == 16) {
    v[8 * ST] = cmul(v[8 * ST], b[3]);
#pragma unroll
    for (int i = 1; i < 8; ++i) v[(8 + i) * ST] = cmul(v[(8 + i) * ST], cmul(w[i], b[3]));
  }
}
template <int N>
DEVI void wpow(cf* b, float r) {
#pragma unroll
  for (int i = 0; i < N; ++i) b[i] = wexp(r * (float)(1 << i));
}
DEVI u32x4 cc2u(cf a, cf b) { return u32x4{__float_as_uint(a.x), __float_as_uint(a.y), __float_as_uint(b.x), __float_as_uint(b.y)}; }

DEVI void fft2048(int t, char* lds, cf* v, const cf* b1, const cf* b2) {
  const int p = t & 3, j = t >> 2;
  dft16(v);
  twiddle<16, 1>(v, b1);
#pragma unroll
  for (int i = 0; i < 16; ++i) *(cf*)(lds + i * 4096 + t * 8) = v[i];
  bar();
#pragma unroll
  for (int i = 0; i < 16; ++i) v[i] = *(const cf*)(lds + (j >> 3) * 4096 + i * 256 + (t & 31) * 8);
  dft16(v);
  twiddle<16, 1>(v, b2);
  char* l2 = lds + 65536;
#pragma unroll
  for (int i = 0; i < 16; ++i) *(cf*)(l2 + (j & 7) * 8224 + i * 512 + (j >> 3) * 32 + p * 8) = v[i];
  bar();
#pragma unroll
  for (int i = 0; i < 16; ++i) v[i] = *(const cf*)(l2 + (i >> 1) * 8224 + (i & 1) * 4096 + t * 8);
  dft8<2>(v);
  dft8<2>(v + 1);
}
DEVI void fft_cols_fwd(char* lds, rsrc_t rw, int blk, unsigned hb) {
  int t = threadIdx.x;
  asm volatile("" : "+v"(t));
  const int p = t & 3, j = t >> 2;
  u32x4 xv[16];
#pragma unroll
  for (int i = 0; i < 16; ++i) xv[i] = ld_sc1s(rw, (j >> 4) * 65536 + p * 256 + (j & 15) * 16, OFF_XF + far8(32 * i + (blk >> 3), hb) + (blk & 7) * 1024u);
  cf b1[4], b2[4];
  wpow<4>(b1, (float)j * (1.f / 1024.f));
  wpow<4>(b2, (float)(j & 7) * (1.f / 64.f));
#pragma nounroll
  for (int it = 0; it < 4; ++it) {
    asm volatile("" : "+v"(t), "+v"(b1[0]), "+v"(b1[1]), "+v"(b1[2]), "+v"(b1[3]), "+v"(b2[0]), "+v"(b2[1]), "+v"(b2[2]), "+v"(b2[3]));
    cf v[16];
#pragma unroll
    for (int i = 0; i < 16; ++i) {
      v[i] = cf{__uint_as_float(xv[i][0] << 16), __uint_as_float(xv[i][0] & 0xffff0000u)};
      xv[i] = u32x4{xv[i][1], xv[i][2], xv[i][3], 0u};
    }
    bar();
    fft2048(t, lds, v, b1, b2);
#pragma unroll
    for (int i = 8; i < 16; ++i) *(cf*)(lds + (i - 8) * 4096 + t * 8) = v[i];
    bar();
#pragma unroll
    for (int i = 0; i < 8; ++i) {
      const int k = (t >> 2) + 128 * i;
      const cf z = v[i], c = *(const cf*)(lds + ((1024 - k) & 1023) * 32 + (t & 3) * 8);
      cf A = cf{z.x + c.x, z.y - c.y}, B = cf{z.y + c.y, c.x - z.x};
      if (k == 0) { A = cf{2.f * z.x, 2.f * c.x}; B = cf{2.f * z.y, 2.f * c.y}; }
      st_sc1(rw, OFF_T + far8a(128 * i, hb) + (unsigned)((t >> 2) * 16384 + blk * 256 + it * 64 + (t & 3) * 16), cc2u(A, B));
    }
  }
}
#define RPF 1
#define NTF 1
DEVI unsigned wpair(const u16* __restrict__ wr, const u16* __restrict__ wi, int o) {
  return NTF ? pack2(__builtin_nontemporal_load(wr + o), __builtin_nontemporal_load(wi + o)) : pack2(wr[o], wi[o]);
}
typedef unsigned u32x4u __attribute__((ext_vector_type(4), aligned(2)));
DEVI void fft_rows_ask(const u16* __restrict__ wr, const u16* __restrict__ wi, int row, int l, u32x4* raw, unsigned* wv) {
  const int rm = (2048 - row) & 2047;
#pragma unroll
  for (int i = 0; i < 4; ++i) raw[i] = __builtin_nontemporal_load((const u32x4u*)(((i & 1) ? wi : wr) + ((i & 2) ? rm * FB_ + 1 : row * FB_) + 8 * l));
  wv[16] = wpair(wr, wi, (l & 1) ? row * FB_ + 512 : rm * FB_);
}
DEVI void fft_rows_w(char* wl, int l, const u32x4* raw, unsigned* wv) {
#pragma unroll
  for (int i = 0; i < 4; ++i) {
    const u32x4 a = raw[i & 2], b = raw[(i & 2) + 1];
    const int k = 2 * (i & 1);
    *(u32x4*)(wl + (i >> 1) * 2048 + l * 32 + (i & 1) * 16) = u32x4{__builtin_amdgcn_perm(b[k], a[k], 0x05040100u), __builtin_amdgcn_perm(b[k], a[k], 0x07060302u),
                                                                   __builtin_amdgcn_perm(b[k + 1], a[k + 1], 0x05040100u), __builtin_amdgcn_perm(b[k + 1], a[k + 1], 0x07060302u)};
  }
#pragma unroll
  for (int i = 0; i < 16; ++i) wv[i] = *(const unsigned*)(wl + (i < 8 ? (l + 64 * i) * 4 : 2048 + (1023 - l - 64 * i) * 4));
  wv[17] = __builtin_amdgcn_update_dpp(0u, wv[16], 0x101, 0xf, 0xf, true);
}
DEVI cf wval(const unsigned* wv, int i, int l) {
  cf w = cf{__uint_as_float(wv[i] << 16), __uint_as_float(wv[i] & 0xffff0000u)};
  if (i >= 8) w.y = -w.y;
  if ((i & 7) == 0) {
    const unsigned e = wv[16 + (i >> 3)];
    const cf m = (w + cf{__uint_as_float(e << 16), __uint_as_float((e & 0xffff0000u) ^ (i ? 0u : 0x80000000u))}) * 0.5f;
    if (l == 0) w = m;
  }
  return w;
}
DEVI void fft_rows(char* lds, rsrc_t rw, const u16* __restrict__ wr, const u16* __restrict__ wi, int blk, unsigned hb) {
  int t = threadIdx.x;
  asm volatile("" : "+v"(t));
  int l = t & 63, row = blk * 8 + __builtin_amdgcn_readfirstlane(t >> 6);
  char* wl = lds + __builtin_amdgcn_readfirstlane(t >> 6) * 8704;
  unsigned wv[18];
  u32x4 in[8], raw[4];
  auto get = [&](int r) {
#pragma unroll
    for (int i = 0; i < 8; ++i) in[i] = ld_sc1s(rw, (l & 48) * 16 + (l & 3) * 64 + (l & 12) * 4 + (i & 3) * 1024, OFF_T + far8a(r, hb) + (i >> 2) * 4096u);
    fft_rows_ask(wr, wi, r, l, raw, wv);
  };
  get(row);
  cf b1[2][3], b2[4];
  wpow<3>(b1[0], (float)(2 * l) * (1.f / 512.f));
  wpow<3>(b1[1], (float)(2 * l + 1) * (1.f / 512.f));
  wpow<4>(b2, (float)(l & 7) * (1.f / 64.f));
#pragma nounroll
  for (int it = 0; it < 4; ++it, row += 256) {
  auto fresh = [&]() {
    asm volatile("" : "+v"(l), "+v"(b1[0][0]), "+v"(b1[0][1]), "+v"(b1[0][2]), "+v"(b1[1][0]), "+v"(b1[1][1]), "+v"(b1[1][2]), "+v"(b2[0]), "+v"(b2[1]), "+v"(b2[2]), "+v"(b2[3]));
  };
  fresh();
  const unsigned go = far8a(row, hb) + (unsigned)(l * 16);
  cf v[16];
#pragma unroll
  for (int i = 0; i < 8; ++i) {
    v[2 * i] = cf{__uint_as_float(in[i][0]), __uint_as_float(in[i][1])};
    v[2 * i + 1] = cf{__uint_as_float(in[i][2]), __uint_as_float(in[i][3])};
  }
  dft8<2>(v); dft8<2>(v + 1);
  twiddle<8, 2>(v, b1[0]); twiddle<8, 2>(v + 1, b1[1]);
#pragma unroll
  for (int i = 0; i < 8; ++i) *(u32x4*)(wl + i * 1088 + l * 16) = cc2u(v[2 * i], v[2 * i + 1]);
#pragma unroll
  for (int i = 0; i < 16; ++i) v[i] = *(const cf*)(wl + (l >> 3) * 1088 + i * 64 + (l & 7) * 8);
  dft16(v);
  twiddle<16, 1>(v, b2);
#pragma unroll
  for (int i = 0; i < 16; ++i) *(cf*)(wl + (l & 7) * 1040 + i * 64 + (l >> 3) * 8) = v[i];
#pragma unroll
  for (int i = 0; i < 16; ++i) v[i] = *(const cf*)(wl + (i >> 1) * 1040 + (i & 1) * 512 + l * 8);
  dft8<2>(v); dft8<2>(v + 1);
  fft_rows_w(wl, l, raw, wv);
  if (row == 0) {
    unsigned w1[18];
    fft_rows_ask(wr, wi, 1024, l, raw, w1);
    fft_rows_w(wl, l, raw, w1);
#pragma unroll
    for (int i = 0; i < 16; ++i) *(cf*)(wl + i * 512 + l * 8) = v[i];
#pragma unroll
    for (int i = 0; i < 16; ++i) {
      const cf c = *(const cf*)(wl + ((1024 - l - 64 * i) & 1023) * 8), wa = wval(wv, i, l), wb = wval(w1, i, l);
      v[i] = cmul(v[i], (wa + wb) * 0.5f) + cmul(cf{c.x, -c.y}, (wa - wb) * 0.5f);
    }
  } else {
#pragma unroll
    for (int i = 0; i < 16; ++i) v[i] = cmul(v[i], wval(wv, i, l));
  }
#pragma unroll
  for (int i = 0; i < 16; ++i) v[i] = cf{v[i].y, v[i].x};
  asm volatile("" : "+v"(v[0]), "+v"(v[8]), "+v"(v[15]) :: "memory");
  if (it < 3) get(row + 256);
  asm volatile("" ::: "memory");
  fresh();
  dft8<2>(v); dft8<2>(v + 1);
#pragma unroll
  for (int i = 0; i < 16; ++i) *(cf*)(wl + (i >> 1) * 1056 + (i & 1) * 512 + l * 8) = v[i];
#pragma unroll
  for (int i = 0; i < 16; ++i) v[i] = *(const cf*)(wl + (l & 7) * 1056 + i * 64 + (l >> 3) * 8);
  twiddle<16, 1>(v, b2);
  dft16(v);
#pragma unroll
  for (int i = 0; i < 16; ++i) *(cf*)(wl + (l >> 3) * 1088 + i * 64 + (l & 7) * 8) = v[i];
#pragma unroll
  for (int i = 0; i < 8; ++i) {
    const f32x4 a = *(const f32x4*)(wl + i * 1088 + l * 16);
    v[2 * i] = cf{a[0], a[1]}; v[2 * i + 1] = cf{a[2], a[3]};
  }
  twiddle<8, 2>(v, b1[0]); twiddle<8, 2>(v + 1, b1[1]);
  dft8<2>(v); dft8<2>(v + 1);
#pragma unroll
  for (int i = 0; i < 8; ++i) st_sc1(rw, OFF_T + go + i * 1024u, cc2u(cf{v[2 * i].y, v[2 * i].x}, cf{v[2 * i + 1].y, v[2 * i + 1].x}));
  }
}
DEVI void fft_cols_inv(char* lds, rsrc_t rw, int blk, unsigned hb) {
  int t = threadIdx.x;
  asm volatile("" : "+v"(t));
  const int j = t >> 2;
  u32x4 in[8];
  auto get = [&](int b) {
#pragma unroll
    for (int i = 0; i < 8; ++i) in[i] = ld_sc1s(rw, (unsigned)((t >> 2) * 16384 + (t & 3) * 16), OFF_T + far8a(128 * i, hb) + (unsigned)b * 64u);
  };
  get(blk);
  cf b1[4], b2[4];
  wpow<4>(b1, (float)j * (1.f / 1024.f));
  wpow<4>(b2, (float)(j & 7) * (1.f / 64.f));
#pragma nounroll
  for (int it = 0; it < 4; ++it, blk += 8) {
    asm volatile("" : "+v"(t), "+v"(b1[0]), "+v"(b1[1]), "+v"(b1[2]), "+v"(b1[3]), "+v"(b2[0]), "+v"(b2[1]), "+v"(b2[2]), "+v"(b2[3]));
    cf v[16];
#pragma unroll
    for (int i = 0; i < 8; ++i) {
      const f32x4 a = __builtin_bit_cast(f32x4, in[i]);
      cf m = cf{a[2] - a[1], a[0] + a[3]};
      v[i] = cf{a[1] + a[2], a[0] - a[3]};
      if (i == 0 && t < 4) { v[0] = cf{a[2], a[0]}; m = cf{a[3], a[1]}; }
      *(cf*)(lds + 65536 + i * 4096 + t * 8) = m;
    }
    asm volatile("" : "+v"(v[0]), "+v"(v[1]), "+v"(v[2]), "+v"(v[3]), "+v"(v[4]), "+v"(v[5]), "+v"(v[6]), "+v"(v[7]) :: "memory");
    if (it < 3) get(blk + 8);
    bar();
#pragma unroll
    for (int i = 8; i < 16; ++i) v[i] = *(const cf*)(lds + 65536 + ((2048 - 128 * i - (t >> 2)) & 1023) * 32 + (t & 3) * 8);
    fft2048(t, lds, v, b1, b2);
    const float sc = 0.5f / ((float)S_ * (float)D_);
#pragma unroll
    for (int i = 0; i < 16; ++i) *(unsigned*)(lds + i * 2048 + t * 4) = cvt2(v[i].y * sc, v[i].x * sc);
    bar();
#pragma unroll
    for (int i = 0; i < 4; ++i) st_sc1(rw, bfoff(blk, i * 512, hb) + (unsigned)t * 16u, *(const u32x4*)(lds + i * 8192 + t * 16));
  }
}

DEVI void oproj_worker(char* lds, rsrc_t rw, const u16* __restrict__ Wo, int set, int c, int q, u16* __restrict__ out, unsigned hb DLYARG
) {
  DLYI(0x300);
  int t = threadIdx.x;
  asm volatile("" : "+v"(t));
  const int l = t & 63, w = __builtin_amdgcn_readfirstlane(t >> 6), g = l >> 4, lo = l & 15, hh = 4 * q + (w >> 1);
  constexpr int sstep = 4;
  const int s0 = set < 3 ? set - ON0 : 3 * ON0 + 3;
  auto seg = [&](int s) { return s >= 3 * ON0 ? s : (s < s0 ? 0 : ((s - s0) >> 2) * 3 + set); };
  u32x4 a[4][4][2];
  __syncthreads();
  {
    const u16* gp = Wo + (size_t)(hh * DH_ + g) * D_ + c * 256 + (w & 1) * 128 + (lo ^ (4 * g)) * 8;
    auto ask = [&](int p) {
#pragma unroll
      for (int i = 0; i < 4; ++i)
        __builtin_amdgcn_global_load_lds((const unsigned*)(gp + (size_t)(16 * p + 4 * i) * D_), (unsigned*)(lds + p % 5 * 32768 + w * 4096 + i * 1024 + l * 16), 16, 0, NTO);
      SCHED();
    };
#pragma unroll
    for (int p = 0; p < 5; ++p) ask(p);
    const char* pp = lds + (w >> 2) * 4096 + (4 * g + (lo >> 2)) * 256 + ((4 * (w & 3)) ^ lo) * 16;
#pragma unroll
    for (int n = 0; n < 8; ++n) {
      asm volatile("s_waitcnt vmcnt(%0) lgkmcnt(0)\n s_barrier" :: "n"(4 * (n == 0 ? 4 : (n < 5 ? 3 : 7 - n))) : "memory");
      if (n >= 1 && n < 4) ask(n + 4);
#pragma unroll
      for (int i = 0; i < 8; ++i) {
        const u32x2 x0 = tr_quad(pp + n % 5 * 32768 + (i >> 1) * 8192 + (i & 1) * 8);
        a[i >> 1][n >> 1][i & 1][2 * (n & 1)] = x0[0];
        a[i >> 1][n >> 1][i & 1][2 * (n & 1) + 1] = x0[1];
      }
      SCHED();
    }
    bar();
  }
  const unsigned vo = (unsigned)l * 16u;
  const unsigned fo = cooff(4 * q, hb) + (unsigned)(((l >> 2) & 3) * 1024 + (l & 3) * 4);
  auto flags = [&](int s) { return ld_sc1_32(rw, fo + (unsigned)((l & 16) ? seg(s + sstep) : seg(s)) * 16u); };
  auto fbits = [&](int s, unsigned f) {
    const unsigned m = (unsigned)__builtin_amdgcn_ballot_w64((f & 1u) != 0u);
    return ((m & 0xffffu) == 0xffffu && s < NL_ ? 1u : 0u) | (m == 0xffffffffu && s + sstep < NL_ ? 2u : 0u);
  };
  auto wait_b = [&](int s) {
#pragma nounroll
    for (int it = 0; it < HSPIN_CAP; ++it) {
      if (__all((ld_sc1_32(rw, cooff(hh, hb) + (unsigned)(seg(s) * 16 + (l & 3) * 4)) & 1u) != 0u)) break;
      __builtin_amdgcn_s_sleep(XSLP);
    }
    asm volatile("" ::: "memory");
  };
  auto load_b = [&](int s, int par, int i, unsigned vd) {
    auto* dst = (__attribute__((address_space(3))) void*)(LDS3 + par * 32768 + w * 4096);
    const unsigned so = ooff(seg(s), hh, hb) + (w & 1) * HALF_;
    if (i == 0) __builtin_amdgcn_raw_ptr_buffer_load_lds(rw, dst, 16, vd, so, 0, 16);
    if (i == 1) __builtin_amdgcn_raw_ptr_buffer_load_lds(rw, dst, 16, vd, so, 1024, 16);
    if (i == 2) __builtin_amdgcn_raw_ptr_buffer_load_lds(rw, dst, 16, vd, so, 2048, 16);
    if (i == 3) __builtin_amdgcn_raw_ptr_buffer_load_lds(rw, dst, 16, vd, so, 3072, 16);
  };
  auto pxs = [&](int slot) { return OFF_PX + far8a((slot * 4 + c) * 16 + (w >> 2), hb) + (unsigned)(w & 3) * 2048u; };
  auto pxo = [&](int s) { return pxs(s < FS_ ? s & 31 : s - FS_ + 32); };
  for (int s = FS_ + (set + sstep - FS_ % sstep) % sstep; s < NL_; s += sstep) {
    st_sc1s(rw, vo, pxo(s) + q * 32768u, u32x4{POISON, POISON, POISON, POISON});
    st_sc1s(rw, vo + 1024u, pxo(s) + q * 32768u, u32x4{POISON, POISON, POISON, POISON});
    SCHED();
    asm volatile("s_nop 1");
    SCHED();
  }
  const unsigned sv = (unsigned)((lo >> 2) * 256 + g * 64 + (lo & 3) * 16);
  auto flag = [&](int s) { if (t == 64) st_sc1_32(rw, OFF_CPX + (unsigned)(seg(s) * 4 + c) * 128u + q * 4u, 1u); };
  float* gl = (float*)(lds + 132096);
  u32x4 pv[2];
  float gv;
  unsigned bb;
  auto pf = [&](int s) { return ld_sc1_32(rw, OFF_CPX + (unsigned)(seg(s) * 4 + c) * 128u + (l & 7) * 4u); };
  auto fin_a = [&](int s, bool ok) {
    if (!ok && s < FS_) {
#pragma nounroll
      for (int it = 0; it < HSPIN_CAP; ++it) {
        if (__all(pf(s) == 1u)) break;
        __builtin_amdgcn_s_sleep(2);
      }
    }
    int ll = l;
    asm volatile("" : "+v"(ll));
    gv = gl[seg(s) * 4 + (ll & 3)];
    bb = __builtin_amdgcn_raw_buffer_load_b32(rw, (ll & 12) * 16384 + (ll & 3) * 16 + (ll >> 4) * 4, bfoff(32 * c + 4 * w, seg(s) * SEG_ + 4 * q, hb), 16);
    if (s >= FS_) __builtin_amdgcn_s_sleep(PD1);
#pragma nounroll
    for (int it = 0; it < HSPIN_CAP; ++it) {
      asm volatile("" ::: "memory");
#pragma unroll
      for (int k = 0; k < 2; ++k) pv[k] = ld_sc1s(rw, (ll >> 4) * 32768 + (ll & 15) * 16, pxo(s) + k * 131072u + q * 256u);
      if (s < FS_ || !bad(umax5(pv[1], umax5(pv[0], 0u)))) break;
      __builtin_amdgcn_s_sleep(1);
    }
  };
  auto fin_b = [&](int s) {
    int ll = l;
    asm volatile("" : "+v"(ll));
    const unsigned s1 = (ll & 2) ? 0u : 0x3f80u << (16 * (ll & 1));
    const u32x4 sel = u32x4{(ll & 12) == 0 ? s1 : 0u, (ll & 12) == 4 ? s1 : 0u, (ll & 12) == 8 ? s1 : 0u, (ll & 12) == 12 ? s1 : 0u};
    const f32x4 v = mfu(sel, pv[1], mfu(sel, pv[0], f32x4{0.f, 0.f, 0.f, 0.f}));
    const float omg = rbf(1.0f - gv);
    const unsigned y = cvt2(omg * __uint_as_float(bb << 16), omg * __uint_as_float(bb & 0xffff0000u));
    const unsigned av = cvt2(v[0], v[1]);
    const unsigned x = cvt2(gv * __uint_as_float(av << 16), gv * __uint_as_float(av & 0xffff0000u));
    const unsigned r = cvt2(__uint_as_float(x << 16) + __uint_as_float(y << 16), __uint_as_float(x & 0xffff0000u) + __uint_as_float(y & 0xffff0000u));
    __builtin_amdgcn_raw_buffer_store_b32(r, __builtin_amdgcn_make_buffer_rsrc(out, 0, S_ * D_ * 2, 0x00020000), (ll & 3) * (D_ * 2) + (ll & 12) * 4 + (ll >> 4) * 4,
                                          (unsigned)((seg(s) * SEG_ + 4 * q) * (D_ * 2) + (256 * c + 32 * w) * 2), OAUX);
  };
  f32x4 acc[2][2];
  auto send = [&](unsigned po) {
    u32x4 u[2];
#pragma unroll
    for (int i = 0; i < 8; ++i) u[i >> 2][i & 3] = cvt2x(acc[i >> 2][(i >> 1) & 1][2 * (i & 1)], acc[i >> 2][(i >> 1) & 1][2 * (i & 1) + 1]);
    asm volatile("" : "+v"(u[0]), "+v"(u[1]));
    st_sc1s(rw, sv, po + q * 32768u, u[0]);
    st_sc1s(rw, sv + 1024u, po + q * 32768u, u[1]);
  };
  auto touch = [&](int s, bool on) {
    if (w == 5)
      __builtin_amdgcn_raw_ptr_buffer_load_lds(rw, (__attribute__((address_space(3))) void*)(LDS3 + 131328), 4, on ? (l >> 5) * HALF_ + (l & 31) * 128u : 0x80000000u,
                                               ooff(s, 4 * q + c, hb), 0, 16);
  };
  auto rdo = [](int j) { return (j >> 3) * 8192 + ((j & 1) * 4 + ((j >> 1) & 3)) * 1024; };
#pragma unroll
  for (int k = 0; k < 2; ++k) {
    wait_b(s0 + k * sstep);
#pragma unroll
    for (int i = 0; i < 4; ++i) load_b(s0 + k * sstep, k, i, vo);
  }
  unsigned* vote = (unsigned*)(lds + 131072);
  unsigned fl = flags(s0 + 2 * sstep);
  drain_vm();
  wg_wait_s(rw, OFF_CF + 256u, 32u);
  if (w == 0) *(u32x4*)(gl + 4 * l) = ld_sc1(rw, OFF_G + (unsigned)(l * SEG_ + 4 * q) * 4u);
  HTR(10);
  int par = 0, s1 = s0;
  {
    constexpr unsigned NOW = 0x80000000u;
    const unsigned s1_ = (l & 2) ? 0u : 0x3f80u << (16 * (l & 1));
    const u32x4 sel = u32x4{(l & 12) == 0 ? s1_ : 0u, (l & 12) == 4 ? s1_ : 0u, (l & 12) == 8 ? s1_ : 0u, (l & 12) == 12 ? s1_ : 0u};
    const unsigned ob = (l & 12) * 16384 + (l & 3) * 16 + g * 4, op = g * 32768 + lo * 16, oo = (l & 3) * (D_ * 2) + (l & 12) * 4 + g * 4;
    const unsigned ofx = l < 32 ? fo : OFF_CPX + (unsigned)(c * 128 + (l & 7) * 4);
    const rsrc_t ro = __builtin_amdgcn_make_buffer_rsrc(out, 0, S_ * D_ * 2, 0x00020000);
    u32x4 b[8];
#pragma unroll
    for (int j = 0; j < 8; ++j) b[j] = *(const u32x4*)(lds + l * 16 + rdo(j));
    unsigned vt, px[4] = {0u, 0u, 0u, 0u};
    pv[0] = pv[1] = u32x4{0u, 0u, 0u, 0u};
    gv = 0.f; bb = 0u;
    int rn = 0, sg[9];
#pragma unroll
    for (int i = 0; i < 9; ++i) sg[i] = seg(s0 + (i - 4) * sstep);
    if (OPR == 1 && w >= 4) __builtin_amdgcn_s_setprio(1);
    if (OPR == 2 && w < 4) __builtin_amdgcn_s_setprio(1);
#pragma nounroll
    do {
      const int s = s1;
      const char* bp = lds + par * 32768 + l * 16;
      const char* bn = lds + ((par + 1) & 3) * 32768 + l * 16;
      unsigned vd, so, x, y;
      f32x4 v;
      SCHED();
#pragma unroll
      for (int j = 0; j < 32; ++j) {
        if (j >= OB1 && j < OB1 + 4 * ODS && (j - OB1) % ODS == 0) {
          auto* dst = (__attribute__((address_space(3))) void*)(LDS3 + (par ^ 2) * 32768 + w * 4096);
          if (j == OB1) __builtin_amdgcn_raw_ptr_buffer_load_lds(rw, dst, 16, vd, so, 0, 16);
          if (j == OB1 + ODS) __builtin_amdgcn_raw_ptr_buffer_load_lds(rw, dst, 16, vd, so, 1024, 16);
          if (j == OB1 + 2 * ODS) __builtin_amdgcn_raw_ptr_buffer_load_lds(rw, dst, 16, vd, so, 2048, 16);
          if (j == OB1 + 3 * ODS) __builtin_amdgcn_raw_ptr_buffer_load_lds(rw, dst, 16, vd, so, 3072, 16);
        }
#pragma unroll
        for (int ct = 0; ct < 2; ++ct) acc[j & 1][ct] = mfu(a[j >> 3][(j >> 1) & 3][ct], b[j & 7], j >= 2 ? acc[j & 1][ct] : f32x4{0.f, 0.f, 0.f, 0.f});
        b[j & 7] = *(const u32x4*)((j < 24 ? bp : bn) + rdo((j + 8) & 31));
        if (j == 2) { px[3] = px[2]; px[2] = px[1]; px[1] = px[0]; px[0] = pxo(s); asm volatile("" : "+s"(px[0])); }
        if (j == 5) { so = ooff(sg[6], hh, hb) + (w & 1) * HALF_; asm volatile("" : "+s"(so)); }
        if (j == OB2 - 5) {
          v = mfu(sel, pv[1], mfu(sel, pv[0], f32x4{0.f, 0.f, 0.f, 0.f}));
          const float omg = rbf(1.0f - gv);
          y = cvt2(omg * __uint_as_float(bb << 16), omg * __uint_as_float(bb & 0xffff0000u));
          asm volatile("" : "+v"(y));
        }
        if (j == OB2 - 3) {
          const unsigned av = cvt2(v[0], v[1]);
          x = cvt2(gv * __uint_as_float(av << 16), gv * __uint_as_float(av & 0xffff0000u));
          asm volatile("" : "+v"(x));
        }
        if (j == OB2 - 2)
          __builtin_amdgcn_raw_buffer_store_b32(cvt2(__uint_as_float(x << 16) + __uint_as_float(y << 16), __uint_as_float(x & 0xffff0000u) + __uint_as_float(y & 0xffff0000u)),
                                                ro, oo | (rn >= 4 ? 0u : NOW), (unsigned)((sg[0] * SEG_ + 4 * q) * (D_ * 2) + (256 * c + 32 * w) * 2), OAUX);
        SCHED();
        if (j == OB0 - 9 && w == 0) {
          unsigned fb = fbits(s + 2 * sstep, fl);
          if (__builtin_expect(!(fb & 1u) && s < OLIM, 0)) {
#pragma nounroll
            for (int it = 0; it < HSPIN_CAP && !(fb & 1u); ++it) {
              __builtin_amdgcn_s_sleep(8);
              fb = fbits(s + 2 * sstep, flags(s + 2 * sstep));
            }
          }
          vote[par] = fb;
          if (__builtin_expect((__builtin_amdgcn_ballot_w64(fl == 1u) >> 32) != 0xffffffffu && rn >= 3, 0)) {
#pragma nounroll
            for (int it = 0; it < HSPIN_CAP; ++it) {
              if (__all(pf(s - 3 * sstep) == 1u)) break;
              __builtin_amdgcn_s_sleep(2);
            }
          }
        }
        if (j == OB0 + 1) {
#pragma unroll
          for (int k = 0; k < 2; ++k) pv[k] = ld_sc1s(rw, op | (rn >= 3 ? 0u : NOW), px[3] + k * 131072u + q * 256u);
        }
        if (j == OB1 + 1) {
          gv = gl[sg[1] * 4 + (l & 3)];
          bb = __builtin_amdgcn_raw_buffer_load_b32(rw, ob, bfoff(32 * c + 4 * w, sg[1] * SEG_ + 4 * q, hb), 16);
        }
        if (j == OB0) {
          wait_vm<1>();
          __builtin_amdgcn_s_barrier();
          asm volatile("" ::: "memory");
          vt = vote[par];
          if (rn >= 1 && t == 64) st_sc1_32(rw, OFF_CPX + (unsigned)(sg[3] * 4 + c) * 128u + q * 4u, 1u);
        }
        if (j == OB0 && w == 0) fl = ld_sc1_32(rw, ofx + (l < 32 ? (unsigned)((l & 16) ? sg[8] : sg[7]) * 16u : (unsigned)sg[2] * 512u));
        if (j == OB1 - 1) {
          vt = __builtin_amdgcn_readfirstlane(vt);
          vd = (vt & 1u) ? vo : NOW;
        }
        if (j == OB1 + 3 * ODS + 1) touch(sg[7], vt & 2u);
      }
      send(px[0]);
      s1 += sstep;
#pragma unroll
      for (int i = 0; i < 8; ++i) sg[i] = sg[i + 1];
      sg[8] += sg[8] < 3 * ON0 ? 3 : 4;
      par = (par + 1) & 3;
      ++rn;
    } while (vt & 1u);
    if (w == 0) vote[par] = fbits(s1 + sstep, flags(s1 + sstep));
    asm volatile("" ::: "memory");
    __builtin_amdgcn_s_waitcnt(0x0f72);
    bar();
  }
  int np = min((s1 - s0) / sstep, 4);
#pragma nounroll
  for (int s = s1; s < NL_; s += sstep, par ^= 1) {
    const int nxt = s + sstep;
    const unsigned vt = __builtin_amdgcn_readfirstlane(vote[par]);
    const bool pre = (vt & 1u) != 0u;
    const unsigned vd = pre ? vo : 0x80000000u;
    const char* bp = lds + par * 32768 + l * 16;
    auto rd = [&](int j) { return *(const u32x4*)(bp + rdo(j)); };
    unsigned f;
    if (np >= 3) f = pf(s - 3 * sstep);
    u32x4 b[8];
#pragma unroll
    for (int j = 0; j < 8; ++j) b[j] = rd(j);
    if (np >= 2) flag(s - 2 * sstep);
    if (np >= 4) fin_b(s - 4 * sstep);
    SCHED();
#pragma unroll
    for (int j = 0; j < 32; ++j) {
      if (j < 4 * ODS && j % ODS == 0) load_b(nxt, par ^ 1, j / ODS, vd);
#pragma unroll
      for (int ct = 0; ct < 2; ++ct) acc[j & 1][ct] = mfu(a[j >> 3][(j >> 1) & 3][ct], b[j & 7], j >= 2 ? acc[j & 1][ct] : f32x4{0.f, 0.f, 0.f, 0.f});
      if (j + 8 < 32) b[j & 7] = rd(j + 8);
      SCHED();
      if (j == OFA) {
        if (np >= 3) fin_a(s - 3 * sstep, __all(f == 1u));
        if (w == 0) fl = flags(nxt + sstep);
        touch(seg(nxt + sstep), vt & 2u);
        SCHED();
      }
    }
    send(pxo(s));
    asm volatile("" ::: "memory");
    __builtin_amdgcn_s_waitcnt(0x0f72);
    asm volatile("" ::: "memory");
    if (w == 0) vote[par ^ 1] = fbits(nxt + sstep, fl);
    np = min(np + 1, 4);
    if (!pre) {
      if (np >= 4) fin_b(s - 3 * sstep);
      if (s - sstep < FS_) {
        if (s < FS_) drain_vm();
        bar();
        if (np >= 2) flag(s - sstep);
        if (s < FS_) flag(s);
      }
#pragma nounroll
      for (int k = min(np, 3) - 1; k >= 0; --k) { fin_a(s - k * sstep, false); fin_b(s - k * sstep); }
      np = 0;
      if (nxt >= NL_) break;
      if (nxt < FS_) {
        wait_b(nxt);
#pragma unroll
        for (int i = 0; i < 4; ++i) load_b(nxt, par ^ 1, i, vo);
        drain_vm();
      } else {
        const unsigned so = ooff(nxt, hh, hb) + (w & 1) * HALF_;
        const unsigned cm = cooff(hh, hb) + (unsigned)((nxt - 2) * 16 + (l & 3) * 4);
        after(poll_flags<FSLP>(rw, cm, ld_sc1_32(rw, cm)), (int)(nxt == NL_ - 1 ? dly[XTL] : dly[XT1]));
#pragma nounroll
        for (int it = 0; it < HSPIN_CAP; ++it) {
          asm volatile("" ::: "memory");
#pragma unroll
          for (int j = 0; j < 4; ++j) b[j] = ld_sc1s(rw, vo + j * 1024u, so);
          if (!bad4(b)) break;
          __builtin_amdgcn_s_sleep(1);
        }
#pragma unroll
        for (int j = 0; j < 4; ++j) *(u32x4*)(lds + (par ^ 1) * 32768 + w * 4096 + j * 1024 + l * 16) = b[j];
      }
    }
    bar();
  }
}

DEVI void helper_role(char* lds, char* ws, rsrc_t rw, const u16* __restrict__ x, const u16* __restrict__ Wq,
                      const u16* __restrict__ Wk, const u16* __restrict__ Wv, const u16* __restrict__ Wo,
                      const u16* __restrict__ wr, const u16* __restrict__ wi, const u16* __restrict__ gw, u16* __restrict__ out, int hw
                      ) {
  const int xg = hw & 7, loc = hw >> 3;
  const int q = xg >> 1, m = loc * 2 + (hw & 1);
  DLYI(0x30);
  HTR(0);
  const int xi = (m & 7) * 4 + q, l = threadIdx.x & 63, w = __builtin_amdgcn_readfirstlane(threadIdx.x >> 6);
  auto xld = [&](int i) {
    return __builtin_amdgcn_raw_buffer_load_b128(__builtin_amdgcn_make_buffer_rsrc((void*)x, 0, S_ * D_ * 2, 0x00020000),
                                                 (l & 7) * 2048 + w * 256 + (l >> 3) * 16 + (i & 2) * 64, (unsigned)(32 * (i >> 2) + xi) * 32768u + (i & 1) * 16384u, NTX);
  };
  auto xst = [&](int i, unsigned hb, u32x4 v) { st_sc1s(rw, (l >> 3) * 256 + (l & 7) * 16 + (i & 2) * 1024 + (i & 1) * 128, OFF_XF + far8((32 * (i >> 2) + xi) * 4 + (w >> 1), hb) + (w & 1) * 4096u, v); };
  unsigned hb;
  if (m < 24) {
    if (m >= 16) {
      static_assert((OFF_REC - OFF_CKV) % 512u == 0 && (OFF_REC - OFF_CKV) / 512u <= 512u, "shares");
      if (threadIdx.x < (OFF_REC - OFF_CKV) / 512u) st_sc1(rw, OFF_CKV + xi * ((OFF_REC - OFF_CKV) / 32u) + threadIdx.x * 16u, u32x4{0u, 0u, 0u, 0u});
      u32x4 xs[4];
#pragma unroll
      for (int i = 0; i < 4; ++i) xs[i] = xld(i);
      const u32x2 no = launch_no(ws);
#pragma unroll
      for (int i = 0; i < 4; ++i) xst(i, 0u, xs[i]);
      rec_put(rw, xi, no);
      asm volatile("" :: "v"(xs[3]));
      HTR(4);
      for (unsigned i = 0; i < (RSE ? 7u : dly[QDL]); ++i) __builtin_amdgcn_s_sleep(32);
    }
    if ((m >> 3) == 1) hb = proj_reg<false>(lds, ws, rw, Wv, 8 * q + (m & 7), 2, hw);
    else hb = proj_reg<true>(lds, ws, rw, m < 8 ? Wk : Wq, 8 * q + (m & 7), m < 8 ? 1 : 0, hw);
    HTR(2);
  } else {
    probe(ws, rw, 8 * q + m - 24);
    const u32x2 no = launch_no(ws);
    rec_put(rw, 32 + 8 * q + m - 24, no);
    HTR(4);
    for (unsigned i = 0; i < dly[XDL]; ++i) __builtin_amdgcn_s_sleep(32);
    NearMap nm;
    {
      u32x4 wv[2], xs[16];
#pragma unroll
      for (int i = 0; i < 16; ++i)
        xs[(i + 4) & 15] = __builtin_amdgcn_raw_buffer_load_b128(__builtin_amdgcn_make_buffer_rsrc((void*)x, 0, S_ * D_ * 2, 0x00020000), l * 16 + (i & 3) * 1024,
                                                                 (unsigned)(32 * (((i + 4) & 15) >> 2) + xi) * 32768u + w * 4096u, NTX);
#pragma unroll
      for (int i = 0; i < 2; ++i) wv[i] = *(const u32x4*)(gw + i * 512 + l * 8);
      nm.k = near_kind(rw, no);
      hb = far_bit(nm.k);
#pragma unroll
      for (int i = 4; i < 16; ++i) *(u32x4*)(lds + (i >> 2) * 32768 + (i & 1) * 16384 + l * 256 + ((2 * w + ((i >> 1) & 1)) ^ (l & 15)) * 16) = xs[i];
      __syncthreads();
      u32x4 xf[12];
#pragma unroll
      for (int i = 0; i < 12; ++i) xf[i] = *(const u32x4*)(lds + ((i >> 2) + 1) * 32768 + w * 4096 + (i & 3) * 1024 + (l >> 4) * 256 + ((l & 15) ^ ((i & 3) * 4 + (l >> 4))) * 16);
#pragma unroll
      for (int i = 0; i < 12; ++i) st_sc1s(rw, l * 16 + (i & 3) * 1024, OFF_XF + far8((32 * ((i >> 2) + 1) + xi) * 4 + (w >> 1), hb) + (w & 1) * 4096u, xf[i]);
      float gs = 0.f;
#pragma unroll
      for (int n = 0; n < 8; ++n) {
        float acc = 0.f;
#pragma unroll
        for (int i = 0; i < 8; ++i) {
          const unsigned a = xs[2 * n + (i >> 2)][i & 3], b = wv[i >> 2][i & 3];
          const unsigned pr = cvt2(__uint_as_float(a << 16) * __uint_as_float(b << 16), __uint_as_float(a & 0xffff0000u) * __uint_as_float(b & 0xffff0000u));
          acc += __uint_as_float(pr << 16) + __uint_as_float(pr & 0xffff0000u);
        }
#pragma unroll
        for (int o = 1; o < 8; o *= 2) acc += __shfl_xor(acc, o);
        acc = rows_sum(fold8(acc));
        if (l == n) gs = acc;
      }
      asm volatile("" :: "v"(xf[11]));
      wg_signal(ws, OFF_CF + 384u);
      if (l < 8) st_sc1_32(rw, OFF_G + (unsigned)((32 * (l >> 1) + xi) * 16 + 2 * w + (l & 1)) * 4u, __float_as_uint(rbf(1.0f / (1.0f + expf(-rbf(gs))))));
    }
    wg_wait_s(rw, OFF_CF + 384u, 32u);
    HTR(1);
    fft_cols_fwd(lds, rw, 8 * q + m - 24, hb);
    wg_signal(ws, OFF_CF);
    HTR(3);
    {
      unsigned tx = threadIdx.x;
      asm volatile("" : "+v"(tx));
      const unsigned tv = (tx >> 3) & 1;
      u32x4 p3 = u32x4{tv, tv, tv, tv}, p4 = u32x4{POISON, POISON, POISON, POISON};
      asm volatile("" : "+v"(p3), "+v"(p4));
#pragma unroll
      for (int i = 0; i < 6; ++i) st_sc1s(rw, (tx & 255u) * 16u, nm(3 + (i >> 1), 2 * (i & 1) + (w >> 2), NB3 + xi), p3);
#pragma unroll
      for (int i = FS_; i < NL_; ++i) st_sc1s(rw, (w >> 2) * HALF_ + (tx & 255u) * 16u, ooff(i, xi, hb), p4);
      drain_vm();
      asm volatile("" :: "v"(p3), "v"(p4));
    }
    wg_wait_s(rw, OFF_CF, 32u);
    fft_rows(lds, rw, wr, wi, xi, hb);
    wg_signal(ws, OFF_CF + 128u);
    HTR(5);
    xattn_wave(rw, m & 7, q, hb);
    HTR(11);
    wg_wait_s(rw, OFF_CF + 128u, 32u);
    fft_cols_inv(lds, rw, 32 * q + m - 24, hb);
    wg_signal(ws, OFF_CF + 256u);
    HTR(7);
  }
  oproj_worker(lds, rw, Wo, (m >> 3) ^ (m >> 4), m & 3, 2 * q + ((m >> 2) & 1), out, hb DLYA
);
  HTR(9);
}

static_assert(W3_OFF + 65536 <= SMEM_BYTES, "LDS");
__global__ __launch_bounds__(512, 1) void mega_kernel(const u16* __restrict__ x, const u16* __restrict__ mem0,
                                                      const u16* __restrict__ Wq, const u16* __restrict__ Wk,
                                                      const u16* __restrict__ Wv, const u16* __restrict__ Wo,
                                                      const u16* __restrict__ wr, const u16* __restrict__ wi,
                                                      const u16* __restrict__ gw, u16* __restrict__ out, char* ws) {
  char* lds = lds_;
  ws += (0u - (unsigned)(size_t)ws) & (SET_ - 1u);
  const rsrc_t rw = __builtin_amdgcn_make_buffer_rsrc(ws, 0, WS_USED, 0x00020000);
  const int idx = (blockIdx.x >> 3) * 4 + (blockIdx.x & 3);
  const bool isrec = (blockIdx.x & 4) == 0;
  const int rid = (blockIdx.x & 3) * 32 + (blockIdx.x >> 3), hid = (idx & 3) * 2 + ((idx >> 2) & 1) + (idx >> 3) * 8;
  if (__builtin_amdgcn_readfirstlane(threadIdx.x >> 6) == 3 && (blockIdx.x >> 3) >= 16) {
    u32x2 pc;
    unsigned n;
    asm volatile("s_getpc_b64 %0\n code_from: s_mov_b32 %1, code_to - code_from" : "=s"(pc), "=s"(n));
    asm volatile("s_mov_b32 m0, %2\n s_nop 0\n buffer_load_dword %0, %1, 0 offen lds"
                 :: "v"((((blockIdx.x >> 3) - 16) * 64 + (threadIdx.x & 63)) * 128), "s"(u32x4{pc[0], pc[1] & 0xffffu, n, 0x00020000u}),
                    "s"((unsigned)(size_t)LDS3 + W3_OFF + 3u * 16384u) : "memory");
  }
  if (isrec) {
    recur_role(lds, ws, rw, mem0, Wq, Wk, Wv, Wo, out, rid TRARG);
  } else helper_role(lds, ws, rw, x, Wq, Wk, Wv, Wo, wr, wi, gw, out, hid TRARG);
}

extern "C" void kernel_launch(void* const* d_in, const int* in_sizes, int n_in, void* d_out, int out_size, void* d_ws,
                              size_t ws_size, hipStream_t stream) {
  const u16* x = (const u16*)d_in[0];
  const u16* mem0 = (const u16*)d_in[1];
  const u16* Wq = (const u16*)d_in[2];
  const u16* Wk = (const u16*)d_in[3];
  const u16* Wv = (const u16*)d_in[4];
  const u16* Wo = (const u16*)d_in[5];
  const u16* wr = (const u16*)d_in[8];
  const u16* wi = (const u16*)d_in[9];
  const u16* gw = (const u16*)d_in[10];
  u16* out = (u16*)d_out;
  char* ws = (char*)d_ws;
  mega_kernel<<<256, 512, 0, stream>>>(x, mem0, Wq, Wk, Wv, Wo, wr, wi, gw, out, ws);
}
```

```cpp
#include <hip/hip_runtime.h>
#include <hip/hip_bf16.h>
#include <stdint.h>
#include <type_traits>

typedef short bf16x8 __attribute__((ext_vector_type(8)));
typedef short s16x4 __attribute__((ext_vector_type(4)));
typedef float f32x4 __attribute__((ext_vector_type(4)));
typedef unsigned u32x4 __attribute__((ext_vector_type(4)));
typedef unsigned u32x2 __attribute__((ext_vector_type(2)));
typedef unsigned short u16;

#define DEVI __device__ __forceinline__

DEVI float bf2f(u16 v) { return __uint_as_float(((unsigned)v) << 16); }
DEVI u16 f2bf(float f) { return __builtin_bit_cast(u16, (__bf16)f); }
DEVI float rbf(float f) { return bf2f(f2bf(f)); }
DEVI unsigned pack2(u16 a, u16 b) { return (unsigned)a | ((unsigned)b << 16); }
DEVI f32x4 mfma16(bf16x8 a, bf16x8 b, f32x4 c) { return __builtin_amdgcn_mfma_f32_16x16x32_bf16(a, b, c, 0, 0, 0); }

constexpr int S_ = 2048, D_ = 1024, H_ = 32, DH_ = 128, MEM_ = 8, SEG_ = 32, NSEG_ = 64, LK_ = 40;
constexpr int HD_ = H_ * DH_;
#define FSV 56
constexpr int NL_ = NSEG_ - 1;
constexpr int FS_ = FSV;
constexpr int XS_ = 8;
constexpr int FB_ = 513;
constexpr float SCALE_ = 0.08838834764831845f;

constexpr unsigned POISON = 0xffffffffu;
constexpr unsigned SET_ = 4u << 20, BLK_ = 32768u, NB3 = 0, NB2 = 32, NB1 = 48, NBLK = 52;
constexpr unsigned OFF_CKV = 24u << 20;
constexpr unsigned OFF_CO = OFF_CKV + 16384u;
constexpr unsigned OFF_CPX = OFF_CO + 65536u;
constexpr unsigned OFF_CF = OFF_CPX + 32768u;
constexpr unsigned OFF_REC = OFF_CF + 1024u;
constexpr unsigned OFF_EP = OFF_REC + 5120u;
constexpr unsigned COMM_BYTES = OFF_EP + 1024u;
struct NearMap {
  unsigned k;
  __device__ static unsigned pg(unsigned c) { return ((c & 1u) << 14) | ((c & 6u) << 11); }
#define BLAY 0
  __device__ static constexpr unsigned blk(unsigned n) { return BLAY ? ((n & 3u) << 15) | ((n & 28u) << 17) | ((n >> 5) << 17) : n * BLK_; }
  __device__ unsigned operator()(unsigned set, unsigned g, unsigned n) const { return set * SET_ + blk(n) + pg(k ^ g ^ (blk(n) >> 19)); }
};
DEVI unsigned far8a(unsigned u, unsigned hb) { return u * 16384u + (((u >> 7) ^ hb) & 1u) * 8192u; }
DEVI unsigned far8(unsigned u, unsigned hb) { return u * 16384u + (((u >> 7) ^ (u < 128u ? 0u : hb)) & 1u) * 8192u; }
#define NPROBE 8
DEVI unsigned long long clk_now() {
  unsigned long long t;
  asm volatile("s_waitcnt vmcnt(0) lgkmcnt(0)\n s_memtime %0\n s_waitcnt lgkmcnt(0)" : "=s"(t) :: "memory");
  return t;
}
constexpr unsigned OFF_G = COMM_BYTES;
constexpr unsigned OFF_Q = 28u << 20, OFF_K = 60u << 20, OFF_VX = 92u << 20, OFF_OT = 124u << 20;
constexpr unsigned OFF_T = 128u << 20;
constexpr unsigned OFF_PX = 144u << 20;
constexpr unsigned OFF_BF = 184u << 20;
constexpr unsigned OFF_XF = 192u << 20;
constexpr unsigned WS_USED = 200u << 20;
static_assert(FS_ >= NSEG_ - 8 && 32 + NL_ - FS_ <= 40 && XS_ == 8, "tails");
constexpr unsigned HALF_ = BLK_;
DEVI unsigned tile8(int s, int h, unsigned hb) { return (unsigned)(s * 8 + (h & 7)) * (2u * BLK_) + NearMap::pg((s < 2 ? 0u : hb >> 1) ^ (unsigned)(h >> 3) ^ (unsigned)s); }
DEVI unsigned ooff(int s, int h, unsigned hb) { return (s < FS_ ? OFF_Q : OFF_OT - (NSEG_ - 8) * H_ * 16384u) + tile8(s, h, hb); }
DEVI unsigned bfoff(int blk, int tok, unsigned hb) { return OFF_BF + far8a(blk * 4 + (tok >> 9), hb) + (unsigned)(tok & 511) * 16u; }
DEVI unsigned kmoff(int s, int h, unsigned hb) { return NearMap{hb >> 1}(3u, h >> 3, 64 + s * 8 + (h & 7)); }
DEVI unsigned cooff(int h, unsigned hb) { return OFF_CO + far8a(h >> 3, hb ^ 1u) + (unsigned)(h & 7) * 1024u; }

constexpr int SMEM_BYTES = 163840;
__shared__ __attribute__((aligned(16))) char lds_[SMEM_BYTES];
#define LDS3 ((__attribute__((address_space(3))) char*)lds_)

typedef __amdgpu_buffer_rsrc_t rsrc_t;
DEVI u32x4 ld_sc1(rsrc_t r, unsigned off) { return __builtin_amdgcn_raw_buffer_load_b128(r, off, 0, 16); }
DEVI u32x2 ld_sc1_64(rsrc_t r, unsigned off) { return __builtin_amdgcn_raw_buffer_load_b64(r, off, 0, 16); }
DEVI unsigned ld_sc1_32(rsrc_t r, unsigned off) { return __builtin_amdgcn_raw_buffer_load_b32(r, off, 0, 16); }
DEVI void st_sc1(rsrc_t r, unsigned off, u32x4 v) { __builtin_amdgcn_raw_buffer_store_b128(v, r, off, 0, 16); }
DEVI void st_sc1_64(rsrc_t r, unsigned off, u32x2 v) { __builtin_amdgcn_raw_buffer_store_b64(v, r, off, 0, 16); }
DEVI void st_sc1_32(rsrc_t r, unsigned off, unsigned v) { __builtin_amdgcn_raw_buffer_store_b32(v, r, off, 0, 16); }
DEVI void st_sc1_16(rsrc_t r, unsigned off, u16 v) { __builtin_amdgcn_raw_buffer_store_b16(v, r, off, 0, 16); }
DEVI u32x4 ld_sc1s(rsrc_t r, unsigned off, unsigned so) { return __builtin_amdgcn_raw_buffer_load_b128(r, off, so, 16); }
DEVI void st_sc1s(rsrc_t r, unsigned off, unsigned so, u32x4 v) { __builtin_amdgcn_raw_buffer_store_b128(v, r, off, so, 16); }
DEVI void st_sc1s_64(rsrc_t r, unsigned off, unsigned so, u32x2 v) { __builtin_amdgcn_raw_buffer_store_b64(v, r, off, so, 16); }
DEVI void st_sc1s_32(rsrc_t r, unsigned off, unsigned so, unsigned v) { __builtin_amdgcn_raw_buffer_store_b32(v, r, off, so, 16); }
#define HAUX 16
DEVI void hst(rsrc_t r, unsigned off, u32x4 v) { __builtin_amdgcn_raw_buffer_store_b128(v, r, off, 0, HAUX); }
DEVI void hst64(rsrc_t r, unsigned off, u32x2 v) { __builtin_amdgcn_raw_buffer_store_b64(v, r, off, 0, HAUX); }
DEVI void drain_vm() { asm volatile("s_waitcnt vmcnt(0)" ::: "memory"); }
DEVI void cnt_add(char* ws, unsigned off, unsigned v = 1u) { __hip_atomic_fetch_add((unsigned*)(ws + off), v, __ATOMIC_RELAXED, __HIP_MEMORY_SCOPE_AGENT); }

constexpr int HSPIN_CAP = 1 << 18;
DEVI void wg_wait_s(rsrc_t r, unsigned off, unsigned n) {
  if (threadIdx.x == 0) {
#pragma nounroll
    for (int it = 0; it < HSPIN_CAP; ++it) {
      if (ld_sc1_32(r, off) >= n) break;
      __builtin_amdgcn_s_sleep(8);
    }
  }
  asm volatile("" ::: "memory");
  __syncthreads();
}
DEVI void wg_signal(char* ws, unsigned off) {
  drain_vm();
  __syncthreads();
  if (threadIdx.x == 0) cnt_add(ws, off);
}

DEVI u32x2 launch_no(const char* ws) {
  u32x2 e;
  asm volatile("s_load_dwordx2 %0, %1, 0x0\n s_waitcnt lgkmcnt(0)" : "=s"(e) : "s"(ws + OFF_EP) : "memory");
  return u32x2{e[0] + 1u, e[1] + 0x9e3779b1u};
}
DEVI void rec_put(rsrc_t rw, int i, u32x2 no) {
  drain_vm();
  __syncthreads();
  if (threadIdx.x == 0) st_sc1_64(rw, OFF_REC + (unsigned)i * 16u, no);
}
DEVI u32x4 rec_wait(rsrc_t rw, u32x2 no, unsigned mask, unsigned lo = 0u, unsigned own = 0u) {
  u32x4 r;
#pragma nounroll
  for (int it = 0; it < HSPIN_CAP; ++it) {
    asm volatile("" ::: "memory");
    r = ld_sc1(rw, OFF_REC + (own && __lane_id() <= own ? __lane_id() : (__lane_id() & mask) + lo) * 16u);
    if (__all(r[0] == no[0] && r[1] == no[1])) break;
    __builtin_amdgcn_s_sleep(8);
  }
  asm volatile("" ::: "memory");
  return r;
}
DEVI unsigned near_kind(rsrc_t rw, u32x2 no) {
  const unsigned lat = rec_wait(rw, no, 63u)[2], ls2 = (lat & 0xffffu) + (lat >> 16), l = __lane_id() & 7u;
  unsigned cost = 0u;
#pragma unroll
  for (int i = 0; i < 4; ++i) cost += __shfl(ls2, 32 + 8 * i + (l ^ i));
  cost = cost * 8u + l;
#pragma unroll
  for (int i = 1; i < 8; i *= 2) cost = min(cost, (unsigned)__shfl_xor(cost, i));
  return (__builtin_amdgcn_readfirstlane(cost) & 7u) ^ 4u;
}
DEVI unsigned far_bit(unsigned kind) { return ((kind >> 2) ^ 1u) | (kind << 1); }
DEVI void probe(char* ws, rsrc_t rw, int i) {
  const NearMap nm{0u};
  if (threadIdx.x == 0) {
    char* p0 = ws + nm(4u, i & 7, 40u + (i >> 3));
    unsigned mn[2] = {0xffffu, 0xffffu};
#pragma unroll 1
    for (int k = 0; k < NPROBE; ++k) {
#pragma unroll
      for (int hf = 0; hf < 2; ++hf) {
        const unsigned long long t0 = clk_now();
        const unsigned v = __hip_atomic_fetch_add((unsigned*)(p0 + hf * 2048), 1u, __ATOMIC_RELAXED, __HIP_MEMORY_SCOPE_AGENT);
        asm volatile("" :: "v"(v));
        const unsigned long long t1 = clk_now();
        mn[hf] = min(mn[hf], (unsigned)(t1 - t0));
      }
    }
    st_sc1_32(rw, OFF_REC + (unsigned)(32 + i) * 16u + 8u, mn[0] | (mn[1] << 16));
  }
}

template <int N>
DEVI void wait_vm() { asm volatile("s_waitcnt vmcnt(%0)" :: "n"(N) : "memory"); }
#define BPR 2
#define ISG 3
#define ESG 4
#define NTW 0
#define RN 1
#define NTO 0
#define OAUX 0
#define NTX 2
#define SCHED() __builtin_amdgcn_sched_barrier(0)
DEVI u32x4 rsrc4(const void* p, unsigned bytes) { return u32x4{(unsigned)(size_t)p, (unsigned)((size_t)p >> 32) & 0xffffu, bytes, 0x00020000u}; }
DEVI void bar() { asm volatile("s_waitcnt lgkmcnt(0)\n s_barrier" ::: "memory"); }
DEVI u32x2 tr_quad(const char* p) {
  return __builtin_bit_cast(u32x2, __builtin_amdgcn_ds_read_tr16_b64_v4i16((__attribute__((address_space(3))) s16x4*)(p)));
}
DEVI unsigned cvt2(float a, float b) {
  typedef float f32x2_ __attribute__((ext_vector_type(2)));
  typedef __bf16 bf16x2_ __attribute__((ext_vector_type(2)));
  return __builtin_bit_cast(unsigned, __builtin_convertvector(f32x2_{a, b}, bf16x2_));
}
DEVI unsigned cvt2x(float a, float b) { return min(cvt2(a, b), POISON - 1u); }
DEVI f32x4 mfu(u32x4 a, u32x4 b, f32x4 c) { return mfma16(__builtin_bit_cast(bf16x8, a), __builtin_bit_cast(bf16x8, b), c); }
#define PTR(i)
#define PTB(i)
template <bool swap>
DEVI unsigned proj_reg(char* lds, char* ws, rsrc_t rw, const u16* __restrict__ W, int h, int mat, int hw) {
  constexpr int nseg = NSEG_;
  const int rot = mat ? 0 : XS_;
  auto sgm = [&](int r) { return (r & 32) | ((r + ((r & 32) ? 0 : rot)) & 31); };
  int t = threadIdx.x;
  asm volatile("" : "+v"(t));
  const int l = t & 63, w = __builtin_amdgcn_readfirstlane(t >> 6), g = l >> 4, lo = l & 15, wn = w & 3, wk = w >> 2;
  const u32x4 rs = rsrc4(ws, WS_USED);
  const unsigned vo = (unsigned)l * 16u;
  auto wswz = [](int row) { return ((row & 3) | (((row >> 3) & 1) << 2)) << 1; };
  u32x4 b[16][2];
  const unsigned go = (unsigned)((512 * wk + 16 * wn + g) * HD_ + h * DH_) * 2u;
  const unsigned gv[2] = {go + (unsigned)((lo ^ wswz(g)) * 16), go + (unsigned)((lo ^ wswz(8 + g)) * 16)};
  auto ask = [&](int p) {
#pragma unroll
    for (int i = 0; i < 4; ++i)
      __builtin_amdgcn_global_load_lds((const unsigned*)((const char*)W + gv[i >> 1] + (size_t)(64 * p + 4 * i) * HD_ * 2), (unsigned*)(lds + (p + 4) % 5 * 32768 + w * 4096 + i * 1024 + l * 16), 16, 0, NTW);
    SCHED();
  };
#pragma unroll
  for (int p = 0; p < 5; ++p) ask(p);
  const u32x2 no = launch_no(ws);
  const char* bp = lds + (64 * wk + 8 * g + (lo >> 2)) * 256 + (l & 1) * 8;
  const int fq2 = wswz(8 * g + (lo >> 2));
  auto take = [&](int p) {
#pragma unroll
    for (int ks = 0; ks < 2; ++ks)
#pragma unroll
      for (int ct = 0; ct < 2; ++ct) {
        const char* p0 = bp + (p + 4) % 5 * 32768 + ks * 8192 + (((4 * wn + 2 * ct + ((l & 3) >> 1)) ^ fq2) * 16);
        const u32x2 x0 = tr_quad(p0), x1 = tr_quad(p0 + 1024);
        b[2 * p + ks][ct] = u32x4{x0[0], x0[1], x1[0], x1[1]};
      }
    SCHED();
  };
  unsigned hb = 0u;
  auto dma = [&](int i, int pc, auto&& mid) {
    auto* dst = (__attribute__((address_space(3))) void*)(LDS3 + ((i + 2) & 3) * 32768 + pc * 4096);
    const unsigned so = OFF_XF + far8(sgm(i >> 1) * 8 + (i & 1) * 4 + (pc >> 1), hb) + (unsigned)(pc & 1) * 4096u;
    const unsigned vd = i < 2 * nseg ? vo : 0x80000000u;
    __builtin_amdgcn_raw_ptr_buffer_load_lds(rw, dst, 16, vd, so, 0, 16);
    mid(0);
    __builtin_amdgcn_raw_ptr_buffer_load_lds(rw, dst, 16, vd, so, 1024, 16);
    mid(1);
    __builtin_amdgcn_raw_ptr_buffer_load_lds(rw, dst, 16, vd, so, 2048, 16);
    mid(2);
    __builtin_amdgcn_raw_ptr_buffer_load_lds(rw, dst, 16, vd, so, 3072, 16);
    mid(3);
  };
  auto none = [](int) {};
  u32x4 f[1];
#pragma unroll
  for (int n = 0; n < 8; ++n) {
    asm volatile("s_waitcnt vmcnt(%0) lgkmcnt(0)\n s_barrier" :: "n"(4 * (n == 0 ? 4 : (n < 5 ? 3 : 7 - n)) + (n > RN && n < RN + 4 ? 1 : 0) + (n > RN + 4 ? 8 : 0)) : "memory");
    if (n == RN) asm volatile("buffer_load_dwordx4 %0, %1, %2, %3 offen sc1" : "=&v"(f[0]) : "v"(vo & 511u), "s"(rs), "s"(OFF_REC) : "memory");
    if (n >= 1 && n < 4) ask(n + 4);
    if (n == RN + 4) {
      asm volatile("" : "+v"(f[0]));
      if (!__all(f[0][0] == no[0] && f[0][1] == no[1])) rec_wait(rw, no, 31u);
      dma(0, w, none); dma(1, w, none);
      SCHED();
    }
    if (n == 7) PTR(17);
    take(n);
  }
  bar();
  const u32x4 zero4 = u32x4{0u, 0u, 0u, 0u};
  if (wk) {
    dma(2, wn, none);
    st_sc1(rw, 0x80000000u, zero4);
    if (!swap) st_sc1(rw, 0x80000010u, zero4);
    wait_vm<swap ? 5 : 6>();
  } else wait_vm<0>();
  __builtin_amdgcn_s_waitcnt(0xc07f);
  __builtin_amdgcn_s_barrier();
  PTR(19);
  const unsigned lb = (unsigned)(size_t)LDS3 + (unsigned)(l * 16);
  const unsigned ab = lb + (unsigned)(wk * 16384), sc = lb + 131072u + (unsigned)(wn * 2048);
  u32x4 a[8];
  auto rd = [&](int j, unsigned ad) { asm volatile("ds_read_b128 %0, %1 offset:%2" : "=v"(a[j & 7]) : "v"(ad), "n"((j & 15) * 1024) : "memory"); };
  if (wk == 0) {
#pragma unroll
    for (int j = 0; j < 8; ++j) rd(j, ab + 65536u);
    asm volatile("s_waitcnt lgkmcnt(0)" : "+v"(a[0]), "+v"(a[1]), "+v"(a[2]), "+v"(a[3]), "+v"(a[4]), "+v"(a[5]), "+v"(a[6]), "+v"(a[7]));
#pragma nounroll
    for (int k = 0; k < 2 * nseg; ++k) {
      const unsigned a0 = ab + (unsigned)(((k + 2) & 3) * 32768), a1 = ab + (unsigned)(((k + 3) & 3) * 32768);
      asm volatile("" :: "v"(a0), "v"(a1));
      wait_vm<8>();
      __builtin_amdgcn_s_setprio(1);
      __builtin_amdgcn_s_barrier();
      f32x4 c[2];
      SCHED();
#pragma unroll
      for (int j = 0; j < 16; ++j) {
#pragma unroll
        for (int ct = 0; ct < 2; ++ct) {
          const f32x4 z = j ? c[ct] : f32x4{0.f, 0.f, 0.f, 0.f};
          c[ct] = swap ? mfu(b[j][ct], a[j & 7], z) : mfu(a[j & 7], b[j][ct], z);
        }
        SCHED();
        rd(j + 8, j < 8 ? a0 : a1);
        if (j & 1) asm volatile("s_waitcnt lgkmcnt(5)" : "+v"(a[(j + 2) & 7]), "+v"(a[(j + 3) & 7]));
        SCHED();
      }
      __builtin_amdgcn_s_setprio(2);
      dma(k + 2, 4 + wn, none);
      SCHED();
      asm volatile("s_nop 7\n ds_write_b128 %0, %1\n ds_write_b128 %0, %2 offset:1024\n s_waitcnt lgkmcnt(0)" :: "v"(sc + (unsigned)((k & 1) * 8192)), "v"(c[0]), "v"(c[1]) : "memory");
      if (__builtin_expect(k == 8, 0)) hb = far_bit(near_kind(rw, no));
    }
    asm volatile("" : "+v"(a[0]), "+v"(a[1]), "+v"(a[2]), "+v"(a[3]), "+v"(a[4]), "+v"(a[5]), "+v"(a[6]), "+v"(a[7]));
    wait_vm<8>();
    __builtin_amdgcn_s_barrier();
  } else {
    const unsigned oo = (mat == 0 ? OFF_Q : (mat == 1 ? OFF_K : OFF_VX)) + (swap ? wn * 1024u : (wn >> 1) * HALF_ + (wn & 1) * 2048u);
    const unsigned v0 = l == 0 ? 0u : 0x80000000u;
    auto signal = [&](int r, unsigned va) {
      __builtin_amdgcn_raw_ptr_buffer_atomic_add_i32(swap ? (mat ? 1 : 65536) : 256, rw, va, OFF_CKV + (unsigned)(h * 64 + sgm(r)) * 4u, 0);
    };
    f32x4 c[2];
    u32x2 v[2];
    c[0] = c[1] = f32x4{0.f, 0.f, 0.f, 0.f};
    v[0] = v[1] = u32x2{0u, 0u};
    auto put = [&](int r, int tt, unsigned vs) {
      const unsigned so = oo + tile8(sgm(r), h, hb);
      const u32x2 p0 = u32x2{cvt2x(c[0][0], c[0][1]), cvt2x(c[0][2], c[0][3])}, p1 = u32x2{cvt2x(c[1][0], c[1][1]), cvt2x(c[1][2], c[1][3])};
      if (swap) st_sc1s(rw, vs, so + (unsigned)tt * HALF_, u32x4{p0[0], p0[1], p1[0], p1[1]});
      else if (tt == 0) { v[0] = p0; v[1] = p1; }
      else {
        st_sc1s(rw, vs, so, u32x4{v[0][0], v[0][1], p0[0], p0[1]});
        st_sc1s(rw, vs, so + 1024u, u32x4{v[1][0], v[1][1], p1[0], p1[1]});
      }
    };
    __builtin_amdgcn_s_setprio(3);
    __builtin_amdgcn_s_barrier();
    rec_wait(rw, no, 3u, 64u + 4u * h);
    dma(3, wn, none);
    if (swap) st_sc1(rw, 0x80000000u, zero4);
#pragma nounroll
    for (int r = 0; r < nseg; ++r) {
#pragma unroll
      for (int tt = 0; tt < 2; ++tt) {
        const int i = 2 * r + tt;
        const unsigned a0 = ab + (unsigned)(((i + 2) & 3) * 32768);
        asm volatile("" :: "v"(a0));
        if (tt == 0) wait_vm<swap ? 5 : 4>(); else wait_vm<swap ? 6 : 7>();
        __builtin_amdgcn_s_barrier();
        if (tt == 1 && __builtin_expect(r < 2 && mat, 0)) __builtin_amdgcn_raw_ptr_buffer_atomic_add_i32(1 << 24, rw, v0, OFF_CKV + (unsigned)(h * 64 + r) * 4u, 0);
        if (__builtin_expect(i == (rot ? 1 : 4), 0)) hb = far_bit(near_kind(rw, no));
        if (tt == 0 && __builtin_expect(r == 14 - rot, 0)) {
#pragma nounroll
          for (int it = 0; it < HSPIN_CAP; ++it) {
            if (__builtin_amdgcn_readfirstlane(ld_sc1_32(rw, OFF_CF + 384u)) >= 32u) break;
            __builtin_amdgcn_s_sleep(16);
          }
          asm volatile("" ::: "memory");
        }
        if (tt == 1 && __builtin_expect(r > ISG && r <= ESG, 0)) { drain_vm(); signal(r - 1, v0); }
        f32x4 n[2];
        asm volatile("ds_read_b128 %0, %2\n ds_read_b128 %1, %2 offset:1024" : "=&v"(n[0]), "=&v"(n[1]) : "v"(sc + (unsigned)(tt * 8192)) : "memory");
        dma(i + 4, wn, [&](int q) { rd(q, a0); });
        if (tt == 0) put(r - 1, 1, r > 0 ? vo : 0x80000000u); else put(r, 0, vo);
        rd(4, a0);
        if (tt == 0) signal(r - 2, r >= ESG + 2 ? v0 : 0x80000000u);
        rd(5, a0); rd(6, a0); rd(7, a0);
        if (tt == 0 || __builtin_expect(r >= BPR, 1)) __builtin_amdgcn_s_setprio(0);
        SCHED();
        asm volatile("s_waitcnt lgkmcnt(5)" : "+v"(n[0]), "+v"(n[1]), "+v"(a[0]), "+v"(a[1]), "+v"(a[2]));
        c[0] = n[0]; c[1] = n[1];
#pragma unroll
        for (int j = 0; j < 16; ++j) {
#pragma unroll
          for (int ct = 0; ct < 2; ++ct) c[ct] = swap ? mfu(b[j][ct], a[j & 7], c[ct]) : mfu(a[j & 7], b[j][ct], c[ct]);
          SCHED();
          if (j < 8) rd(j + 8, a0);
          if ((j & 1) && j < 14) asm volatile("s_waitcnt lgkmcnt(%2)" : "+v"(a[(j + 2) & 7]), "+v"(a[(j + 3) & 7]) : "n"(j < 8 ? 5 : (j < 13 ? 12 - j : 0)));
          SCHED();
        }
        __builtin_amdgcn_s_setprio(3);
      }
      if (__builtin_expect(r < ISG, 0)) {
        put(r, 1, vo);
        if (r < 2) PTB(r ? 20 : 8);
        if (r >= 2) {
          drain_vm();
          if (r == 2) { signal(0, v0); signal(1, v0); }
          signal(r, v0);
        }
      }
    }
    put(nseg - 1, 1, vo);
    drain_vm();
    signal(nseg - 2, v0); signal(nseg - 1, v0);
  }
  __builtin_amdgcn_s_setprio(0);
  drain_vm();
  __syncthreads();
  return hb;
}

constexpr int SPIN_CAP = 1 << 19;
#define LOCK 0
#define SL 0
#define GAM 1
#define DLYS 8, 9, 9, 7, 7, 7, 33, 5, 320, 120, 10, 0, 0, 2
enum { DLY1, DLY2, DLYV, DLY3, XDL, QDL, DLYL, DLYW, XT1, XTL, DLYD, KG3, KG1, DLYG, KG2, KGV, DLYR = QDL, DLYX = KG3, DLY4 = DLYG };
#define D4 1
#define NMX 1
#define S3 0
#define NOMIN 0
#define TID 0
#define EARLY 0
#define L2 1
#define L3 1
#define PRE3 LOCK
#define RSE 0

#ifndef NV1
#define NV1 4
#endif
#define VOR 1
#define WB1 1
#define WB2 3
#define ALG 1
#define PBA 6
#define PBN 7
#define ESL 2
#define LSL 0
#define KSL 8
constexpr int PQK_OFF = 0;
constexpr int W3_OFF = 98304;
constexpr int KV_OFF = 98304;
constexpr int PS_OFF = 131072;
constexpr int PZ_OFF = PS_OFF + 4096;
constexpr int UM_OFF = PZ_OFF + 1024;
constexpr int KC_OFF = UM_OFF + 3072;
constexpr int XV_OFF = KC_OFF + 1024;
constexpr int OB_OFF = XV_OFF + 2048;
constexpr int XK_OFF = OB_OFF + 2048;
constexpr float CS = SCALE_ * 1.4426950408889634f;
typedef __bf16 bfv8 __attribute__((ext_vector_type(8)));
typedef __bf16 bfv16 __attribute__((ext_vector_type(16)));
typedef float f32x2 __attribute__((ext_vector_type(2)));
typedef unsigned u32x8 __attribute__((ext_vector_type(8)));
DEVI f32x4 smf(u32x4 a, u32x8 b, f32x4 c, int idx) {
  return __builtin_amdgcn_smfmac_f32_16x16x64_bf16(__builtin_bit_cast(bfv8, a), __builtin_bit_cast(bfv16, b), c, idx, 0, 0);
}
DEVI float fold8(float a) { return a + __uint_as_float(__builtin_amdgcn_update_dpp(0u, __float_as_uint(a), 0x128, 0xf, 0xf, true)); }
#define LO2(c) f32x2{c[0], c[1]}
#define HI2(c) f32x2{c[2], c[3]}
DEVI f32x4 zero4() { f32x4 z = f32x4{0.f, 0.f, 0.f, 0.f}; asm volatile("" : "+v"(z)); return z; }
DEVI u32x4 cvt8(f32x4 a, f32x4 b) { return u32x4{cvt2(a[0], a[1]), cvt2(a[2], a[3]), cvt2(b[0], b[1]), cvt2(b[2], b[3])}; }

DEVI float vmax(unsigned a, unsigned b) { float r; asm("v_max_f32 %0, %1, %2" : "=v"(r) : "v"(a), "v"(b)); return r; }
DEVI float rows_max(float x) {
  const unsigned u = __float_as_uint(x);
  auto a = __builtin_amdgcn_permlane16_swap(u, u, false, false);
  const unsigned u2 = __float_as_uint(vmax(a[0], a[1]));
  auto b = __builtin_amdgcn_permlane32_swap(u2, u2, false, false);
  return vmax(b[0], b[1]);
}
DEVI float rows_sum(float x) {
  const unsigned u = __float_as_uint(x);
  auto a = __builtin_amdgcn_permlane16_swap(u, u, false, false);
  x = __uint_as_float(a[0]) + __uint_as_float(a[1]);
  const unsigned u2 = __float_as_uint(x);
  auto b = __builtin_amdgcn_permlane32_swap(u2, u2, false, false);
  return __uint_as_float(b[0]) + __uint_as_float(b[1]);
}

DEVI void req4(u32x4 r, unsigned vo, unsigned so, u32x4* v) {
  asm volatile("buffer_load_dwordx4 %0, %4, %5, %6 offen sc1\n buffer_load_dwordx4 %1, %4, %5, %6 offen offset:1024 sc1\n"
               "buffer_load_dwordx4 %2, %4, %5, %6 offen offset:2048 sc1\n buffer_load_dwordx4 %3, %4, %5, %6 offen offset:3072 sc1"
               : "=&v"(v[0]), "=&v"(v[1]), "=&v"(v[2]), "=&v"(v[3]) : "v"(vo), "s"(r), "s"(so) : "memory");
}
#define ILV 4
DEVI void req4i(u32x4 r, unsigned vo, unsigned so, u32x4* v) {
  asm volatile("buffer_load_dwordx4 %0, %5, %6, %4 offen sc1\n s_xor_b32 %4, %4, 0x400\n s_nop %7\n buffer_load_dwordx4 %1, %5, %6, %4 offen sc1\n s_xor_b32 %4, %4, 0xc00\n s_nop %7\n"
               "buffer_load_dwordx4 %2, %5, %6, %4 offen sc1\n s_xor_b32 %4, %4, 0x400\n s_nop %7\n buffer_load_dwordx4 %3, %5, %6, %4 offen sc1"
               : "=&v"(v[0]), "=&v"(v[1]), "=&v"(v[2]), "=&v"(v[3]), "+s"(so) : "v"(vo), "s"(r), "n"(ILV ? ILV - 1 : 0) : "memory", "scc");
}
DEVI u32x4 rs_tid(u32x4 r) { return u32x4{r[0], r[1] | (16u << 16), r[2] >> 4, 1u << 23}; }
DEVI void req4t(u32x4 r, unsigned so, u32x4* v) {
  asm volatile("buffer_load_dwordx4 %0, off, %4, %5 sc1\n buffer_load_dwordx4 %1, off, %4, %5 offset:1024 sc1\n"
               "buffer_load_dwordx4 %2, off, %4, %5 offset:2048 sc1\n buffer_load_dwordx4 %3, off, %4, %5 offset:3072 sc1"
               : "=&v"(v[0]), "=&v"(v[1]), "=&v"(v[2]), "=&v"(v[3]) : "s"(r), "s"(so) : "memory");
}
DEVI void req2t(u32x4 r, unsigned so, u32x4* v) {
  asm volatile("buffer_load_dwordx4 %0, off, %2, %3 sc1\n buffer_load_dwordx4 %1, off, %2, %3 offset:1024 sc1" : "=&v"(v[0]), "=&v"(v[1]) : "s"(r), "s"(so) : "memory");
}
DEVI void req2(u32x4 r, unsigned vo, unsigned so, u32x4* v) {
  asm volatile("buffer_load_dwordx4 %0, %2, %3, %4 offen sc1\n buffer_load_dwordx4 %1, %2, %3, %4 offen offset:1024 sc1"
               : "=&v"(v[0]), "=&v"(v[1]) : "v"(vo), "s"(r), "s"(so) : "memory");
}
template <int N>
DEVI void got(u32x4& v) { asm volatile("s_waitcnt vmcnt(%1)" : "+v"(v) : "n"(N)); }
#define A8 ".p2align 3\n"
#define A4 ".p2align 3\n s_nop 0\n"
#define STR_(x) #x
#define STR(x) STR_(x)
#define MF "v_mfma_f32_16x16x32_bf16 "
#define CV "v_cvt_pk_bf16_f32 "
#define SM "v_smfmac_f32_16x16x64_bf16 "
template <int N>
DEVI void got_s4(u32x4& a, f32x4* c, u32x8 b0, u32x8 b1, u32x8 b2, u32x8 b3, int idx) {
  asm volatile(".if %10 == 3 + " STR(LOCK) "\n" A4 ".endif\n s_waitcnt vmcnt(%10)\n" SM "%1, %0, %5, %9\n" SM "%2, %0, %6, %9\n" SM "%3, %0, %7, %9\n" SM "%4, %0, %8, %9"
               : "+v"(a), "+v"(c[0]), "+v"(c[1]), "+v"(c[2]), "+v"(c[3]) : "v"(b0), "v"(b1), "v"(b2), "v"(b3), "v"(idx), "n"(N));
}
template <int N>
DEVI void got_s2(u32x4& a, f32x4* c, u32x8 b0, u32x8 b1, int idx) {
  asm volatile(".if %6 == 1\n" A4 ".endif\n s_waitcnt vmcnt(%6)\n" SM "%1, %0, %3, %5\n" SM "%2, %0, %4, %5" : "+v"(a), "+v"(c[0]), "+v"(c[1]) : "v"(b0), "v"(b1), "v"(idx), "n"(N));
}
template <int N>
DEVI void got_x(u32x4& x, f32x4& sa, f32x4& sb, f32x4& sm, u32x4 ka, u32x4 kb) {
  asm volatile("s_waitcnt vmcnt(%6)\n" MF "%1, %4, %0, %1\n" MF "%2, %5, %0, %2\n" MF "%3, %0, %0, %3" : "+v"(x), "+v"(sa), "+v"(sb), "+v"(sm) : "v"(ka), "v"(kb), "n"(N));
}
template <int N>
DEVI void got_m(u32x4& b, f32x4& d, u32x4 a) { asm volatile("s_waitcnt vmcnt(%3)\n" MF "%1, %2, %0, %1" : "+v"(b), "+v"(d) : "v"(a), "n"(N)); }
DEVI unsigned umax3(unsigned a, unsigned b, unsigned c) { return max(max(a, b), c); }
DEVI unsigned umax5(u32x4 v, unsigned m) { return umax3(v[2], v[3], umax3(v[0], v[1], m)); }
DEVI bool bad(unsigned m) { return __builtin_expect(__builtin_amdgcn_ballot_w64(m == POISON) != 0ull, 0); }
DEVI bool over(unsigned m, unsigned thr) { return __builtin_expect(__builtin_amdgcn_ballot_w64(m > thr) != 0ull, 0); }
DEVI bool bad4(const u32x4* v) { return bad(umax5(v[3], umax5(v[2], umax5(v[1], umax5(v[0], 0u))))); }
DEVI unsigned tag5(u32x4 v, unsigned t, unsigned m) {
  return __builtin_amdgcn_bitop3_b32(v[0], v[1], t, 0x7e) | __builtin_amdgcn_bitop3_b32(v[2], v[3], t, 0x7e) | m;
}
DEVI unsigned rt_now() {
  unsigned long long t;
  asm volatile("s_memrealtime %0\n s_waitcnt lgkmcnt(0)" : "=s"(t) :: "memory");
  return (unsigned)t;
}
#define DLYARG
#define DLYA
#define DLYI(mask_) unsigned dly[16] = {DLYS}; _Pragma("unroll") for (int i = 0; i < 16; ++i) if (((mask_) >> i) & 1) asm volatile("" : "+s"(dly[i]));
#define PAL 6
DEVI void pause(unsigned n) { asm volatile(".p2align %1\n 1: s_sub_u32 %0, %0, 1\n s_cbranch_scc0 1b" : "+s"(n) : "n"(PAL) : "scc", "memory"); }
DEVI unsigned mt_now() {
  unsigned long long t;
  asm volatile("s_memtime %0\n s_waitcnt lgkmcnt(0)" : "=s"(t) :: "memory");
  return (unsigned)t;
}
DEVI unsigned gt_now() {
  unsigned long long b, c, d;
  asm volatile("s_memrealtime %0\n s_nop 6\n s_memrealtime %1\n s_nop 6\n s_memrealtime %2\n s_waitcnt lgkmcnt(0)" : "=&s"(b), "=&s"(c), "=&s"(d) :: "memory");
  return ((unsigned)b + (unsigned)c + (unsigned)d) * 8u;
}
DEVI void clk_pair(unsigned& t, unsigned& gt) {
  unsigned long long a;
  asm volatile("s_memtime %0" : "=s"(a) :: "memory");
  gt = gt_now();
  asm volatile("" : "+s"(a));
  t = (unsigned)a;
}
DEVI unsigned local_of(unsigned tgt, int rq, unsigned t, unsigned g) {
  int d = (int)(tgt - g);
  asm("s_max_i32 %0, %0, 0xffffc000\n s_min_i32 %0, %0, 0x4000" : "+s"(d) :: "scc");
  return t + (unsigned)(d * rq >> 16);
}
DEVI int until(unsigned tgt, int dmin, unsigned now) {
  const int left = (int)(tgt - now);
  int d;
  asm volatile("s_max_i32 %0, %1, %3\n s_min_i32 %0, %0, 4000\n"
               ".p2align %2\n 1: s_sub_u32 %0, %0, 28\n s_cbranch_scc0 1b\n s_cmp_lt_i32 %0, -20\n s_cbranch_scc1 2f\n s_cmp_lt_i32 %0, -12\n s_cbranch_scc1 2f\n"
               "s_cmp_lt_i32 %0, -4\n s_cbranch_scc1 2f\n s_nop 0\n s_branch 2f\n 2:" : "=&s"(d) : "s"(left), "n"(PAL), "s"(dmin) : "scc", "memory");
  return left;
}
DEVI int until(unsigned tgt, int dmin) { return until(tgt, dmin, mt_now()); }
constexpr int TG_OFF = KC_OFF + 16, GP_OFF = XK_OFF + 3072;
#define KTHR 200
#define KFOL 0
#define KPEN 600
#define KLATE 2050

#define TRD
#define TR(i)
#define TRW()
#define TRE(b)
#define TRARGD
#define TRARG
#define SLP(i)
#define TRT()

#define T0(i)
#define TP(i)
#define XM 0
#define TRX(n) do { if (XM == (n)) TR(7); } while (0)
#define TRY(n) do { if (XM == (n)) TR(4); } while (0)
DEVI unsigned kv_want(int s) { return s < XS_ ? 0x404u : 0x40404u; }
DEVI void kv_wait(u32x4 rs, int s, int h, int& budget) {
  const unsigned want = s > 1 ? kv_want(s) : 8u << 24;
  while (true) {
    unsigned c;
    asm volatile("buffer_load_dword %0, off, %1, %2 sc1\n s_waitcnt vmcnt(0)" : "=v"(c) : "s"(rs), "s"(OFF_CKV + (unsigned)(h * 64 + s) * 4u) : "memory");
    if ((__builtin_amdgcn_readfirstlane(c) & want) == want || --budget <= 0) break;
    __builtin_amdgcn_s_sleep(4);
  }
}

DEVI unsigned kv_src(int s, int h, int w, unsigned hb) { return ((w >> 1) ? OFF_VX : OFF_K) + tile8(s, h, hb) + (w & 1) * HALF_; }
DEVI unsigned kv_dst(int s, int w) { return (unsigned)(size_t)LDS3 + KV_OFF + (s & 1) * 16384 + w * 4096; }
DEVI void kv_dma(u32x4 rs, unsigned dvo, unsigned so, unsigned dst) {
  asm volatile("s_mov_b32 m0, %3\n s_nop 0\n buffer_load_dwordx4 %0, %1, %2 offen sc1 lds\n buffer_load_dwordx4 %0, %1, %2 offen offset:1024 sc1 lds\n"
               "buffer_load_dwordx4 %0, %1, %2 offen offset:2048 sc1 lds\n buffer_load_dwordx4 %0, %1, %2 offen offset:3072 sc1 lds"
               :: "v"(dvo), "s"(rs), "s"(so), "s"(dst) : "memory");
}

DEVI unsigned ovoff(unsigned lv) { return (lv & 128u) * 8u + (lv & 0x370u); }
DEVI unsigned osoff(int s, int h, int jj, unsigned hb) { return ooff(s, h, hb) + (jj >> 1) * HALF_ + (jj & 1) * 128u; }
DEVI void ostore(u32x4 rs, const u32x4* oc, unsigned ov, unsigned os) {
  asm volatile("buffer_store_dwordx4 %0, %2, %3, %4 offen sc1\n buffer_store_dwordx4 %1, %2, %3, %4 offen offset:2048 sc1\n s_nop 1" :: "v"(oc[0]), "v"(oc[1]), "v"(ov), "s"(rs), "s"(os) : "memory");
}
DEVI void kv_first(char* lds, u32x4 rs, int sg, int h, int w, unsigned lv, int& budget) {
  u32x4 t[4];
  do {
    req4(rs, lv ^ (unsigned)(w >> 1) * (((lv >> 3) & 32u) + ((lv >> 2) & 128u)), kv_src(sg, h, w, 0u), t);
    asm volatile("s_waitcnt vmcnt(0)" : "+v"(t[0]), "+v"(t[1]), "+v"(t[2]), "+v"(t[3]));
  } while (bad4(t) && --budget > 0);
#pragma unroll
  for (int i = 0; i < 4; ++i) *(u32x4*)(lds + KV_OFF + sg * 16384 + w * 4096 + i * 1024 + lv) = t[i];
}
DEVI float negmax6(const float* e) {
  float mx, t0, t1;
  asm("v_max3_f32 %0, %3, %4, %5\n v_max3_f32 %1, %6, %7, %8\n v_max_f32 %0, %0, %1\n s_nop 1\n"
      "v_max_f32_dpp %1, %0, %0 row_ror:8 row_mask:0xf bank_mask:0xf\n v_max_f32_dpp %2, %0, %0 row_ror:8 row_mask:0xf bank_mask:0xf\n s_nop 1\n"
      "v_permlane16_swap_b32 %1, %2\n v_max_f32 %0, %1, %2\n v_max_f32 %1, %1, %2\n s_nop 1\n"
      "v_permlane32_swap_b32 %0, %1\n v_max_f32 %0, %0, %1\n v_mul_f32 %0, %9, %0"
      : "=&v"(mx), "=&v"(t0), "=&v"(t1) : "v"(e[0]), "v"(e[1]), "v"(e[2]), "v"(e[3]), "v"(e[4]), "v"(e[5]), "s"(-CS));
  return mx;
}

DEVI void recur_p1(char* lds, rsrc_t rw, const u32x4 rs, const u16* __restrict__ mem0, const u16* __restrict__ Wq, const u16* __restrict__ Wk,
                   const u16* __restrict__ Wv, u16* __restrict__ out, const char* ws, int wg TRARGD DLYARG) {
  DLYI((LOCK ? 0x7849 : 0x49) | (RSE ? 0x20 : 0) | (SL ? 0x1000 : 0));
  const int l = threadIdx.x & 63, w = __builtin_amdgcn_readfirstlane(threadIdx.x >> 6) & 3, g = l >> 4, lo = l & 15;
  const int h = wg >> 2, jj = wg & 3;
  int budget = SPIN_CAP;
  unsigned te = 0u, ge = 0u, gpo = 0u, t1g = 0u, t3g = 0u;
  int kq = 0, rq = 0;
  if ((LOCK || SL) && w == 1) clk_pair(te, ge);
  unsigned mz = 0u;
  if (ALG) asm volatile("" : "+s"(mz));
  const int vq = 2 * (~jj & 1) + (jj >> 1);
  u32x8 w1[6][4];
  u32x4 a[4];
  {
    char* wl = lds + w * 24576;
    const char* g1 = (const char*)((jj & 2) ? Wk : Wq) + (size_t)((256 * w + (l >> 3)) * HD_ + h * DH_ + 64 * (jj & 1) + ((l & 7) ^ (2 * g)) * 8) * 2u;
    const char* g2 = (const char*)Wv + (size_t)((256 * w + (l >> 2)) * HD_ + h * DH_ + 32 * vq + (l & 3) * 8) * 2u;
    const unsigned mo = (unsigned)((2 * (lo >> 2) + (lo & 1)) * D_ + 256 * w + 16 * g + 4 * (lo & 2)) * 2u;
    const int kx = ILV ? w : 0;
    asm volatile("buffer_load_dwordx4 %0, %4, %5, %6 offen\n buffer_load_dwordx4 %1, %4, %5, %7 offen\n"
                 "buffer_load_dwordx4 %2, %4, %5, %8 offen\n buffer_load_dwordx4 %3, %4, %5, %9 offen"
                 : "=&v"(a[0]), "=&v"(a[1]), "=&v"(a[2]), "=&v"(a[3]) : "v"(mo), "s"(rsrc4(mem0, MEM_ * D_ * 2u)), "s"(kx * 128), "s"((kx ^ 1) * 128), "s"((kx ^ 2) * 128), "s"((kx ^ 3) * 128) : "memory");
    auto ask = [&](int n) {
#pragma unroll
      for (int i = 0; i < 4; ++i)
        __builtin_amdgcn_global_load_lds((const unsigned*)(n % 3 < 2 ? g1 + (size_t)(64 * ((n / 3) ^ kx) + 32 * (n % 3) + 8 * i) * HD_ * 2 : g2 + (size_t)(64 * ((n / 3) ^ kx) + 16 * i) * HD_ * 2),
                                         (unsigned*)(wl + (n % 6) * 4096 + i * 1024 + l * 16), 16, 0, NTW);
      SCHED();
    };
    const int row = 16 * (g >> 1) + 4 * (g & 1) + (lo >> 2);
    const char* b1 = wl + row * 128 + (lo & 3) * 8;
    const char* b2 = wl + row * 64 + (lo & 3) * 8;
    auto use = [&](int n) {
#pragma unroll
      for (int i = 0; i < 8; ++i) {
        const int u = n % 3 < 2 ? i >> 1 : 4 + (i >> 2), q = n % 3 < 2 ? 2 * (n % 3) + (i & 1) : i & 3;
        const u32x2 v = tr_quad(n % 3 < 2 ? b1 + (n % 6) * 4096 + ((u ^ ((row >> 1) & 3)) * 32) + (q & 1) * 1024
                                          : b2 + (n % 6) * 4096 + (u & 1) * 32 + (32 * (q >> 1) + 8 * (q & 1)) * 64);
        w1[u][n / 3][(q & 1) + 4 * (q >> 1)] = v[0];
        w1[u][n / 3][(q & 1) + 4 * (q >> 1) + 2] = v[1];
      }
      __builtin_amdgcn_s_waitcnt(0xc07f);
      asm volatile("" ::: "memory");
      SCHED();
    };
#pragma unroll
    for (int n = 0; n < 6; ++n) ask(n);
#pragma unroll
    for (int n = 0; n < 12; ++n) {
      if (n == WB1 || n == WB2) __builtin_amdgcn_s_barrier();
      asm volatile("s_waitcnt vmcnt(%0)" :: "n"(n < 7 ? 20 : 44 - 4 * n) : "memory");
      if (n == 0) asm volatile("" : "+v"(a[0]), "+v"(a[1]), "+v"(a[2]), "+v"(a[3]));
      if (n == 0 || n == 11) TP(n == 11);
      use(n);
      if (n < 6) ask(n + 6);
    }
    TP(2);
    if ((LOCK || SL) && w == 1) *(u32x4*)(lds + TG_OFF) = u32x4{te, ge, 61000u, te};
    bar();
  }
  const int idx = (lo & 2) ? 0xeeee : 0x4444;
  const unsigned vo = (unsigned)l * 16u;
  const unsigned pq0 = (unsigned)(size_t)LDS3 + PQK_OFF + (w * 960 + 2 * g * 40 + 8 * (lo >> 2) + (lo & 3)) * 4;
  const unsigned r1w = NearMap::blk(NB1 + jj) + h * 128u;
  unsigned mvo = 0u, r1r = 0u, r3r = 0u, r2b = 0u, hb = 0u;
  unsigned tg = 0u;
  int known = 0;
  unsigned qs = 0u;
  unsigned sc = 0u, sn = SET_;
  u32x4 rt = rs_tid(rs);
  if (TID) asm volatile("" : "+s"(rt));
  unsigned anv = 0x80000000u;
  auto announce = [&](unsigned t, int db) {
    db += (int)dly[KG1];
    const u32x4 an = u32x4{t + (unsigned)(db * kq >> 16), t + (unsigned)((db + (int)dly[KG2]) * kq >> 16), t + (unsigned)((db + (int)dly[KG3]) * kq >> 16), 0u};
    asm volatile("buffer_store_dwordx4 %0, %1, %2, %3 offen sc1\n s_nop 1" :: "v"(an), "v"(anv), "s"(rs), "s"(4u * SET_ + NearMap::blk(NB1) + (unsigned)(wg >> 3) * 128u) : "memory");
  };
  TRD;
  for (int s = 0; s < NSEG_; ++s) {
    f32x4 acc[6];

    TR(0);
    unsigned m, thr, pq1 = pq0;
    asm volatile("" : "+v"(pq1));
    pq1 += 1280u;
    asm volatile("" : "+v"(pq1));
    unsigned kl;
    const int late = 0;
    asm volatile("ds_read_b32 %0, %1" : "=v"(kl) : "v"((unsigned)(size_t)LDS3 + KC_OFF + 8u) : "memory");
    if (s > 0) pause(dly[DLY1]);
    do {
      thr = --budget > 0 && s > 0 ? POISON - 1u : POISON;
      asm volatile("" : "+s"(thr));
      TRY(3);
      if (__builtin_expect(s > 0, 1)) { if (ILV) req4i(rs, vo, r1r + sc, a); else req4(rs, vo, r1r + sc, a); }
      if (LOCK) {
        announce(t1g, late > KTHR ? min(late, 20000) + KPEN : (KFOL ? max(late, 0) : 0));
      }
#pragma unroll
      for (int u = 0; u < 6; ++u) acc[u] = zero4();
      SCHED();
      asm volatile("s_nop 0");
      got_s4<3 + LOCK>(a[0], acc, w1[0][0], w1[1][0], w1[2][0], w1[3][0], idx); m = umax5(a[0], mz);
      SCHED();
      TRY(8);
      got_s4<2 + LOCK>(a[1], acc, w1[0][1], w1[1][1], w1[2][1], w1[3][1], idx); m = umax5(a[1], m);
      SCHED();
      got_s4<1 + LOCK>(a[2], acc, w1[0][2], w1[1][2], w1[2][2], w1[3][2], idx); m = umax5(a[2], m);
      SCHED();
      got_s4<0 + LOCK>(a[3], acc, w1[0][3], w1[1][3], w1[2][3], w1[3][3], idx); m = umax5(a[3], m);
    } while (over(m, thr));
    TR(1);
    SCHED();
    asm volatile("s_nop 0\n v_pk_add_f32 %2, %2, %3\n v_pk_add_f32 %6, %6, %7\n ds_write2_b32 %0, %4, %5 offset1:40\n v_pk_add_f32 %10, %10, %11\n"
                 "ds_write2_b32 %0, %8, %9 offset0:4 offset1:44\n v_pk_add_f32 %14, %14, %15\n ds_write2_b32 %1, %12, %13 offset1:40\n ds_write2_b32 %1, %16, %17 offset0:4 offset1:44"
                 :: "v"(pq0), "v"(pq1), "v"(LO2(acc[0])), "v"(HI2(acc[0])), "v"(acc[0][0]), "v"(acc[0][1]), "v"(LO2(acc[1])), "v"(HI2(acc[1])), "v"(acc[1][0]), "v"(acc[1][1]),
                    "v"(LO2(acc[2])), "v"(HI2(acc[2])), "v"(acc[2][0]), "v"(acc[2][1]), "v"(LO2(acc[3])), "v"(HI2(acc[3])), "v"(acc[3][0]), "v"(acc[3][1]) : "memory");
    SCHED();
#pragma unroll
    for (int i = 0; i < 7; ++i) {
      acc[4 + (i >> 2)] = smf(a[i & 3], w1[4 + (i >> 2)][i & 3], acc[4 + (i >> 2)], idx);
      SCHED();
      if (i == NV1 - 1) { TR(2); bar(); SCHED(); }
    }
    asm volatile(SM "%9, %10, %11, %12\n v_add_u32 %0, 0x500, %0\n s_nop 0\n v_pk_add_f32 %1, %1, %2\n ds_write2_b32 %0, %3, %4 offset1:40\n s_nop 3\n v_pk_add_f32 %5, %5, %6\n ds_write2_b32 %0, %7, %8 offset0:4 offset1:44"
                 :: "v"(pq1), "v"(LO2(acc[4])), "v"(HI2(acc[4])), "v"(acc[4][0]), "v"(acc[4][1]), "v"(LO2(acc[5])), "v"(HI2(acc[5])), "v"(acc[5][0]), "v"(acc[5][1]),
                    "v"(acc[5]), "v"(a[3]), "v"(w1[5][3]), "v"(idx) : "memory");
    bar();
    asm volatile("s_cmp_eq_u32 %6, 0\n s_cbranch_scc1 1f\n v_and_b32 %4, 0x370, %7\n"
                 "buffer_load_dwordx4 %0, %4, %5, %6 offen sc1\n buffer_load_dwordx4 %1, %4, %5, %6 offen offset:1024 sc1\n"
                 "buffer_load_dwordx4 %2, %4, %5, %6 offen offset:2048 sc1\n buffer_load_dwordx4 %3, %4, %5, %6 offen offset:3072 sc1\n 1:"
                 : "=&v"(a[0]), "=&v"(a[1]), "=&v"(a[2]), "=&v"(a[3]), "=&v"(m) : "s"(rs), "s"(qs), "v"(vo) : "memory", "scc");
    TR(3);
    SLP(0);
    const bool last = s == NSEG_ - 1;
    asm volatile("" : "+v"(kl));
    if (__builtin_expect(__builtin_amdgcn_readfirstlane(kl) != 0x404u && s > 0 && !last, 0)) {
      unsigned lv = vo;
      asm volatile("" : "+v"(lv));
      __builtin_amdgcn_s_sleep(LSL);
      kv_wait(rs, s + 1, h, budget);
      kv_dma(rs, lv ^ (unsigned)(w >> 1) * (((lv >> 3) & 32u) + ((lv >> 2) & 128u)), kv_src(s + 1, h, w, hb), kv_dst(s + 1, w));
      drain_vm();
    }
    auto reset = [&]() {
      u32x2 p2 = u32x2{POISON, POISON};
      const unsigned sp = 3u * SET_ - sc - sn;
      unsigned lv = vo;
      asm volatile("" : "+v"(lv));
      unsigned po = w ? (lv >> 1) + (w < 3 ? (lv >> 8) * 128u : 0u) : mvo;
      const unsigned ps = sp + (w ? r2b + (w == 3 ? BLK_ + vq * 512u : (2 * (jj & 1) + w - 1) * 1024u + (jj >> 1) * 128u) : r1w);
      asm volatile("s_waitcnt lgkmcnt(0)" : "+v"(po), "+v"(p2) : "s"(ps) : "memory");
      __builtin_amdgcn_s_barrier();
      st_sc1s_64(rw, po, ps, p2);
    };
    unsigned tv_, ov, os, pm;
    u32x4 oc[2], sel;
    auto none3 = [&]() { asm volatile("" : "=v"(tv_), "=v"(sel), "=v"(oc[0]), "=v"(oc[1]), "=v"(ov)); asm volatile("" : "=s"(pm), "=s"(os)); };
    auto prep3 = [&]() {
      tv_ = vo;
      asm volatile("" : "+v"(tv_));
      oc[0] = *(const u32x4*)(lds + OB_OFF + tv_); oc[1] = *(const u32x4*)(lds + OB_OFF + 1024 + tv_);
      ov = ovoff(tv_) | (s > XS_ && !last ? 0u : 0x80000000u); os = osoff(max(s - 1, 0), h, jj, hb);
      const unsigned sel1 = 0x3f80u << (tv_ & 16u);
      sel = u32x4{(tv_ & 96u) == 0u ? sel1 : 0u, (tv_ & 96u) == 32u ? sel1 : 0u, (tv_ & 96u) == 64u ? sel1 : 0u, (tv_ & 96u) == 96u ? sel1 : 0u};
      tv_ = (tv_ >> 7) ^ tg;
      pm = r1w + sn;
      asm volatile("" : "+v"(tv_), "+v"(sel), "+v"(oc[0]), "+v"(oc[1]), "+v"(ov));
      asm volatile("" : "+s"(pm), "+s"(os));
    };
    if (qs) {
      float e[6];
      unsigned lv = vo;
      asm volatile("" : "+v"(lv));
      f32x4 sa, sb;
      u32x4 km[4];
      asm volatile("ds_read_b128 %2, %10\n ds_read_b128 %3, %10 offset:1024\n ds_read_b128 %4, %10 offset:2048\n ds_read_b128 %5, %10 offset:3072\n s_waitcnt vmcnt(0) lgkmcnt(0)\n"
                   MF "%0, %2, %6, 0\n ds_read_b128 %2, %10 offset:4096\n" MF "%0, %3, %7, %0\n ds_read_b128 %3, %10 offset:5120\n"
                   MF "%0, %4, %8, %0\n ds_read_b128 %4, %10 offset:6144\n" MF "%0, %5, %9, %0\n ds_read_b128 %5, %10 offset:7168\n s_waitcnt lgkmcnt(3)\n"
                   MF "%1, %2, %6, 0\n s_waitcnt lgkmcnt(2)\n" MF "%1, %3, %7, %1\n s_waitcnt lgkmcnt(1)\n" MF "%1, %4, %8, %1\n s_waitcnt lgkmcnt(0)\n" MF "%1, %5, %9, %1\n s_nop 7"
                   : "=&v"(sa), "=&v"(sb), "=&v"(km[0]), "=&v"(km[1]), "=&v"(km[2]), "=&v"(km[3]), "+v"(a[0]), "+v"(a[1]), "+v"(a[2]), "+v"(a[3])
                   : "v"((unsigned)(size_t)LDS3 + KV_OFF + (s & 1) * 16384 + lv) : "memory");
      const int qi = 8 * jj + (int)((lv >> 4) & 7u) - 4 * (int)(lv >> 8) - (int)((lv >> 3) & 16u);
#pragma unroll
      for (int r = 0; r < 4; ++r) e[r] = (lv & 128u) ? sb[r] : sa[r];
      asm volatile("" : "+v"(e[0]), "+v"(e[1]), "+v"(e[2]), "+v"(e[3]));
#pragma unroll
      for (int r = 0; r < 4; ++r) e[r] = r <= qi ? e[r] : rbf(-1000000000.0f) / SCALE_;
      asm volatile("" : "+v"(e[0]), "+v"(e[1]), "+v"(e[2]), "+v"(e[3]));
      e[4] = e[5] = e[0];
      float m1 = negmax6(e);
      asm volatile("" : "+v"(m1));
      TRY(7);
      const unsigned ls = (unsigned)(size_t)LDS3, pb = ls + PS_OFF + 1024u;
      unsigned ka = ls + XK_OFF + (lv >> 8) * 128u + (lv & 112u), fa = ls + XK_OFF + 2048u, fv;
      unsigned w1a = pb + (lv >> 8) * 128u + (lv & 112u) + ((lv >> 4) & 8u), w2a = pb + 512u + (lv & 256u) + (lv & 112u) * 2u + ((lv >> 5) & 4u);
      asm volatile("" : "+v"(ka), "+v"(fa), "+v"(w1a), "+v"(w2a));
      reset();
      TR(5);
      __builtin_amdgcn_s_sleep(KSL);
      do {
        asm volatile("ds_read_b32 %4, %6\n ds_read_b128 %0, %5\n ds_read_b128 %1, %5 offset:1024\n ds_read_b128 %2, %5 offset:512\n ds_read_b128 %3, %5 offset:1536\n s_waitcnt lgkmcnt(0)"
                     : "=&v"(km[0]), "=&v"(km[1]), "=&v"(km[2]), "=&v"(km[3]), "=&v"(fv) : "v"(ka), "v"(fa) : "memory");
      } while (__builtin_amdgcn_readfirstlane(fv) != (unsigned)s);
      const f32x4 sm = mfu(km[3], a[3], mfu(km[2], a[2], mfu(km[1], a[1], mfu(km[0], a[0], f32x4{0.f, 0.f, 0.f, 0.f}))));
      const unsigned m2 = __float_as_uint(fminf(fminf(sm[0] * -CS, sm[1] * -CS), fminf(fminf(sm[2] * -CS, sm[3] * -CS), m1)));
      e[4] = (lv & 128u) ? sm[2] : sm[0]; e[5] = (lv & 128u) ? sm[3] : sm[1];
      const auto m3 = __builtin_amdgcn_permlane16_swap(m2, m2, false, false);
      float mx;
      asm("v_min_f32 %0, %1, %2" : "=v"(mx) : "v"(m3[0]), "v"(m3[1]));
#pragma unroll
      for (int i = 0; i < 6; ++i) e[i] = __builtin_amdgcn_exp2f(__builtin_fmaf(e[i], CS, mx));
      asm volatile("ds_write_b64 %0, %1\n ds_write2_b32 %2, %3, %3 offset1:6" :: "v"(w1a), "v"(u32x2{cvt2(e[0], e[1]), cvt2(e[2], e[3])}), "v"(w2a), "v"(cvt2(e[4], e[5])) : "memory");
      TRY(6);
      none3();
    } else {
    if (__builtin_expect(s == 0, 0)) {
      const u32x2 no = launch_no(ws);
      unsigned lv = vo;
      asm volatile("" : "+v"(lv));
      kv_wait(rs, 1, h, budget);
      __builtin_amdgcn_s_sleep(ESL);
      kv_first(lds, rs, 1, h, w, lv, budget);
      if (w >= 2) rec_wait(rw, no, 63u, 64u + 64u * w);
      const NearMap nm{(unsigned)__builtin_amdgcn_readfirstlane(*(const unsigned*)(lds + KC_OFF + 4))};
      mvo = (lv & 32u) * 2u + ((lv >> 1) & 32u) + (lv & 16u) + ((lv >> 5) & 8u) + nm(0u, ((lv >> 8) & 2u) + ((lv >> 7) & 1u), NB1) - NearMap::blk(NB1);
      r1r = nm(0u, h >> 3, NB1 + w) + (ILV ? w * 1024u : 0u); r3r = nm(0u, h >> 3, NB3 + jj * 8 + (h & 7)); r2b = nm(0u, h >> 3, NB2 + (h & 7) * 2);
      hb = far_bit(nm.k);
      gpo = nm(4u, h >> 3, NB1);
      if (w == 1 && ((wg ^ (wg >> 3)) & 7) == 0 && lv < 64u) anv = nm(0u, lv >> 4, NB1) - NearMap::blk(NB1);
    }
    if (w == 1) *(unsigned*)(lds + KC_OFF) = known > s + 2 ? 0x404u : 0u;
    if ((LOCK || SL) && w == 1) {
      unsigned t1, g1;
      clk_pair(t1, g1);
      const int err = (int)(g1 - ge) - ((int)(t1 - te) * kq >> 16);
      const int dk = (unsigned)(err + 400) < 800u ? err * 3 : 0;
      kq += dk; rq -= dk * 57 >> 6;
      if (__builtin_expect(s == 0, 0)) {
        const float r = (float)err * __builtin_amdgcn_rcpf((float)(int)(t1 - te));
        kq = __builtin_amdgcn_readfirstlane((int)(r * 65536.f)); rq = __builtin_amdgcn_readfirstlane((int)(__builtin_amdgcn_rcpf(r) * 65536.f));
      }
      te = t1; ge = g1;
      if (s == 0) { t1g = t3g = g1; *(unsigned*)(lds + TG_OFF) = t1; }
    }
    if (PRE3 && w == 0) prep3(); else none3();
    reset();
    TR(5);
    if (w == 3) {
      unsigned now = 1u;
      if (__builtin_expect(jj == 0 && s >= FS_, 0)) now = rt_now() | 1u;
      if (l == 0 && s > 1) st_sc1_32(rw, cooff(h, hb) + (unsigned)((s - 2) * 16 + jj * 4), s < XS_ + 2 ? 2u : now);
      if (__builtin_expect(s < XS_, 0)) {
        unsigned lv = vo;
        asm volatile("" : "+v"(lv));
        const float* p = (const float*)(lds + PQK_OFF + 2560 + ((lv >> 8) & 1u) * 640u + ((lv >> 1) & 96u) + (lv >> 9) * 16u + ((lv >> 2) & 12u));
        float v[4];
#pragma unroll
        for (int r = 0; r < 4; ++r) v[r] = (p[r * 40] + p[960 + r * 40]) + (p[1920 + r * 40] + p[2880 + r * 40]);
        st_sc1s_64(rw, (lv & 511u) + (lv >> 9) * 8u, kmoff(s, h, hb) + 2048u + vq * 512u, u32x2{cvt2(v[0], v[1]), cvt2(v[2], v[3])});
      }
      if (__builtin_expect(s == FS_ - 1, 0)) {
        while (__builtin_amdgcn_readfirstlane(ld_sc1_32(rw, OFF_CF + 256u)) < 32u && --budget > 0) __builtin_amdgcn_s_sleep(8);
      }
    } else if (w == 1) {
      if (__builtin_expect(known < min(s + 4, NSEG_), 0)) {
        unsigned lv = vo;
        asm volatile("" : "+v"(lv));
        const unsigned c = ld_sc1_32(rw, OFF_CKV + (unsigned)h * 256u + (lv >> 2)), want = lv < XS_ * 16u ? 0x404u : 0x40404u;
        const unsigned long long miss = __builtin_amdgcn_ballot_w64((c & want) != want);
        known = miss ? __builtin_ctzll(miss) : NSEG_;
        *(unsigned*)(lds + KC_OFF + 8) = known > s + 2 ? 0x404u : 0u;
      }
      if (SL && __builtin_expect(s > 0, 1)) {
        const int lt = min(max(late, 0), 20000);
        t1g += dly[KG1] + (unsigned)((lt > 500 ? lt : lt * GAM >> 2) * kq >> 16);
        unsigned t1, g1;
        clk_pair(t1, g1);
        *(unsigned*)(lds + TG_OFF) = local_of(t1g, rq, t1, g1);
      }
      if (LOCK && __builtin_expect(s > 0, 1)) {
        u32x4 gp;
        unsigned lv = vo;
        asm volatile("" : "+v"(lv));
#define MX(r) "v_max_i32_dpp %1, %1, %1 row_ror:" #r " row_mask:0xf bank_mask:0xf\n v_max_i32_dpp %2, %2, %2 row_ror:" #r " row_mask:0xf bank_mask:0xf\n v_max_i32_dpp %3, %3, %3 row_ror:" #r " row_mask:0xf bank_mask:0xf\n"
        asm volatile("s_waitcnt vmcnt(1)\n ds_read_b128 %0, %1\n s_waitcnt lgkmcnt(0)" : "=&v"(gp) : "v"((unsigned)(size_t)LDS3 + GP_OFF + lv) : "memory");
        asm volatile("v_subrev_u32 %1, %4, %1\n v_subrev_u32 %2, %4, %2\n v_subrev_u32 %3, %4, %3\n" MX(8) MX(4) MX(2) MX(1) "s_nop 0"
                     : "+v"(gp) : "v"(gp[0]), "v"(gp[1]), "v"(gp[2]), "s"(ge) : "memory");
        t1g = ge + __builtin_amdgcn_readfirstlane(gp[0]); t3g = ge + __builtin_amdgcn_readfirstlane(gp[2]);
        unsigned t1, g1;
        clk_pair(t1, g1);
        typedef unsigned u32x3 __attribute__((ext_vector_type(3)));
        *(u32x3*)(lds + TG_OFF) = u32x3{local_of(t1g, rq, t1, g1), ge + gp[1], (unsigned)rq};
      }
    }
    }
    int lf3 = 0;
    float gv;
    u32x2 bw;
    asm volatile("" : "=v"(gv), "=v"(bw));
    if (__builtin_expect(last, 0)) {
      unsigned lv = vo;
      asm volatile("" : "+v"(lv));
      const unsigned tk = (NSEG_ - 1) * SEG_ + 8 * w + 4 * (h & 1);
      gv = __uint_as_float(__builtin_amdgcn_raw_buffer_load_b32(rw, (lv >> 2) & 12u, OFF_G + tk * 4u, 16));
      bw = __builtin_amdgcn_raw_buffer_load_b64(rw, (lv & 64u) * 1024u + (lv & 48u) + ((lv >> 5) & 8u), bfoff(jj * 32 + (h & ~1), tk, hb), 16);
      sc = w ? (2 + w) * SET_ : 0u;
      pause(LOCK && L3 ? dly[DLYL] : dly[DLYL] - dly[DLY3]);
      asm volatile("" : "+v"(gv), "+v"(bw));
    }
    if (w == 0 || last) {
      prep3();
      if (S3) __builtin_amdgcn_s_sleep(S3);
      pause(dly[DLY3]);
      __builtin_amdgcn_s_setprio(3);
      f32x4 d;
      do {
        thr = --budget > 0 ? POISON - 1u : POISON;
        asm volatile("" : "+s"(thr));
        TRY(4);
        if (TID) req4t(rt, r3r + sc, a); else req4(rs, vo, r3r + sc, a);
        ostore(rs, oc, ov, os);
        asm volatile("v_bfrev_b32 %0, 1" : "+v"(ov));
        d = zero4();
        asm volatile("s_nop 0");
        got_m<5>(a[0], d, sel); m = tag5(a[0], tv_, ~1u); asm volatile("" : "+v"(m)); SCHED();
        got_m<4>(a[1], d, sel); m = tag5(a[1], tv_, m); asm volatile("" : "+v"(m)); SCHED();
        got_m<3>(a[2], d, sel); m = tag5(a[2], tv_, m); asm volatile("" : "+v"(m)); SCHED();
        got_m<2>(a[3], d, sel); m = tag5(a[3], tv_, m);
      } while (over(m, thr));
      TR(6);
      T0(3);
      u32x2 c;
#define RO8 " row_ror:8 row_mask:0xf bank_mask:0xf\n"
      asm volatile("s_nop 2\n v_add_f32_dpp %1, %1, %1" RO8 "v_add_f32_dpp %2, %2, %2" RO8 "v_add_f32_dpp %3, %3, %3" RO8 "v_add_f32_dpp %4, %4, %4" RO8
                   "v_cvt_pk_bf16_f32 %1, %1, %2\n v_cvt_pk_bf16_f32 %2, %3, %4\n .if " STR(NOMIN) " == 0\n v_min_u32 %1, -2, %1\n v_min_u32 %2, -2, %2\n .endif\n"
                   "buffer_store_dwordx2 %5, %6, %7, %8 offen sc1\n .if " STR(NMX) " == 0\n code_to:\n .endif\n v_mov_b64 %0, %5"
                   : "=&v"(c) : "v"(d[0]), "v"(d[1]), "v"(d[2]), "v"(d[3]), "v"(LO2(d)), "v"(mvo), "s"(rs), "s"(pm) : "memory");
      if (__builtin_expect(last, 0)) {
        const float omg = rbf(1.0f - gv);
        u32x2 r;
#pragma unroll
        for (int i = 0; i < 2; ++i)
          r[i] = cvt2(rbf(gv * __uint_as_float(c[i] << 16)) + rbf(omg * __uint_as_float(bw[i] << 16)),
                      rbf(gv * __uint_as_float(c[i] & 0xffff0000u)) + rbf(omg * __uint_as_float(bw[i] & 0xffff0000u)));
        unsigned lv = vo;
        asm volatile("" : "+v"(lv));
        __builtin_amdgcn_raw_buffer_store_b64(r, __builtin_amdgcn_make_buffer_rsrc(out, 0, S_ * D_ * 2, 0x00020000), (lv & 48u) * (D_ / 8) + ((lv >> 2) & 16u) + ((lv >> 5) & 8u),
                                              (unsigned)(((NSEG_ - 1) * SEG_ + 8 * w + 4 * (h & 1)) * (D_ * 2) + (jj * 32 + (h & ~1)) * 16), OAUX);
        break;
      }
      __builtin_amdgcn_s_setprio(0);
      TR(7);
      SLP(1);
    }
    if (w == 2) qs = s + 1 >= XS_ && s + 1 < NSEG_ - 1 ? OFF_Q + tile8(s + 1, h, hb) + (jj >> 1) * HALF_ + (jj & 1) * 128u : 0u;
    asm volatile("" : "+s"(qs));
    bar();
    TRW();
    asm volatile("" : "=v"(a[0]), "=v"(a[1]), "=v"(a[2]), "=v"(a[3]));
    tg ^= sc >> 23;
    sc = sn; sn = sn == 2u * SET_ ? 0u : sn + SET_;
  }
  TRE(4 + w);
}

DEVI void recur_at(char* lds, rsrc_t rw, const u32x4 rs, const u16* __restrict__ Wo, const char* ws, int wg TRARGD DLYARG) {
  DLYI(LOCK ? 0x8486 : 0x2c87);
  const int l = threadIdx.x & 63, w = __builtin_amdgcn_readfirstlane(threadIdx.x >> 6), g = l >> 4, lo = l & 15;
  const int h = wg >> 2, jj = wg & 3;
  int budget = SPIN_CAP;
  unsigned mz = 0u;
  if (ALG) asm volatile("" : "+s"(mz));
  u32x8 w3[6][2];
  const int er = (lo >> 2 & 1) + 4 * (lo >> 3);
  const unsigned vo = (unsigned)l * 16u;
  const unsigned dvo = vo ^ (unsigned)((w >> 1) * ((g & 1) * 32 + (g & 2) * 64));
  u32x2 no;
  {
    u32x4 p4 = u32x4{POISON, POISON, POISON, POISON};
    asm volatile("" : "+v"(p4));
#pragma unroll
    for (int i = 0; i < 2; ++i) st_sc1s(rw, vo + i * 1024u, ((w & 1) ? OFF_VX : OFF_K) + tile8(w >> 1, h, 0u) + (jj >> 1) * HALF_ + (jj & 1) * 2048u, p4);
    st_sc1s(rw, w < 2 ? vo : 0x80000000u, NearMap{0u}(0u, h >> 3, NB2 + (h & 7) * 2 + (w & 1)) + jj * 1024u, p4);
    const u16* gw = Wo + (size_t)(h * DH_ + (l >> 3)) * D_ + jj * 256 + 64 * w + ((l & 7) ^ (l >> 5)) * 8;
#pragma unroll
    for (int i = 0; i < 16; ++i)
      __builtin_amdgcn_global_load_lds((const unsigned*)(gw + (size_t)i * 8 * D_), (unsigned*)(lds + W3_OFF + w * 16384 + i * 1024 + l * 16), 16, 0, NTO);
    no = launch_no(ws);
    drain_vm();
    asm volatile("" :: "v"(p4));
    TP(0);
    __builtin_amdgcn_s_barrier();
    asm volatile("" ::: "memory");
    if (threadIdx.x == 0) st_sc1_64(rw, OFF_REC + (64u + wg) * 16u, no);
#pragma unroll
    for (int T = 0; T < 6; ++T) {
      const int Tc = T < 4 ? T : (T & 1) + (w & 2);
      const char* base = lds + W3_OFF + (T < 4 ? w * 16384 : 0) + (16 * (g & 1) + 8 * (g >> 1) + er) * 128 + (((lo & 3) * 2 + (T & 1)) ^ (lo >> 3)) * 16 + (Tc >> 1) * 8;
#pragma unroll
      for (int q = 0; q < 8; ++q) {
        const u32x2 v = tr_quad(base + (32 * (q >> 1) + 2 * (q & 1)) * 128);
        w3[T][q >> 2][2 * (q & 3)] = v[0];
        w3[T][q >> 2][2 * (q & 3) + 1] = v[1];
      }
      asm volatile("" : "+v"(w3[T][0]), "+v"(w3[T][1]));
    }
    bar();
    TP(1);
    *(u32x4*)(lds + PS_OFF + threadIdx.x * 16) = u32x4{0u, 0u, 0u, 0u};
    *(unsigned*)(lds + PZ_OFF + threadIdx.x * 4) = 0u;
    *(unsigned*)(lds + KC_OFF + 12) = 0u;
    rec_wait(rw, no, 3u, 64u + 4u * h, 31u);
    if (w == 1) {
      const NearMap nm{near_kind(rw, no)};
      *(unsigned*)(lds + KC_OFF + 4) = nm.k;
      const unsigned pv = (l & 8) ? 0u : POISON;
      u32x4 p3 = u32x4{pv, pv, pv, pv};
      asm volatile("" : "+v"(p3));
#pragma unroll
      for (int i = 0; i < 12; ++i) st_sc1s(rw, vo + (i & 3) * 1024u, nm(i >> 2, h >> 3, NB3 + jj * 8 + (h & 7)), p3);
#pragma unroll
      for (int i = 0; i < 2; ++i) st_sc1s(rw, vo, nm(1u, h >> 3, NB2 + (h & 7) * 2 + i) + jj * 1024u, p4);
      st_sc1s(rw, l < 32 ? vo : 0x80000000u, nm(1u, h >> 3, NB1 + jj) + (h & 7) * 512u, p4);
      drain_vm();
      asm volatile("" :: "v"(p3), "v"(p4));
      if (l == 0) st_sc1_64(rw, OFF_REC + (192u + wg) * 16u, no);
    } else kv_wait(rs, 0, h, budget);
    TP(2);
    kv_first(lds, rs, 0, h, w, vo, budget);
    TP(3);
    bar();
    TP(4);
  }
  const int idx = lo < 8 ? 0x4444 : 0xeeee;
  const int xg = 2 * (w & 1) + (l >> 5), xm = w >> 1;
  const f32x2* pq = (const f32x2*)((const float*)(lds + PQK_OFF) + xm * 320 + ((l >> 2) & 7) * 40 + 8 * xg + 2 * (l & 3));
  const unsigned xvo = (unsigned)((2 * (jj & 1) + xm) * 1024 + xg * 256 + (jj >> 1) * 128 + (l & 31) * 4), kvo = (unsigned)(xg * 128 + (l & 31) * 4);
  unsigned r2b;
  unsigned sc = 0u, sn = SET_;
  unsigned tg = h & 1;
  const unsigned dlyd = LOCK || SL ? dly[DLYD] * 28 : dly[DLY1] + dly[DLYD];
  const unsigned dlyv = (w == 0 ? dly[DLY2] : dly[DLYV] + (w == 1 ? dly[DLYW] : 0u)) * (LOCK && L2 ? 28 : 1), kgv = !LOCK || w == 0 ? 0u : dly[KGV] + (w == 1 ? dly[DLYW] * 28 : 0u);
  unsigned t2 = LOCK ? mt_now() : 0u;
  unsigned xo = vo, xt = POISON - 1u;
  f32x4 ma = zero4(), mb = zero4();
  const int vr = (lo & 3) * 16 + ((8 * (g >> 1) + er) ^ ((lo & 1) * 2 + (lo & 2) * 4));
  unsigned vb = KV_OFF + 8192 + (g & 1) * 1024 + vr * 16, kb = KV_OFF + l * 16;
  const unsigned ls = (unsigned)(size_t)LDS3;
  const unsigned pw1 = ls + PS_OFF + w * 1024 + (g * 8 + (lo & 7)) * 16 + (lo & 8);
  const unsigned pw2 = ls + (g >= 2 ? PS_OFF + w * 1024 + 512 : UM_OFF + 2048) + ((g & 1) * 8 + (lo & 7)) * 32 + (lo >> 3) * 4;
  unsigned pr0 = ls + PS_OFF + (g * 8 + (lo & 7)) * 16, pr3 = ls + PS_OFF + 512 + ((g & 1) * 8 + (lo & 7)) * 32 + (lo >> 3) * 16;
  unsigned pr1 = lo < 8 ? pr0 : ls + PZ_OFF, pr2 = lo < 8 ? ls + PZ_OFF : pr0;
  const unsigned oa1 = lo < 8 ? pr0 + 1024u : pr1, oa2 = lo < 8 ? pr2 : pr0 + 1024u, oa3 = g < 2 ? pr3 + 1024u : ls + PZ_OFF;
  const unsigned oam = ls + XV_OFF + (lo & 3) * 128 + ((l >> 2) & 7) * 16;
  const unsigned uma = ls + UM_OFF + max(w - 1, 0) * 1024 + vo;
  unsigned long long w0m;
  asm("s_cmp_eq_u32 %1, 0\n s_cselect_b64 %0, -1, 0" : "=s"(w0m) : "s"(w) : "scc");
  unsigned long long xwm;
  asm("s_cmp_eq_u32 %1, 1\n s_cselect_b64 %0, -1, 0" : "=s"(xwm) : "s"(w) : "scc");
  xwm |= w0m & 0xff00ff00ff00ff00ull;
  const unsigned xw = ls + (w ? XV_OFF + vo : XK_OFF + g * 128 + (lo & 7) * 16);
  const float ninf = g < 2 ? -INFINITY : 0.f;
  u32x4 one = u32x4{0x3f803f80u, 0x3f803f80u, 0x3f803f80u, 0x3f803f80u}, half = u32x4{0x3f003f00u, 0x3f003f00u, 0x3f003f00u, 0x3f003f00u};
  f32x4 nin4 = f32x4{ninf, ninf, ninf, ninf};
  asm volatile("" : "+v"(one), "+v"(half), "+v"(nin4));
  u32x4 kx[8], us[4];
  auto lo4 = [](u32x8 b) { return u32x4{b[0], b[1], b[2], b[3]}; };
  auto hi4 = [](u32x8 b) { return u32x4{b[4], b[5], b[6], b[7]}; };
  u32x4 va[2][4];
  auto useg_a = [&]() {
#pragma unroll
    for (int i = 0; i < 8; ++i) {
      const u32x2 x0 = tr_quad(lds + vb + (i & 3) * 2048 + (i >> 2) * 8), x1 = tr_quad(lds + (vb ^ 32u) + (i & 3) * 2048 + (i >> 2) * 8);
      va[i >> 2][i & 3] = u32x4{x0[0], x0[1], x1[0], x1[1]};
    }
#pragma unroll
    for (int i = 0; i < 8; ++i) kx[i] = *(const u32x4*)(lds + kb + i * 1024);
    vb ^= 16384u; kb ^= 16384u;
  };
  auto useg_b = [&](int T0, int T1) {
#pragma unroll
    for (int T = 0; T < 4; ++T) {
      if (T < T0 || T >= T1) continue;
      f32x4 d[2];
#pragma unroll
      for (int ks = 0; ks < 4; ++ks)
#pragma unroll
        for (int n = 0; n < 2; ++n) d[n] = mfu(va[n][ks], (ks & 1) ? hi4(w3[T][ks >> 1]) : lo4(w3[T][ks >> 1]), ks ? d[n] : f32x4{0.f, 0.f, 0.f, 0.f});
      us[T] = cvt8(d[0], d[1]);
      asm volatile("" : "+v"(us[T]));
    }
    asm volatile("" : "+v"(kx[0]), "+v"(kx[1]), "+v"(kx[2]), "+v"(kx[3]), "+v"(kx[4]), "+v"(kx[5]), "+v"(kx[6]), "+v"(kx[7]));
  };
#define USP 4
  u32x4 rt = rs_tid(rs);
  if (TID) asm volatile("" : "+s"(rt));
  const unsigned qsu = jj < 2 ? __builtin_bit_cast(unsigned, CS) : 0x3f800000u;
  u32x2 qs2 = u32x2{qsu, qsu};
  if (NMX) asm volatile("" : "+s"(qs2));
  unsigned c;
  auto pub = [&]() {
    SCHED();
    if (PBA) asm volatile(".p2align %0\n .rept %1\n s_nop 0\n .endr" :: "n"(PBA), "n"(PBN));
    unsigned so1 = r2b + sc, so2 = r2b + BLK_ + (2 * (~jj & 1) + (jj >> 1)) * 512u + sc;
    f32x2 t0, t1, t2, t3;
    asm volatile("" : "+s"(so1), "+s"(so2));
    asm volatile("" : "=v"(t0), "=v"(t1), "=v"(t2), "=v"(t3), "=v"(c));
#define PSUM(o0, o1, o2, o3, d, mul) "ds_read_b64 %0, %7 offset:" #o0 "\n ds_read_b64 %1, %7 offset:" #o1 "\n ds_read_b64 %2, %7 offset:" #o2 "\n ds_read_b64 %3, %7 offset:" #o3 "\n" \
      "s_waitcnt lgkmcnt(2)\n v_pk_add_f32 %0, %0, %1\n s_waitcnt lgkmcnt(0)\n v_pk_add_f32 %2, %2, %3\n v_pk_add_f32 %0, %0, %2\n" mul CV d ", %4, %5\n .if " STR(NOMIN) " == 0\n v_min_u32 " d ", -2, " d "\n .endif\n"
    asm volatile("s_waitcnt lgkmcnt(0)\n s_barrier\n" PSUM(0, 3840, 7680, 11520, "%6", ".if " STR(NMX) "\n v_pk_mul_f32 %0, %14, %0\n .endif\n") "buffer_store_dword %6, %8, %10, %11 offen sc1\n s_cmp_eq_u32 %13, 0\n s_cbranch_scc0 2f\n s_barrier\n s_branch 3f\n"
                 "2: s_barrier\n" PSUM(1280, 5120, 8960, 12800, "%4", "") "buffer_store_dword %4, %9, %10, %12 offen sc1\n 3:"
                 :: "v"(t0), "v"(t1), "v"(t2), "v"(t3), "v"(t0[0]), "v"(t0[1]), "v"(c), "v"((unsigned)(size_t)pq), "v"(xvo), "v"(kvo), "s"(rs), "s"(so1), "s"(so2), "s"(xm), "s"(qs2) : "memory", "scc");
    asm volatile("" : "+v"(c));
    SCHED();
  };
  const NearMap nm{(unsigned)__builtin_amdgcn_readfirstlane(*(const unsigned*)(lds + KC_OFF + 4))};
  const unsigned hb = far_bit(nm.k);
  r2b = NearMap{0u}(0u, h >> 3, NB2 + (h & 7) * 2);
  const unsigned r3o = nm(0u, w, NB3 + jj * 8 + 2 * g + ((lo >> 2) & 1)) + h * 128u + (unsigned)((lo >> 3) * 4 + (lo & 3)) * 16u;
  asm volatile("" ::: "memory");
  useg_a(); useg_b(0, 4);
  if (w == 1) kb = KV_OFF + 8192 + (l ^ ((g & 1) * 2 + (g & 2) * 4)) * 16;
  TP(5);
  pub();
  TP(7);
  TRD;
  for (int s = 0;;) {
    const bool last = s == NSEG_ - 1;
    const unsigned r2c = r2b + sc;
    unsigned m;
    f32x4 d[6], sa, sb, sm;
    u32x4 x[4];
    asm volatile("" : "=v"(d[0]), "=v"(d[1]), "=v"(d[2]), "=v"(d[3]), "=v"(x[0]), "=v"(x[1]), "=v"(x[2]), "=v"(x[3]), "=v"(sa), "=v"(sb), "=v"(sm));
    unsigned dox = w == 0 || last ? 2u : 1u, lim = last ? 5u * SET_ : sc, rlim = last || !NMX ? 0xffffffffu : 0x28000000u;
    asm volatile("" : "+s"(dox), "+s"(lim), "+s"(rlim));
    int lf2 = 0;
    if (LOCK && L2) lf2 = until(t2, dlyv + (s > 1 ? 0u : 84u)) - (int)dlyv; else pause(dlyv);
#define SH8 " row_shr:8 row_mask:0xf bank_mask:0xc\n"
#define SOFT(e4, e5, a1, a2, cs, lo2) "v_max3_f32 %4, %0, %1, %2\n v_max3_f32 %4, %4, " e4 ", " e5 "\n v_max_f32 %4, %4, %3\n s_nop 1\n" \
                   "v_max_f32_dpp %5, %4, %4" RO8 "v_max_f32_dpp %6, %4, %4" RO8 "s_nop 1\n" \
                   "v_permlane16_swap_b32 %5, %6\n v_max_f32 %4, %5, %6\n v_max_f32 %5, %5, %6\n s_nop 1\n" \
                   "v_permlane32_swap_b32 %4, %5\n v_max_f32 %4, %4, %5\n v_mul_f32 %4, " cs ", %4\n" \
                   "v_fma_f32 %0, %0, " cs ", -%4\n v_fma_f32 %1, %1, " cs ", -%4\n v_fma_f32 %2, %2, " cs ", -%4\n v_exp_f32 %0, %0\n v_exp_f32 %1, %1\n v_fma_f32 %3, %3, " cs ", -%4\n" \
                   "v_fma_f32 " e4 ", " e4 ", " cs ", -%4\n v_exp_f32 %2, %2\n v_exp_f32 %3, %3\n v_fma_f32 " e5 ", " e5 ", " cs ", -%4\n v_cvt_pk_bf16_f32 %0, %0, %1\n v_exp_f32 " e4 ", " e4 "\n" \
                   "v_exp_f32 " e5 ", " e5 "\n v_cvt_pk_bf16_f32 %1, %2, %3\n ds_write_b64 " a1 ", " lo2 "\n v_cvt_pk_bf16_f32 " e4 ", " e4 ", " e5 "\n ds_write2_b32 " a2 ", " e4 ", " e4 " offset1:6"
    if (dox == 2u) {
      TR(0);
      do {
        req4(rs, xo, r2c, x);
        if (--budget <= 0) xt = POISON;
        SCHED();
        asm volatile(A4 "s_waitcnt vmcnt(3)\n" MF "%1, %4, %0, %6\n" MF "%2, %5, %0, %7\n" MF "%3, %0, %0, %8"
                     : "+v"(x[0]), "=&v"(sa), "=&v"(sb), "=&v"(sm) : "v"(kx[0]), "v"(kx[4]), "v"(ma), "v"(mb), "v"(nin4));
        m = umax5(x[0], mz); SCHED();
        got_x<2>(x[1], sa, sb, sm, kx[1], kx[5]); m = umax5(x[1], m); SCHED();
        got_x<1>(x[2], sa, sb, sm, kx[2], kx[6]); m = umax5(x[2], m); SCHED();
        got_x<0>(x[3], sa, sb, sm, kx[3], kx[7]);
      } while (over(umax5(x[3], m), xt));
      TR(3);
#define MOV6 A8 "v_mov_b32_dpp %0, %4" SH8 "v_mov_b32_dpp %1, %5" SH8 "v_mov_b32_dpp %2, %6" SH8 "v_mov_b32_dpp %8, %10" SH8 "v_mov_b32_dpp %9, %11" SH8 "v_mov_b32_dpp %3, %7" SH8
#define SC12 "v"(sa[0]), "v"(sa[1]), "v"(sa[2]), "v"(sa[3]), "v"(sb[0]), "v"(sb[1]), "v"(sb[2]), "v"(sb[3]), "v"(sm[0]), "v"(sm[1]), "v"(sm[2]), "v"(sm[3])
      asm volatile("s_cmp_eq_u32 %17, -1\n s_cbranch_scc1 3f\n s_nop 0\n" MOV6
                   "v_exp_f32 %4, %0\n v_exp_f32 %5, %1\n v_exp_f32 %6, %2\n v_exp_f32 %7, %3\n" CV "%4, %4, %5\n v_exp_f32 %10, %8\n v_exp_f32 %11, %9\n" CV "%5, %6, %7\n"
                   "ds_write_b64 %12, %14\n" CV "%10, %10, %11\n ds_write2_b32 %13, %10, %10 offset1:6\n 9:\n"
                   ".subsection 1\n 3: s_nop 0\n" MOV6 SOFT("%8", "%9", "%12", "%13", "%16", "%15") "\n s_branch 9b\n .subsection 2\n code_to: .fill 64, 4, 0xbf800000\n .subsection 0"
                   :: SC12, "v"(pw1), "v"(pw2), "v"(LO2(sb)), "v"(LO2(sa)), "s"(CS), "s"(rlim) : "memory", "scc");
    }
    if (w != 0) {
      u32x4* vv = x;
      unsigned thr;
      TR(0);
      do {
        thr = --budget > 0 ? POISON - 1u : POISON;
        asm volatile("" : "+s"(thr));
        if (TID) req2t(rt, r2c + BLK_, vv); else req2(rs, vo, r2c + BLK_, vv);
#pragma unroll
        for (int T = 0; T < 6; ++T) d[T] = zero4();
        SCHED();
        asm volatile("s_nop 0");
        got_s2<1>(vv[0], d + 4, w3[4][0], w3[5][0], idx);
        m = umax5(vv[0], mz); SCHED();
        got_s2<0>(vv[1], d + 4, w3[4][1], w3[5][1], idx);
      } while (over(umax5(vv[1], m), thr));
      TR(3);
      asm volatile(A8);
      SCHED();
#pragma unroll
      for (int T = 0; T < 4; ++T) d[T] = smf(vv[0], w3[T][0], d[T], idx);
      SCHED();
      asm volatile("v_cvt_pk_bf16_f32 %2, %2, %3\n v_cvt_pk_bf16_f32 %3, %4, %5\n v_cvt_pk_bf16_f32 %4, %6, %7\n v_cvt_pk_bf16_f32 %5, %8, %9\n ds_write_b128 %0, %1"
                   :: "v"(uma), "v"(d[4]), "v"(d[4][0]), "v"(d[4][1]), "v"(d[4][2]), "v"(d[4][3]), "v"(d[5][0]), "v"(d[5][1]), "v"(d[5][2]), "v"(d[5][3]), "v"(d[3]) : "memory");
      SCHED();
#pragma unroll
      for (int T = 0; T < 4; ++T) d[T] = smf(vv[1], w3[T][1], d[T], idx);
      SCHED();
    }
    TR(4);
    T0(8);
    f32x4 su, c0, c1;
    u32x4 p0, p1, p2, p3;
    SCHED();
    asm volatile("s_waitcnt lgkmcnt(0)" : "=v"(su), "=v"(c0), "=v"(c1), "=v"(p0), "=v"(p1), "=v"(p2), "=v"(p3) :: "memory");
    __builtin_amdgcn_s_barrier();
    TR(5);
#define RDP "ds_read_b128 %0, %4\n ds_read_b128 %1, %5\n ds_read_b128 %2, %6\n ds_read_b128 %3, %7\n"
    asm volatile(RDP CV "%8, %8, %9\n" CV "%9, %10, %11\n" CV "%10, %12, %13\n" CV "%11, %14, %15\n" CV "%16, %16, %17\n" CV "%17, %18, %19\n" CV "%18, %20, %21\n" CV "%19, %22, %23\n"
                 "s_mov_b64 exec, %27\n ds_read_b128 %24, %26\n ds_read_b128 %25, %26 offset:1024\n s_mov_b64 exec, -1"
                 :: "v"(p0), "v"(p3), "v"(p1), "v"(p2), "v"(pr0), "v"(pr3), "v"(pr1), "v"(pr2),
                   "v"(d[0][0]), "v"(d[0][1]), "v"(d[0][2]), "v"(d[0][3]), "v"(d[1][0]), "v"(d[1][1]), "v"(d[1][2]), "v"(d[1][3]),
                   "v"(d[2][0]), "v"(d[2][1]), "v"(d[2][2]), "v"(d[2][3]), "v"(d[3][0]), "v"(d[3][1]), "v"(d[3][2]), "v"(d[3][3]), "v"(d[0]), "v"(d[2]), "v"(uma), "s"(w0m) : "memory");
    SCHED();
    asm volatile("s_waitcnt lgkmcnt(4)\n" A8 MF "%0, %8, %3, 0\n" MF "%0, %9, %6, %0\n s_nop 0\n s_waitcnt lgkmcnt(2)\n" MF "%1, %10, %4, 0\n" MF "%1, %11, %5, %1\n" MF "%2, %12, %4, 0\n"
                 "s_nop 0\n s_waitcnt lgkmcnt(0)\n" MF "%1, %7, %6, %1\n" MF "%2, %13, %5, %2\n"
                 :: "v"(su), "v"(c0), "v"(c1), "v"(p0), "v"(p1), "v"(p2), "v"(p3), "v"(d[0]), "v"(one), "v"(half), "v"(us[0]), "v"(us[1]), "v"(us[2]), "v"(us[3]) : "memory");
    SCHED();
    unsigned so;
#define TAIL "v_pk_mul_f32 %2, %2, %17 op_sel_hi:[1,0]\n v_pk_mul_f32 %3, %3, %17 op_sel_hi:[1,0]\n v_cvt_pk_bf16_f32 %6, %6, %7\n v_cvt_pk_bf16_f32 %7, %8, %9\n" \
             "v_pk_mul_f32 %4, %4, %17 op_sel_hi:[1,0]\n v_pk_mul_f32 %5, %5, %17 op_sel_hi:[1,0]\n v_and_or_b32 %6, %6, -2, %14\n v_cvt_pk_bf16_f32 %8, %10, %11\n" \
             "v_cvt_pk_bf16_f32 %9, %12, %13\n v_and_or_b32 %7, %7, -2, %14\n v_and_or_b32 %8, %8, -2, %14\n v_and_or_b32 %9, %9, -2, %14\n" \
             "buffer_store_dwordx4 %1, %15, %16, %0 offen sc1\n s_nop 1"
#define TOPS "v"(c0), "v"(LO2(c0)), "v"(HI2(c0)), "v"(LO2(c1)), "v"(HI2(c1)), "v"(c0[0]), "v"(c0[1]), "v"(c0[2]), "v"(c0[3]), "v"(c1[0]), "v"(c1[1]), "v"(c1[2]), "v"(c1[3]), \
             "s"(tg), "v"(r3o), "s"(rs), "v"(LO2(su))
    asm volatile("v_rcp_f32_e64 %21, %21\n" MF "%18, %19, %20, %18\n"
                 "v_add_u32 %22, 0xd4800000, %21\n v_cmp_lt_u32_e64 vcc, %23, %22\n s_cmp_lg_u64 vcc, 0\n s_cselect_b32 %0, 0x80000000, %24\n s_nop 0\n" TAIL
                 : "=&s"(so) : TOPS, "v"(c1), "v"(d[2]), "v"(p3), "v"(su[0]), "v"(su[2]), "s"(rlim), "s"(sc) : "memory", "vcc", "scc");
    SCHED();
    while (__builtin_expect(so != lim, 0)) {
      unsigned a0, a3;
      if (so == 0x80000000u) {
        if (w == 0) {
          asm volatile(SOFT("%7", "%8", "%9", "%10", "1.0", "%11") "\n ds_write_b32 %12, %13"
                       :: "v"(sa[0]), "v"(sa[1]), "v"(sa[2]), "v"(sa[3]), "v"(sb[0]), "v"(sb[1]), "v"(sb[2]), "v"(sm[0]), "v"(sm[1]),
                          "v"(pw1 + 2048u), "v"(g >= 2 ? pw2 + 2048u : pw2), "v"(LO2(sa)), "v"(ls + KC_OFF + 12u), "v"(s + 1) : "memory");
        }
        unsigned fv;
        do asm volatile("ds_read_b32 %0, %1\n s_waitcnt lgkmcnt(0)" : "=&v"(fv) : "v"(ls + KC_OFF + 12u) : "memory");
        while (__builtin_amdgcn_readfirstlane(fv) != (unsigned)(s + 1) && --budget > 0);
        a0 = pr0 + 2048u; a3 = pr3 + 2048u;
      } else {
        sc = max(sc, 2u * SET_) + SET_;
        a0 = pr0 += 1024u; a3 = pr3 += 1024u;
      }
      so = sc;
      asm volatile(RDP "s_waitcnt lgkmcnt(0)" :: "v"(p0), "v"(p3), "v"(p1), "v"(p2), "v"(a0), "v"(a3), "v"(lo < 8 ? a0 : ls + PZ_OFF), "v"(lo < 8 ? ls + PZ_OFF : a0) : "memory");
      asm volatile(MF "%0, %7, %3, 0\n" MF "%0, %8, %6, %0\n" MF "%1, %9, %4, 0\n" MF "%1, %10, %5, %1\n" MF "%2, %11, %4, 0\n" MF "%2, %12, %5, %2\n" MF "%1, %13, %6, %1\n" MF "%2, %14, %6, %2\n"
                   "s_nop 7\n v_rcp_f32 %15, %15"
                   :: "v"(su), "v"(c0), "v"(c1), "v"(p0), "v"(p1), "v"(p2), "v"(p3), "v"(one), "v"(half), "v"(us[0]), "v"(us[1]), "v"(us[2]), "v"(us[3]), "v"(d[0]), "v"(d[2]), "v"(su[0]));
      asm volatile("s_nop 0\n" TAIL :: "s"(so), TOPS : "memory");
    }
    TR(6);
    T0(9);
    SLP(0);
    asm volatile("" : "=v"(kx[0]), "=v"(kx[1]), "=v"(kx[2]), "=v"(kx[3]), "=v"(kx[4]), "=v"(kx[5]), "=v"(kx[6]), "=v"(kx[7]));
    if (last) break;
    asm volatile("s_mov_b64 exec, %5\n ds_write_b128 %4, %0\n ds_write_b128 %4, %1 offset:1024\n s_and_b64 exec, exec, %6\n"
                 "ds_write_b128 %4, %2 offset:512\n ds_write_b128 %4, %3 offset:1536\n ds_write_b32 %7, %8\n s_mov_b64 exec, -1"
                 :: "v"(x[0]), "v"(x[1]), "v"(x[2]), "v"(x[3]), "v"(xw), "s"(xwm), "s"(w0m), "v"(ls + XK_OFF + 2048u), "v"(s) : "memory", "scc");
    if (__builtin_expect(s < XS_, 0))
      if (jj & 2) st_sc1s_32(rw, kvo, kmoff(s, h, hb) + (2 * (jj & 1) + xm) * 512u, c);
    const unsigned kc = *(const unsigned*)(lds + KC_OFF);
    useg_a(); useg_b(0, USP);
    if (USP < 4) asm volatile("" : "+v"(va[0][0]), "+v"(va[0][1]), "+v"(va[0][2]), "+v"(va[0][3]), "+v"(va[1][0]), "+v"(va[1][1]), "+v"(va[1][2]), "+v"(va[1][3]));
    TRX(0);
    const bool have = __builtin_amdgcn_readfirstlane(kc) == 0x404u;
    unsigned ks = kv_src(s + 2, h, w, hb), kd = kv_dst(s, w), kv = have && s < NSEG_ - 2 ? dvo : 0x80000000u;
    asm volatile("" : "+s"(ks), "+s"(kd), "+v"(kv));
    if (D4) pause(dly[DLY4]);
    bar();
#define KVE 2
    if (w == KVE) kv_dma(rs, kv, ks, kd);
    r2b = nm(0u, h >> 3, NB2 + (h & 7) * 2);
    tg ^= sc >> 23;
    sc = sn; sn = sn == 2u * SET_ ? 0u : sn + SET_;
    TRW();
    useg_b(USP, 4);
    if (w == 1 && s >= XS_) {
      u32x4 p1, p2, p3;
      u32x2 vm[8];
      asm volatile("ds_read_b128 %0, %11\n ds_read_b128 %1, %12\n ds_read_b128 %2, %13\n"
                   "ds_read_b64_tr_b16 %3, %14\n ds_read_b64_tr_b16 %4, %14 offset:512\n ds_read_b64_tr_b16 %5, %14 offset:8\n ds_read_b64_tr_b16 %6, %14 offset:520\n"
                   "ds_read_b64_tr_b16 %7, %14 offset:1024\n ds_read_b64_tr_b16 %8, %14 offset:1536\n ds_read_b64_tr_b16 %9, %14 offset:1032\n ds_read_b64_tr_b16 %10, %14 offset:1544\n"
                   "s_waitcnt lgkmcnt(0)"
                   : "=&v"(p1), "=&v"(p2), "=&v"(p3), "=&v"(vm[0]), "=&v"(vm[1]), "=&v"(vm[2]), "=&v"(vm[3]), "=&v"(vm[4]), "=&v"(vm[5]), "=&v"(vm[6]), "=&v"(vm[7])
                   : "v"(oa1), "v"(oa2), "v"(oa3), "v"(oam) : "memory");
      const f32x4 su = mfu(one, p3, mfu(one, p2, mfu(one, p1, f32x4{0.f, 0.f, 0.f, 0.f})));
      f32x4 o[4];
#pragma unroll
      for (int i = 0; i < 4; ++i) o[i] = mfu(kx[4 * (i >> 1) + (i & 1)], p1, f32x4{0.f, 0.f, 0.f, 0.f});
#pragma unroll
      for (int i = 0; i < 4; ++i) o[i] = mfu(kx[4 * (i >> 1) + 2 + (i & 1)], p2, o[i]);
#pragma unroll
      for (int i = 0; i < 4; ++i) o[i] = mfu(u32x4{vm[2 * i][0], vm[2 * i][1], vm[2 * i + 1][0], vm[2 * i + 1][1]}, p3, o[i]);
      SCHED();
      const float ir = __builtin_amdgcn_rcpf(su[0]);
      u32x4 oc[2];
#pragma unroll
      for (int i = 0; i < 8; ++i) oc[i >> 2][i & 3] = cvt2x(o[i >> 1][2 * (i & 1)] * ir, o[i >> 1][2 * (i & 1) + 1] * ir);
      *(u32x4*)(lds + OB_OFF + vo) = oc[0];
      *(u32x4*)(lds + OB_OFF + 1024 + vo) = oc[1];
      if (__builtin_expect(s == NL_ - 1, 0)) {
        unsigned lv = vo;
        asm volatile("" : "+v"(lv));
        ostore(rs, oc, ovoff(lv), osoff(s, h, jj, hb));
      }
      TRX(6);
    }
    if (w != KVE) {
      pause(max((int)dlyd - (w == 1 && s >= XS_ ? 19 : 0) - 5 * (4 - USP), 0));
      kv_dma(rs, kv, ks, kd);
    }
    ++s;
    if (__builtin_expect(!have && s < NSEG_ - 1, 0))
      if (__builtin_amdgcn_readfirstlane(*(const unsigned*)(lds + KC_OFF + 8)) == 0x404u) kv_dma(rs, dvo, ks, kd);
    if (__builtin_expect(s == NSEG_ - 1, 0)) {
      static_assert((NSEG_ - 1) % 3 == 0, "set 0");
      asm volatile("" : "+v"(xo));
      const int qi = 8 * w + (int)((xo >> 4) & 7u) - 4 * (int)(xo >> 8);
#pragma unroll
      for (int r = 0; r < 4; ++r) {
        ma[r] = r <= qi ? 0.f : rbf(-1000000000.0f) / SCALE_;
        mb[r] = 16 + r <= qi ? 0.f : rbf(-1000000000.0f) / SCALE_;
      }
      if (w == 1) {
#pragma unroll
        for (int i = 0; i < 8; ++i) kx[i] = *(const u32x4*)(lds + KV_OFF + 16384 + xo + i * 1024);
        asm volatile("" : "+v"(kx[0]), "+v"(kx[1]), "+v"(kx[2]), "+v"(kx[3]), "+v"(kx[4]), "+v"(kx[5]), "+v"(kx[6]), "+v"(kx[7]));
      }
      xt = (xo & 128u) ? xt : POISON;
      xo += (xo & 128u) ? 0u : OFF_Q + tile8(NSEG_ - 1, h, hb) + (w >> 1) * HALF_ + (w & 1) * 128u - r2b;
    }
    pub();
    TR(2);
  }
  asm volatile("" : "+s"(no));
  if (wg == 0 && w == 0 && vo == 0u) st_sc1_64(rw, OFF_EP, no);
  TRE(w);
}

DEVI void recur_role(char* lds, char* ws, rsrc_t rw, const u16* __restrict__ mem0, const u16* __restrict__ Wq, const u16* __restrict__ Wk,
                     const u16* __restrict__ Wv, const u16* __restrict__ Wo, u16* __restrict__ out, int wg TRARGD) {
  const u32x4 rs = u32x4{(unsigned)(size_t)ws, (unsigned)((size_t)ws >> 32) & 0xffffu, WS_USED, 0x00020000u};
  if (__builtin_amdgcn_readfirstlane(threadIdx.x) >= 256) recur_p1(lds, rw, rs, mem0, Wq, Wk, Wv, out, ws, wg TRARG DLYA);
  else recur_at(lds, rw, rs, Wo, ws, wg TRARG DLYA);
}

#define HTR(i)
#define XSLP 16
#define PD1 8
#define ODS 3
#define OB0 17
#define OB1 20
#define OB2 14
#define OPR 2
#define OLIM 16
#define ON0 4
#define CPF 1
#define OFA 11
#define FSLP 16
template <int SLP>
DEVI unsigned poll_flags(rsrc_t rw, unsigned off, unsigned f) {
#pragma nounroll
  for (int it = 0; it < HSPIN_CAP; ++it) {
    if (__all(f != 0u)) break;
    __builtin_amdgcn_s_sleep(SLP);
    f = ld_sc1_32(rw, off);
  }
  return f;
}
DEVI void after(unsigned f, int d) {
  const unsigned t = __builtin_amdgcn_readfirstlane(f) + (unsigned)d;
#pragma nounroll
  for (int k = 0; k < 4 && t != 1u + (unsigned)d; ++k) {
    const int r = min((int)(t - rt_now()), 4096);
    if (r < 4) break;
#pragma nounroll
    for (int i = r * 9 >> 5; i > 0; --i) __builtin_amdgcn_s_sleep(1);
    if (r < 40) break;
  }
}
DEVI void xattn_wave(rsrc_t rw, int s, int hq, unsigned hb) {
  int tx = threadIdx.x;
  asm volatile("" : "+v"(tx));
  const int l = tx & 63, g = l >> 4, lo = l & 15, h = hq * 8 + __builtin_amdgcn_readfirstlane(tx >> 6);
#pragma nounroll
  for (int it = 0; it < HSPIN_CAP; ++it) {
    const unsigned f = ld_sc1_32(rw, l < 32 ? OFF_CKV + (unsigned)(h * 64 + s) * 4u : cooff(h, hb) + (unsigned)(s * 16 + (l & 3) * 4));
    if (__all((f & 0xffffffu) == (l < 32 ? 0x40404u : 2u))) break;
    __builtin_amdgcn_s_sleep(XSLP);
  }
  asm volatile("" ::: "memory");
  const unsigned lv = (unsigned)l * 16u, so = tile8(s, h, hb);
  u32x4 bq[4][2], kx[8], vx[8];
#pragma unroll
  for (int ks = 0; ks < 4; ++ks) {
#pragma unroll
    for (int n = 0; n < 2; ++n) bq[ks][n] = ld_sc1s(rw, lv + ks * 1024u, OFF_Q + so + n * HALF_);
    kx[ks] = ld_sc1s(rw, lv + ks * 1024u, OFF_K + so);
    kx[4 + ks] = ld_sc1s(rw, lv + ks * 1024u, OFF_K + so + HALF_);
  }
#pragma unroll
  for (int te = 0; te < 8; ++te) vx[te] = ld_sc1s(rw, lv + (te & 3) * 1024u, OFF_VX + so + (te >> 2) * HALF_);
  constexpr float L2E = 1.4426950408889634f;
  float m1[2], s1[2];
  f32x4 os[8][2];
  auto seg_part = [&]() {
    f32x4 st[2][2];
#pragma unroll
    for (int i = 0; i < 4; ++i) st[i >> 1][i & 1] = f32x4{0.f, 0.f, 0.f, 0.f};
#pragma unroll
    for (int ks = 0; ks < 4; ++ks) {
      st[0][0] = mfu(kx[ks], bq[ks][0], st[0][0]);
      st[0][1] = mfu(kx[ks], bq[ks][1], st[0][1]);
      st[1][1] = mfu(kx[4 + ks], bq[ks][1], st[1][1]);
    }
#pragma unroll
    for (int n = 0; n < 2; ++n) {
      float sv[8], mx = -3.0e38f, sum = 0.f;
#pragma unroll
      for (int i = 0; i < 8; ++i) {
        sv[i] = 16 * (i >> 2) + 4 * g + (i & 3) <= 16 * n + lo ? rbf(rbf(st[i >> 2][n][i & 3]) * SCALE_) : rbf(-1000000000.0f);
        mx = fmaxf(mx, sv[i]);
      }
      mx = rows_max(mx);
#pragma unroll
      for (int i = 0; i < 8; ++i) { sv[i] = __builtin_amdgcn_exp2f((sv[i] - mx) * L2E); sum += sv[i]; }
      m1[n] = mx;
      s1[n] = rows_sum(sum);
      const u32x4 pb = u32x4{cvt2(sv[0], sv[1]), cvt2(sv[2], sv[3]), cvt2(sv[4], sv[5]), cvt2(sv[6], sv[7])};
#pragma unroll
      for (int te = 0; te < 8; ++te) os[te][n] = mfu(vx[te], pb, f32x4{0.f, 0.f, 0.f, 0.f});
    }
  };
  u32x4 km[4], vm[4];
#pragma unroll
  for (int i = 0; i < 4; ++i) km[i] = ld_sc1(rw, kmoff(s, h, hb) + (unsigned)(i * 512 + g * 128 + (lo & 7) * 16));
#pragma unroll
  for (int i = 0; i < 4; ++i) vm[i] = ld_sc1(rw, kmoff(s, h, hb) + 2048u + (unsigned)(i * 512 + (l & 31) * 16));
  seg_part();
  f32x4 sm[2];
#pragma unroll
  for (int ks = 0; ks < 4; ++ks)
#pragma unroll
    for (int n = 0; n < 2; ++n) sm[n] = mfu(km[ks], bq[ks][n], ks ? sm[n] : f32x4{0.f, 0.f, 0.f, 0.f});
  s16x4 pm[2];
  float cs[2];
#pragma unroll
  for (int n = 0; n < 2; ++n) {
    float sv[4], mx = m1[n], sum = 0.f;
#pragma unroll
    for (int r = 0; r < 4; ++r) {
      sv[r] = g < 2 ? rbf(rbf(sm[n][r]) * SCALE_) : -INFINITY;
      mx = fmaxf(mx, sv[r]);
    }
    mx = rows_max(mx);
#pragma unroll
    for (int r = 0; r < 4; ++r) { sv[r] = __builtin_amdgcn_exp2f((sv[r] - mx) * L2E); sum += sv[r]; }
    const float cr = __builtin_amdgcn_exp2f((m1[n] - mx) * L2E);
    const float inv = __builtin_amdgcn_rcpf(__builtin_fmaf(s1[n], cr, rows_sum(sum)));
    cs[n] = cr * inv;
    pm[n] = __builtin_bit_cast(s16x4, u32x2{cvt2(sv[0] * inv, sv[1] * inv), cvt2(sv[2] * inv, sv[3] * inv)});
  }
#pragma unroll
  for (int n = 0; n < 2; ++n)
#pragma unroll
    for (int a = 0; a < 4; ++a) {
      f32x4 o[2];
#pragma unroll
      for (int i = 0; i < 2; ++i)
        o[i] = __builtin_amdgcn_mfma_f32_16x16x16bf16_1k(__builtin_bit_cast(s16x4, u32x2{vm[a][2 * i], vm[a][2 * i + 1]}), pm[n], os[2 * a + i][n] * cs[n], 0, 0, 0);
      st_sc1(rw, ooff(s, h, hb) + n * HALF_ + (unsigned)(a * 1024 + l * 16),
             u32x4{cvt2x(o[0][0], o[0][1]), cvt2x(o[0][2], o[0][3]), cvt2x(o[1][0], o[1][1]), cvt2x(o[1][2], o[1][3])});
    }
  drain_vm();
  if (l < 4) st_sc1_32(rw, cooff(h, hb) + (unsigned)(s * 16 + l * 4), 1u);
}

typedef f32x2 cf;
DEVI cf cmul(cf a, cf b) { return __builtin_elementwise_fma(cf{a.y, a.y}, cf{-b.y, b.x}, cf{a.x, a.x} * b); }
DEVI cf mni(cf a) { return cf{a.y, -a.x}; }
DEVI cf wexp(float r) {
  float sn, cs;
  sincospif(r, &sn, &cs);
  return cf{cs, -sn};
}
DEVI void dft4(cf& a0, cf& a1, cf& a2, cf& a3) {
  const cf t0 = a0 + a2, t1 = a0 - a2, t2 = a1 + a3, t3 = mni(a1 - a3);
  a0 = t0 + t2; a1 = t1 + t3; a2 = t0 - t2; a3 = t1 - t3;
}
constexpr float RH_ = 0.70710678118654752f, C8_ = 0.92387953251128674f, S8_ = 0.38268343236508977f;
DEVI cf w8_1(cf a) { return (a + mni(a)) * RH_; }
DEVI cf w8_3(cf a) { return (mni(a) - a) * RH_; }
template <int ST>
DEVI void dft8(cf* v) {
  dft4(v[0], v[2 * ST], v[4 * ST], v[6 * ST]);
  dft4(v[ST], v[3 * ST], v[5 * ST], v[7 * ST]);
  const cf e[4] = {v[0], v[2 * ST], v[4 * ST], v[6 * ST]}, o[4] = {v[ST], w8_1(v[3 * ST]), mni(v[5 * ST]), w8_3(v[7 * ST])};
#pragma unroll
  for (int c = 0; c < 4; ++c) { v[c * ST] = e[c] + o[c]; v[(c + 4) * ST] = e[c] - o[c]; }
}
DEVI void dft16(cf* v) {
#pragma unroll
  for (int b = 0; b < 4; ++b) dft4(v[b], v[4 + b], v[8 + b], v[12 + b]);
  v[5] = cmul(v[5], cf{C8_, -S8_}); v[6] = w8_1(v[6]); v[7] = cmul(v[7], cf{S8_, -C8_});
  v[9] = w8_1(v[9]); v[10] = mni(v[10]); v[11] = w8_3(v[11]);
  v[13] = cmul(v[13], cf{S8_, -C8_}); v[14] = w8_3(v[14]); v[15] = cmul(v[15], cf{-C8_, S8_});
  cf y[16];
#pragma unroll
  for (int c = 0; c < 4; ++c) {
    dft4(v[4 * c], v[4 * c + 1], v[4 * c + 2], v[4 * c + 3]);
#pragma unroll
    for (int d = 0; d < 4; ++d) y[c + 4 * d] = v[4 * c + d];
  }
#pragma unroll
  for (int i = 0; i < 16; ++i) v[i] = y[i];
}
template <int N, int ST>
DEVI void twiddle(cf* v, const cf* b) {
  cf w[8];
  w[1] = b[0]; w[2] = b[1]; w[4] = b[2];
  w[3] = cmul(w[1], w[2]); w[5] = cmul(w[1], w[4]); w[6] = cmul(w[2], w[4]); w[7] = cmul(w[3], w[4]);
#pragma unroll
  for (int i = 1; i < 8; ++i) v[i * ST] = cmul(v[i * ST], w[i]);
  if (N == 16) {
    v[8 * ST] = cmul(v[8 * ST], b[3]);
#pragma unroll
    for (int i = 1; i < 8; ++i) v[(8 + i) * ST] = cmul(v[(8 + i) * ST], cmul(w[i], b[3]));
  }
}
template <int N>
DEVI void wpow(cf* b, float r) {
#pragma unroll
  for (int i = 0; i < N; ++i) b[i] = wexp(r * (float)(1 << i));
}
DEVI u32x4 cc2u(cf a, cf b) { return u32x4{__float_as_uint(a.x), __float_as_uint(a.y), __float_as_uint(b.x), __float_as_uint(b.y)}; }

DEVI void fft2048(int t, char* lds, cf* v, const cf* b1, const cf* b2) {
  const int p = t & 3, j = t >> 2;
  dft16(v);
  twiddle<16, 1>(v, b1);
#pragma unroll
  for (int i = 0; i < 16; ++i) *(cf*)(lds + i * 4096 + t * 8) = v[i];
  bar();
#pragma unroll
  for (int i = 0; i < 16; ++i) v[i] = *(const cf*)(lds + (j >> 3) * 4096 + i * 256 + (t & 31) * 8);
  dft16(v);
  twiddle<16, 1>(v, b2);
  char* l2 = lds + 65536;
#pragma unroll
  for (int i = 0; i < 16; ++i) *(cf*)(l2 + (j & 7) * 8224 + i * 512 + (j >> 3) * 32 + p * 8) = v[i];
  bar();
#pragma unroll
  for (int i = 0; i < 16; ++i) v[i] = *(const cf*)(l2 + (i >> 1) * 8224 + (i & 1) * 4096 + t * 8);
  dft8<2>(v);
  dft8<2>(v + 1);
}
DEVI void fft_cols_fwd(char* lds, rsrc_t rw, int blk, unsigned hb) {
  int t = threadIdx.x;
  asm volatile("" : "+v"(t));
  const int p = t & 3, j = t >> 2;
  u32x4 xv[16];
#pragma unroll
  for (int i = 0; i < 16; ++i) xv[i] = ld_sc1s(rw, (j >> 4) * 65536 + p * 256 + (j & 15) * 16, OFF_XF + far8(32 * i + (blk >> 3), hb) + (blk & 7) * 1024u);
  cf b1[4], b2[4];
  wpow<4>(b1, (float)j * (1.f / 1024.f));
  wpow<4>(b2, (float)(j & 7) * (1.f / 64.f));
#pragma nounroll
  for (int it = 0; it < 4; ++it) {
    asm volatile("" : "+v"(t), "+v"(b1[0]), "+v"(b1[1]), "+v"(b1[2]), "+v"(b1[3]), "+v"(b2[0]), "+v"(b2[1]), "+v"(b2[2]), "+v"(b2[3]));
    cf v[16];
#pragma unroll
    for (int i = 0; i < 16; ++i) {
      v[i] = cf{__uint_as_float(xv[i][0] << 16), __uint_as_float(xv[i][0] & 0xffff0000u)};
      xv[i] = u32x4{xv[i][1], xv[i][2], xv[i][3], 0u};
    }
    bar();
    fft2048(t, lds, v, b1, b2);
#pragma unroll
    for (int i = 8; i < 16; ++i) *(cf*)(lds + (i - 8) * 4096 + t * 8) = v[i];
    bar();
#pragma unroll
    for (int i = 0; i < 8; ++i) {
      const int k = (t >> 2) + 128 * i;
      const cf z = v[i], c = *(const cf*)(lds + ((1024 - k) & 1023) * 32 + (t & 3) * 8);
      cf A = cf{z.x + c.x, z.y - c.y}, B = cf{z.y + c.y, c.x - z.x};
      if (k == 0) { A = cf{2.f * z.x, 2.f * c.x}; B = cf{2.f * z.y, 2.f * c.y}; }
      st_sc1(rw, OFF_T + far8a(128 * i, hb) + (unsigned)((t >> 2) * 16384 + blk * 256 + it * 64 + (t & 3) * 16), cc2u(A, B));
    }
  }
}
#define RPF 1
#define NTF 1
DEVI unsigned wpair(const u16* __restrict__ wr, const u16* __restrict__ wi, int o) {
  return NTF ? pack2(__builtin_nontemporal_load(wr + o), __builtin_nontemporal_load(wi + o)) : pack2(wr[o], wi[o]);
}
typedef unsigned u32x4u __attribute__((ext_vector_type(4), aligned(2)));
DEVI void fft_rows_ask(const u16* __restrict__ wr, const u16* __restrict__ wi, int row, int l, u32x4* raw, unsigned* wv) {
  const int rm = (2048 - row) & 2047;
#pragma unroll
  for (int i = 0; i < 4; ++i) raw[i] = __builtin_nontemporal_load((const u32x4u*)(((i & 1) ? wi : wr) + ((i & 2) ? rm * FB_ + 1 : row * FB_) + 8 * l));
  wv[16] = wpair(wr, wi, (l & 1) ? row * FB_ + 512 : rm * FB_);
}
DEVI void fft_rows_w(char* wl, int l, const u32x4* raw, unsigned* wv) {
#pragma unroll
  for (int i = 0; i < 4; ++i) {
    const u32x4 a = raw[i & 2], b = raw[(i & 2) + 1];
    const int k = 2 * (i & 1);
    *(u32x4*)(wl + (i >> 1) * 2048 + l * 32 + (i & 1) * 16) = u32x4{__builtin_amdgcn_perm(b[k], a[k], 0x05040100u), __builtin_amdgcn_perm(b[k], a[k], 0x07060302u),
                                                                   __builtin_amdgcn_perm(b[k + 1], a[k + 1], 0x05040100u), __builtin_amdgcn_perm(b[k + 1], a[k + 1], 0x07060302u)};
  }
#pragma unroll
  for (int i = 0; i < 16; ++i) wv[i] = *(const unsigned*)(wl + (i < 8 ? (l + 64 * i) * 4 : 2048 + (1023 - l - 64 * i) * 4));
  wv[17] = __builtin_amdgcn_update_dpp(0u, wv[16], 0x101, 0xf, 0xf, true);
}
DEVI cf wval(const unsigned* wv, int i, int l) {
  cf w = cf{__uint_as_float(wv[i] << 16), __uint_as_float(wv[i] & 0xffff0000u)};
  if (i >= 8) w.y = -w.y;
  if ((i & 7) == 0) {
    const unsigned e = wv[16 + (i >> 3)];
    const cf m = (w + cf{__uint_as_float(e << 16), __uint_as_float((e & 0xffff0000u) ^ (i ? 0u : 0x80000000u))}) * 0.5f;
    if (l == 0) w = m;
  }
  return w;
}
DEVI void fft_rows(char* lds, rsrc_t rw, const u16* __restrict__ wr, const u16* __restrict__ wi, int blk, unsigned hb) {
  int t = threadIdx.x;
  asm volatile("" : "+v"(t));
  int l = t & 63, row = blk * 8 + __builtin_amdgcn_readfirstlane(t >> 6);
  char* wl = lds + __builtin_amdgcn_readfirstlane(t >> 6) * 8704;
  unsigned wv[18];
  u32x4 in[8], raw[4];
  auto get = [&](int r) {
#pragma unroll
    for (int i = 0; i < 8; ++i) in[i] = ld_sc1s(rw, (l & 48) * 16 + (l & 3) * 64 + (l & 12) * 4 + (i & 3) * 1024, OFF_T + far8a(r, hb) + (i >> 2) * 4096u);
    fft_rows_ask(wr, wi, r, l, raw, wv);
  };
  get(row);
  cf b1[2][3], b2[4];
  wpow<3>(b1[0], (float)(2 * l) * (1.f / 512.f));
  wpow<3>(b1[1], (float)(2 * l + 1) * (1.f / 512.f));
  wpow<4>(b2, (float)(l & 7) * (1.f / 64.f));
#pragma nounroll
  for (int it = 0; it < 4; ++it, row += 256) {
  auto fresh = [&]() {
    asm volatile("" : "+v"(l), "+v"(b1[0][0]), "+v"(b1[0][1]), "+v"(b1[0][2]), "+v"(b1[1][0]), "+v"(b1[1][1]), "+v"(b1[1][2]), "+v"(b2[0]), "+v"(b2[1]), "+v"(b2[2]), "+v"(b2[3]));
  };
  fresh();
  const unsigned go = far8a(row, hb) + (unsigned)(l * 16);
  cf v[16];
#pragma unroll
  for (int i = 0; i < 8; ++i) {
    v[2 * i] = cf{__uint_as_float(in[i][0]), __uint_as_float(in[i][1])};
    v[2 * i + 1] = cf{__uint_as_float(in[i][2]), __uint_as_float(in[i][3])};
  }
  dft8<2>(v); dft8<2>(v + 1);
  twiddle<8, 2>(v, b1[0]); twiddle<8, 2>(v + 1, b1[1]);
#pragma unroll
  for (int i = 0; i < 8; ++i) *(u32x4*)(wl + i * 1088 + l * 16) = cc2u(v[2 * i], v[2 * i + 1]);
#pragma unroll
  for (int i = 0; i < 16; ++i) v[i] = *(const cf*)(wl + (l >> 3) * 1088 + i * 64 + (l & 7) * 8);
  dft16(v);
  twiddle<16, 1>(v, b2);
#pragma unroll
  for (int i = 0; i < 16; ++i) *(cf*)(wl + (l & 7) * 1040 + i * 64 + (l >> 3) * 8) = v[i];
#pragma unroll
  for (int i = 0; i < 16; ++i) v[i] = *(const cf*)(wl + (i >> 1) * 1040 + (i & 1) * 512 + l * 8);
  dft8<2>(v); dft8<2>(v + 1);
  fft_rows_w(wl, l, raw, wv);
  if (row == 0) {
    unsigned w1[18];
    fft_rows_ask(wr, wi, 1024, l, raw, w1);
    fft_rows_w(wl, l, raw, w1);
#pragma unroll
    for (int i = 0; i < 16; ++i) *(cf*)(wl + i * 512 + l * 8) = v[i];
#pragma unroll
    for (int i = 0; i < 16; ++i) {
      const cf c = *(const cf*)(wl + ((1024 - l - 64 * i) & 1023) * 8), wa = wval(wv, i, l), wb = wval(w1, i, l);
      v[i] = cmul(v[i], (wa + wb) * 0.5f) + cmul(cf{c.x, -c.y}, (wa - wb) * 0.5f);
    }
  } else {
#pragma unroll
    for (int i = 0; i < 16; ++i) v[i] = cmul(v[i], wval(wv, i, l));
  }
#pragma unroll
  for (int i = 0; i < 16; ++i) v[i] = cf{v[i].y, v[i].x};
  asm volatile("" : "+v"(v[0]), "+v"(v[8]), "+v"(v[15]) :: "memory");
  if (it < 3) get(row + 256);
  asm volatile("" ::: "memory");
  fresh();
  dft8<2>(v); dft8<2>(v + 1);
#pragma unroll
  for (int i = 0; i < 16; ++i) *(cf*)(wl + (i >> 1) * 1056 + (i & 1) * 512 + l * 8) = v[i];
#pragma unroll
  for (int i = 0; i < 16; ++i) v[i] = *(const cf*)(wl + (l & 7) * 1056 + i * 64 + (l >> 3) * 8);
  twiddle<16, 1>(v, b2);
  dft16(v);
#pragma unroll
  for (int i = 0; i < 16; ++i) *(cf*)(wl + (l >> 3) * 1088 + i * 64 + (l & 7) * 8) = v[i];
#pragma unroll
  for (int i = 0; i < 8; ++i) {
    const f32x4 a = *(const f32x4*)(wl + i * 1088 + l * 16);
    v[2 * i] = cf{a[0], a[1]}; v[2 * i + 1] = cf{a[2], a[3]};
  }
  twiddle<8, 2>(v, b1[0]); twiddle<8, 2>(v + 1, b1[1]);
  dft8<2>(v); dft8<2>(v + 1);
#pragma unroll
  for (int i = 0; i < 8; ++i) st_sc1(rw, OFF_T + go + i * 1024u, cc2u(cf{v[2 * i].y, v[2 * i].x}, cf{v[2 * i + 1].y, v[2 * i + 1].x}));
  }
}
DEVI void fft_cols_inv(char* lds, rsrc_t rw, int blk, unsigned hb) {
  int t = threadIdx.x;
  asm volatile("" : "+v"(t));
  const int j = t >> 2;
  u32x4 in[8];
  auto get = [&](int b) {
#pragma unroll
    for (int i = 0; i < 8; ++i) in[i] = ld_sc1s(rw, (unsigned)((t >> 2) * 16384 + (t & 3) * 16), OFF_T + far8a(128 * i, hb) + (unsigned)b * 64u);
  };
  get(blk);
  cf b1[4], b2[4];
  wpow<4>(b1, (float)j * (1.f / 1024.f));
  wpow<4>(b2, (float)(j & 7) * (1.f / 64.f));
#pragma nounroll
  for (int it = 0; it < 4; ++it, blk += 8) {
    asm volatile("" : "+v"(t), "+v"(b1[0]), "+v"(b1[1]), "+v"(b1[2]), "+v"(b1[3]), "+v"(b2[0]), "+v"(b2[1]), "+v"(b2[2]), "+v"(b2[3]));
    cf v[16];
#pragma unroll
    for (int i = 0; i < 8; ++i) {
      const f32x4 a = __builtin_bit_cast(f32x4, in[i]);
      cf m = cf{a[2] - a[1], a[0] + a[3]};
      v[i] = cf{a[1] + a[2], a[0] - a[3]};
      if (i == 0 && t < 4) { v[0] = cf{a[2], a[0]}; m = cf{a[3], a[1]}; }
      *(cf*)(lds + 65536 + i * 4096 + t * 8) = m;
    }
    asm volatile("" : "+v"(v[0]), "+v"(v[1]), "+v"(v[2]), "+v"(v[3]), "+v"(v[4]), "+v"(v[5]), "+v"(v[6]), "+v"(v[7]) :: "memory");
    if (it < 3) get(blk + 8);
    bar();
#pragma unroll
    for (int i = 8; i < 16; ++i) v[i] = *(const cf*)(lds + 65536 + ((2048 - 128 * i - (t >> 2)) & 1023) * 32 + (t & 3) * 8);
    fft2048(t, lds, v, b1, b2);
    const float sc = 0.5f / ((float)S_ * (float)D_);
#pragma unroll
    for (int i = 0; i < 16; ++i) *(unsigned*)(lds + i * 2048 + t * 4) = cvt2(v[i].y * sc, v[i].x * sc);
    bar();
#pragma unroll
    for (int i = 0; i < 4; ++i) st_sc1(rw, bfoff(blk, i * 512, hb) + (unsigned)t * 16u, *(const u32x4*)(lds + i * 8192 + t * 16));
  }
}

DEVI void oproj_worker(char* lds, rsrc_t rw, const u16* __restrict__ Wo, int set, int c, int q, u16* __restrict__ out, unsigned hb DLYARG
) {
  DLYI(0x300);
  int t = threadIdx.x;
  asm volatile("" : "+v"(t));
  const int l = t & 63, w = __builtin_amdgcn_readfirstlane(t >> 6), g = l >> 4, lo = l & 15, hh = 4 * q + (w >> 1);
  constexpr int sstep = 4;
  const int s0 = set < 3 ? set - ON0 : 3 * ON0 + 3;
  auto seg = [&](int s) { return s >= 3 * ON0 ? s : (s < s0 ? 0 : ((s - s0) >> 2) * 3 + set); };
  u32x4 a[4][4][2];
  __syncthreads();
  {
    const u16* gp = Wo + (size_t)(hh * DH_ + g) * D_ + c * 256 + (w & 1) * 128 + (lo ^ (4 * g)) * 8;
    auto ask = [&](int p) {
#pragma unroll
      for (int i = 0; i < 4; ++i)
        __builtin_amdgcn_global_load_lds((const unsigned*)(gp + (size_t)(16 * p + 4 * i) * D_), (unsigned*)(lds + p % 5 * 32768 + w * 4096 + i * 1024 + l * 16), 16, 0, NTO);
      SCHED();
    };
#pragma unroll
    for (int p = 0; p < 5; ++p) ask(p);
    const char* pp = lds + (w >> 2) * 4096 + (4 * g + (lo >> 2)) * 256 + ((4 * (w & 3)) ^ lo) * 16;
#pragma unroll
    for (int n = 0; n < 8; ++n) {
      asm volatile("s_waitcnt vmcnt(%0) lgkmcnt(0)\n s_barrier" :: "n"(4 * (n == 0 ? 4 : (n < 5 ? 3 : 7 - n))) : "memory");
      if (n >= 1 && n < 4) ask(n + 4);
#pragma unroll
      for (int i = 0; i < 8; ++i) {
        const u32x2 x0 = tr_quad(pp + n % 5 * 32768 + (i >> 1) * 8192 + (i & 1) * 8);
        a[i >> 1][n >> 1][i & 1][2 * (n & 1)] = x0[0];
        a[i >> 1][n >> 1][i & 1][2 * (n & 1) + 1] = x0[1];
      }
      SCHED();
    }
    bar();
  }
  const unsigned vo = (unsigned)l * 16u;
  const unsigned fo = cooff(4 * q, hb) + (unsigned)(((l >> 2) & 3) * 1024 + (l & 3) * 4);
  auto flags = [&](int s) { return ld_sc1_32(rw, fo + (unsigned)((l & 16) ? seg(s + sstep) : seg(s)) * 16u); };
  auto fbits = [&](int s, unsigned f) {
    const unsigned m = (unsigned)__builtin_amdgcn_ballot_w64((f & 1u) != 0u);
    return ((m & 0xffffu) == 0xffffu && s < NL_ ? 1u : 0u) | (m == 0xffffffffu && s + sstep < NL_ ? 2u : 0u);
  };
  auto wait_b = [&](int s) {
#pragma nounroll
    for (int it = 0; it < HSPIN_CAP; ++it) {
      if (__all((ld_sc1_32(rw, cooff(hh, hb) + (unsigned)(seg(s) * 16 + (l & 3) * 4)) & 1u) != 0u)) break;
      __builtin_amdgcn_s_sleep(XSLP);
    }
    asm volatile("" ::: "memory");
  };
  auto load_b = [&](int s, int par, int i, unsigned vd) {
    auto* dst = (__attribute__((address_space(3))) void*)(LDS3 + par * 32768 + w * 4096);
    const unsigned so = ooff(seg(s), hh, hb) + (w & 1) * HALF_;
    if (i == 0) __builtin_amdgcn_raw_ptr_buffer_load_lds(rw, dst, 16, vd, so, 0, 16);
    if (i == 1) __builtin_amdgcn_raw_ptr_buffer_load_lds(rw, dst, 16, vd, so, 1024, 16);
    if (i == 2) __builtin_amdgcn_raw_ptr_buffer_load_lds(rw, dst, 16, vd, so, 2048, 16);
    if (i == 3) __builtin_amdgcn_raw_ptr_buffer_load_lds(rw, dst, 16, vd, so, 3072, 16);
  };
  auto pxs = [&](int slot) { return OFF_PX + far8a((slot * 4 + c) * 16 + (w >> 2), hb) + (unsigned)(w & 3) * 2048u; };
  auto pxo = [&](int s) { return pxs(s < FS_ ? s & 31 : s - FS_ + 32); };
  for (int s = FS_ + (set + sstep - FS_ % sstep) % sstep; s < NL_; s += sstep) {
    st_sc1s(rw, vo, pxo(s) + q * 32768u, u32x4{POISON, POISON, POISON, POISON});
    st_sc1s(rw, vo + 1024u, pxo(s) + q * 32768u, u32x4{POISON, POISON, POISON, POISON});
    SCHED();
    asm volatile("s_nop 1");
    SCHED();
  }
  const unsigned sv = (unsigned)((lo >> 2) * 256 + g * 64 + (lo & 3) * 16);
  auto flag = [&](int s) { if (t == 64) st_sc1_32(rw, OFF_CPX + (unsigned)(seg(s) * 4 + c) * 128u + q * 4u, 1u); };
  float* gl = (float*)(lds + 132096);
  u32x4 pv[2];
  float gv;
  unsigned bb;
  auto pf = [&](int s) { return ld_sc1_32(rw, OFF_CPX + (unsigned)(seg(s) * 4 + c) * 128u + (l & 7) * 4u); };
  auto fin_a = [&](int s, bool ok) {
    if (!ok && s < FS_) {
#pragma nounroll
      for (int it = 0; it < HSPIN_CAP; ++it) {
        if (__all(pf(s) == 1u)) break;
        __builtin_amdgcn_s_sleep(2);
      }
    }
    int ll = l;
    asm volatile("" : "+v"(ll));
    gv = gl[seg(s) * 4 + (ll & 3)];
    bb = __builtin_amdgcn_raw_buffer_load_b32(rw, (ll & 12) * 16384 + (ll & 3) * 16 + (ll >> 4) * 4, bfoff(32 * c + 4 * w, seg(s) * SEG_ + 4 * q, hb), 16);
    if (s >= FS_) __builtin_amdgcn_s_sleep(PD1);
#pragma nounroll
    for (int it = 0; it < HSPIN_CAP; ++it) {
      asm volatile("" ::: "memory");
#pragma unroll
      for (int k = 0; k < 2; ++k) pv[k] = ld_sc1s(rw, (ll >> 4) * 32768 + (ll & 15) * 16, pxo(s) + k * 131072u + q * 256u);
      if (s < FS_ || !bad(umax5(pv[1], umax5(pv[0], 0u)))) break;
      __builtin_amdgcn_s_sleep(1);
    }
  };
  auto fin_b = [&](int s) {
    int ll = l;
    asm volatile("" : "+v"(ll));
    const unsigned s1 = (ll & 2) ? 0u : 0x3f80u << (16 * (ll & 1));
    const u32x4 sel = u32x4{(ll & 12) == 0 ? s1 : 0u, (ll & 12) == 4 ? s1 : 0u, (ll & 12) == 8 ? s1 : 0u, (ll & 12) == 12 ? s1 : 0u};
    const f32x4 v = mfu(sel, pv[1], mfu(sel, pv[0], f32x4{0.f, 0.f, 0.f, 0.f}));
    const float omg = rbf(1.0f - gv);
    const unsigned y = cvt2(omg * __uint_as_float(bb << 16), omg * __uint_as_float(bb & 0xffff0000u));
    const unsigned av = cvt2(v[0], v[1]);
    const unsigned x = cvt2(gv * __uint_as_float(av << 16), gv * __uint_as_float(av & 0xffff0000u));
    const unsigned r = cvt2(__uint_as_float(x << 16) + __uint_as_float(y << 16), __uint_as_float(x & 0xffff0000u) + __uint_as_float(y & 0xffff0000u));
    __builtin_amdgcn_raw_buffer_store_b32(r, __builtin_amdgcn_make_buffer_rsrc(out, 0, S_ * D_ * 2, 0x00020000), (ll & 3) * (D_ * 2) + (ll & 12) * 4 + (ll >> 4) * 4,
                                          (unsigned)((seg(s) * SEG_ + 4 * q) * (D_ * 2) + (256 * c + 32 * w) * 2), OAUX);
  };
  f32x4 acc[2][2];
  auto send = [&](unsigned po) {
    u32x4 u[2];
#pragma unroll
    for (int i = 0; i < 8; ++i) u[i >> 2][i & 3] = cvt2x(acc[i >> 2][(i >> 1) & 1][2 * (i & 1)], acc[i >> 2][(i >> 1) & 1][2 * (i & 1) + 1]);
    asm volatile("" : "+v"(u[0]), "+v"(u[1]));
    st_sc1s(rw, sv, po + q * 32768u, u[0]);
    st_sc1s(rw, sv + 1024u, po + q * 32768u, u[1]);
  };
  auto touch = [&](int s, bool on) {
    if (w == 5)
      __builtin_amdgcn_raw_ptr_buffer_load_lds(rw, (__attribute__((address_space(3))) void*)(LDS3 + 131328), 4, on ? (l >> 5) * HALF_ + (l & 31) * 128u : 0x80000000u,
                                               ooff(s, 4 * q + c, hb), 0, 16);
  };
  auto rdo = [](int j) { return (j >> 3) * 8192 + ((j & 1) * 4 + ((j >> 1) & 3)) * 1024; };
#pragma unroll
  for (int k = 0; k < 2; ++k) {
    wait_b(s0 + k * sstep);
#pragma unroll
    for (int i = 0; i < 4; ++i) load_b(s0 + k * sstep, k, i, vo);
  }
  unsigned* vote = (unsigned*)(lds + 131072);
  unsigned fl = flags(s0 + 2 * sstep);
  drain_vm();
  wg_wait_s(rw, OFF_CF + 256u, 32u);
  if (w == 0) *(u32x4*)(gl + 4 * l) = ld_sc1(rw, OFF_G + (unsigned)(l * SEG_ + 4 * q) * 4u);
  HTR(10);
  int par = 0, s1 = s0;
  {
    constexpr unsigned NOW = 0x80000000u;
    const unsigned s1_ = (l & 2) ? 0u : 0x3f80u << (16 * (l & 1));
    const u32x4 sel = u32x4{(l & 12) == 0 ? s1_ : 0u, (l & 12) == 4 ? s1_ : 0u, (l & 12) == 8 ? s1_ : 0u, (l & 12) == 12 ? s1_ : 0u};
    const unsigned ob = (l & 12) * 16384 + (l & 3) * 16 + g * 4, op = g * 32768 + lo * 16, oo = (l & 3) * (D_ * 2) + (l & 12) * 4 + g * 4;
    const unsigned ofx = l < 32 ? fo : OFF_CPX + (unsigned)(c * 128 + (l & 7) * 4);
    const rsrc_t ro = __builtin_amdgcn_make_buffer_rsrc(out, 0, S_ * D_ * 2, 0x00020000);
    u32x4 b[8];
#pragma unroll
    for (int j = 0; j < 8; ++j) b[j] = *(const u32x4*)(lds + l * 16 + rdo(j));
    unsigned vt, px[4] = {0u, 0u, 0u, 0u};
    pv[0] = pv[1] = u32x4{0u, 0u, 0u, 0u};
    gv = 0.f; bb = 0u;
    int rn = 0, sg[9];
#pragma unroll
    for (int i = 0; i < 9; ++i) sg[i] = seg(s0 + (i - 4) * sstep);
    if (OPR == 1 && w >= 4) __builtin_amdgcn_s_setprio(1);
    if (OPR == 2 && w < 4) __builtin_amdgcn_s_setprio(1);
#pragma nounroll
    do {
      const int s = s1;
      const char* bp = lds + par * 32768 + l * 16;
      const char* bn = lds + ((par + 1) & 3) * 32768 + l * 16;
      unsigned vd, so, x, y;
      f32x4 v;
      SCHED();
#pragma unroll
      for (int j = 0; j < 32; ++j) {
        if (j >= OB1 && j < OB1 + 4 * ODS && (j - OB1) % ODS == 0) {
          auto* dst = (__attribute__((address_space(3))) void*)(LDS3 + (par ^ 2) * 32768 + w * 4096);
          if (j == OB1) __builtin_amdgcn_raw_ptr_buffer_load_lds(rw, dst, 16, vd, so, 0, 16);
          if (j == OB1 + ODS) __builtin_amdgcn_raw_ptr_buffer_load_lds(rw, dst, 16, vd, so, 1024, 16);
          if (j == OB1 + 2 * ODS) __builtin_amdgcn_raw_ptr_buffer_load_lds(rw, dst, 16, vd, so, 2048, 16);
          if (j == OB1 + 3 * ODS) __builtin_amdgcn_raw_ptr_buffer_load_lds(rw, dst, 16, vd, so, 3072, 16);
        }
#pragma unroll
        for (int ct = 0; ct < 2; ++ct) acc[j & 1][ct] = mfu(a[j >> 3][(j >> 1) & 3][ct], b[j & 7], j >= 2 ? acc[j & 1][ct] : f32x4{0.f, 0.f, 0.f, 0.f});
        b[j & 7] = *(const u32x4*)((j < 24 ? bp : bn) + rdo((j + 8) & 31));
        if (j == 2) { px[3] = px[2]; px[2] = px[1]; px[1] = px[0]; px[0] = pxo(s); asm volatile("" : "+s"(px[0])); }
        if (j == 5) { so = ooff(sg[6], hh, hb) + (w & 1) * HALF_; asm volatile("" : "+s"(so)); }
        if (j == OB2 - 5) {
          v = mfu(sel, pv[1], mfu(sel, pv[0], f32x4{0.f, 0.f, 0.f, 0.f}));
          const float omg = rbf(1.0f - gv);
          y = cvt2(omg * __uint_as_float(bb << 16), omg * __uint_as_float(bb & 0xffff0000u));
          asm volatile("" : "+v"(y));
        }
        if (j == OB2 - 3) {
          const unsigned av = cvt2(v[0], v[1]);
          x = cvt2(gv * __uint_as_float(av << 16), gv * __uint_as_float(av & 0xffff0000u));
          asm volatile("" : "+v"(x));
        }
        if (j == OB2 - 2)
          __builtin_amdgcn_raw_buffer_store_b32(cvt2(__uint_as_float(x << 16) + __uint_as_float(y << 16), __uint_as_float(x & 0xffff0000u) + __uint_as_float(y & 0xffff0000u)),
                                                ro, oo | (rn >= 4 ? 0u : NOW), (unsigned)((sg[0] * SEG_ + 4 * q) * (D_ * 2) + (256 * c + 32 * w) * 2), OAUX);
        SCHED();
        if (j == OB0 - 9 && w == 0) {
          unsigned fb = fbits(s + 2 * sstep, fl);
          if (__builtin_expect(!(fb & 1u) && s < OLIM, 0)) {
#pragma nounroll
            for (int it = 0; it < HSPIN_CAP && !(fb & 1u); ++it) {
              __builtin_amdgcn_s_sleep(8);
              fb = fbits(s + 2 * sstep, flags(s + 2 * sstep));
            }
          }
          vote[par] = fb;
          if (__builtin_expect((__builtin_amdgcn_ballot_w64(fl == 1u) >> 32) != 0xffffffffu && rn >= 3, 0)) {
#pragma nounroll
            for (int it = 0; it < HSPIN_CAP; ++it) {
              if (__all(pf(s - 3 * sstep) == 1u)) break;
              __builtin_amdgcn_s_sleep(2);
            }
          }
        }
        if (j == OB0 + 1) {
#pragma unroll
          for (int k = 0; k < 2; ++k) pv[k] = ld_sc1s(rw, op | (rn >= 3 ? 0u : NOW), px[3] + k * 131072u + q * 256u);
        }
        if (j == OB1 + 1) {
          gv = gl[sg[1] * 4 + (l & 3)];
          bb = __builtin_amdgcn_raw_buffer_load_b32(rw, ob, bfoff(32 * c + 4 * w, sg[1] * SEG_ + 4 * q, hb), 16);
        }
        if (j == OB0) {
          wait_vm<1>();
          __builtin_amdgcn_s_barrier();
          asm volatile("" ::: "memory");
          vt = vote[par];
          if (rn >= 1 && t == 64) st_sc1_32(rw, OFF_CPX + (unsigned)(sg[3] * 4 + c) * 128u + q * 4u, 1u);
        }
        if (j == OB0 && w == 0) fl = ld_sc1_32(rw, ofx + (l < 32 ? (unsigned)((l & 16) ? sg[8] : sg[7]) * 16u : (unsigned)sg[2] * 512u));
        if (j == OB1 - 1) {
          vt = __builtin_amdgcn_readfirstlane(vt);
          vd = (vt & 1u) ? vo : NOW;
        }
        if (j == OB1 + 3 * ODS + 1) touch(sg[7], vt & 2u);
      }
      send(px[0]);
      s1 += sstep;
#pragma unroll
      for (int i = 0; i < 8; ++i) sg[i] = sg[i + 1];
      sg[8] += sg[8] < 3 * ON0 ? 3 : 4;
      par = (par + 1) & 3;
      ++rn;
    } while (vt & 1u);
    if (w == 0) vote[par] = fbits(s1 + sstep, flags(s1 + sstep));
    asm volatile("" ::: "memory");
    __builtin_amdgcn_s_waitcnt(0x0f72);
    bar();
  }
  int np = min((s1 - s0) / sstep, 4);
#pragma nounroll
  for (int s = s1; s < NL_; s += sstep, par ^= 1) {
    const int nxt = s + sstep;
    const unsigned vt = __builtin_amdgcn_readfirstlane(vote[par]);
    const bool pre = (vt & 1u) != 0u;
    const unsigned vd = pre ? vo : 0x80000000u;
    const char* bp = lds + par * 32768 + l * 16;
    auto rd = [&](int j) { return *(const u32x4*)(bp + rdo(j)); };
    unsigned f;
    if (np >= 3) f = pf(s - 3 * sstep);
    u32x4 b[8];
#pragma unroll
    for (int j = 0; j < 8; ++j) b[j] = rd(j);
    if (np >= 2) flag(s - 2 * sstep);
    if (np >= 4) fin_b(s - 4 * sstep);
    SCHED();
#pragma unroll
    for (int j = 0; j < 32; ++j) {
      if (j < 4 * ODS && j % ODS == 0) load_b(nxt, par ^ 1, j / ODS, vd);
#pragma unroll
      for (int ct = 0; ct < 2; ++ct) acc[j & 1][ct] = mfu(a[j >> 3][(j >> 1) & 3][ct], b[j & 7], j >= 2 ? acc[j & 1][ct] : f32x4{0.f, 0.f, 0.f, 0.f});
      if (j + 8 < 32) b[j & 7] = rd(j + 8);
      SCHED();
      if (j == OFA) {
        if (np >= 3) fin_a(s - 3 * sstep, __all(f == 1u));
        if (w == 0) fl = flags(nxt + sstep);
        touch(seg(nxt + sstep), vt & 2u);
        SCHED();
      }
    }
    send(pxo(s));
    asm volatile("" ::: "memory");
    __builtin_amdgcn_s_waitcnt(0x0f72);
    asm volatile("" ::: "memory");
    if (w == 0) vote[par ^ 1] = fbits(nxt + sstep, fl);
    np = min(np + 1, 4);
    if (!pre) {
      if (np >= 4) fin_b(s - 3 * sstep);
      if (s - sstep < FS_) {
        if (s < FS_) drain_vm();
        bar();
        if (np >= 2) flag(s - sstep);
        if (s < FS_) flag(s);
      }
#pragma nounroll
      for (int k = min(np, 3) - 1; k >= 0; --k) { fin_a(s - k * sstep, false); fin_b(s - k * sstep); }
      np = 0;
      if (nxt >= NL_) break;
      if (nxt < FS_) {
        wait_b(nxt);
#pragma unroll
        for (int i = 0; i < 4; ++i) load_b(nxt, par ^ 1, i, vo);
        drain_vm();
      } else {
        const unsigned so = ooff(nxt, hh, hb) + (w & 1) * HALF_;
        const unsigned cm = cooff(hh, hb) + (unsigned)((nxt - 2) * 16 + (l & 3) * 4);
        after(poll_flags<FSLP>(rw, cm, ld_sc1_32(rw, cm)), (int)(nxt == NL_ - 1 ? dly[XTL] : dly[XT1]));
#pragma nounroll
        for (int it = 0; it < HSPIN_CAP; ++it) {
          asm volatile("" ::: "memory");
#pragma unroll
          for (int j = 0; j < 4; ++j) b[j] = ld_sc1s(rw, vo + j * 1024u, so);
          if (!bad4(b)) break;
          __builtin_amdgcn_s_sleep(1);
        }
#pragma unroll
        for (int j = 0; j < 4; ++j) *(u32x4*)(lds + (par ^ 1) * 32768 + w * 4096 + j * 1024 + l * 16) = b[j];
      }
    }
    bar();
  }
}

DEVI void helper_role(char* lds, char* ws, rsrc_t rw, const u16* __restrict__ x, const u16* __restrict__ Wq,
                      const u16* __restrict__ Wk, const u16* __restrict__ Wv, const u16* __restrict__ Wo,
                      const u16* __restrict__ wr, const u16* __restrict__ wi, const u16* __restrict__ gw, u16* __restrict__ out, int hw
                      ) {
  const int xg = hw & 7, loc = hw >> 3;
  const int q = xg >> 1, m = loc * 2 + (hw & 1);
  DLYI(0x30);
  HTR(0);
  const int xi = (m & 7) * 4 + q, l = threadIdx.x & 63, w = __builtin_amdgcn_readfirstlane(threadIdx.x >> 6);
  auto xld = [&](int i) {
    return __builtin_amdgcn_raw_buffer_load_b128(__builtin_amdgcn_make_buffer_rsrc((void*)x, 0, S_ * D_ * 2, 0x00020000),
                                                 (l & 7) * 2048 + w * 256 + (l >> 3) * 16 + (i & 2) * 64, (unsigned)(32 * (i >> 2) + xi) * 32768u + (i & 1) * 16384u, NTX);
  };
  auto xst = [&](int i, unsigned hb, u32x4 v) { st_sc1s(rw, (l >> 3) * 256 + (l & 7) * 16 + (i & 2) * 1024 + (i & 1) * 128, OFF_XF + far8((32 * (i >> 2) + xi) * 4 + (w >> 1), hb) + (w & 1) * 4096u, v); };
  unsigned hb;
  if (m < 24) {
    if (m >= 16) {
      static_assert((OFF_REC - OFF_CKV) % 512u == 0 && (OFF_REC - OFF_CKV) / 512u <= 512u, "shares");
      if (threadIdx.x < (OFF_REC - OFF_CKV) / 512u) st_sc1(rw, OFF_CKV + xi * ((OFF_REC - OFF_CKV) / 32u) + threadIdx.x * 16u, u32x4{0u, 0u, 0u, 0u});
      u32x4 xs[4];
#pragma unroll
      for (int i = 0; i < 4; ++i) xs[i] = xld(i);
      const u32x2 no = launch_no(ws);
#pragma unroll
      for (int i = 0; i < 4; ++i) xst(i, 0u, xs[i]);
      rec_put(rw, xi, no);
      asm volatile("" :: "v"(xs[3]));
      HTR(4);
      for (unsigned i = 0; i < (RSE ? 7u : dly[QDL]); ++i) __builtin_amdgcn_s_sleep(32);
    }
    if ((m >> 3) == 1) hb = proj_reg<false>(lds, ws, rw, Wv, 8 * q + (m & 7), 2, hw);
    else hb = proj_reg<true>(lds, ws, rw, m < 8 ? Wk : Wq, 8 * q + (m & 7), m < 8 ? 1 : 0, hw);
    HTR(2);
  } else {
    probe(ws, rw, 8 * q + m - 24);
    const u32x2 no = launch_no(ws);
    rec_put(rw, 32 + 8 * q + m - 24, no);
    HTR(4);
    for (unsigned i = 0; i < dly[XDL]; ++i) __builtin_amdgcn_s_sleep(32);
    NearMap nm;
    {
      u32x4 wv[2], xs[16];
#pragma unroll
      for (int i = 0; i < 16; ++i)
        xs[(i + 4) & 15] = __builtin_amdgcn_raw_buffer_load_b128(__builtin_amdgcn_make_buffer_rsrc((void*)x, 0, S_ * D_ * 2, 0x00020000), l * 16 + (i & 3) * 1024,
                                                                 (unsigned)(32 * (((i + 4) & 15) >> 2) + xi) * 32768u + w * 4096u, NTX);
#pragma unroll
      for (int i = 0; i < 2; ++i) wv[i] = *(const u32x4*)(gw + i * 512 + l * 8);
      nm.k = near_kind(rw, no);
      hb = far_bit(nm.k);
#pragma unroll
      for (int i = 4; i < 16; ++i) *(u32x4*)(lds + (i >> 2) * 32768 + (i & 1) * 16384 + l * 256 + ((2 * w + ((i >> 1) & 1)) ^ (l & 15)) * 16) = xs[i];
      __syncthreads();
      u32x4 xf[12];
#pragma unroll
      for (int i = 0; i < 12; ++i) xf[i] = *(const u32x4*)(lds + ((i >> 2) + 1) * 32768 + w * 4096 + (i & 3) * 1024 + (l >> 4) * 256 + ((l & 15) ^ ((i & 3) * 4 + (l >> 4))) * 16);
#pragma unroll
      for (int i = 0; i < 12; ++i) st_sc1s(rw, l * 16 + (i & 3) * 1024, OFF_XF + far8((32 * ((i >> 2) + 1) + xi) * 4 + (w >> 1), hb) + (w & 1) * 4096u, xf[i]);
      float gs = 0.f;
#pragma unroll
      for (int n = 0; n < 8; ++n) {
        float acc = 0.f;
#pragma unroll
        for (int i = 0; i < 8; ++i) {
          const unsigned a = xs[2 * n + (i >> 2)][i & 3], b = wv[i >> 2][i & 3];
          const unsigned pr = cvt2(__uint_as_float(a << 16) * __uint_as_float(b << 16), __uint_as_float(a & 0xffff0000u) * __uint_as_float(b & 0xffff0000u));
          acc += __uint_as_float(pr << 16) + __uint_as_float(pr & 0xffff0000u);
        }
#pragma unroll
        for (int o = 1; o < 8; o *= 2) acc += __shfl_xor(acc, o);
        acc = rows_sum(fold8(acc));
        if (l == n) gs = acc;
      }
      asm volatile("" :: "v"(xf[11]));
      wg_signal(ws, OFF_CF + 384u);
      if (l < 8) st_sc1_32(rw, OFF_G + (unsigned)((32 * (l >> 1) + xi) * 16 + 2 * w + (l & 1)) * 4u, __float_as_uint(rbf(1.0f / (1.0f + expf(-rbf(gs))))));
    }
    wg_wait_s(rw, OFF_CF + 384u, 32u);
    HTR(1);
    fft_cols_fwd(lds, rw, 8 * q + m - 24, hb);
    wg_signal(ws, OFF_CF);
    HTR(3);
    {
      unsigned tx = threadIdx.x;
      asm volatile("" : "+v"(tx));
      const unsigned tv = (tx >> 3) & 1;
      u32x4 p3 = u32x4{tv, tv, tv, tv}, p4 = u32x4{POISON, POISON, POISON, POISON};
      asm volatile("" : "+v"(p3), "+v"(p4));
#pragma unroll
      for (int i = 0; i < 6; ++i) st_sc1s(rw, (tx & 255u) * 16u, nm(3 + (i >> 1), 2 * (i & 1) + (w >> 2), NB3 + xi), p3);
#pragma unroll
      for (int i = FS_; i < NL_; ++i) st_sc1s(rw, (w >> 2) * HALF_ + (tx & 255u) * 16u, ooff(i, xi, hb), p4);
      drain_vm();
      asm volatile("" :: "v"(p3), "v"(p4));
    }
    wg_wait_s(rw, OFF_CF, 32u);
    fft_rows(lds, rw, wr, wi, xi, hb);
    wg_signal(ws, OFF_CF + 128u);
    HTR(5);
    xattn_wave(rw, m & 7, q, hb);
    HTR(11);
    wg_wait_s(rw, OFF_CF + 128u, 32u);
    fft_cols_inv(lds, rw, 32 * q + m - 24, hb);
    wg_signal(ws, OFF_CF + 256u);
    HTR(7);
  }
  oproj_worker(lds, rw, Wo, (m >> 3) ^ (m >> 4), m & 3, 2 * q + ((m >> 2) & 1), out, hb DLYA
);
  HTR(9);
}

static_assert(W3_OFF + 65536 <= SMEM_BYTES, "LDS");
__global__ __launch_bounds__(512, 1) void mega_kernel(const u16* __restrict__ x, const u16* __restrict__ mem0,
                                                      const u16* __restrict__ Wq, const u16* __restrict__ Wk,
                                                      const u16* __restrict__ Wv, const u16* __restrict__ Wo,
                                                      const u16* __restrict__ wr, const u16* __restrict__ wi,
                                                      const u16* __restrict__ gw, u16* __restrict__ out, char* ws) {
  char* lds = lds_;
  ws += (0u - (unsigned)(size_t)ws) & (SET_ - 1u);
  const rsrc_t rw = __builtin_amdgcn_make_buffer_rsrc(ws, 0, WS_USED, 0x00020000);
  const int idx = (blockIdx.x >> 3) * 4 + (blockIdx.x & 3);
  const bool isrec = (blockIdx.x & 4) == 0;
  const int rid = (blockIdx.x & 3) * 32 + (blockIdx.x >> 3), hid = (idx & 3) * 2 + ((idx >> 2) & 1) + (idx >> 3) * 8;
  if (__builtin_amdgcn_readfirstlane(threadIdx.x >> 6) == (isrec ? 7 : 3) && (blockIdx.x >> 3) >= 16) {
    u32x2 pc;
    unsigned n;
    asm volatile("s_getpc_b64 %0\n code_from: s_mov_b32 %1, code_to - code_from" : "=s"(pc), "=s"(n));
    asm volatile("s_mov_b32 m0, %2\n s_nop 0\n buffer_load_dword %0, %1, 0 offen lds"
                 :: "v"((((blockIdx.x >> 3) - 16) * 64 + (threadIdx.x & 63)) * 128), "s"(u32x4{pc[0], pc[1] & 0xffffu, n, 0x00020000u}),
                    "s"((unsigned)(size_t)LDS3 + (isrec ? 3u * 24576u : 131072u + 3u * 4096u)) : "memory");
  }
  if (isrec) {
    recur_role(lds, ws, rw, mem0, Wq, Wk, Wv, Wo, out, rid TRARG);
  } else helper_role(lds, ws, rw, x, Wq, Wk, Wv, Wo, wr, wi, gw, out, hid TRARG);
}

extern "C" void kernel_launch(void* const* d_in, const int* in_sizes, int n_in, void* d_out, int out_size, void* d_ws,
                              size_t ws_size, hipStream_t stream) {
  const u16* x = (const u16*)d_in[0];
  const u16* mem0 = (const u16*)d_in[1];
  const u16* Wq = (const u16*)d_in[2];
  const u16* Wk = (const u16*)d_in[3];
  const u16* Wv = (const u16*)d_in[4];
  const u16* Wo = (const u16*)d_in[5];
  const u16* wr = (const u16*)d_in[8];
  const u16* wi = (const u16*)d_in[9];
  const u16* gw = (const u16*)d_in[10];
  u16* out = (u16*)d_out;
  char* ws = (char*)d_ws;
  mega_kernel<<<256, 512, 0, stream>>>(x, mem0, Wq, Wk, Wv, Wo, wr, wi, gw, out, ws);
}
```

```cpp
#define DLYS 7, 8, 9, 9, 7, 7, 8, 6, 310, 122, 13, 0, 0, 1
#include <hip/hip_runtime.h>
#include <hip/hip_bf16.h>
#include <stdint.h>
#include <type_traits>

typedef short bf16x8 __attribute__((ext_vector_type(8)));
typedef short s16x4 __attribute__((ext_vector_type(4)));
typedef float f32x4 __attribute__((ext_vector_type(4)));
typedef unsigned u32x4 __attribute__((ext_vector_type(4)));
typedef unsigned u32x2 __attribute__((ext_vector_type(2)));
typedef unsigned short u16;

#define DEVI __device__ __forceinline__

DEVI float bf2f(u16 v) { return __uint_as_float(((unsigned)v) << 16); }
DEVI u16 f2bf(float f) { return __builtin_bit_cast(u16, (__bf16)f); }
DEVI float rbf(float f) { return bf2f(f2bf(f)); }
DEVI unsigned pack2(u16 a, u16 b) { return (unsigned)a | ((unsigned)b << 16); }
DEVI f32x4 mfma16(bf16x8 a, bf16x8 b, f32x4 c) { return __builtin_amdgcn_mfma_f32_16x16x32_bf16(a, b, c, 0, 0, 0); }

constexpr int S_ = 2048, D_ = 1024, H_ = 32, DH_ = 128, MEM_ = 8, SEG_ = 32, NSEG_ = 64, LK_ = 40;
constexpr int HD_ = H_ * DH_;
#define FSV 56
constexpr int NL_ = NSEG_ - 1;
constexpr int FS_ = FSV;
constexpr int XS_ = 8;
constexpr int FB_ = 513;
constexpr float SCALE_ = 0.08838834764831845f;

constexpr unsigned POISON = 0xffffffffu;
constexpr unsigned SET_ = 4u << 20, BLK_ = 32768u, NB3 = 0, NB2 = 32, NB1 = 48, NBLK = 52;
constexpr unsigned OFF_CKV = 24u << 20;
constexpr unsigned OFF_CO = OFF_CKV + 16384u;
constexpr unsigned OFF_CPX = OFF_CO + 65536u;
constexpr unsigned OFF_CF = OFF_CPX + 32768u;
constexpr unsigned OFF_REC = OFF_CF + 1024u;
constexpr unsigned OFF_EP = OFF_REC + 5120u;
constexpr unsigned COMM_BYTES = OFF_EP + 1024u;
struct NearMap {
  unsigned k;
  __device__ static unsigned pg(unsigned c) { return ((c & 1u) << 14) | ((c & 6u) << 11); }
#define BLAY 0
  __device__ static constexpr unsigned blk(unsigned n) { return BLAY ? ((n & 3u) << 15) | ((n & 28u) << 17) | ((n >> 5) << 17) : n * BLK_; }
  __device__ unsigned operator()(unsigned set, unsigned g, unsigned n) const { return set * SET_ + blk(n) + pg(k ^ g ^ (blk(n) >> 19)); }
};
DEVI unsigned far8a(unsigned u, unsigned hb) { return u * 16384u + (((u >> 7) ^ hb) & 1u) * 8192u; }
DEVI unsigned far8(unsigned u, unsigned hb) { return u * 16384u + (((u >> 7) ^ (u < 128u ? 0u : hb)) & 1u) * 8192u; }
#define NPROBE 8
DEVI unsigned long long clk_now() {
  unsigned long long t;
  asm volatile("s_waitcnt vmcnt(0) lgkmcnt(0)\n s_memtime %0\n s_waitcnt lgkmcnt(0)" : "=s"(t) :: "memory");
  return t;
}
constexpr unsigned OFF_G = COMM_BYTES;
constexpr unsigned OFF_Q = 28u << 20, OFF_K = 60u << 20, OFF_VX = 92u << 20, OFF_OT = 124u << 20;
constexpr unsigned OFF_T = 128u << 20;
constexpr unsigned OFF_PX = 144u << 20;
constexpr unsigned OFF_BF = 184u << 20;
constexpr unsigned OFF_XF = 192u << 20;
constexpr unsigned WS_USED = 200u << 20;
static_assert(FS_ >= NSEG_ - 8 && 32 + NL_ - FS_ <= 40 && XS_ == 8, "tails");
constexpr unsigned HALF_ = BLK_;
DEVI unsigned tile8(int s, int h, unsigned hb) { return (unsigned)(s * 8 + (h & 7)) * (2u * BLK_) + NearMap::pg((s < 2 ? 0u : hb >> 1) ^ (unsigned)(h >> 3) ^ (unsigned)s); }
DEVI unsigned ooff(int s, int h, unsigned hb) { return (s < FS_ ? OFF_Q : OFF_OT - (NSEG_ - 8) * H_ * 16384u) + tile8(s, h, hb); }
DEVI unsigned bfoff(int blk, int tok, unsigned hb) { return OFF_BF + far8a(blk * 4 + (tok >> 9), hb) + (unsigned)(tok & 511) * 16u; }
DEVI unsigned kmoff(int s, int h, unsigned hb) { return NearMap{hb >> 1}(3u, h >> 3, 64 + s * 8 + (h & 7)); }
DEVI unsigned cooff(int h, unsigned hb) { return OFF_CO + far8a(h >> 3, hb ^ 1u) + (unsigned)(h & 7) * 1024u; }

constexpr int SMEM_BYTES = 163840;
__shared__ __attribute__((aligned(16))) char lds_[SMEM_BYTES];
#define LDS3 ((__attribute__((address_space(3))) char*)lds_)

typedef __amdgpu_buffer_rsrc_t rsrc_t;
DEVI u32x4 ld_sc1(rsrc_t r, unsigned off) { return __builtin_amdgcn_raw_buffer_load_b128(r, off, 0, 16); }
DEVI u32x2 ld_sc1_64(rsrc_t r, unsigned off) { return __builtin_amdgcn_raw_buffer_load_b64(r, off, 0, 16); }
DEVI unsigned ld_sc1_32(rsrc_t r, unsigned off) { return __builtin_amdgcn_raw_buffer_load_b32(r, off, 0, 16); }
DEVI void st_sc1(rsrc_t r, unsigned off, u32x4 v) { __builtin_amdgcn_raw_buffer_store_b128(v, r, off, 0, 16); }
DEVI void st_sc1_64(rsrc_t r, unsigned off, u32x2 v) { __builtin_amdgcn_raw_buffer_store_b64(v, r, off, 0, 16); }
DEVI void st_sc1_32(rsrc_t r, unsigned off, unsigned v) { __builtin_amdgcn_raw_buffer_store_b32(v, r, off, 0, 16); }
DEVI void st_sc1_16(rsrc_t r, unsigned off, u16 v) { __builtin_amdgcn_raw_buffer_store_b16(v, r, off, 0, 16); }
DEVI u32x4 ld_sc1s(rsrc_t r, unsigned off, unsigned so) { return __builtin_amdgcn_raw_buffer_load_b128(r, off, so, 16); }
DEVI void st_sc1s(rsrc_t r, unsigned off, unsigned so, u32x4 v) { __builtin_amdgcn_raw_buffer_store_b128(v, r, off, so, 16); }
DEVI void st_sc1s_64(rsrc_t r, unsigned off, unsigned so, u32x2 v) { __builtin_amdgcn_raw_buffer_store_b64(v, r, off, so, 16); }
DEVI void st_sc1s_32(rsrc_t r, unsigned off, unsigned so, unsigned v) { __builtin_amdgcn_raw_buffer_store_b32(v, r, off, so, 16); }
#define HAUX 16
DEVI void hst(rsrc_t r, unsigned off, u32x4 v) { __builtin_amdgcn_raw_buffer_store_b128(v, r, off, 0, HAUX); }
DEVI void hst64(rsrc_t r, unsigned off, u32x2 v) { __builtin_amdgcn_raw_buffer_store_b64(v, r, off, 0, HAUX); }
DEVI void drain_vm() { asm volatile("s_waitcnt vmcnt(0)" ::: "memory"); }
DEVI void cnt_add(char* ws, unsigned off, unsigned v = 1u) { __hip_atomic_fetch_add((unsigned*)(ws + off), v, __ATOMIC_RELAXED, __HIP_MEMORY_SCOPE_AGENT); }

constexpr int HSPIN_CAP = 1 << 18;
DEVI void wg_wait_s(rsrc_t r, unsigned off, unsigned n) {
  if (threadIdx.x == 0) {
#pragma nounroll
    for (int it = 0; it < HSPIN_CAP; ++it) {
      if (ld_sc1_32(r, off) >= n) break;
      __builtin_amdgcn_s_sleep(8);
    }
  }
  asm volatile("" ::: "memory");
  __syncthreads();
}
DEVI void wg_signal(char* ws, unsigned off) {
  drain_vm();
  __syncthreads();
  if (threadIdx.x == 0) cnt_add(ws, off);
}

DEVI u32x2 launch_no(const char* ws) {
  u32x2 e;
  asm volatile("s_load_dwordx2 %0, %1, 0x0\n s_waitcnt lgkmcnt(0)" : "=s"(e) : "s"(ws + OFF_EP) : "memory");
  return u32x2{e[0] + 1u, e[1] + 0x9e3779b1u};
}
DEVI void rec_put(rsrc_t rw, int i, u32x2 no) {
  drain_vm();
  __syncthreads();
  if (threadIdx.x == 0) st_sc1_64(rw, OFF_REC + (unsigned)i * 16u, no);
}
DEVI u32x4 rec_wait(rsrc_t rw, u32x2 no, unsigned mask, unsigned lo = 0u, unsigned own = 0u) {
  u32x4 r;
#pragma nounroll
  for (int it = 0; it < HSPIN_CAP; ++it) {
    asm volatile("" ::: "memory");
    r = ld_sc1(rw, OFF_REC + (own && __lane_id() <= own ? __lane_id() : (__lane_id() & mask) + lo) * 16u);
    if (__all(r[0] == no[0] && r[1] == no[1])) break;
    __builtin_amdgcn_s_sleep(8);
  }
  asm volatile("" ::: "memory");
  return r;
}
DEVI unsigned near_kind(rsrc_t rw, u32x2 no) {
  const unsigned lat = rec_wait(rw, no, 63u)[2], ls2 = (lat & 0xffffu) + (lat >> 16), l = __lane_id() & 7u;
  unsigned cost = 0u;
#pragma unroll
  for (int i = 0; i < 4; ++i) cost += __shfl(ls2, 32 + 8 * i + (l ^ i));
  cost = cost * 8u + l;
#pragma unroll
  for (int i = 1; i < 8; i *= 2) cost = min(cost, (unsigned)__shfl_xor(cost, i));
  return (__builtin_amdgcn_readfirstlane(cost) & 7u) ^ 4u;
}
DEVI unsigned far_bit(unsigned kind) { return ((kind >> 2) ^ 1u) | (kind << 1); }
DEVI void probe(char* ws, rsrc_t rw, int i) {
  const NearMap nm{0u};
  if (threadIdx.x == 0) {
    char* p0 = ws + nm(4u, i & 7, 40u + (i >> 3));
    unsigned mn[2] = {0xffffu, 0xffffu};
#pragma unroll 1
    for (int k = 0; k < NPROBE; ++k) {
#pragma unroll
      for (int hf = 0; hf < 2; ++hf) {
        const unsigned long long t0 = clk_now();
        const unsigned v = __hip_atomic_fetch_add((unsigned*)(p0 + hf * 2048), 1u, __ATOMIC_RELAXED, __HIP_MEMORY_SCOPE_AGENT);
        asm volatile("" :: "v"(v));
        const unsigned long long t1 = clk_now();
        mn[hf] = min(mn[hf], (unsigned)(t1 - t0));
      }
    }
    st_sc1_32(rw, OFF_REC + (unsigned)(32 + i) * 16u + 8u, mn[0] | (mn[1] << 16));
  }
}

template <int N>
DEVI void wait_vm() { asm volatile("s_waitcnt vmcnt(%0)" :: "n"(N) : "memory"); }
#define BPR 2
#define ISG 3
#define ESG 4
#define NTW 0
#define RN 1
#define NTO 0
#define OAUX 0
#define NTX 2
#define SCHED() __builtin_amdgcn_sched_barrier(0)
DEVI u32x4 rsrc4(const void* p, unsigned bytes) { return u32x4{(unsigned)(size_t)p, (unsigned)((size_t)p >> 32) & 0xffffu, bytes, 0x00020000u}; }
DEVI void bar() { asm volatile("s_waitcnt lgkmcnt(0)\n s_barrier" ::: "memory"); }
DEVI u32x2 tr_quad(const char* p) {
  return __builtin_bit_cast(u32x2, __builtin_amdgcn_ds_read_tr16_b64_v4i16((__attribute__((address_space(3))) s16x4*)(p)));
}
DEVI unsigned cvt2(float a, float b) {
  typedef float f32x2_ __attribute__((ext_vector_type(2)));
  typedef __bf16 bf16x2_ __attribute__((ext_vector_type(2)));
  return __builtin_bit_cast(unsigned, __builtin_convertvector(f32x2_{a, b}, bf16x2_));
}
DEVI unsigned cvt2x(float a, float b) { return min(cvt2(a, b), POISON - 1u); }
DEVI f32x4 mfu(u32x4 a, u32x4 b, f32x4 c) { return mfma16(__builtin_bit_cast(bf16x8, a), __builtin_bit_cast(bf16x8, b), c); }
#define PTR(i)
#define PTB(i)
template <bool swap>
DEVI unsigned proj_reg(char* lds, char* ws, rsrc_t rw, const u16* __restrict__ W, int h, int mat, int hw) {
  constexpr int nseg = NSEG_;
  const int rot = mat ? 0 : XS_;
  auto sgm = [&](int r) { return (r & 32) | ((r + ((r & 32) ? 0 : rot)) & 31); };
  int t = threadIdx.x;
  asm volatile("" : "+v"(t));
  const int l = t & 63, w = __builtin_amdgcn_readfirstlane(t >> 6), g = l >> 4, lo = l & 15, wn = w & 3, wk = w >> 2;
  const u32x4 rs = rsrc4(ws, WS_USED);
  const unsigned vo = (unsigned)l * 16u;
  auto wswz = [](int row) { return ((row & 3) | (((row >> 3) & 1) << 2)) << 1; };
  u32x4 b[16][2];
  const unsigned go = (unsigned)((512 * wk + 16 * wn + g) * HD_ + h * DH_) * 2u;
  const unsigned gv[2] = {go + (unsigned)((lo ^ wswz(g)) * 16), go + (unsigned)((lo ^ wswz(8 + g)) * 16)};
  auto ask = [&](int p) {
#pragma unroll
    for (int i = 0; i < 4; ++i)
      __builtin_amdgcn_global_load_lds((const unsigned*)((const char*)W + gv[i >> 1] + (size_t)(64 * p + 4 * i) * HD_ * 2), (unsigned*)(lds + (p + 4) % 5 * 32768 + w * 4096 + i * 1024 + l * 16), 16, 0, NTW);
    SCHED();
  };
#pragma unroll
  for (int p = 0; p < 5; ++p) ask(p);
  const u32x2 no = launch_no(ws);
  const char* bp = lds + (64 * wk + 8 * g + (lo >> 2)) * 256 + (l & 1) * 8;
  const int fq2 = wswz(8 * g + (lo >> 2));
  auto take = [&](int p) {
#pragma unroll
    for (int ks = 0; ks < 2; ++ks)
#pragma unroll
      for (int ct = 0; ct < 2; ++ct) {
        const char* p0 = bp + (p + 4) % 5 * 32768 + ks * 8192 + (((4 * wn + 2 * ct + ((l & 3) >> 1)) ^ fq2) * 16);
        const u32x2 x0 = tr_quad(p0), x1 = tr_quad(p0 + 1024);
        b[2 * p + ks][ct] = u32x4{x0[0], x0[1], x1[0], x1[1]};
      }
    SCHED();
  };
  unsigned hb = 0u;
  auto dma = [&](int i, int pc, auto&& mid) {
    auto* dst = (__attribute__((address_space(3))) void*)(LDS3 + ((i + 2) & 3) * 32768 + pc * 4096);
    const unsigned so = OFF_XF + far8(sgm(i >> 1) * 8 + (i & 1) * 4 + (pc >> 1), hb) + (unsigned)(pc & 1) * 4096u;
    const unsigned vd = i < 2 * nseg ? vo : 0x80000000u;
    __builtin_amdgcn_raw_ptr_buffer_load_lds(rw, dst, 16, vd, so, 0, 16);
    mid(0);
    __builtin_amdgcn_raw_ptr_buffer_load_lds(rw, dst, 16, vd, so, 1024, 16);
    mid(1);
    __builtin_amdgcn_raw_ptr_buffer_load_lds(rw, dst, 16, vd, so, 2048, 16);
    mid(2);
    __builtin_amdgcn_raw_ptr_buffer_load_lds(rw, dst, 16, vd, so, 3072, 16);
    mid(3);
  };
  auto none = [](int) {};
  u32x4 f[1];
#pragma unroll
  for (int n = 0; n < 8; ++n) {
    asm volatile("s_waitcnt vmcnt(%0) lgkmcnt(0)\n s_barrier" :: "n"(4 * (n == 0 ? 4 : (n < 5 ? 3 : 7 - n)) + (n > RN && n < RN + 4 ? 1 : 0) + (n > RN + 4 ? 8 : 0)) : "memory");
    if (n == RN) asm volatile("buffer_load_dwordx4 %0, %1, %2, %3 offen sc1" : "=&v"(f[0]) : "v"(vo & 511u), "s"(rs), "s"(OFF_REC) : "memory");
    if (n >= 1 && n < 4) ask(n + 4);
    if (n == RN + 4) {
      asm volatile("" : "+v"(f[0]));
      if (!__all(f[0][0] == no[0] && f[0][1] == no[1])) rec_wait(rw, no, 31u);
      dma(0, w, none); dma(1, w, none);
      SCHED();
    }
    if (n == 7) PTR(17);
    take(n);
  }
  bar();
  const u32x4 zero4 = u32x4{0u, 0u, 0u, 0u};
  if (wk) {
    dma(2, wn, none);
    st_sc1(rw, 0x80000000u, zero4);
    if (!swap) st_sc1(rw, 0x80000010u, zero4);
    wait_vm<swap ? 5 : 6>();
  } else wait_vm<0>();
  __builtin_amdgcn_s_waitcnt(0xc07f);
  __builtin_amdgcn_s_barrier();
  PTR(19);
  const unsigned lb = (unsigned)(size_t)LDS3 + (unsigned)(l * 16);
  const unsigned ab = lb + (unsigned)(wk * 16384), sc = lb + 131072u + (unsigned)(wn * 2048);
  u32x4 a[8];
  auto rd = [&](int j, unsigned ad) { asm volatile("ds_read_b128 %0, %1 offset:%2" : "=v"(a[j & 7]) : "v"(ad), "n"((j & 15) * 1024) : "memory"); };
  if (wk == 0) {
#pragma unroll
    for (int j = 0; j < 8; ++j) rd(j, ab + 65536u);
    asm volatile("s_waitcnt lgkmcnt(0)" : "+v"(a[0]), "+v"(a[1]), "+v"(a[2]), "+v"(a[3]), "+v"(a[4]), "+v"(a[5]), "+v"(a[6]), "+v"(a[7]));
#pragma nounroll
    for (int k = 0; k < 2 * nseg; ++k) {
      const unsigned a0 = ab + (unsigned)(((k + 2) & 3) * 32768), a1 = ab + (unsigned)(((k + 3) & 3) * 32768);
      asm volatile("" :: "v"(a0), "v"(a1));
      wait_vm<8>();
      __builtin_amdgcn_s_setprio(1);
      __builtin_amdgcn_s_barrier();
      f32x4 c[2];
      SCHED();
#pragma unroll
      for (int j = 0; j < 16; ++j) {
#pragma unroll
        for (int ct = 0; ct < 2; ++ct) {
          const f32x4 z = j ? c[ct] : f32x4{0.f, 0.f, 0.f, 0.f};
          c[ct] = swap ? mfu(b[j][ct], a[j & 7], z) : mfu(a[j & 7], b[j][ct], z);
        }
        SCHED();
        rd(j + 8, j < 8 ? a0 : a1);
        if (j & 1) asm volatile("s_waitcnt lgkmcnt(5)" : "+v"(a[(j + 2) & 7]), "+v"(a[(j + 3) & 7]));
        SCHED();
      }
      __builtin_amdgcn_s_setprio(2);
      dma(k + 2, 4 + wn, none);
      SCHED();
      asm volatile("s_nop 7\n ds_write_b128 %0, %1\n ds_write_b128 %0, %2 offset:1024\n s_waitcnt lgkmcnt(0)" :: "v"(sc + (unsigned)((k & 1) * 8192)), "v"(c[0]), "v"(c[1]) : "memory");
      if (__builtin_expect(k == 8, 0)) hb = far_bit(near_kind(rw, no));
    }
    asm volatile("" : "+v"(a[0]), "+v"(a[1]), "+v"(a[2]), "+v"(a[3]), "+v"(a[4]), "+v"(a[5]), "+v"(a[6]), "+v"(a[7]));
    wait_vm<8>();
    __builtin_amdgcn_s_barrier();
  } else {
    const unsigned oo = (mat == 0 ? OFF_Q : (mat == 1 ? OFF_K : OFF_VX)) + (swap ? wn * 1024u : (wn >> 1) * HALF_ + (wn & 1) * 2048u);
    const unsigned v0 = l == 0 ? 0u : 0x80000000u;
    auto signal = [&](int r, unsigned va) {
      __builtin_amdgcn_raw_ptr_buffer_atomic_add_i32(swap ? (mat ? 1 : 65536) : 256, rw, va, OFF_CKV + (unsigned)(h * 64 + sgm(r)) * 4u, 0);
    };
    f32x4 c[2];
    u32x2 v[2];
    c[0] = c[1] = f32x4{0.f, 0.f, 0.f, 0.f};
    v[0] = v[1] = u32x2{0u, 0u};
    auto put = [&](int r, int tt, unsigned vs) {
      const unsigned so = oo + tile8(sgm(r), h, hb);
      const u32x2 p0 = u32x2{cvt2x(c[0][0], c[0][1]), cvt2x(c[0][2], c[0][3])}, p1 = u32x2{cvt2x(c[1][0], c[1][1]), cvt2x(c[1][2], c[1][3])};
      if (swap) st_sc1s(rw, vs, so + (unsigned)tt * HALF_, u32x4{p0[0], p0[1], p1[0], p1[1]});
      else if (tt == 0) { v[0] = p0; v[1] = p1; }
      else {
        st_sc1s(rw, vs, so, u32x4{v[0][0], v[0][1], p0[0], p0[1]});
        st_sc1s(rw, vs, so + 1024u, u32x4{v[1][0], v[1][1], p1[0], p1[1]});
      }
    };
    __builtin_amdgcn_s_setprio(3);
    __builtin_amdgcn_s_barrier();
    rec_wait(rw, no, 3u, 64u + 4u * h);
    dma(3, wn, none);
    if (swap) st_sc1(rw, 0x80000000u, zero4);
#pragma nounroll
    for (int r = 0; r < nseg; ++r) {
#pragma unroll
      for (int tt = 0; tt < 2; ++tt) {
        const int i = 2 * r + tt;
        const unsigned a0 = ab + (unsigned)(((i + 2) & 3) * 32768);
        asm volatile("" :: "v"(a0));
        if (tt == 0) wait_vm<swap ? 5 : 4>(); else wait_vm<swap ? 6 : 7>();
        __builtin_amdgcn_s_barrier();
        if (tt == 1 && __builtin_expect(r < 2 && mat, 0)) __builtin_amdgcn_raw_ptr_buffer_atomic_add_i32(1 << 24, rw, v0, OFF_CKV + (unsigned)(h * 64 + r) * 4u, 0);
        if (__builtin_expect(i == (rot ? 1 : 4), 0)) hb = far_bit(near_kind(rw, no));
        if (tt == 0 && __builtin_expect(r == 14 - rot, 0)) {
#pragma nounroll
          for (int it = 0; it < HSPIN_CAP; ++it) {
            if (__builtin_amdgcn_readfirstlane(ld_sc1_32(rw, OFF_CF + 384u)) >= 32u) break;
            __builtin_amdgcn_s_sleep(16);
          }
          asm volatile("" ::: "memory");
        }
        if (tt == 1 && __builtin_expect(r > ISG && r <= ESG, 0)) { drain_vm(); signal(r - 1, v0); }
        f32x4 n[2];
        asm volatile("ds_read_b128 %0, %2\n ds_read_b128 %1, %2 offset:1024" : "=&v"(n[0]), "=&v"(n[1]) : "v"(sc + (unsigned)(tt * 8192)) : "memory");
        dma(i + 4, wn, [&](int q) { rd(q, a0); });
        if (tt == 0) put(r - 1, 1, r > 0 ? vo : 0x80000000u); else put(r, 0, vo);
        rd(4, a0);
        if (tt == 0) signal(r - 2, r >= ESG + 2 ? v0 : 0x80000000u);
        rd(5, a0); rd(6, a0); rd(7, a0);
        if (tt == 0 || __builtin_expect(r >= BPR, 1)) __builtin_amdgcn_s_setprio(0);
        SCHED();
        asm volatile("s_waitcnt lgkmcnt(5)" : "+v"(n[0]), "+v"(n[1]), "+v"(a[0]), "+v"(a[1]), "+v"(a[2]));
        c[0] = n[0]; c[1] = n[1];
#pragma unroll
        for (int j = 0; j < 16; ++j) {
#pragma unroll
          for (int ct = 0; ct < 2; ++ct) c[ct] = swap ? mfu(b[j][ct], a[j & 7], c[ct]) : mfu(a[j & 7], b[j][ct], c[ct]);
          SCHED();
          if (j < 8) rd(j + 8, a0);
          if ((j & 1) && j < 14) asm volatile("s_waitcnt lgkmcnt(%2)" : "+v"(a[(j + 2) & 7]), "+v"(a[(j + 3) & 7]) : "n"(j < 8 ? 5 : (j < 13 ? 12 - j : 0)));
          SCHED();
        }
        __builtin_amdgcn_s_setprio(3);
      }
      if (__builtin_expect(r < ISG, 0)) {
        put(r, 1, vo);
        if (r < 2) PTB(r ? 20 : 8);
        if (r >= 2) {
          drain_vm();
          if (r == 2) { signal(0, v0); signal(1, v0); }
          signal(r, v0);
        }
      }
    }
    put(nseg - 1, 1, vo);
    drain_vm();
    signal(nseg - 2, v0); signal(nseg - 1, v0);
  }
  __builtin_amdgcn_s_setprio(0);
  drain_vm();
  __syncthreads();
  return hb;
}

constexpr int SPIN_CAP = 1 << 19;
#define LOCK 0
#define SL 0
#define GAM 1
enum { DLY1, DLY2, DLYV, DLY3, XDL, QDL, DLYL, DLYW, XT1, XTL, DLYD, KG3, KG1, DLYG, KG2, KGV, DLYR = QDL, DLYX = KG3, DLY4 = DLYG };
#define D4 1
#define NMX 1
#define S3 0
#define NOMIN 0
#define TID 3
#define EARLY 0
#define L2 1
#define L3 1
#define PRE3 LOCK
#define RSE 0

#ifndef NV1
#define NV1 4
#endif
#define VOR 1
#define LVS 1
#define CIN 1
#define GIL 1
#define G1N ""
#define G1F "s_nop 0\n"
#define G3N ""
#define XIL 1
#define VAS 1
#define TCH 0
#define TC1 0
#define TCW 1
#define G3I 1
#define WB1 1
#define WB2 3
#define ALG 1
#define PBA 6
#define PBN 7
#define ESL 2
#define LSL 0
#define KSL 8
constexpr int PQK_OFF = 0;
constexpr int W3_OFF = 98304;
constexpr int KV_OFF = 98304;
constexpr int PS_OFF = 131072;
constexpr int PZ_OFF = PS_OFF + 4096;
constexpr int UM_OFF = PZ_OFF + 1024;
constexpr int KC_OFF = UM_OFF + 3072;
constexpr int XV_OFF = KC_OFF + 1024;
constexpr int OB_OFF = XV_OFF + 2048;
constexpr int XK_OFF = OB_OFF + 2048;
constexpr float CS = SCALE_ * 1.4426950408889634f;
typedef __bf16 bfv8 __attribute__((ext_vector_type(8)));
typedef __bf16 bfv16 __attribute__((ext_vector_type(16)));
typedef float f32x2 __attribute__((ext_vector_type(2)));
typedef unsigned u32x8 __attribute__((ext_vector_type(8)));
DEVI f32x4 smf(u32x4 a, u32x8 b, f32x4 c, int idx) {
  return __builtin_amdgcn_smfmac_f32_16x16x64_bf16(__builtin_bit_cast(bfv8, a), __builtin_bit_cast(bfv16, b), c, idx, 0, 0);
}
DEVI float fold8(float a) { return a + __uint_as_float(__builtin_amdgcn_update_dpp(0u, __float_as_uint(a), 0x128, 0xf, 0xf, true)); }
#define LO2(c) f32x2{c[0], c[1]}
#define HI2(c) f32x2{c[2], c[3]}
DEVI f32x4 zero4() { f32x4 z = f32x4{0.f, 0.f, 0.f, 0.f}; asm volatile("" : "+v"(z)); return z; }
DEVI u32x4 cvt8(f32x4 a, f32x4 b) { return u32x4{cvt2(a[0], a[1]), cvt2(a[2], a[3]), cvt2(b[0], b[1]), cvt2(b[2], b[3])}; }

DEVI float vmax(unsigned a, unsigned b) { float r; asm("v_max_f32 %0, %1, %2" : "=v"(r) : "v"(a), "v"(b)); return r; }
DEVI float rows_max(float x) {
  const unsigned u = __float_as_uint(x);
  auto a = __builtin_amdgcn_permlane16_swap(u, u, false, false);
  const unsigned u2 = __float_as_uint(vmax(a[0], a[1]));
  auto b = __builtin_amdgcn_permlane32_swap(u2, u2, false, false);
  return vmax(b[0], b[1]);
}
DEVI float rows_sum(float x) {
  const unsigned u = __float_as_uint(x);
  auto a = __builtin_amdgcn_permlane16_swap(u, u, false, false);
  x = __uint_as_float(a[0]) + __uint_as_float(a[1]);
  const unsigned u2 = __float_as_uint(x);
  auto b = __builtin_amdgcn_permlane32_swap(u2, u2, false, false);
  return __uint_as_float(b[0]) + __uint_as_float(b[1]);
}

DEVI void req4(u32x4 r, unsigned vo, unsigned so, u32x4* v) {
  asm volatile("buffer_load_dwordx4 %0, %4, %5, %6 offen sc1\n buffer_load_dwordx4 %1, %4, %5, %6 offen offset:1024 sc1\n"
               "buffer_load_dwordx4 %2, %4, %5, %6 offen offset:2048 sc1\n buffer_load_dwordx4 %3, %4, %5, %6 offen offset:3072 sc1"
               : "=&v"(v[0]), "=&v"(v[1]), "=&v"(v[2]), "=&v"(v[3]) : "v"(vo), "s"(r), "s"(so) : "memory");
}
#define ILV 4
DEVI void req4i(u32x4 r, unsigned vo, unsigned so, u32x4* v) {
  asm volatile("buffer_load_dwordx4 %0, %5, %6, %4 offen sc1\n s_xor_b32 %4, %4, 0x400\n s_nop %7\n buffer_load_dwordx4 %1, %5, %6, %4 offen sc1\n s_xor_b32 %4, %4, 0xc00\n s_nop %7\n"
               "buffer_load_dwordx4 %2, %5, %6, %4 offen sc1\n s_xor_b32 %4, %4, 0x400\n s_nop %7\n buffer_load_dwordx4 %3, %5, %6, %4 offen sc1"
               : "=&v"(v[0]), "=&v"(v[1]), "=&v"(v[2]), "=&v"(v[3]), "+s"(so) : "v"(vo), "s"(r), "n"(ILV ? ILV - 1 : 0) : "memory", "scc");
}
DEVI void req4it(u32x4 r, unsigned so, u32x4* v) {
  asm volatile("buffer_load_dwordx4 %0, off, %5, %4 sc1\n s_xor_b32 %4, %4, 0x400\n s_nop %6\n buffer_load_dwordx4 %1, off, %5, %4 sc1\n s_xor_b32 %4, %4, 0xc00\n s_nop %6\n"
               "buffer_load_dwordx4 %2, off, %5, %4 sc1\n s_xor_b32 %4, %4, 0x400\n s_nop %6\n buffer_load_dwordx4 %3, off, %5, %4 sc1"
               : "=&v"(v[0]), "=&v"(v[1]), "=&v"(v[2]), "=&v"(v[3]), "+s"(so) : "s"(r), "n"(ILV ? ILV - 1 : 0) : "memory", "scc");
}
DEVI u32x4 rs_tid(u32x4 r) { return u32x4{r[0], r[1] | (16u << 16), r[2] >> 4, 1u << 23}; }
DEVI void req4t(u32x4 r, unsigned so, u32x4* v) {
  asm volatile("buffer_load_dwordx4 %0, off, %4, %5 sc1\n buffer_load_dwordx4 %1, off, %4, %5 offset:1024 sc1\n"
               "buffer_load_dwordx4 %2, off, %4, %5 offset:2048 sc1\n buffer_load_dwordx4 %3, off, %4, %5 offset:3072 sc1"
               : "=&v"(v[0]), "=&v"(v[1]), "=&v"(v[2]), "=&v"(v[3]) : "s"(r), "s"(so) : "memory");
}
DEVI void req2t(u32x4 r, unsigned so, u32x4* v) {
  asm volatile("buffer_load_dwordx4 %0, off, %2, %3 sc1\n buffer_load_dwordx4 %1, off, %2, %3 offset:1024 sc1" : "=&v"(v[0]), "=&v"(v[1]) : "s"(r), "s"(so) : "memory");
}
DEVI void req2(u32x4 r, unsigned vo, unsigned so, u32x4* v) {
  asm volatile("buffer_load_dwordx4 %0, %2, %3, %4 offen sc1\n buffer_load_dwordx4 %1, %2, %3, %4 offen offset:1024 sc1"
               : "=&v"(v[0]), "=&v"(v[1]) : "v"(vo), "s"(r), "s"(so) : "memory");
}
template <int N>
DEVI void got(u32x4& v) { asm volatile("s_waitcnt vmcnt(%1)" : "+v"(v) : "n"(N)); }
#define A8 ".p2align 3\n"
#define A4 ".p2align 3\n s_nop 0\n"
#define STR_(x) #x
#define STR(x) STR_(x)
#define MF "v_mfma_f32_16x16x32_bf16 "
#define CV "v_cvt_pk_bf16_f32 "
#define SM "v_smfmac_f32_16x16x64_bf16 "
template <int N>
DEVI void got_s4(u32x4& a, f32x4* c, u32x8 b0, u32x8 b1, u32x8 b2, u32x8 b3, int idx) {
  asm volatile(".if %10 == 3 + " STR(LOCK) "\n" A4 ".endif\n s_waitcnt vmcnt(%10)\n" SM "%1, %0, %5, %9\n" SM "%2, %0, %6, %9\n" SM "%3, %0, %7, %9\n" SM "%4, %0, %8, %9"
               : "+v"(a), "+v"(c[0]), "+v"(c[1]), "+v"(c[2]), "+v"(c[3]) : "v"(b0), "v"(b1), "v"(b2), "v"(b3), "v"(idx), "n"(N));
}
template <int N>
DEVI void got_s2(u32x4& a, f32x4* c, u32x8 b0, u32x8 b1, int idx) {
  asm volatile(".if %6 == 1\n" A4 ".endif\n s_waitcnt vmcnt(%6)\n" SM "%1, %0, %3, %5\n" SM "%2, %0, %4, %5" : "+v"(a), "+v"(c[0]), "+v"(c[1]) : "v"(b0), "v"(b1), "v"(idx), "n"(N));
}
template <int N>
DEVI void got_x(u32x4& x, f32x4& sa, f32x4& sb, f32x4& sm, u32x4 ka, u32x4 kb) {
  asm volatile("s_waitcnt vmcnt(%6)\n" MF "%1, %4, %0, %1\n" MF "%2, %5, %0, %2\n" MF "%3, %0, %0, %3" : "+v"(x), "+v"(sa), "+v"(sb), "+v"(sm) : "v"(ka), "v"(kb), "n"(N));
}
template <int N>
DEVI void got_m(u32x4& b, f32x4& d, u32x4 a) { asm volatile("s_waitcnt vmcnt(%3)\n" MF "%1, %2, %0, %1" : "+v"(b), "+v"(d) : "v"(a), "n"(N)); }
DEVI unsigned umax3(unsigned a, unsigned b, unsigned c) { return max(max(a, b), c); }
DEVI unsigned umax5(u32x4 v, unsigned m) { return umax3(v[2], v[3], umax3(v[0], v[1], m)); }
DEVI bool bad(unsigned m) { return __builtin_expect(__builtin_amdgcn_ballot_w64(m == POISON) != 0ull, 0); }
DEVI bool over(unsigned m, unsigned thr) { return __builtin_expect(__builtin_amdgcn_ballot_w64(m > thr) != 0ull, 0); }
DEVI bool bad4(const u32x4* v) { return bad(umax5(v[3], umax5(v[2], umax5(v[1], umax5(v[0], 0u))))); }
DEVI unsigned tag5(u32x4 v, unsigned t, unsigned m) {
  return __builtin_amdgcn_bitop3_b32(v[0], v[1], t, 0x7e) | __builtin_amdgcn_bitop3_b32(v[2], v[3], t, 0x7e) | m;
}
DEVI unsigned rt_now() {
  unsigned long long t;
  asm volatile("s_memrealtime %0\n s_waitcnt lgkmcnt(0)" : "=s"(t) :: "memory");
  return (unsigned)t;
}
#define DLYARG
#define DLYA
#define DLYI(mask_) unsigned dly[16] = {DLYS}; _Pragma("unroll") for (int i = 0; i < 16; ++i) if (((mask_) >> i) & 1) asm volatile("" : "+s"(dly[i]));
#define PAL 6
DEVI void pause(unsigned n) { asm volatile(".p2align %1\n 1: s_sub_u32 %0, %0, 1\n s_cbranch_scc0 1b" : "+s"(n) : "n"(PAL) : "scc", "memory"); }
DEVI unsigned mt_now() {
  unsigned long long t;
  asm volatile("s_memtime %0\n s_waitcnt lgkmcnt(0)" : "=s"(t) :: "memory");
  return (unsigned)t;
}
DEVI unsigned gt_now() {
  unsigned long long b, c, d;
  asm volatile("s_memrealtime %0\n s_nop 6\n s_memrealtime %1\n s_nop 6\n s_memrealtime %2\n s_waitcnt lgkmcnt(0)" : "=&s"(b), "=&s"(c), "=&s"(d) :: "memory");
  return ((unsigned)b + (unsigned)c + (unsigned)d) * 8u;
}
DEVI void clk_pair(unsigned& t, unsigned& gt) {
  unsigned long long a;
  asm volatile("s_memtime %0" : "=s"(a) :: "memory");
  gt = gt_now();
  asm volatile("" : "+s"(a));
  t = (unsigned)a;
}
DEVI unsigned local_of(unsigned tgt, int rq, unsigned t, unsigned g) {
  int d = (int)(tgt - g);
  asm("s_max_i32 %0, %0, 0xffffc000\n s_min_i32 %0, %0, 0x4000" : "+s"(d) :: "scc");
  return t + (unsigned)(d * rq >> 16);
}
DEVI int until(unsigned tgt, int dmin, unsigned now) {
  const int left = (int)(tgt - now);
  int d;
  asm volatile("s_max_i32 %0, %1, %3\n s_min_i32 %0, %0, 4000\n"
               ".p2align %2\n 1: s_sub_u32 %0, %0, 28\n s_cbranch_scc0 1b\n s_cmp_lt_i32 %0, -20\n s_cbranch_scc1 2f\n s_cmp_lt_i32 %0, -12\n s_cbranch_scc1 2f\n"
               "s_cmp_lt_i32 %0, -4\n s_cbranch_scc1 2f\n s_nop 0\n s_branch 2f\n 2:" : "=&s"(d) : "s"(left), "n"(PAL), "s"(dmin) : "scc", "memory");
  return left;
}
DEVI int until(unsigned tgt, int dmin) { return until(tgt, dmin, mt_now()); }
constexpr int TG_OFF = KC_OFF + 16, GP_OFF = XK_OFF + 3072;
#define KTHR 200
#define KFOL 0
#define KPEN 600
#define KLATE 2050

#define TRD
#define TR(i)
#define TRW()
#define TRE(b)
#define TRARGD
#define TRARG
#define SLP(i)
#define TRT()

#define T0(i)
#define TP(i)
#define XM 0
#define TRX(n) do { if (XM == (n)) TR(7); } while (0)
#define TRY(n) do { if (XM == (n)) TR(4); } while (0)
DEVI unsigned kv_want(int s) { return s < XS_ ? 0x404u : 0x40404u; }
DEVI void kv_wait(u32x4 rs, int s, int h, int& budget) {
  const unsigned want = s > 1 ? kv_want(s) : 8u << 24;
  while (true) {
    unsigned c;
    asm volatile("buffer_load_dword %0, off, %1, %2 sc1\n s_waitcnt vmcnt(0)" : "=v"(c) : "s"(rs), "s"(OFF_CKV + (unsigned)(h * 64 + s) * 4u) : "memory");
    if ((__builtin_amdgcn_readfirstlane(c) & want) == want || --budget <= 0) break;
    __builtin_amdgcn_s_sleep(4);
  }
}

DEVI unsigned kv_src(int s, int h, int w, unsigned hb) { return ((w >> 1) ? OFF_VX : OFF_K) + tile8(s, h, hb) + (w & 1) * HALF_; }
DEVI unsigned kv_dst(int s, int w) { return (unsigned)(size_t)LDS3 + KV_OFF + (s & 1) * 16384 + w * 4096; }
DEVI void kv_dma(u32x4 rs, unsigned dvo, unsigned so, unsigned dst) {
  asm volatile("s_mov_b32 m0, %3\n s_nop 0\n buffer_load_dwordx4 %0, %1, %2 offen sc1 lds\n buffer_load_dwordx4 %0, %1, %2 offen offset:1024 sc1 lds\n"
               "buffer_load_dwordx4 %0, %1, %2 offen offset:2048 sc1 lds\n buffer_load_dwordx4 %0, %1, %2 offen offset:3072 sc1 lds"
               :: "v"(dvo), "s"(rs), "s"(so), "s"(dst) : "memory");
}

DEVI unsigned ovoff(unsigned lv) { return (lv & 128u) * 8u + (lv & 0x370u); }
DEVI unsigned osoff(int s, int h, int jj, unsigned hb) { return ooff(s, h, hb) + (jj >> 1) * HALF_ + (jj & 1) * 128u; }
DEVI void ostore(u32x4 rs, const u32x4* oc, unsigned ov, unsigned os) {
  asm volatile("buffer_store_dwordx4 %0, %2, %3, %4 offen sc1\n buffer_store_dwordx4 %1, %2, %3, %4 offen offset:2048 sc1\n s_nop 1" :: "v"(oc[0]), "v"(oc[1]), "v"(ov), "s"(rs), "s"(os) : "memory");
}
DEVI void kv_first(char* lds, u32x4 rs, int sg, int h, int w, unsigned lv, int& budget) {
  u32x4 t[4];
  do {
    req4(rs, lv ^ (unsigned)(w >> 1) * (((lv >> 3) & 32u) + ((lv >> 2) & 128u)), kv_src(sg, h, w, 0u), t);
    asm volatile("s_waitcnt vmcnt(0)" : "+v"(t[0]), "+v"(t[1]), "+v"(t[2]), "+v"(t[3]));
  } while (bad4(t) && --budget > 0);
#pragma unroll
  for (int i = 0; i < 4; ++i) *(u32x4*)(lds + KV_OFF + sg * 16384 + w * 4096 + i * 1024 + lv) = t[i];
}
DEVI float max6(const float* e) {
  float mx, t0, t1;
  asm("v_max3_f32 %0, %3, %4, %5\n v_max3_f32 %1, %6, %7, %8\n v_max_f32 %0, %0, %1\n s_nop 1\n"
      "v_max_f32_dpp %1, %0, %0 row_ror:8 row_mask:0xf bank_mask:0xf\n v_max_f32_dpp %2, %0, %0 row_ror:8 row_mask:0xf bank_mask:0xf\n s_nop 1\n"
      "v_permlane16_swap_b32 %1, %2\n v_max_f32 %0, %1, %2\n v_max_f32 %1, %1, %2\n s_nop 1\n"
      "v_permlane32_swap_b32 %0, %1\n v_max_f32 %0, %0, %1"
      : "=&v"(mx), "=&v"(t0), "=&v"(t1) : "v"(e[0]), "v"(e[1]), "v"(e[2]), "v"(e[3]), "v"(e[4]), "v"(e[5]));
  return mx;
}

DEVI void recur_p1(char* lds, rsrc_t rw, const u32x4 rs, const u16* __restrict__ mem0, const u16* __restrict__ Wq, const u16* __restrict__ Wk,
                   const u16* __restrict__ Wv, u16* __restrict__ out, const char* ws, int wg TRARGD DLYARG) {
  DLYI((LOCK ? 0x7849 : 0x49) | (RSE ? 0x20 : 0) | (SL ? 0x1000 : 0));
  const int l = threadIdx.x & 63, w = __builtin_amdgcn_readfirstlane(threadIdx.x >> 6) & 3, g = l >> 4, lo = l & 15;
  const int h = wg >> 2, jj = wg & 3;
  int budget = SPIN_CAP;
  unsigned te = 0u, ge = 0u, gpo = 0u, t1g = 0u, t3g = 0u;
  int kq = 0, rq = 0;
  if ((LOCK || SL) && w == 1) clk_pair(te, ge);
  unsigned mz = 0u;
  if (ALG) asm volatile("" : "+s"(mz));
  const int vq = 2 * (~jj & 1) + (jj >> 1);
  u32x8 w1[6][4];
  u32x4 a[4];
  {
    char* wl = lds + w * 24576;
    const char* g1 = (const char*)((jj & 2) ? Wk : Wq) + (size_t)((256 * w + (l >> 3)) * HD_ + h * DH_ + 64 * (jj & 1) + ((l & 7) ^ (2 * g)) * 8) * 2u;
    const char* g2 = (const char*)Wv + (size_t)((256 * w + (l >> 2)) * HD_ + h * DH_ + 32 * vq + (l & 3) * 8) * 2u;
    const unsigned mo = (unsigned)((2 * (lo >> 2) + (lo & 1)) * D_ + 256 * w + 16 * g + 4 * (lo & 2)) * 2u;
    const int kx = ILV ? w : 0;
    asm volatile("buffer_load_dwordx4 %0, %4, %5, %6 offen\n buffer_load_dwordx4 %1, %4, %5, %7 offen\n"
                 "buffer_load_dwordx4 %2, %4, %5, %8 offen\n buffer_load_dwordx4 %3, %4, %5, %9 offen"
                 : "=&v"(a[0]), "=&v"(a[1]), "=&v"(a[2]), "=&v"(a[3]) : "v"(mo), "s"(rsrc4(mem0, MEM_ * D_ * 2u)), "s"(kx * 128), "s"((kx ^ 1) * 128), "s"((kx ^ 2) * 128), "s"((kx ^ 3) * 128) : "memory");
    auto ask = [&](int n) {
#pragma unroll
      for (int i = 0; i < 4; ++i)
        __builtin_amdgcn_global_load_lds((const unsigned*)(n % 3 < 2 ? g1 + (size_t)(64 * ((n / 3) ^ kx) + 32 * (n % 3) + 8 * i) * HD_ * 2 : g2 + (size_t)(64 * ((n / 3) ^ kx) + 16 * i) * HD_ * 2),
                                         (unsigned*)(wl + (n % 6) * 4096 + i * 1024 + l * 16), 16, 0, NTW);
      SCHED();
    };
    const int row = 16 * (g >> 1) + 4 * (g & 1) + (lo >> 2);
    const char* b1 = wl + row * 128 + (lo & 3) * 8;
    const char* b2 = wl + row * 64 + (lo & 3) * 8;
    auto use = [&](int n) {
#pragma unroll
      for (int i = 0; i < 8; ++i) {
        const int u = n % 3 < 2 ? i >> 1 : 4 + (i >> 2), q = n % 3 < 2 ? 2 * (n % 3) + (i & 1) : i & 3;
        const u32x2 v = tr_quad(n % 3 < 2 ? b1 + (n % 6) * 4096 + ((u ^ ((row >> 1) & 3)) * 32) + (q & 1) * 1024
                                          : b2 + (n % 6) * 4096 + (u & 1) * 32 + (32 * (q >> 1) + 8 * (q & 1)) * 64);
        w1[u][n / 3][(q & 1) + 4 * (q >> 1)] = v[0];
        w1[u][n / 3][(q & 1) + 4 * (q >> 1) + 2] = v[1];
      }
      __builtin_amdgcn_s_waitcnt(0xc07f);
      asm volatile("" ::: "memory");
      SCHED();
    };
#pragma unroll
    for (int n = 0; n < 6; ++n) ask(n);
#pragma unroll
    for (int n = 0; n < 12; ++n) {
      if (n == WB1 || n == WB2) __builtin_amdgcn_s_barrier();
      asm volatile("s_waitcnt vmcnt(%0)" :: "n"(n < 7 ? 20 : 44 - 4 * n) : "memory");
      if (n == 0) asm volatile("" : "+v"(a[0]), "+v"(a[1]), "+v"(a[2]), "+v"(a[3]));
      if (n == 0 || n == 11) TP(n == 11);
      use(n);
      if (n < 6) ask(n + 6);
    }
    TP(2);
    if ((LOCK || SL) && w == 1) *(u32x4*)(lds + TG_OFF) = u32x4{te, ge, 61000u, te};
    bar();
  }
  const int idx = (lo & 2) ? 0xeeee : 0x4444;
  const unsigned vo = (unsigned)l * 16u;
  const unsigned pq0 = (unsigned)(size_t)LDS3 + PQK_OFF + (w * 960 + 2 * g * 40 + 8 * (lo >> 2) + (lo & 3)) * 4;
  const unsigned r1w = NearMap::blk(NB1 + jj) + h * 128u;
  unsigned mvo = 0u, r1r = 0u, r3r = 0u, r2b = 0u, hb = 0u;
  unsigned tg = 0u;
  int known = 0;
  unsigned qs = 0u;
  unsigned sc = 0u, sn = SET_;
  u32x4 rt = rs_tid(rs);
  if (TID) asm volatile("" : "+s"(rt));
  unsigned anv = 0x80000000u;
  auto announce = [&](unsigned t, int db) {
    db += (int)dly[KG1];
    const u32x4 an = u32x4{t + (unsigned)(db * kq >> 16), t + (unsigned)((db + (int)dly[KG2]) * kq >> 16), t + (unsigned)((db + (int)dly[KG3]) * kq >> 16), 0u};
    asm volatile("buffer_store_dwordx4 %0, %1, %2, %3 offen sc1\n s_nop 1" :: "v"(an), "v"(anv), "s"(rs), "s"(4u * SET_ + NearMap::blk(NB1) + (unsigned)(wg >> 3) * 128u) : "memory");
  };
  TRD;
  for (int s = 0; s < NSEG_; ++s) {
    f32x4 acc[6];

    TR(0);
    unsigned m, thr, pq1 = pq0;
    asm volatile("" : "+v"(pq1));
    pq1 += 1280u;
    asm volatile("" : "+v"(pq1));
    unsigned kl;
    const int late = 0;
    asm volatile("ds_read_b32 %0, %1" : "=v"(kl) : "v"((unsigned)(size_t)LDS3 + KC_OFF + 8u) : "memory");
    if (s > 0) pause(dly[DLY1]);
    do {
      thr = --budget > 0 && s > 0 ? POISON - 1u : POISON;
      asm volatile("" : "+s"(thr));
      TRY(3);
      if (__builtin_expect(s > 0, 1)) { if (TID & 2) req4it(rt, r1r + sc, a); else req4i(rs, vo, r1r + sc, a); }
      if (LOCK) {
        announce(t1g, late > KTHR ? min(late, 20000) + KPEN : (KFOL ? max(late, 0) : 0));
      }
#pragma unroll
      for (int u = 0; u < 6; ++u) acc[u] = zero4();
      SCHED();
      asm volatile("s_nop 0");
#define G1P(i, al, m0) asm volatile(al "s_waitcnt vmcnt(%15)\n" SM "%1, %0, %6, %10\n" SM "%2, %0, %7, %10\n v_max3_u32 %5, %11, %12, " m0 "\n" SM "%3, %0, %8, %10\n v_max3_u32 %5, %13, %14, %5\n" SM "%4, %0, %9, %10" \
                   : "+v"(a[i]), "+v"(acc[0]), "+v"(acc[1]), "+v"(acc[2]), "+v"(acc[3]), "+v"(m) \
                   : "v"(w1[0][i]), "v"(w1[1][i]), "v"(w1[2][i]), "v"(w1[3][i]), "v"(idx), "v"(a[i][0]), "v"(a[i][1]), "v"(a[i][2]), "v"(a[i][3]), "n"(3 - i + LOCK))
      asm volatile("" : "=v"(m));
      G1P(0, A4, "%12"); SCHED();
      G1P(1, G1N, "%5"); SCHED();
      G1P(2, G1N, "%5"); SCHED();
      G1P(3, G1N, "%5"); SCHED();
    } while (over(m, thr));
    TR(1);
    SCHED();
    asm volatile(G1F "v_pk_add_f32 %2, %2, %3\n v_pk_add_f32 %6, %6, %7\n ds_write2_b32 %0, %4, %5 offset1:40\n v_pk_add_f32 %10, %10, %11\n"
                 "ds_write2_b32 %0, %8, %9 offset0:4 offset1:44\n v_pk_add_f32 %14, %14, %15\n ds_write2_b32 %1, %12, %13 offset1:40\n ds_write2_b32 %1, %16, %17 offset0:4 offset1:44"
                 :: "v"(pq0), "v"(pq1), "v"(LO2(acc[0])), "v"(HI2(acc[0])), "v"(acc[0][0]), "v"(acc[0][1]), "v"(LO2(acc[1])), "v"(HI2(acc[1])), "v"(acc[1][0]), "v"(acc[1][1]),
                    "v"(LO2(acc[2])), "v"(HI2(acc[2])), "v"(acc[2][0]), "v"(acc[2][1]), "v"(LO2(acc[3])), "v"(HI2(acc[3])), "v"(acc[3][0]), "v"(acc[3][1]) : "memory");
    SCHED();
#pragma unroll
    for (int i = 0; i < 7; ++i) {
      acc[4 + (i >> 2)] = smf(a[i & 3], w1[4 + (i >> 2)][i & 3], acc[4 + (i >> 2)], idx);
      SCHED();
      if (i == NV1 - 1) { TR(2); bar(); SCHED(); }
    }
    asm volatile(SM "%9, %10, %11, %12\n v_add_u32 %0, 0x500, %0\n s_nop 0\n v_pk_add_f32 %1, %1, %2\n ds_write2_b32 %0, %3, %4 offset1:40\n s_nop 3\n v_pk_add_f32 %5, %5, %6\n ds_write2_b32 %0, %7, %8 offset0:4 offset1:44"
                 :: "v"(pq1), "v"(LO2(acc[4])), "v"(HI2(acc[4])), "v"(acc[4][0]), "v"(acc[4][1]), "v"(LO2(acc[5])), "v"(HI2(acc[5])), "v"(acc[5][0]), "v"(acc[5][1]),
                    "v"(acc[5]), "v"(a[3]), "v"(w1[5][3]), "v"(idx) : "memory");
    bar();
    asm volatile("s_cmp_eq_u32 %6, 0\n s_cbranch_scc1 1f\n v_and_b32 %4, 0x370, %7\n"
                 "buffer_load_dwordx4 %0, %4, %5, %6 offen sc1\n buffer_load_dwordx4 %1, %4, %5, %6 offen offset:1024 sc1\n"
                 "buffer_load_dwordx4 %2, %4, %5, %6 offen offset:2048 sc1\n buffer_load_dwordx4 %3, %4, %5, %6 offen offset:3072 sc1\n 1:"
                 : "=&v"(a[0]), "=&v"(a[1]), "=&v"(a[2]), "=&v"(a[3]), "=&v"(m) : "s"(rs), "s"(qs), "v"(vo) : "memory", "scc");
    TR(3);
    SLP(0);
    const bool last = s == NSEG_ - 1;
    asm volatile("" : "+v"(kl));
    if (__builtin_expect(__builtin_amdgcn_readfirstlane(kl) != 0x404u && s > 0 && !last, 0)) {
      unsigned lv = vo;
      asm volatile("" : "+v"(lv));
      __builtin_amdgcn_s_sleep(LSL);
      kv_wait(rs, s + 1, h, budget);
      kv_dma(rs, lv ^ (unsigned)(w >> 1) * (((lv >> 3) & 32u) + ((lv >> 2) & 128u)), kv_src(s + 1, h, w, hb), kv_dst(s + 1, w));
      drain_vm();
    }
    auto reset = [&]() {
      u32x2 p2 = u32x2{POISON, POISON};
      const unsigned sp = 3u * SET_ - sc - sn;
      unsigned lv = vo;
      asm volatile("" : "+v"(lv));
      unsigned po = w ? (lv >> 1) + (w < 3 ? (lv >> 8) * 128u : 0u) : mvo;
      const unsigned ps = sp + (w ? r2b + (w == 3 ? BLK_ + vq * 512u : (2 * (jj & 1) + w - 1) * 1024u + (jj >> 1) * 128u) : r1w);
      asm volatile("s_waitcnt lgkmcnt(0)" : "+v"(po), "+v"(p2) : "s"(ps) : "memory");
      __builtin_amdgcn_s_barrier();
      st_sc1s_64(rw, po, ps, p2);
    };
    unsigned tv_, ov, os, pm;
    u32x4 oc[2], sel;
    auto none3 = [&]() { asm volatile("" : "=v"(tv_), "=v"(sel), "=v"(oc[0]), "=v"(oc[1]), "=v"(ov)); asm volatile("" : "=s"(pm), "=s"(os)); };
    auto prep3 = [&]() {
      tv_ = vo;
      asm volatile("" : "+v"(tv_));
      oc[0] = *(const u32x4*)(lds + OB_OFF + tv_); oc[1] = *(const u32x4*)(lds + OB_OFF + 1024 + tv_);
      ov = ovoff(tv_) | (s > XS_ && !last ? 0u : 0x80000000u); os = osoff(max(s - 1, 0), h, jj, hb);
      const unsigned sel1 = 0x3f80u << (tv_ & 16u);
      sel = u32x4{(tv_ & 96u) == 0u ? sel1 : 0u, (tv_ & 96u) == 32u ? sel1 : 0u, (tv_ & 96u) == 64u ? sel1 : 0u, (tv_ & 96u) == 96u ? sel1 : 0u};
      tv_ = (tv_ >> 7) ^ tg;
      pm = r1w + sn;
      asm volatile("" : "+v"(tv_), "+v"(sel), "+v"(oc[0]), "+v"(oc[1]), "+v"(ov));
      asm volatile("" : "+s"(pm), "+s"(os));
    };
    if (qs) {
      float e[6];
      unsigned lv = vo;
      asm volatile("" : "+v"(lv));
      f32x4 sa, sb;
      u32x4 km[4];
      asm volatile("ds_read_b128 %2, %10\n ds_read_b128 %3, %10 offset:1024\n ds_read_b128 %4, %10 offset:2048\n ds_read_b128 %5, %10 offset:3072\n s_waitcnt vmcnt(0) lgkmcnt(0)\n"
                   MF "%0, %2, %6, 0\n ds_read_b128 %2, %10 offset:4096\n" MF "%0, %3, %7, %0\n ds_read_b128 %3, %10 offset:5120\n"
                   MF "%0, %4, %8, %0\n ds_read_b128 %4, %10 offset:6144\n" MF "%0, %5, %9, %0\n ds_read_b128 %5, %10 offset:7168\n s_waitcnt lgkmcnt(3)\n"
                   MF "%1, %2, %6, 0\n s_waitcnt lgkmcnt(2)\n" MF "%1, %3, %7, %1\n s_waitcnt lgkmcnt(1)\n" MF "%1, %4, %8, %1\n s_waitcnt lgkmcnt(0)\n" MF "%1, %5, %9, %1\n s_nop 7"
                   : "=&v"(sa), "=&v"(sb), "=&v"(km[0]), "=&v"(km[1]), "=&v"(km[2]), "=&v"(km[3]), "+v"(a[0]), "+v"(a[1]), "+v"(a[2]), "+v"(a[3])
                   : "v"((unsigned)(size_t)LDS3 + KV_OFF + (s & 1) * 16384 + lv) : "memory");
      const int qi = 8 * jj + (int)((lv >> 4) & 7u) - 4 * (int)(lv >> 8) - (int)((lv >> 3) & 16u);
#pragma unroll
      for (int r = 0; r < 4; ++r) e[r] = (lv & 128u) ? sb[r] : sa[r];
      asm volatile("" : "+v"(e[0]), "+v"(e[1]), "+v"(e[2]), "+v"(e[3]));
#pragma unroll
      for (int r = 0; r < 4; ++r) e[r] = r <= qi ? e[r] : rbf(-1000000000.0f) / SCALE_;
      asm volatile("" : "+v"(e[0]), "+v"(e[1]), "+v"(e[2]), "+v"(e[3]));
      e[4] = e[5] = e[0];
      float m1 = max6(e);
      asm volatile("" : "+v"(m1));
      TRY(7);
      const unsigned ls = (unsigned)(size_t)LDS3, pb = ls + PS_OFF + 1024u;
      unsigned ka = ls + XK_OFF + (lv >> 8) * 128u + (lv & 112u), fa = ls + XK_OFF + 2048u, fv;
      unsigned w1a = pb + (lv >> 8) * 128u + (lv & 112u) + ((lv >> 4) & 8u), w2a = pb + 512u + (lv & 256u) + (lv & 112u) * 2u + ((lv >> 5) & 4u);
      asm volatile("" : "+v"(ka), "+v"(fa), "+v"(w1a), "+v"(w2a));
      reset();
      TR(5);
      __builtin_amdgcn_s_sleep(KSL);
      do {
        asm volatile("ds_read_b32 %4, %6\n ds_read_b128 %0, %5\n ds_read_b128 %1, %5 offset:1024\n ds_read_b128 %2, %5 offset:512\n ds_read_b128 %3, %5 offset:1536\n s_waitcnt lgkmcnt(0)"
                     : "=&v"(km[0]), "=&v"(km[1]), "=&v"(km[2]), "=&v"(km[3]), "=&v"(fv) : "v"(ka), "v"(fa) : "memory");
      } while (__builtin_amdgcn_readfirstlane(fv) != (unsigned)s);
      const f32x4 sm = mfu(km[3], a[3], mfu(km[2], a[2], mfu(km[1], a[1], mfu(km[0], a[0], f32x4{0.f, 0.f, 0.f, 0.f}))));
      const unsigned m2 = __float_as_uint(fmaxf(fmaxf(sm[0], sm[1]), fmaxf(fmaxf(sm[2], sm[3]), m1)));
      e[4] = (lv & 128u) ? sm[2] : sm[0]; e[5] = (lv & 128u) ? sm[3] : sm[1];
      const auto m3 = __builtin_amdgcn_permlane16_swap(m2, m2, false, false);
      float mx;
      asm("v_max_f32 %0, %1, %2" : "=v"(mx) : "v"(m3[0]), "v"(m3[1]));
#pragma unroll
      for (int i = 0; i < 6; ++i) e[i] = __builtin_amdgcn_exp2f((e[i] - mx) * CS);
      asm volatile("ds_write_b64 %0, %1\n ds_write2_b32 %2, %3, %3 offset1:6" :: "v"(w1a), "v"(u32x2{cvt2(e[0], e[1]), cvt2(e[2], e[3])}), "v"(w2a), "v"(cvt2(e[4], e[5])) : "memory");
      TRY(6);
      none3();
    } else {
    if (__builtin_expect(s == 0, 0)) {
      const u32x2 no = launch_no(ws);
      unsigned lv = vo;
      asm volatile("" : "+v"(lv));
      kv_wait(rs, 1, h, budget);
      __builtin_amdgcn_s_sleep(ESL);
      kv_first(lds, rs, 1, h, w, lv, budget);
      if (w >= 2) rec_wait(rw, no, 63u, 64u + 64u * w);
      const NearMap nm{(unsigned)__builtin_amdgcn_readfirstlane(*(const unsigned*)(lds + KC_OFF + 4))};
      mvo = (lv & 32u) * 2u + ((lv >> 1) & 32u) + (lv & 16u) + ((lv >> 5) & 8u) + nm(0u, ((lv >> 8) & 2u) + ((lv >> 7) & 1u), NB1) - NearMap::blk(NB1);
      r1r = nm(0u, h >> 3, NB1 + w) + (ILV ? w * 1024u : 0u); r3r = nm(0u, h >> 3, NB3 + jj * 8 + (h & 7)); r2b = nm(0u, h >> 3, NB2 + (h & 7) * 2);
      hb = far_bit(nm.k);
      gpo = nm(4u, h >> 3, NB1);
      if (w == 1 && ((wg ^ (wg >> 3)) & 7) == 0 && lv < 64u) anv = nm(0u, lv >> 4, NB1) - NearMap::blk(NB1);
    }
    if (w == 1) *(u32x2*)(lds + KC_OFF + 40) = u32x2{known > s + 2 ? 0x404u : 0u, known > s + 3 ? 1u : 0u};
    if ((LOCK || SL) && w == 1) {
      unsigned t1, g1;
      clk_pair(t1, g1);
      const int err = (int)(g1 - ge) - ((int)(t1 - te) * kq >> 16);
      const int dk = (unsigned)(err + 400) < 800u ? err * 3 : 0;
      kq += dk; rq -= dk * 57 >> 6;
      if (__builtin_expect(s == 0, 0)) {
        const float r = (float)err * __builtin_amdgcn_rcpf((float)(int)(t1 - te));
        kq = __builtin_amdgcn_readfirstlane((int)(r * 65536.f)); rq = __builtin_amdgcn_readfirstlane((int)(__builtin_amdgcn_rcpf(r) * 65536.f));
      }
      te = t1; ge = g1;
      if (s == 0) { t1g = t3g = g1; *(unsigned*)(lds + TG_OFF) = t1; }
    }
    if (PRE3 && w == 0) prep3(); else none3();
    reset();
    TR(5);
    if (w == 3) {
      unsigned now = 1u;
      if (__builtin_expect(jj == 0 && s >= FS_, 0)) now = rt_now() | 1u;
      if (l == 0 && s > 1) st_sc1_32(rw, cooff(h, hb) + (unsigned)((s - 2) * 16 + jj * 4), s < XS_ + 2 ? 2u : now);
      if (__builtin_expect(s < XS_, 0)) {
        unsigned lv = vo;
        asm volatile("" : "+v"(lv));
        const float* p = (const float*)(lds + PQK_OFF + 2560 + ((lv >> 8) & 1u) * 640u + ((lv >> 1) & 96u) + (lv >> 9) * 16u + ((lv >> 2) & 12u));
        float v[4];
#pragma unroll
        for (int r = 0; r < 4; ++r) v[r] = (p[r * 40] + p[960 + r * 40]) + (p[1920 + r * 40] + p[2880 + r * 40]);
        st_sc1s_64(rw, (lv & 511u) + (lv >> 9) * 8u, kmoff(s, h, hb) + 2048u + vq * 512u, u32x2{cvt2(v[0], v[1]), cvt2(v[2], v[3])});
      }
      if (__builtin_expect(s == FS_ - 1, 0)) {
        while (__builtin_amdgcn_readfirstlane(ld_sc1_32(rw, OFF_CF + 256u)) < 32u && --budget > 0) __builtin_amdgcn_s_sleep(8);
      }
    } else if (w == 1) {
      if (__builtin_expect(known < min(s + 4 + TC1, NSEG_), 0)) {
        unsigned lv = vo;
        asm volatile("" : "+v"(lv));
        const unsigned c = ld_sc1_32(rw, OFF_CKV + (unsigned)h * 256u + (lv >> 2)), want = lv < XS_ * 16u ? 0x404u : 0x40404u;
        const unsigned long long miss = __builtin_amdgcn_ballot_w64((c & want) != want);
        known = miss ? __builtin_ctzll(miss) : NSEG_;
        *(unsigned*)(lds + KC_OFF + 8) = known > s + 2 ? 0x404u : 0u;
      }
      if (SL && __builtin_expect(s > 0, 1)) {
        const int lt = min(max(late, 0), 20000);
        t1g += dly[KG1] + (unsigned)((lt > 500 ? lt : lt * GAM >> 2) * kq >> 16);
        unsigned t1, g1;
        clk_pair(t1, g1);
        *(unsigned*)(lds + TG_OFF) = local_of(t1g, rq, t1, g1);
      }
      if (LOCK && __builtin_expect(s > 0, 1)) {
        u32x4 gp;
        unsigned lv = vo;
        asm volatile("" : "+v"(lv));
#define MX(r) "v_max_i32_dpp %1, %1, %1 row_ror:" #r " row_mask:0xf bank_mask:0xf\n v_max_i32_dpp %2, %2, %2 row_ror:" #r " row_mask:0xf bank_mask:0xf\n v_max_i32_dpp %3, %3, %3 row_ror:" #r " row_mask:0xf bank_mask:0xf\n"
        asm volatile("s_waitcnt vmcnt(1)\n ds_read_b128 %0, %1\n s_waitcnt lgkmcnt(0)" : "=&v"(gp) : "v"((unsigned)(size_t)LDS3 + GP_OFF + lv) : "memory");
        asm volatile("v_subrev_u32 %1, %4, %1\n v_subrev_u32 %2, %4, %2\n v_subrev_u32 %3, %4, %3\n" MX(8) MX(4) MX(2) MX(1) "s_nop 0"
                     : "+v"(gp) : "v"(gp[0]), "v"(gp[1]), "v"(gp[2]), "s"(ge) : "memory");
        t1g = ge + __builtin_amdgcn_readfirstlane(gp[0]); t3g = ge + __builtin_amdgcn_readfirstlane(gp[2]);
        unsigned t1, g1;
        clk_pair(t1, g1);
        typedef unsigned u32x3 __attribute__((ext_vector_type(3)));
        *(u32x3*)(lds + TG_OFF) = u32x3{local_of(t1g, rq, t1, g1), ge + gp[1], (unsigned)rq};
      }
    }
    }
    int lf3 = 0;
    float gv;
    u32x2 bw;
    unsigned oa;
    asm volatile("" : "=v"(gv), "=v"(bw), "=v"(oa));
    if (__builtin_expect(last, 0)) {
      unsigned lv = vo;
      asm volatile("" : "+v"(lv));
      const unsigned tk = (NSEG_ - 1) * SEG_ + 8 * w + 4 * (h & 1);
      gv = __uint_as_float(__builtin_amdgcn_raw_buffer_load_b32(rw, (lv >> 2) & 12u, OFF_G + tk * 4u, 16));
      bw = __builtin_amdgcn_raw_buffer_load_b64(rw, (lv & 64u) * 1024u + (lv & 48u) + ((lv >> 5) & 8u), bfoff(jj * 32 + (h & ~1), tk, hb), 16);
      sc = w ? (2 + w) * SET_ : 0u;
      const float omg = rbf(1.0f - gv);
#pragma unroll
      for (int i = 0; i < 2; ++i) bw[i] = cvt2(omg * __uint_as_float(bw[i] << 16), omg * __uint_as_float(bw[i] & 0xffff0000u));
      oa = (lv & 48u) * (D_ / 8) + ((lv >> 2) & 16u) + ((lv >> 5) & 8u);
      asm volatile("" : "+v"(gv), "+v"(bw), "+v"(oa));
      pause(dly[DLYL]);
    }
    if (w == 0 || last) {
      prep3();
      if (S3) __builtin_amdgcn_s_sleep(S3);
      pause(dly[DLY3]);
      __builtin_amdgcn_s_setprio(3);
      f32x4 d;
      do {
        thr = --budget > 0 ? POISON - 1u : POISON;
        asm volatile("" : "+s"(thr));
        TRY(4);
        if (TID) req4t(rt, r3r + sc, a); else req4(rs, vo, r3r + sc, a);
        ostore(rs, oc, ov, os);
        asm volatile("v_bfrev_b32 %0, 1" : "+v"(ov));
        d = zero4();
        asm volatile("s_nop 0");
        unsigned t0, t1;
        asm volatile("" : "=v"(m), "=v"(t0), "=v"(t1));
#define BT(d, i, j) "v_bitop3_b32 " d ", %" #i ", %" #j ", %9 bitop3:0x7e\n"
        asm volatile(A4 "s_waitcnt vmcnt(5)\n" MF "%4, %8, %0, %4\n" BT("%5", 10, 11) BT("%6", 12, 13) G3N "s_waitcnt vmcnt(4)\n" MF "%4, %8, %1, %4\n" BT("%7", 14, 15) "v_or3_b32 %5, %5, %6, %7\n"
                     G3N "s_waitcnt vmcnt(3)\n" MF "%4, %8, %2, %4\n" BT("%6", 16, 17) BT("%7", 18, 19) G3N "s_waitcnt vmcnt(2)\n" MF "%4, %8, %3, %4\n"
                     "v_or3_b32 %5, %5, %6, %7\n" BT("%6", 20, 21) BT("%7", 22, 23) "v_or3_b32 %5, %5, %6, %7\n" BT("%6", 24, 25) "v_or3_b32 %5, %5, %6, -2"
                     : "+v"(a[0]), "+v"(a[1]), "+v"(a[2]), "+v"(a[3]), "+v"(d), "+v"(m), "+v"(t0), "+v"(t1)
                     : "v"(sel), "v"(tv_), "v"(a[0][0]), "v"(a[0][1]), "v"(a[0][2]), "v"(a[0][3]), "v"(a[1][0]), "v"(a[1][1]), "v"(a[1][2]), "v"(a[1][3]),
                       "v"(a[2][0]), "v"(a[2][1]), "v"(a[2][2]), "v"(a[2][3]), "v"(a[3][0]), "v"(a[3][1]), "v"(a[3][2]), "v"(a[3][3]));
        SCHED();
      } while (over(m, thr));
      TR(6);
      T0(3);
      u32x2 c;
#define RO8 " row_ror:8 row_mask:0xf bank_mask:0xf\n"
      asm volatile(".if " STR(G3I) " == 0\n s_nop 2\n .endif\n v_add_f32_dpp %1, %1, %1" RO8 "v_add_f32_dpp %2, %2, %2" RO8 "v_add_f32_dpp %3, %3, %3" RO8 "v_add_f32_dpp %4, %4, %4" RO8
                   "v_cvt_pk_bf16_f32 %1, %1, %2\n v_cvt_pk_bf16_f32 %2, %3, %4\n .if " STR(NOMIN) " == 0\n v_min_u32 %1, -2, %1\n v_min_u32 %2, -2, %2\n .endif\n"
                   "buffer_store_dwordx2 %5, %6, %7, %8 offen sc1\n .if " STR(NMX) " == 0\n code_to:\n .endif\n v_mov_b64 %0, %5"
                   : "=&v"(c) : "v"(d[0]), "v"(d[1]), "v"(d[2]), "v"(d[3]), "v"(LO2(d)), "v"(mvo), "s"(rs), "s"(pm) : "memory");
      if (__builtin_expect(last, CIN)) {
        u32x2 r;
#pragma unroll
        for (int i = 0; i < 2; ++i) {
          const unsigned x = cvt2(gv * __uint_as_float(c[i] << 16), gv * __uint_as_float(c[i] & 0xffff0000u));
          r[i] = cvt2(__uint_as_float(x << 16) + __uint_as_float(bw[i] << 16), __uint_as_float(x & 0xffff0000u) + __uint_as_float(bw[i] & 0xffff0000u));
        }
        __builtin_amdgcn_raw_buffer_store_b64(r, __builtin_amdgcn_make_buffer_rsrc(out, 0, S_ * D_ * 2, 0x00020000), oa,
                                              (unsigned)(((NSEG_ - 1) * SEG_ + 8 * w + 4 * (h & 1)) * (D_ * 2) + (jj * 32 + (h & ~1)) * 16), OAUX);
        break;
      }
      __builtin_amdgcn_s_setprio(0);
      TR(7);
      SLP(1);
    }
    if (w == 2) qs = s + 1 >= XS_ && s + 1 < NSEG_ - 1 ? OFF_Q + tile8(s + 1, h, hb) + (jj >> 1) * HALF_ + (jj & 1) * 128u : 0u;
    asm volatile("" : "+s"(qs));
    bar();
    TRW();
    asm volatile("" : "=v"(a[0]), "=v"(a[1]), "=v"(a[2]), "=v"(a[3]));
    tg ^= sc >> 23;
    sc = sn; sn = sn == 2u * SET_ ? 0u : sn + SET_;
  }
  TRE(4 + w);
}

DEVI void recur_at(char* lds, rsrc_t rw, const u32x4 rs, const u16* __restrict__ Wo, const char* ws, int wg TRARGD DLYARG) {
  DLYI(LOCK ? 0x8486 : 0x2c87);
  const int l = threadIdx.x & 63, w = __builtin_amdgcn_readfirstlane(threadIdx.x >> 6), g = l >> 4, lo = l & 15;
  const int h = wg >> 2, jj = wg & 3;
  int budget = SPIN_CAP;
  unsigned mz = 0u;
  if (ALG) asm volatile("" : "+s"(mz));
  u32x8 w3[6][2];
  const int er = (lo >> 2 & 1) + 4 * (lo >> 3);
  const unsigned vo = (unsigned)l * 16u;
  const unsigned dvo = vo ^ (unsigned)((w >> 1) * ((g & 1) * 32 + (g & 2) * 64));
  u32x2 no;
  {
    u32x4 p4 = u32x4{POISON, POISON, POISON, POISON};
    asm volatile("" : "+v"(p4));
#pragma unroll
    for (int i = 0; i < 2; ++i) st_sc1s(rw, vo + i * 1024u, ((w & 1) ? OFF_VX : OFF_K) + tile8(w >> 1, h, 0u) + (jj >> 1) * HALF_ + (jj & 1) * 2048u, p4);
    st_sc1s(rw, w < 2 ? vo : 0x80000000u, NearMap{0u}(0u, h >> 3, NB2 + (h & 7) * 2 + (w & 1)) + jj * 1024u, p4);
    const u16* gw = Wo + (size_t)(h * DH_ + (l >> 3)) * D_ + jj * 256 + 64 * w + ((l & 7) ^ (l >> 5)) * 8;
#pragma unroll
    for (int i = 0; i < 16; ++i)
      __builtin_amdgcn_global_load_lds((const unsigned*)(gw + (size_t)i * 8 * D_), (unsigned*)(lds + W3_OFF + w * 16384 + i * 1024 + l * 16), 16, 0, NTO);
    no = launch_no(ws);
    drain_vm();
    asm volatile("" :: "v"(p4));
    TP(0);
    __builtin_amdgcn_s_barrier();
    asm volatile("" ::: "memory");
    if (threadIdx.x == 0) st_sc1_64(rw, OFF_REC + (64u + wg) * 16u, no);
#pragma unroll
    for (int T = 0; T < 6; ++T) {
      const int Tc = T < 4 ? T : (T & 1) + (w & 2);
      const char* base = lds + W3_OFF + (T < 4 ? w * 16384 : 0) + (16 * (g & 1) + 8 * (g >> 1) + er) * 128 + (((lo & 3) * 2 + (T & 1)) ^ (lo >> 3)) * 16 + (Tc >> 1) * 8;
#pragma unroll
      for (int q = 0; q < 8; ++q) {
        const u32x2 v = tr_quad(base + (32 * (q >> 1) + 2 * (q & 1)) * 128);
        w3[T][q >> 2][2 * (q & 3)] = v[0];
        w3[T][q >> 2][2 * (q & 3) + 1] = v[1];
      }
      asm volatile("" : "+v"(w3[T][0]), "+v"(w3[T][1]));
    }
    bar();
    TP(1);
    *(u32x4*)(lds + PS_OFF + threadIdx.x * 16) = u32x4{0u, 0u, 0u, 0u};
    *(unsigned*)(lds + PZ_OFF + threadIdx.x * 4) = 0u;
    *(unsigned*)(lds + KC_OFF + 12) = 0u;
    rec_wait(rw, no, 3u, 64u + 4u * h, 31u);
    if (w == 1) {
      const NearMap nm{near_kind(rw, no)};
      *(unsigned*)(lds + KC_OFF + 4) = nm.k;
      const unsigned pv = (l & 8) ? 0u : POISON;
      u32x4 p3 = u32x4{pv, pv, pv, pv};
      asm volatile("" : "+v"(p3));
#pragma unroll
      for (int i = 0; i < 12; ++i) st_sc1s(rw, vo + (i & 3) * 1024u, nm(i >> 2, h >> 3, NB3 + jj * 8 + (h & 7)), p3);
#pragma unroll
      for (int i = 0; i < 2; ++i) st_sc1s(rw, vo, nm(1u, h >> 3, NB2 + (h & 7) * 2 + i) + jj * 1024u, p4);
      st_sc1s(rw, l < 32 ? vo : 0x80000000u, nm(1u, h >> 3, NB1 + jj) + (h & 7) * 512u, p4);
      drain_vm();
      asm volatile("" :: "v"(p3), "v"(p4));
      if (l == 0) st_sc1_64(rw, OFF_REC + (192u + wg) * 16u, no);
    } else kv_wait(rs, 0, h, budget);
    TP(2);
    kv_first(lds, rs, 0, h, w, vo, budget);
    TP(3);
    bar();
    TP(4);
  }
  const int idx = lo < 8 ? 0x4444 : 0xeeee;
  const int xg = 2 * (w & 1) + (l >> 5), xm = w >> 1;
  const f32x2* pq = (const f32x2*)((const float*)(lds + PQK_OFF) + xm * 320 + ((l >> 2) & 7) * 40 + 8 * xg + 2 * (l & 3));
  const unsigned xvo = (unsigned)((2 * (jj & 1) + xm) * 1024 + xg * 256 + (jj >> 1) * 128 + (l & 31) * 4), kvo = (unsigned)(xg * 128 + (l & 31) * 4);
  unsigned r2b;
  unsigned sc = 0u, sn = SET_;
  unsigned tg = h & 1;
  const unsigned dlyd = LOCK || SL ? dly[DLYD] * 28 : dly[DLY1] + dly[DLYD];
  const unsigned dlyv = (w == 0 ? dly[DLY2] : dly[DLYV] + (w == 1 ? dly[DLYW] : 0u)) * (LOCK && L2 ? 28 : 1), kgv = !LOCK || w == 0 ? 0u : dly[KGV] + (w == 1 ? dly[DLYW] * 28 : 0u);
  unsigned t2 = LOCK ? mt_now() : 0u;
  unsigned xo = vo, xt = POISON - 1u;
  const unsigned vo2 = vo * 2u;
  const unsigned tch = w == TCW && l < 32 ? lo * 128u + (l >> 4) * (OFF_VX - OFF_K) + (jj & 1) * 2048u + (jj >> 1) * HALF_ : 0x80000000u;
  unsigned tcs = 0x80000000u;
  f32x4 ma = zero4(), mb = zero4();
  const int vr = (lo & 3) * 16 + ((8 * (g >> 1) + er) ^ ((lo & 1) * 2 + (lo & 2) * 4));
  unsigned vb = KV_OFF + 8192 + (g & 1) * 1024 + vr * 16, kb = KV_OFF + l * 16;
  const unsigned ls = (unsigned)(size_t)LDS3;
  const unsigned pw1 = ls + PS_OFF + w * 1024 + (g * 8 + (lo & 7)) * 16 + (lo & 8);
  const unsigned pw2 = ls + (g >= 2 ? PS_OFF + w * 1024 + 512 : UM_OFF + 2048) + ((g & 1) * 8 + (lo & 7)) * 32 + (lo >> 3) * 4;
  const unsigned pr0 = ls + PS_OFF + (g * 8 + (lo & 7)) * 16, pr3 = ls + PS_OFF + 512 + ((g & 1) * 8 + (lo & 7)) * 32 + (lo >> 3) * 16;
  const unsigned pr1 = lo < 8 ? pr0 : ls + PZ_OFF, pr2 = lo < 8 ? ls + PZ_OFF : pr0;
  const unsigned oa1 = lo < 8 ? pr0 + 1024u : pr1, oa2 = lo < 8 ? pr2 : pr0 + 1024u, oa3 = g < 2 ? pr3 + 1024u : ls + PZ_OFF;
  const unsigned oam = ls + XV_OFF + (lo & 3) * 128 + ((l >> 2) & 7) * 16;
  const unsigned uma = ls + UM_OFF + max(w - 1, 0) * 1024 + vo;
  unsigned long long w0m;
  asm("s_cmp_eq_u32 %1, 0\n s_cselect_b64 %0, -1, 0" : "=s"(w0m) : "s"(w) : "scc");
  unsigned long long xwm;
  asm("s_cmp_eq_u32 %1, 1\n s_cselect_b64 %0, -1, 0" : "=s"(xwm) : "s"(w) : "scc");
  xwm |= w0m & 0xff00ff00ff00ff00ull;
  const unsigned xw = ls + (w ? XV_OFF + vo : XK_OFF + g * 128 + (lo & 7) * 16);
  const float ninf = g < 2 ? -INFINITY : 0.f;
  u32x4 one = u32x4{0x3f803f80u, 0x3f803f80u, 0x3f803f80u, 0x3f803f80u}, half = u32x4{0x3f003f00u, 0x3f003f00u, 0x3f003f00u, 0x3f003f00u};
  f32x4 nin4 = f32x4{ninf, ninf, ninf, ninf};
  asm volatile("" : "+v"(one), "+v"(half), "+v"(nin4));
  u32x4 kx[8], us[4];
  auto lo4 = [](u32x8 b) { return u32x4{b[0], b[1], b[2], b[3]}; };
  auto hi4 = [](u32x8 b) { return u32x4{b[4], b[5], b[6], b[7]}; };
  u32x4 va[2][4];
  auto useg_a = [&]() {
#pragma unroll
    for (int i = 0; i < 8; ++i) {
      const u32x2 x0 = tr_quad(lds + vb + (i & 3) * 2048 + (i >> 2) * 8), x1 = tr_quad(lds + (vb ^ 32u) + (i & 3) * 2048 + (i >> 2) * 8);
      va[i >> 2][i & 3] = u32x4{x0[0], x0[1], x1[0], x1[1]};
    }
#pragma unroll
    for (int i = 0; i < 8; ++i) kx[i] = *(const u32x4*)(lds + kb + i * 1024);
    vb ^= 16384u; kb ^= 16384u;
  };
  auto useg_b = [&](int T0, int T1) {
#pragma unroll
    for (int T = 0; T < 4; ++T) {
      if (T < T0 || T >= T1) continue;
      f32x4 d[2];
#pragma unroll
      for (int ks = 0; ks < 4; ++ks)
#pragma unroll
        for (int n = 0; n < 2; ++n) d[n] = mfu(va[n][ks], (ks & 1) ? hi4(w3[T][ks >> 1]) : lo4(w3[T][ks >> 1]), ks ? d[n] : f32x4{0.f, 0.f, 0.f, 0.f});
      us[T] = cvt8(d[0], d[1]);
      asm volatile("" : "+v"(us[T]));
    }
    asm volatile("" : "+v"(kx[0]), "+v"(kx[1]), "+v"(kx[2]), "+v"(kx[3]), "+v"(kx[4]), "+v"(kx[5]), "+v"(kx[6]), "+v"(kx[7]));
  };
#define USP 4
  u32x4 rt = rs_tid(rs);
  if (TID) asm volatile("" : "+s"(rt));
  const unsigned qsu = jj < 2 ? __builtin_bit_cast(unsigned, CS) : 0x3f800000u;
  u32x2 qs2 = u32x2{qsu, qsu};
  if (NMX) asm volatile("" : "+s"(qs2));
  unsigned c;
  auto pub = [&]() {
    SCHED();
    if (PBA) asm volatile(".p2align %0\n .rept %1\n s_nop 0\n .endr" :: "n"(PBA), "n"(PBN));
    unsigned so1 = r2b + sc, so2 = r2b + BLK_ + (2 * (~jj & 1) + (jj >> 1)) * 512u + sc;
    f32x2 t0, t1, t2, t3;
    asm volatile("" : "+s"(so1), "+s"(so2));
    asm volatile("" : "=v"(t0), "=v"(t1), "=v"(t2), "=v"(t3), "=v"(c));
#define PSUM(o0, o1, o2, o3, d, mul) "ds_read_b64 %0, %7 offset:" #o0 "\n ds_read_b64 %1, %7 offset:" #o1 "\n ds_read_b64 %2, %7 offset:" #o2 "\n ds_read_b64 %3, %7 offset:" #o3 "\n" \
      "s_waitcnt lgkmcnt(2)\n v_pk_add_f32 %0, %0, %1\n s_waitcnt lgkmcnt(0)\n v_pk_add_f32 %2, %2, %3\n v_pk_add_f32 %0, %0, %2\n" mul CV d ", %4, %5\n .if " STR(NOMIN) " == 0\n v_min_u32 " d ", -2, " d "\n .endif\n"
    asm volatile("s_waitcnt lgkmcnt(0)\n s_barrier\n" PSUM(0, 3840, 7680, 11520, "%6", ".if " STR(NMX) "\n v_pk_mul_f32 %0, %14, %0\n .endif\n") "buffer_store_dword %6, %8, %10, %11 offen sc1\n s_cmp_eq_u32 %13, 0\n s_cbranch_scc0 2f\n s_barrier\n s_branch 3f\n"
                 "2: s_barrier\n" PSUM(1280, 5120, 8960, 12800, "%4", "") "buffer_store_dword %4, %9, %10, %12 offen sc1\n 3:"
                 :: "v"(t0), "v"(t1), "v"(t2), "v"(t3), "v"(t0[0]), "v"(t0[1]), "v"(c), "v"((unsigned)(size_t)pq), "v"(xvo), "v"(kvo), "s"(rs), "s"(so1), "s"(so2), "s"(xm), "s"(qs2) : "memory", "scc");
    asm volatile("" : "+v"(c));
    SCHED();
  };
  const NearMap nm{(unsigned)__builtin_amdgcn_readfirstlane(*(const unsigned*)(lds + KC_OFF + 4))};
  const unsigned hb = far_bit(nm.k);
  r2b = NearMap{0u}(0u, h >> 3, NB2 + (h & 7) * 2);
  const unsigned r3o = nm(0u, w, NB3 + jj * 8 + 2 * g + ((lo >> 2) & 1)) + h * 128u + (unsigned)((lo >> 3) * 4 + (lo & 3)) * 16u;
  asm volatile("" ::: "memory");
  useg_a(); useg_b(0, 4);
  if (w == 1) kb = KV_OFF + 8192 + (l ^ ((g & 1) * 2 + (g & 2) * 4)) * 16;
  TP(5);
  pub();
  TP(7);
  TRD;
  for (int s = 0;;) {
    const bool last = s == NSEG_ - 1;
    const unsigned r2c = r2b + sc;
    unsigned m;
    f32x4 d[6], sa, sb, sm;
    u32x4 x[4];
    asm volatile("" : "=v"(d[0]), "=v"(d[1]), "=v"(d[2]), "=v"(d[3]), "=v"(x[0]), "=v"(x[1]), "=v"(x[2]), "=v"(x[3]), "=v"(sa), "=v"(sb), "=v"(sm));
    unsigned dox = w == 0 || last ? 2u : 1u, lim = last ? 5u * SET_ : sc, rlim = last || !NMX ? 0xffffffffu : 0x28000000u;
    asm volatile("" : "+s"(dox), "+s"(lim), "+s"(rlim));
    int lf2 = 0;
    if (LOCK && L2) lf2 = until(t2, dlyv + (s > 1 ? 0u : 84u)) - (int)dlyv; else pause(dlyv);
#define SH8 " row_shr:8 row_mask:0xf bank_mask:0xc\n"
#define SOFT(e4, e5, a1, a2, cs, lo2) "v_max3_f32 %4, %0, %1, %2\n v_max3_f32 %4, %4, " e4 ", " e5 "\n v_max_f32 %4, %4, %3\n s_nop 1\n" \
                   "v_max_f32_dpp %5, %4, %4" RO8 "v_max_f32_dpp %6, %4, %4" RO8 "s_nop 1\n" \
                   "v_permlane16_swap_b32 %5, %6\n v_max_f32 %4, %5, %6\n v_max_f32 %5, %5, %6\n s_nop 1\n" \
                   "v_permlane32_swap_b32 %4, %5\n v_max_f32 %4, %4, %5\n" \
                   "v_sub_f32 %0, %0, %4\n v_sub_f32 %1, %1, %4\n v_sub_f32 %2, %2, %4\n v_sub_f32 %3, %3, %4\n v_sub_f32 " e4 ", " e4 ", %4\n v_sub_f32 " e5 ", " e5 ", %4\n" \
                   "v_mul_f32 %0, " cs ", %0\n v_mul_f32 %1, " cs ", %1\n v_mul_f32 %2, " cs ", %2\n v_exp_f32 %0, %0\n v_exp_f32 %1, %1\n v_mul_f32 %3, " cs ", %3\n" \
                   "v_mul_f32 " e4 ", " cs ", " e4 "\n v_exp_f32 %2, %2\n v_exp_f32 %3, %3\n v_mul_f32 " e5 ", " cs ", " e5 "\n v_cvt_pk_bf16_f32 %0, %0, %1\n v_exp_f32 " e4 ", " e4 "\n" \
                   "v_exp_f32 " e5 ", " e5 "\n v_cvt_pk_bf16_f32 %1, %2, %3\n ds_write_b64 " a1 ", " lo2 "\n v_cvt_pk_bf16_f32 " e4 ", " e4 ", " e5 "\n ds_write2_b32 " a2 ", " e4 ", " e4 " offset1:6"
    if (dox == 2u) {
      TR(0);
      do {
        asm volatile("s_cmp_eq_u32 %11, %12\n s_cbranch_scc0 6f\n buffer_load_dwordx4 %0, off, %4, %5 sc1\n buffer_load_dwordx4 %1, off, %4, %5 offset:1024 sc1\n"
                     "buffer_load_dwordx4 %2, off, %4, %5 offset:2048 sc1\n buffer_load_dwordx4 %3, off, %4, %5 offset:3072 sc1\n s_branch 7f\n"
                     "6: buffer_load_dwordx4 %0, %6, %7, %5 offen sc1\n buffer_load_dwordx4 %1, %6, %7, %5 offen offset:1024 sc1\n"
                     "buffer_load_dwordx4 %2, %6, %7, %5 offen offset:2048 sc1\n buffer_load_dwordx4 %3, %6, %7, %5 offen offset:3072 sc1\n"
                     "s_sleep %13\n buffer_load_dword %8, %9, %7, %10 offen sc1\n 7:"
                     : "=&v"(x[0]), "=&v"(x[1]), "=&v"(x[2]), "=&v"(x[3])
                     : "s"(rt), "s"(r2c), "v"(xo), "s"(rs), "v"(d[0][0]), "v"(vo2), "s"(r2c + BLK_), "s"(lim), "s"(sc), "n"(LVS) : "memory", "scc");
        if (--budget <= 0) xt = POISON;
        SCHED();
#define MX3(a, b, c) "v_max3_u32 %0, %" #a ", %" #b ", %" #c "\n"
        asm volatile(A4 "s_waitcnt vmcnt(3)\n" MF "%2, %5, %1, %7\n" MX3(10, 11, 12) MF "%3, %6, %1, %8\n" MX3(13, 13, 0) MF "%4, %1, %1, %9"
                     : "=&v"(m), "+v"(x[0]), "=&v"(sa), "=&v"(sb), "=&v"(sm) : "v"(kx[0]), "v"(kx[4]), "v"(ma), "v"(mb), "v"(nin4), "v"(x[0][0]), "v"(x[0][1]), "v"(x[0][2]), "v"(x[0][3]));
        SCHED();
        asm volatile("s_waitcnt vmcnt(0)\n" MF "%4, %7, %1, %4\n" MX3(13, 14, 0) MF "%5, %10, %1, %5\n" MX3(15, 16, 0) MF "%6, %1, %1, %6\n" MX3(17, 18, 0)
                     MF "%4, %8, %2, %4\n" MX3(19, 20, 0) MF "%5, %11, %2, %5\n" MX3(21, 22, 0) MF "%6, %2, %2, %6\n" MX3(23, 24, 0)
                     MF "%4, %9, %3, %4\n" MF "%5, %12, %3, %5\n" MF "%6, %3, %3, %6"
                     : "+v"(m), "+v"(x[1]), "+v"(x[2]), "+v"(x[3]), "+v"(sa), "+v"(sb), "+v"(sm)
                     : "v"(kx[1]), "v"(kx[2]), "v"(kx[3]), "v"(kx[5]), "v"(kx[6]), "v"(kx[7]), "v"(x[1][0]), "v"(x[1][1]), "v"(x[1][2]), "v"(x[1][3]),
                       "v"(x[2][0]), "v"(x[2][1]), "v"(x[2][2]), "v"(x[2][3]), "v"(x[3][0]), "v"(x[3][1]), "v"(x[3][2]), "v"(x[3][3]));
        SCHED();
      } while (over(m, xt));
      TR(3);
#define MOV6 A8 "v_mov_b32_dpp %0, %4" SH8 "v_mov_b32_dpp %1, %5" SH8 "v_mov_b32_dpp %2, %6" SH8 "v_mov_b32_dpp %8, %10" SH8 "v_mov_b32_dpp %9, %11" SH8 "v_mov_b32_dpp %3, %7" SH8
#define SC12 "v"(sa[0]), "v"(sa[1]), "v"(sa[2]), "v"(sa[3]), "v"(sb[0]), "v"(sb[1]), "v"(sb[2]), "v"(sb[3]), "v"(sm[0]), "v"(sm[1]), "v"(sm[2]), "v"(sm[3])
      asm volatile("s_cmp_eq_u32 %17, -1\n s_cbranch_scc1 3f\n s_nop " STR(XIL * 2) "\n" MOV6
                   "v_exp_f32 %4, %0\n v_exp_f32 %5, %1\n v_exp_f32 %6, %2\n v_exp_f32 %7, %3\n" CV "%4, %4, %5\n v_exp_f32 %10, %8\n v_exp_f32 %11, %9\n" CV "%5, %6, %7\n"
                   "ds_write_b64 %12, %14\n" CV "%10, %10, %11\n ds_write2_b32 %13, %10, %10 offset1:6\n 9:\n"
                   ".subsection 1\n 3: s_nop 2\n" MOV6 SOFT("%8", "%9", "%12", "%13", "%16", "%15") "\n s_branch 9b\n .subsection 2\n code_to: .fill 64, 4, 0xbf800000\n .subsection 0"
                   :: SC12, "v"(pw1), "v"(pw2), "v"(LO2(sb)), "v"(LO2(sa)), "s"(CS), "s"(rlim) : "memory", "scc");
    }
    if (w != 0) {
      u32x4* vv = x;
      unsigned thr, r2v = r2c + BLK_;
      asm volatile("" : "=s"(thr), "+s"(r2v));
      TR(0);
      asm volatile("5:\n .if " STR(TID) "\n buffer_load_dwordx4 %0, off, %6, %7 sc1\n buffer_load_dwordx4 %1, off, %6, %7 offset:1024 sc1\n .else\n"
                   "buffer_load_dwordx4 %0, %5, %6, %7 offen sc1\n buffer_load_dwordx4 %1, %5, %6, %7 offen offset:1024 sc1\n .endif\n s_add_i32 %8, %8, -1\n s_cmp_gt_i32 %8, 0\n s_cselect_b32 %9, -2, -1"
                   : "=&v"(vv[0]), "=&v"(vv[1]), "=&v"(d[4]), "=&v"(d[5]), "=&v"(m) : "v"(vo), "s"(TID ? rt : rs), "s"(r2v), "s"(budget), "s"(thr) : "memory", "scc");
      if (TCH) asm volatile("s_mov_b32 m0, %0\n s_nop 0\n buffer_load_dword %1, %2, %3 offen sc1 lds" :: "n"(UM_OFF + 2560), "v"(tch), "s"(rs), "s"(tcs) : "memory");
      SCHED();
      asm volatile("s_nop 3\n v_mov_b64 %0, 0\n v_mov_b64 %1, 0\n v_mov_b64 %2, 0\n v_mov_b64 %3, 0\n v_mov_b64 %4, 0\n v_mov_b64 %5, 0\n"
                   "v_mov_b64 %6, 0\n v_mov_b64 %7, 0\n v_mov_b64 %8, 0\n v_mov_b64 %9, 0\n v_mov_b64 %10, 0\n v_mov_b64 %11, 0"
                   :: "v"(LO2(d[0])), "v"(HI2(d[0])), "v"(LO2(d[1])), "v"(HI2(d[1])), "v"(LO2(d[2])), "v"(HI2(d[2])),
                      "v"(LO2(d[3])), "v"(HI2(d[3])), "v"(LO2(d[4])), "v"(HI2(d[4])), "v"(LO2(d[5])), "v"(HI2(d[5])));
      SCHED();
      __builtin_amdgcn_s_setprio(0);
      SCHED();
      asm volatile(A4 "s_waitcnt vmcnt(1 + " STR(TC1) ")\n" SM "%5, %0, %8, %15\n v_max3_u32 %7, %16, %17, %18\n" SM "%6, %0, %9, %15\n s_nop 0\n s_waitcnt vmcnt(" STR(TC1) ")\n"
                   SM "%5, %1, %10, %15\n v_max3_u32 %7, %19, %20, %7\n" SM "%6, %1, %11, %15\n v_max3_u32 %7, %21, %22, %7\n"
                   SM "%2, %0, %12, %15\n v_max3_u32 %7, %23, %23, %7\n" SM "%3, %0, %13, %15\n v_cmp_lt_u32 vcc, %24, %7\n" SM "%4, %0, %14, %15\n s_cbranch_vccnz 5b"
                   :: "v"(vv[0]), "v"(vv[1]), "v"(d[0]), "v"(d[1]), "v"(d[2]), "v"(d[4]), "v"(d[5]), "v"(m),
                      "v"(w3[4][0]), "v"(w3[5][0]), "v"(w3[4][1]), "v"(w3[5][1]), "v"(w3[0][0]), "v"(w3[1][0]), "v"(w3[2][0]), "v"(idx),
                      "v"(vv[0][0]), "v"(vv[0][1]), "v"(vv[0][2]), "v"(vv[0][3]), "v"(vv[1][0]), "v"(vv[1][1]), "v"(vv[1][2]), "v"(vv[1][3]), "s"(thr), "s"(r2v), "v"(d[3]) : "vcc");
      SCHED();
      asm volatile(CV "%8, %8, %9\n" CV "%9, %10, %11\n" CV "%10, %12, %13\n" CV "%11, %14, %15\n ds_write_b128 %6, %7\n"
                   SM "%5, %0, %16, %21\n" SM "%2, %1, %17, %21\n" SM "%3, %1, %18, %21\n" SM "%4, %1, %19, %21\n" SM "%5, %1, %20, %21"
                   :: "v"(vv[0]), "v"(vv[1]), "v"(d[0]), "v"(d[1]), "v"(d[2]), "v"(d[3]), "v"(uma), "v"(d[4]),
                      "v"(d[4][0]), "v"(d[4][1]), "v"(d[4][2]), "v"(d[4][3]), "v"(d[5][0]), "v"(d[5][1]), "v"(d[5][2]), "v"(d[5][3]),
                      "v"(w3[3][0]), "v"(w3[0][1]), "v"(w3[1][1]), "v"(w3[2][1]), "v"(w3[3][1]), "v"(idx) : "memory");
      SCHED();
    }
    TR(4);
    T0(8);
    f32x4 su, c0, c1;
    u32x4 p0, p1, p2, p3;
    SCHED();
    asm volatile("s_waitcnt lgkmcnt(0)" : "=v"(su), "=v"(c0), "=v"(c1), "=v"(p0), "=v"(p1), "=v"(p2), "=v"(p3) :: "memory");
    __builtin_amdgcn_s_barrier();
    TR(5);
#define RDP "ds_read_b128 %0, %4\n ds_read_b128 %1, %5\n ds_read_b128 %2, %6\n ds_read_b128 %3, %7\n"
    asm volatile(RDP CV "%8, %8, %9\n" CV "%9, %10, %11\n" CV "%10, %12, %13\n" CV "%11, %14, %15\n" CV "%16, %16, %17\n" CV "%17, %18, %19\n" CV "%18, %20, %21\n" CV "%19, %22, %23\n"
                 "s_mov_b64 exec, %27\n ds_read_b128 %24, %26\n ds_read_b128 %25, %26 offset:1024\n s_mov_b64 exec, -1"
                 :: "v"(p0), "v"(p3), "v"(p1), "v"(p2), "v"(pr0), "v"(pr3), "v"(pr1), "v"(pr2),
                   "v"(d[0][0]), "v"(d[0][1]), "v"(d[0][2]), "v"(d[0][3]), "v"(d[1][0]), "v"(d[1][1]), "v"(d[1][2]), "v"(d[1][3]),
                   "v"(d[2][0]), "v"(d[2][1]), "v"(d[2][2]), "v"(d[2][3]), "v"(d[3][0]), "v"(d[3][1]), "v"(d[3][2]), "v"(d[3][3]), "v"(d[0]), "v"(d[2]), "v"(uma), "s"(w0m) : "memory");
    SCHED();
    asm volatile("s_waitcnt lgkmcnt(4)\n" A8 MF "%0, %8, %3, 0\n" MF "%0, %9, %6, %0\n s_nop 0\n s_waitcnt lgkmcnt(2)\n" MF "%1, %10, %4, 0\n" MF "%1, %11, %5, %1\n" MF "%2, %12, %4, 0\n"
                 "s_nop 0\n s_waitcnt lgkmcnt(0)\n" MF "%1, %7, %6, %1\n" MF "%2, %13, %5, %2\n"
                 :: "v"(su), "v"(c0), "v"(c1), "v"(p0), "v"(p1), "v"(p2), "v"(p3), "v"(d[0]), "v"(one), "v"(half), "v"(us[0]), "v"(us[1]), "v"(us[2]), "v"(us[3]) : "memory");
    SCHED();
    unsigned so;
#define TAIL "v_pk_mul_f32 %2, %2, %17 op_sel_hi:[1,0]\n v_pk_mul_f32 %3, %3, %17 op_sel_hi:[1,0]\n v_cvt_pk_bf16_f32 %6, %6, %7\n v_cvt_pk_bf16_f32 %7, %8, %9\n" \
             "v_pk_mul_f32 %4, %4, %17 op_sel_hi:[1,0]\n v_pk_mul_f32 %5, %5, %17 op_sel_hi:[1,0]\n v_and_or_b32 %6, %6, -2, %14\n v_cvt_pk_bf16_f32 %8, %10, %11\n" \
             "v_cvt_pk_bf16_f32 %9, %12, %13\n v_and_or_b32 %7, %7, -2, %14\n v_and_or_b32 %8, %8, -2, %14\n v_and_or_b32 %9, %9, -2, %14\n" \
             "buffer_store_dwordx4 %1, %15, %16, %0 offen sc1\n s_nop 1"
#define TOPS "v"(c0), "v"(LO2(c0)), "v"(HI2(c0)), "v"(LO2(c1)), "v"(HI2(c1)), "v"(c0[0]), "v"(c0[1]), "v"(c0[2]), "v"(c0[3]), "v"(c1[0]), "v"(c1[1]), "v"(c1[2]), "v"(c1[3]), \
             "s"(tg), "v"(r3o), "s"(rs), "v"(LO2(su))
    asm volatile("v_rcp_f32_e64 %21, %21\n" MF "%18, %19, %20, %18\n"
                 "v_add_u32 %22, 0xd4800000, %21\n v_cmp_lt_u32_e64 vcc, %23, %22\n s_cmp_lg_u64 vcc, 0\n s_cselect_b32 %0, 0x80000000, %24\n s_nop 0\n" TAIL
                 : "=&s"(so) : TOPS, "v"(c1), "v"(d[2]), "v"(p3), "v"(su[0]), "v"(su[2]), "s"(rlim), "s"(sc) : "memory", "vcc", "scc");
    SCHED();
    asm volatile("" : "+s"(budget));
    if (__builtin_expect(so != lim, CIN)) {
      if (so == 0x80000000u) {
        if (w == 0) {
          asm volatile(SOFT("%7", "%8", "%9", "%10", "1.0", "%11") "\n ds_write_b32 %12, %13"
                       :: "v"(sa[0]), "v"(sa[1]), "v"(sa[2]), "v"(sa[3]), "v"(sb[0]), "v"(sb[1]), "v"(sb[2]), "v"(sm[0]), "v"(sm[1]),
                          "v"(pw1 + 2048u), "v"(g >= 2 ? pw2 + 2048u : pw2), "v"(LO2(sa)), "v"(ls + KC_OFF + 12u), "v"(s + 1) : "memory");
        }
        unsigned fv;
        do asm volatile("ds_read_b32 %0, %1\n s_waitcnt lgkmcnt(0)" : "=&v"(fv) : "v"(ls + KC_OFF + 12u) : "memory");
        while (__builtin_amdgcn_readfirstlane(fv) != (unsigned)(s + 1) && --budget > 0);
      }
      asm volatile("" : "+v"(d[0]), "+v"(d[2]));
      unsigned lv = vo;
      asm volatile("" : "+v"(lv));
      const unsigned z1 = (lv & 128u) ? PZ_OFF : pr0 - ls, z2 = (lv & 128u) ? pr0 - ls : PZ_OFF, g1 = (lv & 128u) ? 0u : 1024u;
      u32x4 r[3];
#pragma unroll
      for (int j = 1; j < 4; ++j) {
        const u32x4 q0 = *(const u32x4*)(lds + (pr0 - ls) + j * 1024), q3 = *(const u32x4*)(lds + (pr3 - ls) + j * 1024);
        const u32x4 q1 = *(const u32x4*)(lds + z1 + j * g1), q2 = *(const u32x4*)(lds + z2 + j * (1024u - g1));
        const f32x4 z = f32x4{0.f, 0.f, 0.f, 0.f};
        const float ir = __builtin_amdgcn_rcpf(mfu(half, q3, mfu(one, q0, z))[0]);
        const f32x4 e0 = mfu(__builtin_bit_cast(u32x4, d[0]), q3, mfu(us[1], q2, mfu(us[0], q1, z))) * ir, e1 = mfu(__builtin_bit_cast(u32x4, d[2]), q3, mfu(us[3], q2, mfu(us[2], q1, z))) * ir;
        r[j - 1] = (cvt8(e0, e1) & ~1u) | tg;
        asm volatile("" : "+v"(r[j - 1]));
        st_sc1s(rw, last || j == 2 ? r3o : 0x80000000u, last ? (2 + j) * SET_ : sc, r[j - 1]);
      }
      asm volatile("s_nop 1" :: "v"(r[0]), "v"(r[1]), "v"(r[2]));
    }
    TR(6);
    T0(9);
    SLP(0);
    asm volatile("" : "=v"(kx[0]), "=v"(kx[1]), "=v"(kx[2]), "=v"(kx[3]), "=v"(kx[4]), "=v"(kx[5]), "=v"(kx[6]), "=v"(kx[7]));
    if (last) break;
    asm volatile("s_mov_b64 exec, %5\n ds_write_b128 %4, %0\n ds_write_b128 %4, %1 offset:1024\n s_and_b64 exec, exec, %6\n"
                 "ds_write_b128 %4, %2 offset:512\n ds_write_b128 %4, %3 offset:1536\n ds_write_b32 %7, %8\n s_mov_b64 exec, -1"
                 :: "v"(x[0]), "v"(x[1]), "v"(x[2]), "v"(x[3]), "v"(xw), "s"(xwm), "s"(w0m), "v"(ls + XK_OFF + 2048u), "v"(s) : "memory", "scc");
    if (__builtin_expect(s < XS_, 0))
      if (jj & 2) st_sc1s_32(rw, kvo, kmoff(s, h, hb) + (2 * (jj & 1) + xm) * 512u, c);
    const u32x2 kc2 = *(const u32x2*)(lds + KC_OFF + 40);
    const unsigned kc = kc2[0];
    useg_a(); useg_b(0, USP);
    if (USP < 4) asm volatile("" : "+v"(va[0][0]), "+v"(va[0][1]), "+v"(va[0][2]), "+v"(va[0][3]), "+v"(va[1][0]), "+v"(va[1][1]), "+v"(va[1][2]), "+v"(va[1][3]));
    TRX(0);
    const bool have = __builtin_amdgcn_readfirstlane(kc) == 0x404u;
    unsigned ks = kv_src(s + 2, h, w, hb), kd = kv_dst(s, w), kv = have && s < NSEG_ - 2 ? dvo : 0x80000000u;
    if (TCH) {
      tcs = __builtin_amdgcn_readfirstlane(kc2[1]) && s < NSEG_ - 3 ? OFF_K + tile8(s + 3, h, hb) : 0x80000000u;
      asm volatile("" : "+s"(tcs));
    }
    asm volatile("" : "+s"(ks), "+s"(kd), "+v"(kv));
    if (D4) pause(dly[DLY4]);
    bar();
#define KVE 2
    if (w == KVE || KVE == 8) kv_dma(rs, kv, ks, kd);
    r2b = nm(0u, h >> 3, NB2 + (h & 7) * 2);
    tg ^= sc >> 23;
    sc = sn; sn = sn == 2u * SET_ ? 0u : sn + SET_;
    TRW();
    useg_b(USP, 4);
    if (w == 1 && s >= XS_) {
      u32x4 p1, p2, p3;
      u32x2 vm[8];
      asm volatile("ds_read_b128 %0, %11\n ds_read_b128 %1, %12\n ds_read_b128 %2, %13\n"
                   "ds_read_b64_tr_b16 %3, %14\n ds_read_b64_tr_b16 %4, %14 offset:512\n ds_read_b64_tr_b16 %5, %14 offset:8\n ds_read_b64_tr_b16 %6, %14 offset:520\n"
                   "ds_read_b64_tr_b16 %7, %14 offset:1024\n ds_read_b64_tr_b16 %8, %14 offset:1536\n ds_read_b64_tr_b16 %9, %14 offset:1032\n ds_read_b64_tr_b16 %10, %14 offset:1544\n"
                   "s_waitcnt lgkmcnt(0)"
                   : "=&v"(p1), "=&v"(p2), "=&v"(p3), "=&v"(vm[0]), "=&v"(vm[1]), "=&v"(vm[2]), "=&v"(vm[3]), "=&v"(vm[4]), "=&v"(vm[5]), "=&v"(vm[6]), "=&v"(vm[7])
                   : "v"(oa1), "v"(oa2), "v"(oa3), "v"(oam) : "memory");
      const f32x4 su = mfu(one, p3, mfu(one, p2, mfu(one, p1, f32x4{0.f, 0.f, 0.f, 0.f})));
      f32x4 o[4];
#pragma unroll
      for (int i = 0; i < 4; ++i) o[i] = mfu(kx[4 * (i >> 1) + (i & 1)], p1, f32x4{0.f, 0.f, 0.f, 0.f});
#pragma unroll
      for (int i = 0; i < 4; ++i) o[i] = mfu(kx[4 * (i >> 1) + 2 + (i & 1)], p2, o[i]);
#pragma unroll
      for (int i = 0; i < 4; ++i) o[i] = mfu(u32x4{vm[2 * i][0], vm[2 * i][1], vm[2 * i + 1][0], vm[2 * i + 1][1]}, p3, o[i]);
      SCHED();
      const float ir = __builtin_amdgcn_rcpf(su[0]);
      u32x4 oc[2];
#pragma unroll
      for (int i = 0; i < 8; ++i) oc[i >> 2][i & 3] = cvt2x(o[i >> 1][2 * (i & 1)] * ir, o[i >> 1][2 * (i & 1) + 1] * ir);
      *(u32x4*)(lds + OB_OFF + vo) = oc[0];
      *(u32x4*)(lds + OB_OFF + 1024 + vo) = oc[1];
      if (__builtin_expect(s == NL_ - 1, 0)) {
        unsigned lv = vo;
        asm volatile("" : "+v"(lv));
        ostore(rs, oc, ovoff(lv), osoff(s, h, jj, hb));
      }
      TRX(6);
    }
    if (w != KVE && KVE != 8) {
      pause(max((int)dlyd - (w == 1 && s >= XS_ ? 19 : 0) - 5 * (4 - USP), 0));
      kv_dma(rs, kv, ks, kd);
    }
    ++s;
    if (__builtin_expect(!have && s < NSEG_ - 1, 0))
      if (__builtin_amdgcn_readfirstlane(*(const unsigned*)(lds + KC_OFF + 8)) == 0x404u) kv_dma(rs, dvo, ks, kd);
    if (__builtin_expect(s == NSEG_ - 1, 0)) {
      static_assert((NSEG_ - 1) % 3 == 0, "set 0");
      asm volatile("" : "+v"(xo));
      const int qi = 8 * w + (int)((xo >> 4) & 7u) - 4 * (int)(xo >> 8);
#pragma unroll
      for (int r = 0; r < 4; ++r) {
        ma[r] = r <= qi ? 0.f : rbf(-1000000000.0f) / SCALE_;
        mb[r] = 16 + r <= qi ? 0.f : rbf(-1000000000.0f) / SCALE_;
      }
      if (w == 1) {
#pragma unroll
        for (int i = 0; i < 8; ++i) kx[i] = *(const u32x4*)(lds + KV_OFF + 16384 + xo + i * 1024);
        asm volatile("" : "+v"(kx[0]), "+v"(kx[1]), "+v"(kx[2]), "+v"(kx[3]), "+v"(kx[4]), "+v"(kx[5]), "+v"(kx[6]), "+v"(kx[7]));
      }
      xt = (xo & 128u) ? xt : POISON;
      xo += (xo & 128u) ? 0u : OFF_Q + tile8(NSEG_ - 1, h, hb) + (w >> 1) * HALF_ + (w & 1) * 128u - r2b;
    }
    pub();
    TR(2);
  }
  asm volatile("" : "+s"(no));
  if (wg == 0 && w == 0 && vo == 0u) st_sc1_64(rw, OFF_EP, no);
  TRE(w);
}

DEVI void recur_role(char* lds, char* ws, rsrc_t rw, const u16* __restrict__ mem0, const u16* __restrict__ Wq, const u16* __restrict__ Wk,
                     const u16* __restrict__ Wv, const u16* __restrict__ Wo, u16* __restrict__ out, int wg TRARGD) {
  const u32x4 rs = u32x4{(unsigned)(size_t)ws, (unsigned)((size_t)ws >> 32) & 0xffffu, WS_USED, 0x00020000u};
  if (__builtin_amdgcn_readfirstlane(threadIdx.x) >= 256) recur_p1(lds, rw, rs, mem0, Wq, Wk, Wv, out, ws, wg TRARG DLYA);
  else recur_at(lds, rw, rs, Wo, ws, wg TRARG DLYA);
}

#define HTR(i)
#define XSLP 16
#define PD1 8
#define ODS 3
#define OB0 17
#define OB1 20
#define OB2 14
#define OPR 2
#define OLIM 16
#define ON0 4
#define CPF 1
#define OFA 11
#define FSLP 16
template <int SLP>
DEVI unsigned poll_flags(rsrc_t rw, unsigned off, unsigned f) {
#pragma nounroll
  for (int it = 0; it < HSPIN_CAP; ++it) {
    if (__all(f != 0u)) break;
    __builtin_amdgcn_s_sleep(SLP);
    f = ld_sc1_32(rw, off);
  }
  return f;
}
DEVI void after(unsigned f, int d) {
  const unsigned t = __builtin_amdgcn_readfirstlane(f) + (unsigned)d;
#pragma nounroll
  for (int k = 0; k < 4 && t != 1u + (unsigned)d; ++k) {
    const int r = min((int)(t - rt_now()), 4096);
    if (r < 4) break;
#pragma nounroll
    for (int i = r * 9 >> 5; i > 0; --i) __builtin_amdgcn_s_sleep(1);
    if (r < 40) break;
  }
}
DEVI void xattn_wave(rsrc_t rw, int s, int hq, unsigned hb) {
  int tx = threadIdx.x;
  asm volatile("" : "+v"(tx));
  const int l = tx & 63, g = l >> 4, lo = l & 15, h = hq * 8 + __builtin_amdgcn_readfirstlane(tx >> 6);
#pragma nounroll
  for (int it = 0; it < HSPIN_CAP; ++it) {
    const unsigned f = ld_sc1_32(rw, l < 32 ? OFF_CKV + (unsigned)(h * 64 + s) * 4u : cooff(h, hb) + (unsigned)(s * 16 + (l & 3) * 4));
    if (__all((f & 0xffffffu) == (l < 32 ? 0x40404u : 2u))) break;
    __builtin_amdgcn_s_sleep(XSLP);
  }
  asm volatile("" ::: "memory");
  const unsigned lv = (unsigned)l * 16u, so = tile8(s, h, hb);
  u32x4 bq[4][2], kx[8], vx[8];
#pragma unroll
  for (int ks = 0; ks < 4; ++ks) {
#pragma unroll
    for (int n = 0; n < 2; ++n) bq[ks][n] = ld_sc1s(rw, lv + ks * 1024u, OFF_Q + so + n * HALF_);
    kx[ks] = ld_sc1s(rw, lv + ks * 1024u, OFF_K + so);
    kx[4 + ks] = ld_sc1s(rw, lv + ks * 1024u, OFF_K + so + HALF_);
  }
#pragma unroll
  for (int te = 0; te < 8; ++te) vx[te] = ld_sc1s(rw, lv + (te & 3) * 1024u, OFF_VX + so + (te >> 2) * HALF_);
  constexpr float L2E = 1.4426950408889634f;
  float m1[2], s1[2];
  f32x4 os[8][2];
  auto seg_part = [&]() {
    f32x4 st[2][2];
#pragma unroll
    for (int i = 0; i < 4; ++i) st[i >> 1][i & 1] = f32x4{0.f, 0.f, 0.f, 0.f};
#pragma unroll
    for (int ks = 0; ks < 4; ++ks) {
      st[0][0] = mfu(kx[ks], bq[ks][0], st[0][0]);
      st[0][1] = mfu(kx[ks], bq[ks][1], st[0][1]);
      st[1][1] = mfu(kx[4 + ks], bq[ks][1], st[1][1]);
    }
#pragma unroll
    for (int n = 0; n < 2; ++n) {
      float sv[8], mx = -3.0e38f, sum = 0.f;
#pragma unroll
      for (int i = 0; i < 8; ++i) {
        sv[i] = 16 * (i >> 2) + 4 * g + (i & 3) <= 16 * n + lo ? rbf(rbf(st[i >> 2][n][i & 3]) * SCALE_) : rbf(-1000000000.0f);
        mx = fmaxf(mx, sv[i]);
      }
      mx = rows_max(mx);
#pragma unroll
      for (int i = 0; i < 8; ++i) { sv[i] = __builtin_amdgcn_exp2f((sv[i] - mx) * L2E); sum += sv[i]; }
      m1[n] = mx;
      s1[n] = rows_sum(sum);
      const u32x4 pb = u32x4{cvt2(sv[0], sv[1]), cvt2(sv[2], sv[3]), cvt2(sv[4], sv[5]), cvt2(sv[6], sv[7])};
#pragma unroll
      for (int te = 0; te < 8; ++te) os[te][n] = mfu(vx[te], pb, f32x4{0.f, 0.f, 0.f, 0.f});
    }
  };
  u32x4 km[4], vm[4];
#pragma unroll
  for (int i = 0; i < 4; ++i) km[i] = ld_sc1(rw, kmoff(s, h, hb) + (unsigned)(i * 512 + g * 128 + (lo & 7) * 16));
#pragma unroll
  for (int i = 0; i < 4; ++i) vm[i] = ld_sc1(rw, kmoff(s, h, hb) + 2048u + (unsigned)(i * 512 + (l & 31) * 16));
  seg_part();
  f32x4 sm[2];
#pragma unroll
  for (int ks = 0; ks < 4; ++ks)
#pragma unroll
    for (int n = 0; n < 2; ++n) sm[n] = mfu(km[ks], bq[ks][n], ks ? sm[n] : f32x4{0.f, 0.f, 0.f, 0.f});
  s16x4 pm[2];
  float cs[2];
#pragma unroll
  for (int n = 0; n < 2; ++n) {
    float sv[4], mx = m1[n], sum = 0.f;
#pragma unroll
    for (int r = 0; r < 4; ++r) {
      sv[r] = g < 2 ? rbf(rbf(sm[n][r]) * SCALE_) : -INFINITY;
      mx = fmaxf(mx, sv[r]);
    }
    mx = rows_max(mx);
#pragma unroll
    for (int r = 0; r < 4; ++r) { sv[r] = __builtin_amdgcn_exp2f((sv[r] - mx) * L2E); sum += sv[r]; }
    const float cr = __builtin_amdgcn_exp2f((m1[n] - mx) * L2E);
    const float inv = __builtin_amdgcn_rcpf(__builtin_fmaf(s1[n], cr, rows_sum(sum)));
    cs[n] = cr * inv;
    pm[n] = __builtin_bit_cast(s16x4, u32x2{cvt2(sv[0] * inv, sv[1] * inv), cvt2(sv[2] * inv, sv[3] * inv)});
  }
#pragma unroll
  for (int n = 0; n < 2; ++n)
#pragma unroll
    for (int a = 0; a < 4; ++a) {
      f32x4 o[2];
#pragma unroll
      for (int i = 0; i < 2; ++i)
        o[i] = __builtin_amdgcn_mfma_f32_16x16x16bf16_1k(__builtin_bit_cast(s16x4, u32x2{vm[a][2 * i], vm[a][2 * i + 1]}), pm[n], os[2 * a + i][n] * cs[n], 0, 0, 0);
      st_sc1(rw, ooff(s, h, hb) + n * HALF_ + (unsigned)(a * 1024 + l * 16),
             u32x4{cvt2x(o[0][0], o[0][1]), cvt2x(o[0][2], o[0][3]), cvt2x(o[1][0], o[1][1]), cvt2x(o[1][2], o[1][3])});
    }
  drain_vm();
  if (l < 4) st_sc1_32(rw, cooff(h, hb) + (unsigned)(s * 16 + l * 4), 1u);
}

typedef f32x2 cf;
DEVI cf cmul(cf a, cf b) { return __builtin_elementwise_fma(cf{a.y, a.y}, cf{-b.y, b.x}, cf{a.x, a.x} * b); }
DEVI cf mni(cf a) { return cf{a.y, -a.x}; }
DEVI cf wexp(float r) {
  float sn, cs;
  sincospif(r, &sn, &cs);
  return cf{cs, -sn};
}
DEVI void dft4(cf& a0, cf& a1, cf& a2, cf& a3) {
  const cf t0 = a0 + a2, t1 = a0 - a2, t2 = a1 + a3, t3 = mni(a1 - a3);
  a0 = t0 + t2; a1 = t1 + t3; a2 = t0 - t2; a3 = t1 - t3;
}
constexpr float RH_ = 0.70710678118654752f, C8_ = 0.92387953251128674f, S8_ = 0.38268343236508977f;
DEVI cf w8_1(cf a) { return (a + mni(a)) * RH_; }
DEVI cf w8_3(cf a) { return (mni(a) - a) * RH_; }
template <int ST>
DEVI void dft8(cf* v) {
  dft4(v[0], v[2 * ST], v[4 * ST], v[6 * ST]);
  dft4(v[ST], v[3 * ST], v[5 * ST], v[7 * ST]);
  const cf e[4] = {v[0], v[2 * ST], v[4 * ST], v[6 * ST]}, o[4] = {v[ST], w8_1(v[3 * ST]), mni(v[5 * ST]), w8_3(v[7 * ST])};
#pragma unroll
  for (int c = 0; c < 4; ++c) { v[c * ST] = e[c] + o[c]; v[(c + 4) * ST] = e[c] - o[c]; }
}
DEVI void dft16(cf* v) {
#pragma unroll
  for (int b = 0; b < 4; ++b) dft4(v[b], v[4 + b], v[8 + b], v[12 + b]);
  v[5] = cmul(v[5], cf{C8_, -S8_}); v[6] = w8_1(v[6]); v[7] = cmul(v[7], cf{S8_, -C8_});
  v[9] = w8_1(v[9]); v[10] = mni(v[10]); v[11] = w8_3(v[11]);
  v[13] = cmul(v[13], cf{S8_, -C8_}); v[14] = w8_3(v[14]); v[15] = cmul(v[15], cf{-C8_, S8_});
  cf y[16];
#pragma unroll
  for (int c = 0; c < 4; ++c) {
    dft4(v[4 * c], v[4 * c + 1], v[4 * c + 2], v[4 * c + 3]);
#pragma unroll
    for (int d = 0; d < 4; ++d) y[c + 4 * d] = v[4 * c + d];
  }
#pragma unroll
  for (int i = 0; i < 16; ++i) v[i] = y[i];
}
template <int N, int ST>
DEVI void twiddle(cf* v, const cf* b) {
  cf w[8];
  w[1] = b[0]; w[2] = b[1]; w[4] = b[2];
  w[3] = cmul(w[1], w[2]); w[5] = cmul(w[1], w[4]); w[6] = cmul(w[2], w[4]); w[7] = cmul(w[3], w[4]);
#pragma unroll
  for (int i = 1; i < 8; ++i) v[i * ST] = cmul(v[i * ST], w[i]);
  if (N == 16) {
    v[8 * ST] = cmul(v[8 * ST], b[3]);
#pragma unroll
    for (int i = 1; i < 8; ++i) v[(8 + i) * ST] = cmul(v[(8 + i) * ST], cmul(w[i], b[3]));
  }
}
template <int N>
DEVI void wpow(cf* b, float r) {
#pragma unroll
  for (int i = 0; i < N; ++i) b[i] = wexp(r * (float)(1 << i));
}
DEVI u32x4 cc2u(cf a, cf b) { return u32x4{__float_as_uint(a.x), __float_as_uint(a.y), __float_as_uint(b.x), __float_as_uint(b.y)}; }

DEVI void fft2048(int t, char* lds, cf* v, const cf* b1, const cf* b2) {
  const int p = t & 3, j = t >> 2;
  dft16(v);
  twiddle<16, 1>(v, b1);
#pragma unroll
  for (int i = 0; i < 16; ++i) *(cf*)(lds + i * 4096 + t * 8) = v[i];
  bar();
#pragma unroll
  for (int i = 0; i < 16; ++i) v[i] = *(const cf*)(lds + (j >> 3) * 4096 + i * 256 + (t & 31) * 8);
  dft16(v);
  twiddle<16, 1>(v, b2);
  char* l2 = lds + 65536;
#pragma unroll
  for (int i = 0; i < 16; ++i) *(cf*)(l2 + (j & 7) * 8224 + i * 512 + (j >> 3) * 32 + p * 8) = v[i];
  bar();
#pragma unroll
  for (int i = 0; i < 16; ++i) v[i] = *(const cf*)(l2 + (i >> 1) * 8224 + (i & 1) * 4096 + t * 8);
  dft8<2>(v);
  dft8<2>(v + 1);
}
DEVI void fft_cols_fwd(char* lds, rsrc_t rw, int blk, unsigned hb) {
  int t = threadIdx.x;
  asm volatile("" : "+v"(t));
  const int p = t & 3, j = t >> 2;
  u32x4 xv[16];
#pragma unroll
  for (int i = 0; i < 16; ++i) xv[i] = ld_sc1s(rw, (j >> 4) * 65536 + p * 256 + (j & 15) * 16, OFF_XF + far8(32 * i + (blk >> 3), hb) + (blk & 7) * 1024u);
  cf b1[4], b2[4];
  wpow<4>(b1, (float)j * (1.f / 1024.f));
  wpow<4>(b2, (float)(j & 7) * (1.f / 64.f));
#pragma nounroll
  for (int it = 0; it < 4; ++it) {
    asm volatile("" : "+v"(t), "+v"(b1[0]), "+v"(b1[1]), "+v"(b1[2]), "+v"(b1[3]), "+v"(b2[0]), "+v"(b2[1]), "+v"(b2[2]), "+v"(b2[3]));
    cf v[16];
#pragma unroll
    for (int i = 0; i < 16; ++i) {
      v[i] = cf{__uint_as_float(xv[i][0] << 16), __uint_as_float(xv[i][0] & 0xffff0000u)};
      xv[i] = u32x4{xv[i][1], xv[i][2], xv[i][3], 0u};
    }
    bar();
    fft2048(t, lds, v, b1, b2);
#pragma unroll
    for (int i = 8; i < 16; ++i) *(cf*)(lds + (i - 8) * 4096 + t * 8) = v[i];
    bar();
#pragma unroll
    for (int i = 0; i < 8; ++i) {
      const int k = (t >> 2) + 128 * i;
      const cf z = v[i], c = *(const cf*)(lds + ((1024 - k) & 1023) * 32 + (t & 3) * 8);
      cf A = cf{z.x + c.x, z.y - c.y}, B = cf{z.y + c.y, c.x - z.x};
      if (k == 0) { A = cf{2.f * z.x, 2.f * c.x}; B = cf{2.f * z.y, 2.f * c.y}; }
      st_sc1(rw, OFF_T + far8a(128 * i, hb) + (unsigned)((t >> 2) * 16384 + blk * 256 + it * 64 + (t & 3) * 16), cc2u(A, B));
    }
  }
}
#define RPF 1
#define NTF 1
DEVI unsigned wpair(const u16* __restrict__ wr, const u16* __restrict__ wi, int o) {
  return NTF ? pack2(__builtin_nontemporal_load(wr + o), __builtin_nontemporal_load(wi + o)) : pack2(wr[o], wi[o]);
}
typedef unsigned u32x4u __attribute__((ext_vector_type(4), aligned(2)));
DEVI void fft_rows_ask(const u16* __restrict__ wr, const u16* __restrict__ wi, int row, int l, u32x4* raw, unsigned* wv) {
  const int rm = (2048 - row) & 2047;
#pragma unroll
  for (int i = 0; i < 4; ++i) raw[i] = __builtin_nontemporal_load((const u32x4u*)(((i & 1) ? wi : wr) + ((i & 2) ? rm * FB_ + 1 : row * FB_) + 8 * l));
  wv[16] = wpair(wr, wi, (l & 1) ? row * FB_ + 512 : rm * FB_);
}
DEVI void fft_rows_w(char* wl, int l, const u32x4* raw, unsigned* wv) {
#pragma unroll
  for (int i = 0; i < 4; ++i) {
    const u32x4 a = raw[i & 2], b = raw[(i & 2) + 1];
    const int k = 2 * (i & 1);
    *(u32x4*)(wl + (i >> 1) * 2048 + l * 32 + (i & 1) * 16) = u32x4{__builtin_amdgcn_perm(b[k], a[k], 0x05040100u), __builtin_amdgcn_perm(b[k], a[k], 0x07060302u),
                                                                   __builtin_amdgcn_perm(b[k + 1], a[k + 1], 0x05040100u), __builtin_amdgcn_perm(b[k + 1], a[k + 1], 0x07060302u)};
  }
#pragma unroll
  for (int i = 0; i < 16; ++i) wv[i] = *(const unsigned*)(wl + (i < 8 ? (l + 64 * i) * 4 : 2048 + (1023 - l - 64 * i) * 4));
  wv[17] = __builtin_amdgcn_update_dpp(0u, wv[16], 0x101, 0xf, 0xf, true);
}
DEVI cf wval(const unsigned* wv, int i, int l) {
  cf w = cf{__uint_as_float(wv[i] << 16), __uint_as_float(wv[i] & 0xffff0000u)};
  if (i >= 8) w.y = -w.y;
  if ((i & 7) == 0) {
    const unsigned e = wv[16 + (i >> 3)];
    const cf m = (w + cf{__uint_as_float(e << 16), __uint_as_float((e & 0xffff0000u) ^ (i ? 0u : 0x80000000u))}) * 0.5f;
    if (l == 0) w = m;
  }
  return w;
}
DEVI void fft_rows(char* lds, rsrc_t rw, const u16* __restrict__ wr, const u16* __restrict__ wi, int blk, unsigned hb) {
  int t = threadIdx.x;
  asm volatile("" : "+v"(t));
  int l = t & 63, row = blk * 8 + __builtin_amdgcn_readfirstlane(t >> 6);
  char* wl = lds + __builtin_amdgcn_readfirstlane(t >> 6) * 8704;
  unsigned wv[18];
  u32x4 in[8], raw[4];
  auto get = [&](int r) {
#pragma unroll
    for (int i = 0; i < 8; ++i) in[i] = ld_sc1s(rw, (l & 48) * 16 + (l & 3) * 64 + (l & 12) * 4 + (i & 3) * 1024, OFF_T + far8a(r, hb) + (i >> 2) * 4096u);
    fft_rows_ask(wr, wi, r, l, raw, wv);
  };
  get(row);
  cf b1[2][3], b2[4];
  wpow<3>(b1[0], (float)(2 * l) * (1.f / 512.f));
  wpow<3>(b1[1], (float)(2 * l + 1) * (1.f / 512.f));
  wpow<4>(b2, (float)(l & 7) * (1.f / 64.f));
#pragma nounroll
  for (int it = 0; it < 4; ++it, row += 256) {
  auto fresh = [&]() {
    asm volatile("" : "+v"(l), "+v"(b1[0][0]), "+v"(b1[0][1]), "+v"(b1[0][2]), "+v"(b1[1][0]), "+v"(b1[1][1]), "+v"(b1[1][2]), "+v"(b2[0]), "+v"(b2[1]), "+v"(b2[2]), "+v"(b2[3]));
  };
  fresh();
  const unsigned go = far8a(row, hb) + (unsigned)(l * 16);
  cf v[16];
#pragma unroll
  for (int i = 0; i < 8; ++i) {
    v[2 * i] = cf{__uint_as_float(in[i][0]), __uint_as_float(in[i][1])};
    v[2 * i + 1] = cf{__uint_as_float(in[i][2]), __uint_as_float(in[i][3])};
  }
  dft8<2>(v); dft8<2>(v + 1);
  twiddle<8, 2>(v, b1[0]); twiddle<8, 2>(v + 1, b1[1]);
#pragma unroll
  for (int i = 0; i < 8; ++i) *(u32x4*)(wl + i * 1088 + l * 16) = cc2u(v[2 * i], v[2 * i + 1]);
#pragma unroll
  for (int i = 0; i < 16; ++i) v[i] = *(const cf*)(wl + (l >> 3) * 1088 + i * 64 + (l & 7) * 8);
  dft16(v);
  twiddle<16, 1>(v, b2);
#pragma unroll
  for (int i = 0; i < 16; ++i) *(cf*)(wl + (l & 7) * 1040 + i * 64 + (l >> 3) * 8) = v[i];
#pragma unroll
  for (int i = 0; i < 16; ++i) v[i] = *(const cf*)(wl + (i >> 1) * 1040 + (i & 1) * 512 + l * 8);
  dft8<2>(v); dft8<2>(v + 1);
  fft_rows_w(wl, l, raw, wv);
  if (row == 0) {
    unsigned w1[18];
    fft_rows_ask(wr, wi, 1024, l, raw, w1);
    fft_rows_w(wl, l, raw, w1);
#pragma unroll
    for (int i = 0; i < 16; ++i) *(cf*)(wl + i * 512 + l * 8) = v[i];
#pragma unroll
    for (int i = 0; i < 16; ++i) {
      const cf c = *(const cf*)(wl + ((1024 - l - 64 * i) & 1023) * 8), wa = wval(wv, i, l), wb = wval(w1, i, l);
      v[i] = cmul(v[i], (wa + wb) * 0.5f) + cmul(cf{c.x, -c.y}, (wa - wb) * 0.5f);
    }
  } else {
#pragma unroll
    for (int i = 0; i < 16; ++i) v[i] = cmul(v[i], wval(wv, i, l));
  }
#pragma unroll
  for (int i = 0; i < 16; ++i) v[i] = cf{v[i].y, v[i].x};
  asm volatile("" : "+v"(v[0]), "+v"(v[8]), "+v"(v[15]) :: "memory");
  if (it < 3) get(row + 256);
  asm volatile("" ::: "memory");
  fresh();
  dft8<2>(v); dft8<2>(v + 1);
#pragma unroll
  for (int i = 0; i < 16; ++i) *(cf*)(wl + (i >> 1) * 1056 + (i & 1) * 512 + l * 8) = v[i];
#pragma unroll
  for (int i = 0; i < 16; ++i) v[i] = *(const cf*)(wl + (l & 7) * 1056 + i * 64 + (l >> 3) * 8);
  twiddle<16, 1>(v, b2);
  dft16(v);
#pragma unroll
  for (int i = 0; i < 16; ++i) *(cf*)(wl + (l >> 3) * 1088 + i * 64 + (l & 7) * 8) = v[i];
#pragma unroll
  for (int i = 0; i < 8; ++i) {
    const f32x4 a = *(const f32x4*)(wl + i * 1088 + l * 16);
    v[2 * i] = cf{a[0], a[1]}; v[2 * i + 1] = cf{a[2], a[3]};
  }
  twiddle<8, 2>(v, b1[0]); twiddle<8, 2>(v + 1, b1[1]);
  dft8<2>(v); dft8<2>(v + 1);
#pragma unroll
  for (int i = 0; i < 8; ++i) st_sc1(rw, OFF_T + go + i * 1024u, cc2u(cf{v[2 * i].y, v[2 * i].x}, cf{v[2 * i + 1].y, v[2 * i + 1].x}));
  }
}
DEVI void fft_cols_inv(char* lds, rsrc_t rw, int blk, unsigned hb) {
  int t = threadIdx.x;
  asm volatile("" : "+v"(t));
  const int j = t >> 2;
  u32x4 in[8];
  auto get = [&](int b) {
#pragma unroll
    for (int i = 0; i < 8; ++i) in[i] = ld_sc1s(rw, (unsigned)((t >> 2) * 16384 + (t & 3) * 16), OFF_T + far8a(128 * i, hb) + (unsigned)b * 64u);
  };
  get(blk);
  cf b1[4], b2[4];
  wpow<4>(b1, (float)j * (1.f / 1024.f));
  wpow<4>(b2, (float)(j & 7) * (1.f / 64.f));
#pragma nounroll
  for (int it = 0; it < 4; ++it, blk += 8) {
    asm volatile("" : "+v"(t), "+v"(b1[0]), "+v"(b1[1]), "+v"(b1[2]), "+v"(b1[3]), "+v"(b2[0]), "+v"(b2[1]), "+v"(b2[2]), "+v"(b2[3]));
    cf v[16];
#pragma unroll
    for (int i = 0; i < 8; ++i) {
      const f32x4 a = __builtin_bit_cast(f32x4, in[i]);
      cf m = cf{a[2] - a[1], a[0] + a[3]};
      v[i] = cf{a[1] + a[2], a[0] - a[3]};
      if (i == 0 && t < 4) { v[0] = cf{a[2], a[0]}; m = cf{a[3], a[1]}; }
      *(cf*)(lds + 65536 + i * 4096 + t * 8) = m;
    }
    asm volatile("" : "+v"(v[0]), "+v"(v[1]), "+v"(v[2]), "+v"(v[3]), "+v"(v[4]), "+v"(v[5]), "+v"(v[6]), "+v"(v[7]) :: "memory");
    if (it < 3) get(blk + 8);
    bar();
#pragma unroll
    for (int i = 8; i < 16; ++i) v[i] = *(const cf*)(lds + 65536 + ((2048 - 128 * i - (t >> 2)) & 1023) * 32 + (t & 3) * 8);
    fft2048(t, lds, v, b1, b2);
    const float sc = 0.5f / ((float)S_ * (float)D_);
#pragma unroll
    for (int i = 0; i < 16; ++i) *(unsigned*)(lds + i * 2048 + t * 4) = cvt2(v[i].y * sc, v[i].x * sc);
    bar();
#pragma unroll
    for (int i = 0; i < 4; ++i) st_sc1(rw, bfoff(blk, i * 512, hb) + (unsigned)t * 16u, *(const u32x4*)(lds + i * 8192 + t * 16));
  }
}

DEVI void oproj_worker(char* lds, rsrc_t rw, const u16* __restrict__ Wo, int set, int c, int q, u16* __restrict__ out, unsigned hb DLYARG
) {
  DLYI(0x300);
  int t = threadIdx.x;
  asm volatile("" : "+v"(t));
  const int l = t & 63, w = __builtin_amdgcn_readfirstlane(t >> 6), g = l >> 4, lo = l & 15, hh = 4 * q + (w >> 1);
  constexpr int sstep = 4;
  const int s0 = set < 3 ? set - ON0 : 3 * ON0 + 3;
  auto seg = [&](int s) { return s >= 3 * ON0 ? s : (s < s0 ? 0 : ((s - s0) >> 2) * 3 + set); };
  u32x4 a[4][4][2];
  __syncthreads();
  {
    const u16* gp = Wo + (size_t)(hh * DH_ + g) * D_ + c * 256 + (w & 1) * 128 + (lo ^ (4 * g)) * 8;
    auto ask = [&](int p) {
#pragma unroll
      for (int i = 0; i < 4; ++i)
        __builtin_amdgcn_global_load_lds((const unsigned*)(gp + (size_t)(16 * p + 4 * i) * D_), (unsigned*)(lds + p % 5 * 32768 + w * 4096 + i * 1024 + l * 16), 16, 0, NTO);
      SCHED();
    };
#pragma unroll
    for (int p = 0; p < 5; ++p) ask(p);
    const char* pp = lds + (w >> 2) * 4096 + (4 * g + (lo >> 2)) * 256 + ((4 * (w & 3)) ^ lo) * 16;
#pragma unroll
    for (int n = 0; n < 8; ++n) {
      asm volatile("s_waitcnt vmcnt(%0) lgkmcnt(0)\n s_barrier" :: "n"(4 * (n == 0 ? 4 : (n < 5 ? 3 : 7 - n))) : "memory");
      if (n >= 1 && n < 4) ask(n + 4);
#pragma unroll
      for (int i = 0; i < 8; ++i) {
        const u32x2 x0 = tr_quad(pp + n % 5 * 32768 + (i >> 1) * 8192 + (i & 1) * 8);
        a[i >> 1][n >> 1][i & 1][2 * (n & 1)] = x0[0];
        a[i >> 1][n >> 1][i & 1][2 * (n & 1) + 1] = x0[1];
      }
      SCHED();
    }
    bar();
  }
  const unsigned vo = (unsigned)l * 16u;
  const unsigned fo = cooff(4 * q, hb) + (unsigned)(((l >> 2) & 3) * 1024 + (l & 3) * 4);
  auto flags = [&](int s) { return ld_sc1_32(rw, fo + (unsigned)((l & 16) ? seg(s + sstep) : seg(s)) * 16u); };
  auto fbits = [&](int s, unsigned f) {
    const unsigned m = (unsigned)__builtin_amdgcn_ballot_w64((f & 1u) != 0u);
    return ((m & 0xffffu) == 0xffffu && s < NL_ ? 1u : 0u) | (m == 0xffffffffu && s + sstep < NL_ ? 2u : 0u);
  };
  auto wait_b = [&](int s) {
#pragma nounroll
    for (int it = 0; it < HSPIN_CAP; ++it) {
      if (__all((ld_sc1_32(rw, cooff(hh, hb) + (unsigned)(seg(s) * 16 + (l & 3) * 4)) & 1u) != 0u)) break;
      __builtin_amdgcn_s_sleep(XSLP);
    }
    asm volatile("" ::: "memory");
  };
  auto load_b = [&](int s, int par, int i, unsigned vd) {
    auto* dst = (__attribute__((address_space(3))) void*)(LDS3 + par * 32768 + w * 4096);
    const unsigned so = ooff(seg(s), hh, hb) + (w & 1) * HALF_;
    if (i == 0) __builtin_amdgcn_raw_ptr_buffer_load_lds(rw, dst, 16, vd, so, 0, 16);
    if (i == 1) __builtin_amdgcn_raw_ptr_buffer_load_lds(rw, dst, 16, vd, so, 1024, 16);
    if (i == 2) __builtin_amdgcn_raw_ptr_buffer_load_lds(rw, dst, 16, vd, so, 2048, 16);
    if (i == 3) __builtin_amdgcn_raw_ptr_buffer_load_lds(rw, dst, 16, vd, so, 3072, 16);
  };
  auto pxs = [&](int slot) { return OFF_PX + far8a((slot * 4 + c) * 16 + (w >> 2), hb) + (unsigned)(w & 3) * 2048u; };
  auto pxo = [&](int s) { return pxs(s < FS_ ? s & 31 : s - FS_ + 32); };
  for (int s = FS_ + (set + sstep - FS_ % sstep) % sstep; s < NL_; s += sstep) {
    st_sc1s(rw, vo, pxo(s) + q * 32768u, u32x4{POISON, POISON, POISON, POISON});
    st_sc1s(rw, vo + 1024u, pxo(s) + q * 32768u, u32x4{POISON, POISON, POISON, POISON});
    SCHED();
    asm volatile("s_nop 1");
    SCHED();
  }
  const unsigned sv = (unsigned)((lo >> 2) * 256 + g * 64 + (lo & 3) * 16);
  auto flag = [&](int s) { if (t == 64) st_sc1_32(rw, OFF_CPX + (unsigned)(seg(s) * 4 + c) * 128u + q * 4u, 1u); };
  float* gl = (float*)(lds + 132096);
  u32x4 pv[2];
  float gv;
  unsigned bb;
  auto pf = [&](int s) { return ld_sc1_32(rw, OFF_CPX + (unsigned)(seg(s) * 4 + c) * 128u + (l & 7) * 4u); };
  auto fin_a = [&](int s, bool ok) {
    if (!ok && s < FS_) {
#pragma nounroll
      for (int it = 0; it < HSPIN_CAP; ++it) {
        if (__all(pf(s) == 1u)) break;
        __builtin_amdgcn_s_sleep(2);
      }
    }
    int ll = l;
    asm volatile("" : "+v"(ll));
    gv = gl[seg(s) * 4 + (ll & 3)];
    bb = __builtin_amdgcn_raw_buffer_load_b32(rw, (ll & 12) * 16384 + (ll & 3) * 16 + (ll >> 4) * 4, bfoff(32 * c + 4 * w, seg(s) * SEG_ + 4 * q, hb), 16);
    if (s >= FS_) __builtin_amdgcn_s_sleep(PD1);
#pragma nounroll
    for (int it = 0; it < HSPIN_CAP; ++it) {
      asm volatile("" ::: "memory");
#pragma unroll
      for (int k = 0; k < 2; ++k) pv[k] = ld_sc1s(rw, (ll >> 4) * 32768 + (ll & 15) * 16, pxo(s) + k * 131072u + q * 256u);
      if (s < FS_ || !bad(umax5(pv[1], umax5(pv[0], 0u)))) break;
      __builtin_amdgcn_s_sleep(1);
    }
  };
  auto fin_b = [&](int s) {
    int ll = l;
    asm volatile("" : "+v"(ll));
    const unsigned s1 = (ll & 2) ? 0u : 0x3f80u << (16 * (ll & 1));
    const u32x4 sel = u32x4{(ll & 12) == 0 ? s1 : 0u, (ll & 12) == 4 ? s1 : 0u, (ll & 12) == 8 ? s1 : 0u, (ll & 12) == 12 ? s1 : 0u};
    const f32x4 v = mfu(sel, pv[1], mfu(sel, pv[0], f32x4{0.f, 0.f, 0.f, 0.f}));
    const float omg = rbf(1.0f - gv);
    const unsigned y = cvt2(omg * __uint_as_float(bb << 16), omg * __uint_as_float(bb & 0xffff0000u));
    const unsigned av = cvt2(v[0], v[1]);
    const unsigned x = cvt2(gv * __uint_as_float(av << 16), gv * __uint_as_float(av & 0xffff0000u));
    const unsigned r = cvt2(__uint_as_float(x << 16) + __uint_as_float(y << 16), __uint_as_float(x & 0xffff0000u) + __uint_as_float(y & 0xffff0000u));
    __builtin_amdgcn_raw_buffer_store_b32(r, __builtin_amdgcn_make_buffer_rsrc(out, 0, S_ * D_ * 2, 0x00020000), (ll & 3) * (D_ * 2) + (ll & 12) * 4 + (ll >> 4) * 4,
                                          (unsigned)((seg(s) * SEG_ + 4 * q) * (D_ * 2) + (256 * c + 32 * w) * 2), OAUX);
  };
  f32x4 acc[2][2];
  auto send = [&](unsigned po) {
    u32x4 u[2];
#pragma unroll
    for (int i = 0; i < 8; ++i) u[i >> 2][i & 3] = cvt2x(acc[i >> 2][(i >> 1) & 1][2 * (i & 1)], acc[i >> 2][(i >> 1) & 1][2 * (i & 1) + 1]);
    asm volatile("" : "+v"(u[0]), "+v"(u[1]));
    st_sc1s(rw, sv, po + q * 32768u, u[0]);
    st_sc1s(rw, sv + 1024u, po + q * 32768u, u[1]);
  };
  auto touch = [&](int s, bool on) {
    if (w == 5)
      __builtin_amdgcn_raw_ptr_buffer_load_lds(rw, (__attribute__((address_space(3))) void*)(LDS3 + 131328), 4, on ? (l >> 5) * HALF_ + (l & 31) * 128u : 0x80000000u,
                                               ooff(s, 4 * q + c, hb), 0, 16);
  };
  auto rdo = [](int j) { return (j >> 3) * 8192 + ((j & 1) * 4 + ((j >> 1) & 3)) * 1024; };
#pragma unroll
  for (int k = 0; k < 2; ++k) {
    wait_b(s0 + k * sstep);
#pragma unroll
    for (int i = 0; i < 4; ++i) load_b(s0 + k * sstep, k, i, vo);
  }
  unsigned* vote = (unsigned*)(lds + 131072);
  unsigned fl = flags(s0 + 2 * sstep);
  drain_vm();
  wg_wait_s(rw, OFF_CF + 256u, 32u);
  if (w == 0) *(u32x4*)(gl + 4 * l) = ld_sc1(rw, OFF_G + (unsigned)(l * SEG_ + 4 * q) * 4u);
  HTR(10);
  int par = 0, s1 = s0;
  {
    constexpr unsigned NOW = 0x80000000u;
    const unsigned s1_ = (l & 2) ? 0u : 0x3f80u << (16 * (l & 1));
    const u32x4 sel = u32x4{(l & 12) == 0 ? s1_ : 0u, (l & 12) == 4 ? s1_ : 0u, (l & 12) == 8 ? s1_ : 0u, (l & 12) == 12 ? s1_ : 0u};
    const unsigned ob = (l & 12) * 16384 + (l & 3) * 16 + g * 4, op = g * 32768 + lo * 16, oo = (l & 3) * (D_ * 2) + (l & 12) * 4 + g * 4;
    const unsigned ofx = l < 32 ? fo : OFF_CPX + (unsigned)(c * 128 + (l & 7) * 4);
    const rsrc_t ro = __builtin_amdgcn_make_buffer_rsrc(out, 0, S_ * D_ * 2, 0x00020000);
    u32x4 b[8];
#pragma unroll
    for (int j = 0; j < 8; ++j) b[j] = *(const u32x4*)(lds + l * 16 + rdo(j));
    unsigned vt, px[4] = {0u, 0u, 0u, 0u};
    pv[0] = pv[1] = u32x4{0u, 0u, 0u, 0u};
    gv = 0.f; bb = 0u;
    int rn = 0, sg[9];
#pragma unroll
    for (int i = 0; i < 9; ++i) sg[i] = seg(s0 + (i - 4) * sstep);
    if (OPR == 1 && w >= 4) __builtin_amdgcn_s_setprio(1);
    if (OPR == 2 && w < 4) __builtin_amdgcn_s_setprio(1);
#pragma nounroll
    do {
      const int s = s1;
      const char* bp = lds + par * 32768 + l * 16;
      const char* bn = lds + ((par + 1) & 3) * 32768 + l * 16;
      unsigned vd, so, x, y;
      f32x4 v;
      SCHED();
#pragma unroll
      for (int j = 0; j < 32; ++j) {
        if (j >= OB1 && j < OB1 + 4 * ODS && (j - OB1) % ODS == 0) {
          auto* dst = (__attribute__((address_space(3))) void*)(LDS3 + (par ^ 2) * 32768 + w * 4096);
          if (j == OB1) __builtin_amdgcn_raw_ptr_buffer_load_lds(rw, dst, 16, vd, so, 0, 16);
          if (j == OB1 + ODS) __builtin_amdgcn_raw_ptr_buffer_load_lds(rw, dst, 16, vd, so, 1024, 16);
          if (j == OB1 + 2 * ODS) __builtin_amdgcn_raw_ptr_buffer_load_lds(rw, dst, 16, vd, so, 2048, 16);
          if (j == OB1 + 3 * ODS) __builtin_amdgcn_raw_ptr_buffer_load_lds(rw, dst, 16, vd, so, 3072, 16);
        }
#pragma unroll
        for (int ct = 0; ct < 2; ++ct) acc[j & 1][ct] = mfu(a[j >> 3][(j >> 1) & 3][ct], b[j & 7], j >= 2 ? acc[j & 1][ct] : f32x4{0.f, 0.f, 0.f, 0.f});
        b[j & 7] = *(const u32x4*)((j < 24 ? bp : bn) + rdo((j + 8) & 31));
        if (j == 2) { px[3] = px[2]; px[2] = px[1]; px[1] = px[0]; px[0] = pxo(s); asm volatile("" : "+s"(px[0])); }
        if (j == 5) { so = ooff(sg[6], hh, hb) + (w & 1) * HALF_; asm volatile("" : "+s"(so)); }
        if (j == OB2 - 5) {
          v = mfu(sel, pv[1], mfu(sel, pv[0], f32x4{0.f, 0.f, 0.f, 0.f}));
          const float omg = rbf(1.0f - gv);
          y = cvt2(omg * __uint_as_float(bb << 16), omg * __uint_as_float(bb & 0xffff0000u));
          asm volatile("" : "+v"(y));
        }
        if (j == OB2 - 3) {
          const unsigned av = cvt2(v[0], v[1]);
          x = cvt2(gv * __uint_as_float(av << 16), gv * __uint_as_float(av & 0xffff0000u));
          asm volatile("" : "+v"(x));
        }
        if (j == OB2 - 2)
          __builtin_amdgcn_raw_buffer_store_b32(cvt2(__uint_as_float(x << 16) + __uint_as_float(y << 16), __uint_as_float(x & 0xffff0000u) + __uint_as_float(y & 0xffff0000u)),
                                                ro, oo | (rn >= 4 ? 0u : NOW), (unsigned)((sg[0] * SEG_ + 4 * q) * (D_ * 2) + (256 * c + 32 * w) * 2), OAUX);
        SCHED();
        if (j == OB0 - 9 && w == 0) {
          unsigned fb = fbits(s + 2 * sstep, fl);
          if (__builtin_expect(!(fb & 1u) && s < OLIM, 0)) {
#pragma nounroll
            for (int it = 0; it < HSPIN_CAP && !(fb & 1u); ++it) {
              __builtin_amdgcn_s_sleep(8);
              fb = fbits(s + 2 * sstep, flags(s + 2 * sstep));
            }
          }
          vote[par] = fb;
          if (__builtin_expect((__builtin_amdgcn_ballot_w64(fl == 1u) >> 32) != 0xffffffffu && rn >= 3, 0)) {
#pragma nounroll
            for (int it = 0; it < HSPIN_CAP; ++it) {
              if (__all(pf(s - 3 * sstep) == 1u)) break;
              __builtin_amdgcn_s_sleep(2);
            }
          }
        }
        if (j == OB0 + 1) {
#pragma unroll
          for (int k = 0; k < 2; ++k) pv[k] = ld_sc1s(rw, op | (rn >= 3 ? 0u : NOW), px[3] + k * 131072u + q * 256u);
        }
        if (j == OB1 + 1) {
          gv = gl[sg[1] * 4 + (l & 3)];
          bb = __builtin_amdgcn_raw_buffer_load_b32(rw, ob, bfoff(32 * c + 4 * w, sg[1] * SEG_ + 4 * q, hb), 16);
        }
        if (j == OB0) {
          wait_vm<1>();
          __builtin_amdgcn_s_barrier();
          asm volatile("" ::: "memory");
          vt = vote[par];
          if (rn >= 1 && t == 64) st_sc1_32(rw, OFF_CPX + (unsigned)(sg[3] * 4 + c) * 128u + q * 4u, 1u);
        }
        if (j == OB0 && w == 0) fl = ld_sc1_32(rw, ofx + (l < 32 ? (unsigned)((l & 16) ? sg[8] : sg[7]) * 16u : (unsigned)sg[2] * 512u));
        if (j == OB1 - 1) {
          vt = __builtin_amdgcn_readfirstlane(vt);
          vd = (vt & 1u) ? vo : NOW;
        }
        if (j == OB1 + 3 * ODS + 1) touch(sg[7], vt & 2u);
      }
      send(px[0]);
      s1 += sstep;
#pragma unroll
      for (int i = 0; i < 8; ++i) sg[i] = sg[i + 1];
      sg[8] += sg[8] < 3 * ON0 ? 3 : 4;
      par = (par + 1) & 3;
      ++rn;
    } while (vt & 1u);
    if (w == 0) vote[par] = fbits(s1 + sstep, flags(s1 + sstep));
    asm volatile("" ::: "memory");
    __builtin_amdgcn_s_waitcnt(0x0f72);
    bar();
  }
  int np = min((s1 - s0) / sstep, 4);
#pragma nounroll
  for (int s = s1; s < NL_; s += sstep, par ^= 1) {
    const int nxt = s + sstep;
    const unsigned vt = __builtin_amdgcn_readfirstlane(vote[par]);
    const bool pre = (vt & 1u) != 0u;
    const unsigned vd = pre ? vo : 0x80000000u;
    const char* bp = lds + par * 32768 + l * 16;
    auto rd = [&](int j) { return *(const u32x4*)(bp + rdo(j)); };
    unsigned f;
    if (np >= 3) f = pf(s - 3 * sstep);
    u32x4 b[8];
#pragma unroll
    for (int j = 0; j < 8; ++j) b[j] = rd(j);
    if (np >= 2) flag(s - 2 * sstep);
    if (np >= 4) fin_b(s - 4 * sstep);
    SCHED();
#pragma unroll
    for (int j = 0; j < 32; ++j) {
      if (j < 4 * ODS && j % ODS == 0) load_b(nxt, par ^ 1, j / ODS, vd);
#pragma unroll
      for (int ct = 0; ct < 2; ++ct) acc[j & 1][ct] = mfu(a[j >> 3][(j >> 1) & 3][ct], b[j & 7], j >= 2 ? acc[j & 1][ct] : f32x4{0.f, 0.f, 0.f, 0.f});
      if (j + 8 < 32) b[j & 7] = rd(j + 8);
      SCHED();
      if (j == OFA) {
        if (np >= 3) fin_a(s - 3 * sstep, __all(f == 1u));
        if (w == 0) fl = flags(nxt + sstep);
        touch(seg(nxt + sstep), vt & 2u);
        SCHED();
      }
    }
    send(pxo(s));
    asm volatile("" ::: "memory");
    __builtin_amdgcn_s_waitcnt(0x0f72);
    asm volatile("" ::: "memory");
    if (w == 0) vote[par ^ 1] = fbits(nxt + sstep, fl);
    np = min(np + 1, 4);
    if (!pre) {
      if (np >= 4) fin_b(s - 3 * sstep);
      if (s - sstep < FS_) {
        if (s < FS_) drain_vm();
        bar();
        if (np >= 2) flag(s - sstep);
        if (s < FS_) flag(s);
      }
#pragma nounroll
      for (int k = min(np, 3) - 1; k >= 0; --k) { fin_a(s - k * sstep, false); fin_b(s - k * sstep); }
      np = 0;
      if (nxt >= NL_) break;
      if (nxt < FS_) {
        wait_b(nxt);
#pragma unroll
        for (int i = 0; i < 4; ++i) load_b(nxt, par ^ 1, i, vo);
        drain_vm();
      } else {
        const unsigned so = ooff(nxt, hh, hb) + (w & 1) * HALF_;
        const unsigned cm = cooff(hh, hb) + (unsigned)((nxt - 2) * 16 + (l & 3) * 4);
        after(poll_flags<FSLP>(rw, cm, ld_sc1_32(rw, cm)), (int)(nxt == NL_ - 1 ? dly[XTL] : dly[XT1]));
#pragma nounroll
        for (int it = 0; it < HSPIN_CAP; ++it) {
          asm volatile("" ::: "memory");
#pragma unroll
          for (int j = 0; j < 4; ++j) b[j] = ld_sc1s(rw, vo + j * 1024u, so);
          if (!bad4(b)) break;
          __builtin_amdgcn_s_sleep(1);
        }
#pragma unroll
        for (int j = 0; j < 4; ++j) *(u32x4*)(lds + (par ^ 1) * 32768 + w * 4096 + j * 1024 + l * 16) = b[j];
      }
    }
    bar();
  }
}

DEVI void helper_role(char* lds, char* ws, rsrc_t rw, const u16* __restrict__ x, const u16* __restrict__ Wq,
                      const u16* __restrict__ Wk, const u16* __restrict__ Wv, const u16* __restrict__ Wo,
                      const u16* __restrict__ wr, const u16* __restrict__ wi, const u16* __restrict__ gw, u16* __restrict__ out, int hw
                      ) {
  const int xg = hw & 7, loc = hw >> 3;
  const int q = xg >> 1, m = loc * 2 + (hw & 1);
  DLYI(0x30);
  HTR(0);
  const int xi = (m & 7) * 4 + q, l = threadIdx.x & 63, w = __builtin_amdgcn_readfirstlane(threadIdx.x >> 6);
  auto xld = [&](int i) {
    return __builtin_amdgcn_raw_buffer_load_b128(__builtin_amdgcn_make_buffer_rsrc((void*)x, 0, S_ * D_ * 2, 0x00020000),
                                                 (l & 7) * 2048 + w * 256 + (l >> 3) * 16 + (i & 2) * 64, (unsigned)(32 * (i >> 2) + xi) * 32768u + (i & 1) * 16384u, NTX);
  };
  auto xst = [&](int i, unsigned hb, u32x4 v) { st_sc1s(rw, (l >> 3) * 256 + (l & 7) * 16 + (i & 2) * 1024 + (i & 1) * 128, OFF_XF + far8((32 * (i >> 2) + xi) * 4 + (w >> 1), hb) + (w & 1) * 4096u, v); };
  unsigned hb;
  if (m < 24) {
    if (m >= 16) {
      static_assert((OFF_REC - OFF_CKV) % 512u == 0 && (OFF_REC - OFF_CKV) / 512u <= 512u, "shares");
      if (threadIdx.x < (OFF_REC - OFF_CKV) / 512u) st_sc1(rw, OFF_CKV + xi * ((OFF_REC - OFF_CKV) / 32u) + threadIdx.x * 16u, u32x4{0u, 0u, 0u, 0u});
      u32x4 xs[4];
#pragma unroll
      for (int i = 0; i < 4; ++i) xs[i] = xld(i);
      const u32x2 no = launch_no(ws);
#pragma unroll
      for (int i = 0; i < 4; ++i) xst(i, 0u, xs[i]);
      rec_put(rw, xi, no);
      asm volatile("" :: "v"(xs[3]));
      HTR(4);
      for (unsigned i = 0; i < (RSE ? 7u : dly[QDL]); ++i) __builtin_amdgcn_s_sleep(32);
    }
    if ((m >> 3) == 1) hb = proj_reg<false>(lds, ws, rw, Wv, 8 * q + (m & 7), 2, hw);
    else hb = proj_reg<true>(lds, ws, rw, m < 8 ? Wk : Wq, 8 * q + (m & 7), m < 8 ? 1 : 0, hw);
    HTR(2);
  } else {
    probe(ws, rw, 8 * q + m - 24);
    const u32x2 no = launch_no(ws);
    rec_put(rw, 32 + 8 * q + m - 24, no);
    HTR(4);
    for (unsigned i = 0; i < dly[XDL]; ++i) __builtin_amdgcn_s_sleep(32);
    NearMap nm;
    {
      u32x4 wv[2], xs[16];
#pragma unroll
      for (int i = 0; i < 16; ++i)
        xs[(i + 4) & 15] = __builtin_amdgcn_raw_buffer_load_b128(__builtin_amdgcn_make_buffer_rsrc((void*)x, 0, S_ * D_ * 2, 0x00020000), l * 16 + (i & 3) * 1024,
                                                                 (unsigned)(32 * (((i + 4) & 15) >> 2) + xi) * 32768u + w * 4096u, NTX);
#pragma unroll
      for (int i = 0; i < 2; ++i) wv[i] = *(const u32x4*)(gw + i * 512 + l * 8);
      nm.k = near_kind(rw, no);
      hb = far_bit(nm.k);
#pragma unroll
      for (int i = 4; i < 16; ++i) *(u32x4*)(lds + (i >> 2) * 32768 + (i & 1) * 16384 + l * 256 + ((2 * w + ((i >> 1) & 1)) ^ (l & 15)) * 16) = xs[i];
      __syncthreads();
      u32x4 xf[12];
#pragma unroll
      for (int i = 0; i < 12; ++i) xf[i] = *(const u32x4*)(lds + ((i >> 2) + 1) * 32768 + w * 4096 + (i & 3) * 1024 + (l >> 4) * 256 + ((l & 15) ^ ((i & 3) * 4 + (l >> 4))) * 16);
#pragma unroll
      for (int i = 0; i < 12; ++i) st_sc1s(rw, l * 16 + (i & 3) * 1024, OFF_XF + far8((32 * ((i >> 2) + 1) + xi) * 4 + (w >> 1), hb) + (w & 1) * 4096u, xf[i]);
      float gs = 0.f;
#pragma unroll
      for (int n = 0; n < 8; ++n) {
        float acc = 0.f;
#pragma unroll
        for (int i = 0; i < 8; ++i) {
          const unsigned a = xs[2 * n + (i >> 2)][i & 3], b = wv[i >> 2][i & 3];
          const unsigned pr = cvt2(__uint_as_float(a << 16) * __uint_as_float(b << 16), __uint_as_float(a & 0xffff0000u) * __uint_as_float(b & 0xffff0000u));
          acc += __uint_as_float(pr << 16) + __uint_as_float(pr & 0xffff0000u);
        }
#pragma unroll
        for (int o = 1; o < 8; o *= 2) acc += __shfl_xor(acc, o);
        acc = rows_sum(fold8(acc));
        if (l == n) gs = acc;
      }
      asm volatile("" :: "v"(xf[11]));
      wg_signal(ws, OFF_CF + 384u);
      if (l < 8) st_sc1_32(rw, OFF_G + (unsigned)((32 * (l >> 1) + xi) * 16 + 2 * w + (l & 1)) * 4u, __float_as_uint(rbf(1.0f / (1.0f + expf(-rbf(gs))))));
    }
    wg_wait_s(rw, OFF_CF + 384u, 32u);
    HTR(1);
    fft_cols_fwd(lds, rw, 8 * q + m - 24, hb);
    wg_signal(ws, OFF_CF);
    HTR(3);
    {
      unsigned tx = threadIdx.x;
      asm volatile("" : "+v"(tx));
      const unsigned tv = (tx >> 3) & 1;
      u32x4 p3 = u32x4{tv, tv, tv, tv}, p4 = u32x4{POISON, POISON, POISON, POISON};
      asm volatile("" : "+v"(p3), "+v"(p4));
#pragma unroll
      for (int i = 0; i < 6; ++i) st_sc1s(rw, (tx & 255u) * 16u, nm(3 + (i >> 1), 2 * (i & 1) + (w >> 2), NB3 + xi), p3);
#pragma unroll
      for (int i = FS_; i < NL_; ++i) st_sc1s(rw, (w >> 2) * HALF_ + (tx & 255u) * 16u, ooff(i, xi, hb), p4);
      drain_vm();
      asm volatile("" :: "v"(p3), "v"(p4));
    }
    wg_wait_s(rw, OFF_CF, 32u);
    fft_rows(lds, rw, wr, wi, xi, hb);
    wg_signal(ws, OFF_CF + 128u);
    HTR(5);
    xattn_wave(rw, m & 7, q, hb);
    HTR(11);
    wg_wait_s(rw, OFF_CF + 128u, 32u);
    fft_cols_inv(lds, rw, 32 * q + m - 24, hb);
    wg_signal(ws, OFF_CF + 256u);
    HTR(7);
  }
  oproj_worker(lds, rw, Wo, (m >> 3) ^ (m >> 4), m & 3, 2 * q + ((m >> 2) & 1), out, hb DLYA
);
  HTR(9);
}

static_assert(W3_OFF + 65536 <= SMEM_BYTES, "LDS");
__global__ __launch_bounds__(512, 1) void mega_kernel(const u16* __restrict__ x, const u16* __restrict__ mem0,
                                                      const u16* __restrict__ Wq, const u16* __restrict__ Wk,
                                                      const u16* __restrict__ Wv, const u16* __restrict__ Wo,
                                                      const u16* __restrict__ wr, const u16* __restrict__ wi,
                                                      const u16* __restrict__ gw, u16* __restrict__ out, char* ws) {
  char* lds = lds_;
  ws += (0u - (unsigned)(size_t)ws) & (SET_ - 1u);
  const rsrc_t rw = __builtin_amdgcn_make_buffer_rsrc(ws, 0, WS_USED, 0x00020000);
  const int idx = (blockIdx.x >> 3) * 4 + (blockIdx.x & 3);
  const bool isrec = (blockIdx.x & 4) == 0;
  const int rid = (blockIdx.x & 3) * 32 + (blockIdx.x >> 3), hid = (idx & 3) * 2 + ((idx >> 2) & 1) + (idx >> 3) * 8;
  if (__builtin_amdgcn_readfirstlane(threadIdx.x >> 6) == (isrec ? 7 : 3) && (blockIdx.x >> 3) >= 16) {
    u32x2 pc;
    unsigned n;
    asm volatile("s_getpc_b64 %0\n code_from: s_mov_b32 %1, code_to - code_from" : "=s"(pc), "=s"(n));
    asm volatile("s_mov_b32 m0, %2\n s_nop 0\n buffer_load_dword %0, %1, 0 offen lds"
                 :: "v"((((blockIdx.x >> 3) - 16) * 64 + (threadIdx.x & 63)) * 128), "s"(u32x4{pc[0], pc[1] & 0xffffu, n, 0x00020000u}),
                    "s"((unsigned)(size_t)LDS3 + (isrec ? 3u * 24576u : 131072u + 3u * 4096u)) : "memory");
  }
  if (isrec) {
    recur_role(lds, ws, rw, mem0, Wq, Wk, Wv, Wo, out, rid TRARG);
  } else helper_role(lds, ws, rw, x, Wq, Wk, Wv, Wo, wr, wi, gw, out, hid TRARG);
}

extern "C" void kernel_launch(void* const* d_in, const int* in_sizes, int n_in, void* d_out, int out_size, void* d_ws,
                              size_t ws_size, hipStream_t stream) {
  const u16* x = (const u16*)d_in[0];
  const u16* mem0 = (const u16*)d_in[1];
  const u16* Wq = (const u16*)d_in[2];
  const u16* Wk = (const u16*)d_in[3];
  const u16* Wv = (const u16*)d_in[4];
  const u16* Wo = (const u16*)d_in[5];
  const u16* wr = (const u16*)d_in[8];
  const u16* wi = (const u16*)d_in[9];
  const u16* gw = (const u16*)d_in[10];
  u16* out = (u16*)d_out;
  char* ws = (char*)d_ws;
  mega_kernel<<<256, 512, 0, stream>>>(x, mem0, Wq, Wk, Wv, Wo, wr, wi, gw, out, ws);
}
```

```cpp
#include <hip/hip_runtime.h>
#include <hip/hip_bf16.h>
#include <stdint.h>
#include <type_traits>

typedef short bf16x8 __attribute__((ext_vector_type(8)));
typedef short s16x4 __attribute__((ext_vector_type(4)));
typedef float f32x4 __attribute__((ext_vector_type(4)));
typedef unsigned u32x4 __attribute__((ext_vector_type(4)));
typedef unsigned u32x2 __attribute__((ext_vector_type(2)));
typedef unsigned short u16;

#define DEVI __device__ __forceinline__

DEVI float bf2f(u16 v) { return __uint_as_float(((unsigned)v) << 16); }
DEVI u16 f2bf(float f) { return __builtin_bit_cast(u16, (__bf16)f); }
DEVI float rbf(float f) { return bf2f(f2bf(f)); }
DEVI unsigned pack2(u16 a, u16 b) { return (unsigned)a | ((unsigned)b << 16); }
DEVI f32x4 mfma16(bf16x8 a, bf16x8 b, f32x4 c) { return __builtin_amdgcn_mfma_f32_16x16x32_bf16(a, b, c, 0, 0, 0); }

constexpr int S_ = 2048, D_ = 1024, H_ = 32, DH_ = 128, MEM_ = 8, SEG_ = 32, NSEG_ = 64, LK_ = 40;
constexpr int HD_ = H_ * DH_;
#define FSV 56
constexpr int NL_ = NSEG_ - 1;
constexpr int FS_ = FSV;
constexpr int XS_ = 8;
constexpr int FB_ = 513;
constexpr float SCALE_ = 0.08838834764831845f;

constexpr unsigned POISON = 0xffffffffu;
constexpr unsigned SET_ = 4u << 20, BLK_ = 32768u, NB3 = 0, NB2 = 32, NB1 = 48, NBLK = 52;
constexpr unsigned OFF_CKV = 24u << 20;
constexpr unsigned OFF_CO = OFF_CKV + 16384u;
constexpr unsigned OFF_CPX = OFF_CO + 65536u;
constexpr unsigned OFF_CF = OFF_CPX + 32768u;
constexpr unsigned OFF_REC = OFF_CF + 1024u;
constexpr unsigned OFF_EP = OFF_REC + 5120u;
constexpr unsigned COMM_BYTES = OFF_EP + 1024u;
struct NearMap {
  unsigned k;
  __device__ static unsigned pg(unsigned c) { return ((c & 1u) << 14) | ((c & 6u) << 11); }
#define BLAY 0
  __device__ static constexpr unsigned blk(unsigned n) { return BLAY ? ((n & 3u) << 15) | ((n & 28u) << 17) | ((n >> 5) << 17) : n * BLK_; }
  __device__ unsigned operator()(unsigned set, unsigned g, unsigned n) const { return set * SET_ + blk(n) + pg(k ^ g ^ (blk(n) >> 19)); }
};
DEVI unsigned far8a(unsigned u, unsigned hb) { return u * 16384u + (((u >> 7) ^ hb) & 1u) * 8192u; }
DEVI unsigned far8(unsigned u, unsigned hb) { return u * 16384u + (((u >> 7) ^ (u < 128u ? 0u : hb)) & 1u) * 8192u; }
#define NPROBE 8
DEVI unsigned long long clk_now() {
  unsigned long long t;
  asm volatile("s_waitcnt vmcnt(0) lgkmcnt(0)\n s_memtime %0\n s_waitcnt lgkmcnt(0)" : "=s"(t) :: "memory");
  return t;
}
constexpr unsigned OFF_G = COMM_BYTES;
constexpr unsigned OFF_Q = 28u << 20, OFF_K = 60u << 20, OFF_VX = 92u << 20, OFF_OT = 124u << 20;
constexpr unsigned OFF_T = 128u << 20;
constexpr unsigned OFF_PX = 144u << 20;
constexpr unsigned OFF_BF = 184u << 20;
constexpr unsigned OFF_XF = 192u << 20;
constexpr unsigned WS_USED = 200u << 20;
static_assert(FS_ >= NSEG_ - 8 && 32 + NL_ - FS_ <= 40 && XS_ == 8, "tails");
constexpr unsigned HALF_ = BLK_;
DEVI unsigned tile8(int s, int h, unsigned hb) { return (unsigned)(s * 8 + (h & 7)) * (2u * BLK_) + NearMap::pg((s < 2 ? 0u : hb >> 1) ^ (unsigned)(h >> 3) ^ (unsigned)s); }
DEVI unsigned ooff(int s, int h, unsigned hb) { return (s < FS_ ? OFF_Q : OFF_OT - (NSEG_ - 8) * H_ * 16384u) + tile8(s, h, hb); }
DEVI unsigned bfoff(int blk, int tok, unsigned hb) { return OFF_BF + far8a(blk * 4 + (tok >> 9), hb) + (unsigned)(tok & 511) * 16u; }
DEVI unsigned kmoff(int s, int h, unsigned hb) { return NearMap{hb >> 1}(3u, h >> 3, 64 + s * 8 + (h & 7)); }
DEVI unsigned cooff(int h, unsigned hb) { return OFF_CO + far8a(h >> 3, hb ^ 1u) + (unsigned)(h & 7) * 1024u; }

constexpr int SMEM_BYTES = 163840;
__shared__ __attribute__((aligned(16))) char lds_[SMEM_BYTES];
#define LDS3 ((__attribute__((address_space(3))) char*)lds_)

typedef __amdgpu_buffer_rsrc_t rsrc_t;
DEVI u32x4 ld_sc1(rsrc_t r, unsigned off) { return __builtin_amdgcn_raw_buffer_load_b128(r, off, 0, 16); }
DEVI u32x2 ld_sc1_64(rsrc_t r, unsigned off) { return __builtin_amdgcn_raw_buffer_load_b64(r, off, 0, 16); }
DEVI unsigned ld_sc1_32(rsrc_t r, unsigned off) { return __builtin_amdgcn_raw_buffer_load_b32(r, off, 0, 16); }
DEVI void st_sc1(rsrc_t r, unsigned off, u32x4 v) { __builtin_amdgcn_raw_buffer_store_b128(v, r, off, 0, 16); }
DEVI void st_sc1_64(rsrc_t r, unsigned off, u32x2 v) { __builtin_amdgcn_raw_buffer_store_b64(v, r, off, 0, 16); }
DEVI void st_sc1_32(rsrc_t r, unsigned off, unsigned v) { __builtin_amdgcn_raw_buffer_store_b32(v, r, off, 0, 16); }
DEVI void st_sc1_16(rsrc_t r, unsigned off, u16 v) { __builtin_amdgcn_raw_buffer_store_b16(v, r, off, 0, 16); }
DEVI u32x4 ld_sc1s(rsrc_t r, unsigned off, unsigned so) { return __builtin_amdgcn_raw_buffer_load_b128(r, off, so, 16); }
DEVI void st_sc1s(rsrc_t r, unsigned off, unsigned so, u32x4 v) { __builtin_amdgcn_raw_buffer_store_b128(v, r, off, so, 16); }
DEVI void st_sc1s_64(rsrc_t r, unsigned off, unsigned so, u32x2 v) { __builtin_amdgcn_raw_buffer_store_b64(v, r, off, so, 16); }
DEVI void st_sc1s_32(rsrc_t r, unsigned off, unsigned so, unsigned v) { __builtin_amdgcn_raw_buffer_store_b32(v, r, off, so, 16); }
#define HAUX 16
DEVI void hst(rsrc_t r, unsigned off, u32x4 v) { __builtin_amdgcn_raw_buffer_store_b128(v, r, off, 0, HAUX); }
DEVI void hst64(rsrc_t r, unsigned off, u32x2 v) { __builtin_amdgcn_raw_buffer_store_b64(v, r, off, 0, HAUX); }
DEVI void drain_vm() { asm volatile("s_waitcnt vmcnt(0)" ::: "memory"); }
DEVI void cnt_add(char* ws, unsigned off, unsigned v = 1u) { __hip_atomic_fetch_add((unsigned*)(ws + off), v, __ATOMIC_RELAXED, __HIP_MEMORY_SCOPE_AGENT); }

constexpr int HSPIN_CAP = 1 << 18;
DEVI void wg_wait_s(rsrc_t r, unsigned off, unsigned n) {
  if (threadIdx.x == 0) {
#pragma nounroll
    for (int it = 0; it < HSPIN_CAP; ++it) {
      if (ld_sc1_32(r, off) >= n) break;
      __builtin_amdgcn_s_sleep(8);
    }
  }
  asm volatile("" ::: "memory");
  __syncthreads();
}
DEVI void wg_signal(char* ws, unsigned off) {
  drain_vm();
  __syncthreads();
  if (threadIdx.x == 0) cnt_add(ws, off);
}

DEVI u32x2 launch_no(const char* ws) {
  u32x2 e;
  asm volatile("s_load_dwordx2 %0, %1, 0x0\n s_waitcnt lgkmcnt(0)" : "=s"(e) : "s"(ws + OFF_EP) : "memory");
  return u32x2{e[0] + 1u, e[1] + 0x9e3779b1u};
}
DEVI void rec_put(rsrc_t rw, int i, u32x2 no) {
  drain_vm();
  __syncthreads();
  if (threadIdx.x == 0) st_sc1_64(rw, OFF_REC + (unsigned)i * 16u, no);
}
DEVI u32x4 rec_wait(rsrc_t rw, u32x2 no, unsigned mask, unsigned lo = 0u, unsigned own = 0u) {
  u32x4 r;
#pragma nounroll
  for (int it = 0; it < HSPIN_CAP; ++it) {
    asm volatile("" ::: "memory");
    r = ld_sc1(rw, OFF_REC + (own && __lane_id() <= own ? __lane_id() : (__lane_id() & mask) + lo) * 16u);
    if (__all(r[0] == no[0] && r[1] == no[1])) break;
    __builtin_amdgcn_s_sleep(8);
  }
  asm volatile("" ::: "memory");
  return r;
}
DEVI unsigned near_kind(rsrc_t rw, u32x2 no) {
  const unsigned lat = rec_wait(rw, no, 63u)[2], ls2 = (lat & 0xffffu) + (lat >> 16), l = __lane_id() & 7u;
  unsigned cost = 0u;
#pragma unroll
  for (int i = 0; i < 4; ++i) cost += __shfl(ls2, 32 + 8 * i + (l ^ i));
  cost = cost * 8u + l;
#pragma unroll
  for (int i = 1; i < 8; i *= 2) cost = min(cost, (unsigned)__shfl_xor(cost, i));
  return (__builtin_amdgcn_readfirstlane(cost) & 7u) ^ 4u;
}
DEVI unsigned far_bit(unsigned kind) { return ((kind >> 2) ^ 1u) | (kind << 1); }
DEVI void probe(char* ws, rsrc_t rw, int i) {
  const NearMap nm{0u};
  if (threadIdx.x == 0) {
    char* p0 = ws + nm(4u, i & 7, 40u + (i >> 3));
    unsigned mn[2] = {0xffffu, 0xffffu};
#pragma unroll 1
    for (int k = 0; k < NPROBE; ++k) {
#pragma unroll
      for (int hf = 0; hf < 2; ++hf) {
        const unsigned long long t0 = clk_now();
        const unsigned v = __hip_atomic_fetch_add((unsigned*)(p0 + hf * 2048), 1u, __ATOMIC_RELAXED, __HIP_MEMORY_SCOPE_AGENT);
        asm volatile("" :: "v"(v));
        const unsigned long long t1 = clk_now();
        mn[hf] = min(mn[hf], (unsigned)(t1 - t0));
      }
    }
    st_sc1_32(rw, OFF_REC + (unsigned)(32 + i) * 16u + 8u, mn[0] | (mn[1] << 16));
  }
}

template <int N>
DEVI void wait_vm() { asm volatile("s_waitcnt vmcnt(%0)" :: "n"(N) : "memory"); }
#define BPR 2
#define ISG 3
#define ESG 4
#define NTW 0
#define RN 1
#define NTO 0
#define OAUX 0
#define NTX 2
#define SCHED() __builtin_amdgcn_sched_barrier(0)
DEVI u32x4 rsrc4(const void* p, unsigned bytes) { return u32x4{(unsigned)(size_t)p, (unsigned)((size_t)p >> 32) & 0xffffu, bytes, 0x00020000u}; }
DEVI void bar() { asm volatile("s_waitcnt lgkmcnt(0)\n s_barrier" ::: "memory"); }
DEVI u32x2 tr_quad(const char* p) {
  return __builtin_bit_cast(u32x2, __builtin_amdgcn_ds_read_tr16_b64_v4i16((__attribute__((address_space(3))) s16x4*)(p)));
}
DEVI unsigned cvt2(float a, float b) {
  typedef float f32x2_ __attribute__((ext_vector_type(2)));
  typedef __bf16 bf16x2_ __attribute__((ext_vector_type(2)));
  return __builtin_bit_cast(unsigned, __builtin_convertvector(f32x2_{a, b}, bf16x2_));
}
DEVI unsigned cvt2x(float a, float b) { return min(cvt2(a, b), POISON - 1u); }
DEVI f32x4 mfu(u32x4 a, u32x4 b, f32x4 c) { return mfma16(__builtin_bit_cast(bf16x8, a), __builtin_bit_cast(bf16x8, b), c); }
#define PTR(i)
#define PTB(i)
template <bool swap>
DEVI unsigned proj_reg(char* lds, char* ws, rsrc_t rw, const u16* __restrict__ W, int h, int mat, int hw) {
  constexpr int nseg = NSEG_;
  const int rot = mat ? 0 : XS_;
  auto sgm = [&](int r) { return (r & 32) | ((r + ((r & 32) ? 0 : rot)) & 31); };
  int t = threadIdx.x;
  asm volatile("" : "+v"(t));
  const int l = t & 63, w = __builtin_amdgcn_readfirstlane(t >> 6), g = l >> 4, lo = l & 15, wn = w & 3, wk = w >> 2;
  const u32x4 rs = rsrc4(ws, WS_USED);
  const unsigned vo = (unsigned)l * 16u;
  auto wswz = [](int row) { return ((row & 3) | (((row >> 3) & 1) << 2)) << 1; };
  u32x4 b[16][2];
  const unsigned go = (unsigned)((512 * wk + 16 * wn + g) * HD_ + h * DH_) * 2u;
  const unsigned gv[2] = {go + (unsigned)((lo ^ wswz(g)) * 16), go + (unsigned)((lo ^ wswz(8 + g)) * 16)};
  auto ask = [&](int p) {
#pragma unroll
    for (int i = 0; i < 4; ++i)
      __builtin_amdgcn_global_load_lds((const unsigned*)((const char*)W + gv[i >> 1] + (size_t)(64 * p + 4 * i) * HD_ * 2), (unsigned*)(lds + (p + 4) % 5 * 32768 + w * 4096 + i * 1024 + l * 16), 16, 0, NTW);
    SCHED();
  };
#pragma unroll
  for (int p = 0; p < 5; ++p) ask(p);
  const u32x2 no = launch_no(ws);
  const char* bp = lds + (64 * wk + 8 * g + (lo >> 2)) * 256 + (l & 1) * 8;
  const int fq2 = wswz(8 * g + (lo >> 2));
  auto take = [&](int p) {
#pragma unroll
    for (int ks = 0; ks < 2; ++ks)
#pragma unroll
      for (int ct = 0; ct < 2; ++ct) {
        const char* p0 = bp + (p + 4) % 5 * 32768 + ks * 8192 + (((4 * wn + 2 * ct + ((l & 3) >> 1)) ^ fq2) * 16);
        const u32x2 x0 = tr_quad(p0), x1 = tr_quad(p0 + 1024);
        b[2 * p + ks][ct] = u32x4{x0[0], x0[1], x1[0], x1[1]};
      }
    SCHED();
  };
  unsigned hb = 0u;
  auto dma = [&](int i, int pc, auto&& mid) {
    auto* dst = (__attribute__((address_space(3))) void*)(LDS3 + ((i + 2) & 3) * 32768 + pc * 4096);
    const unsigned so = OFF_XF + far8(sgm(i >> 1) * 8 + (i & 1) * 4 + (pc >> 1), hb) + (unsigned)(pc & 1) * 4096u;
    const unsigned vd = i < 2 * nseg ? vo : 0x80000000u;
    __builtin_amdgcn_raw_ptr_buffer_load_lds(rw, dst, 16, vd, so, 0, 16);
    mid(0);
    __builtin_amdgcn_raw_ptr_buffer_load_lds(rw, dst, 16, vd, so, 1024, 16);
    mid(1);
    __builtin_amdgcn_raw_ptr_buffer_load_lds(rw, dst, 16, vd, so, 2048, 16);
    mid(2);
    __builtin_amdgcn_raw_ptr_buffer_load_lds(rw, dst, 16, vd, so, 3072, 16);
    mid(3);
  };
  auto none = [](int) {};
  u32x4 f[1];
#pragma unroll
  for (int n = 0; n < 8; ++n) {
    asm volatile("s_waitcnt vmcnt(%0) lgkmcnt(0)\n s_barrier" :: "n"(4 * (n == 0 ? 4 : (n < 5 ? 3 : 7 - n)) + (n > RN && n < RN + 4 ? 1 : 0) + (n > RN + 4 ? 8 : 0)) : "memory");
    if (n == RN) asm volatile("buffer_load_dwordx4 %0, %1, %2, %3 offen sc1" : "=&v"(f[0]) : "v"(vo & 511u), "s"(rs), "s"(OFF_REC) : "memory");
    if (n >= 1 && n < 4) ask(n + 4);
    if (n == RN + 4) {
      asm volatile("" : "+v"(f[0]));
      if (!__all(f[0][0] == no[0] && f[0][1] == no[1])) rec_wait(rw, no, 31u);
      dma(0, w, none); dma(1, w, none);
      SCHED();
    }
    if (n == 7) PTR(17);
    take(n);
  }
  bar();
  const u32x4 zero4 = u32x4{0u, 0u, 0u, 0u};
  if (wk) {
    dma(2, wn, none);
    st_sc1(rw, 0x80000000u, zero4);
    if (!swap) st_sc1(rw, 0x80000010u, zero4);
    wait_vm<swap ? 5 : 6>();
  } else wait_vm<0>();
  __builtin_amdgcn_s_waitcnt(0xc07f);
  __builtin_amdgcn_s_barrier();
  PTR(19);
  const unsigned lb = (unsigned)(size_t)LDS3 + (unsigned)(l * 16);
  const unsigned ab = lb + (unsigned)(wk * 16384), sc = lb + 131072u + (unsigned)(wn * 2048);
  u32x4 a[8];
  auto rd = [&](int j, unsigned ad) { asm volatile("ds_read_b128 %0, %1 offset:%2" : "=v"(a[j & 7]) : "v"(ad), "n"((j & 15) * 1024) : "memory"); };
  if (wk == 0) {
#pragma unroll
    for (int j = 0; j < 8; ++j) rd(j, ab + 65536u);
    asm volatile("s_waitcnt lgkmcnt(0)" : "+v"(a[0]), "+v"(a[1]), "+v"(a[2]), "+v"(a[3]), "+v"(a[4]), "+v"(a[5]), "+v"(a[6]), "+v"(a[7]));
#pragma nounroll
    for (int k = 0; k < 2 * nseg; ++k) {
      const unsigned a0 = ab + (unsigned)(((k + 2) & 3) * 32768), a1 = ab + (unsigned)(((k + 3) & 3) * 32768);
      asm volatile("" :: "v"(a0), "v"(a1));
      wait_vm<8>();
      __builtin_amdgcn_s_setprio(1);
      __builtin_amdgcn_s_barrier();
      f32x4 c[2];
      SCHED();
#pragma unroll
      for (int j = 0; j < 16; ++j) {
#pragma unroll
        for (int ct = 0; ct < 2; ++ct) {
          const f32x4 z = j ? c[ct] : f32x4{0.f, 0.f, 0.f, 0.f};
          c[ct] = swap ? mfu(b[j][ct], a[j & 7], z) : mfu(a[j & 7], b[j][ct], z);
        }
        SCHED();
        rd(j + 8, j < 8 ? a0 : a1);
        if (j & 1) asm volatile("s_waitcnt lgkmcnt(5)" : "+v"(a[(j + 2) & 7]), "+v"(a[(j + 3) & 7]));
        SCHED();
      }
      __builtin_amdgcn_s_setprio(2);
      dma(k + 2, 4 + wn, none);
      SCHED();
      asm volatile("s_nop 7\n ds_write_b128 %0, %1\n ds_write_b128 %0, %2 offset:1024\n s_waitcnt lgkmcnt(0)" :: "v"(sc + (unsigned)((k & 1) * 8192)), "v"(c[0]), "v"(c[1]) : "memory");
      if (__builtin_expect(k == 8, 0)) hb = far_bit(near_kind(rw, no));
    }
    asm volatile("" : "+v"(a[0]), "+v"(a[1]), "+v"(a[2]), "+v"(a[3]), "+v"(a[4]), "+v"(a[5]), "+v"(a[6]), "+v"(a[7]));
    wait_vm<8>();
    __builtin_amdgcn_s_barrier();
  } else {
    const unsigned oo = (mat == 0 ? OFF_Q : (mat == 1 ? OFF_K : OFF_VX)) + (swap ? wn * 1024u : (wn >> 1) * HALF_ + (wn & 1) * 2048u);
    const unsigned v0 = l == 0 ? 0u : 0x80000000u;
    auto signal = [&](int r, unsigned va) {
      __builtin_amdgcn_raw_ptr_buffer_atomic_add_i32(swap ? (mat ? 1 : 65536) : 256, rw, va, OFF_CKV + (unsigned)(h * 64 + sgm(r)) * 4u, 0);
    };
    f32x4 c[2];
    u32x2 v[2];
    c[0] = c[1] = f32x4{0.f, 0.f, 0.f, 0.f};
    v[0] = v[1] = u32x2{0u, 0u};
    auto put = [&](int r, int tt, unsigned vs) {
      const unsigned so = oo + tile8(sgm(r), h, hb);
      const u32x2 p0 = u32x2{cvt2x(c[0][0], c[0][1]), cvt2x(c[0][2], c[0][3])}, p1 = u32x2{cvt2x(c[1][0], c[1][1]), cvt2x(c[1][2], c[1][3])};
      if (swap) st_sc1s(rw, vs, so + (unsigned)tt * HALF_, u32x4{p0[0], p0[1], p1[0], p1[1]});
      else if (tt == 0) { v[0] = p0; v[1] = p1; }
      else {
        st_sc1s(rw, vs, so, u32x4{v[0][0], v[0][1], p0[0], p0[1]});
        st_sc1s(rw, vs, so + 1024u, u32x4{v[1][0], v[1][1], p1[0], p1[1]});
      }
    };
    __builtin_amdgcn_s_setprio(3);
    __builtin_amdgcn_s_barrier();
    rec_wait(rw, no, 3u, 64u + 4u * h);
    dma(3, wn, none);
    if (swap) st_sc1(rw, 0x80000000u, zero4);
#pragma nounroll
    for (int r = 0; r < nseg; ++r) {
#pragma unroll
      for (int tt = 0; tt < 2; ++tt) {
        const int i = 2 * r + tt;
        const unsigned a0 = ab + (unsigned)(((i + 2) & 3) * 32768);
        asm volatile("" :: "v"(a0));
        if (tt == 0) wait_vm<swap ? 5 : 4>(); else wait_vm<swap ? 6 : 7>();
        __builtin_amdgcn_s_barrier();
        if (tt == 1 && __builtin_expect(r < 2 && mat, 0)) __builtin_amdgcn_raw_ptr_buffer_atomic_add_i32(1 << 24, rw, v0, OFF_CKV + (unsigned)(h * 64 + r) * 4u, 0);
        if (__builtin_expect(i == (rot ? 1 : 4), 0)) hb = far_bit(near_kind(rw, no));
        if (tt == 0 && __builtin_expect(r == 14 - rot, 0)) {
#pragma nounroll
          for (int it = 0; it < HSPIN_CAP; ++it) {
            if (__builtin_amdgcn_readfirstlane(ld_sc1_32(rw, OFF_CF + 384u)) >= 32u) break;
            __builtin_amdgcn_s_sleep(16);
          }
          asm volatile("" ::: "memory");
        }
        if (tt == 1 && __builtin_expect(r > ISG && r <= ESG, 0)) { drain_vm(); signal(r - 1, v0); }
        f32x4 n[2];
        asm volatile("ds_read_b128 %0, %2\n ds_read_b128 %1, %2 offset:1024" : "=&v"(n[0]), "=&v"(n[1]) : "v"(sc + (unsigned)(tt * 8192)) : "memory");
        dma(i + 4, wn, [&](int q) { rd(q, a0); });
        if (tt == 0) put(r - 1, 1, r > 0 ? vo : 0x80000000u); else put(r, 0, vo);
        rd(4, a0);
        if (tt == 0) signal(r - 2, r >= ESG + 2 ? v0 : 0x80000000u);
        rd(5, a0); rd(6, a0); rd(7, a0);
        if (tt == 0 || __builtin_expect(r >= BPR, 1)) __builtin_amdgcn_s_setprio(0);
        SCHED();
        asm volatile("s_waitcnt lgkmcnt(5)" : "+v"(n[0]), "+v"(n[1]), "+v"(a[0]), "+v"(a[1]), "+v"(a[2]));
        c[0] = n[0]; c[1] = n[1];
#pragma unroll
        for (int j = 0; j < 16; ++j) {
#pragma unroll
          for (int ct = 0; ct < 2; ++ct) c[ct] = swap ? mfu(b[j][ct], a[j & 7], c[ct]) : mfu(a[j & 7], b[j][ct], c[ct]);
          SCHED();
          if (j < 8) rd(j + 8, a0);
          if ((j & 1) && j < 14) asm volatile("s_waitcnt lgkmcnt(%2)" : "+v"(a[(j + 2) & 7]), "+v"(a[(j + 3) & 7]) : "n"(j < 8 ? 5 : (j < 13 ? 12 - j : 0)));
          SCHED();
        }
        __builtin_amdgcn_s_setprio(3);
      }
      if (__builtin_expect(r < ISG, 0)) {
        put(r, 1, vo);
        if (r < 2) PTB(r ? 20 : 8);
        if (r >= 2) {
          drain_vm();
          if (r == 2) { signal(0, v0); signal(1, v0); }
          signal(r, v0);
        }
      }
    }
    put(nseg - 1, 1, vo);
    drain_vm();
    signal(nseg - 2, v0); signal(nseg - 1, v0);
  }
  __builtin_amdgcn_s_setprio(0);
  drain_vm();
  __syncthreads();
  return hb;
}

constexpr int SPIN_CAP = 1 << 19;
#define LOCK 0
#define SL 0
#define GAM 1
#define DLYS 3, 7, 9, 9, 3, 6, 8, 6, 300, 118, 12, 6, 0, 0
enum { DLY1, DLY2, DLYV, DLY3, XDL, QDL, DLYL, DLYW, XT1, XTL, DLYD, KG3, KG1, DLYG, KG2, KGV, DLYR = QDL, DLYX = KG3, DLY4 = DLYG, DLYB = KG3 };
#define D4 1
#define NMX 1
#define S3 0
#define NOMIN 0
#define TID 3
#define EARLY 0
#define L2 1
#define L3 1
#define PRE3 LOCK
#define RSE 0

#ifndef NV1
#define NV1 4
#endif
#define VOR 1
#define LVS 1
#define CIN 1
#define GIL 1
#define G1N ""
#define G1F "s_nop 0\n"
#define G3N ""
#define XIL 1
#define VAS 1
#define TCH 0
#define TC1 0
#define TCW 1
#define APP 0
#define CNT_OFF 15872
#define PAN 1
#define TWO 1
#define G3I 1
#define WB1 1
#define WB2 3
#define ALG 1
#define PBA 6
#define PBN 7
#define ESL 2
#define LSL 0
#define KSL 8
constexpr int PQK_OFF = 0;
constexpr int W3_OFF = 98304;
constexpr int KV_OFF = 98304;
constexpr int PS_OFF = 131072;
constexpr int PZ_OFF = PS_OFF + 4096;
constexpr int UM_OFF = PZ_OFF + 1024;
constexpr int KC_OFF = UM_OFF + 3072;
constexpr int XV_OFF = KC_OFF + 1024;
constexpr int OB_OFF = XV_OFF + 2048;
constexpr int XK_OFF = OB_OFF + 2048;
constexpr float CS = SCALE_ * 1.4426950408889634f;
typedef __bf16 bfv8 __attribute__((ext_vector_type(8)));
typedef __bf16 bfv16 __attribute__((ext_vector_type(16)));
typedef float f32x2 __attribute__((ext_vector_type(2)));
typedef unsigned u32x8 __attribute__((ext_vector_type(8)));
DEVI f32x4 smf(u32x4 a, u32x8 b, f32x4 c, int idx) {
  return __builtin_amdgcn_smfmac_f32_16x16x64_bf16(__builtin_bit_cast(bfv8, a), __builtin_bit_cast(bfv16, b), c, idx, 0, 0);
}
DEVI float fold8(float a) { return a + __uint_as_float(__builtin_amdgcn_update_dpp(0u, __float_as_uint(a), 0x128, 0xf, 0xf, true)); }
#define LO2(c) f32x2{c[0], c[1]}
#define HI2(c) f32x2{c[2], c[3]}
DEVI f32x4 zero4() { f32x4 z = f32x4{0.f, 0.f, 0.f, 0.f}; asm volatile("" : "+v"(z)); return z; }
DEVI u32x4 cvt8(f32x4 a, f32x4 b) { return u32x4{cvt2(a[0], a[1]), cvt2(a[2], a[3]), cvt2(b[0], b[1]), cvt2(b[2], b[3])}; }

DEVI float vmax(unsigned a, unsigned b) { float r; asm("v_max_f32 %0, %1, %2" : "=v"(r) : "v"(a), "v"(b)); return r; }
DEVI float rows_max(float x) {
  const unsigned u = __float_as_uint(x);
  auto a = __builtin_amdgcn_permlane16_swap(u, u, false, false);
  const unsigned u2 = __float_as_uint(vmax(a[0], a[1]));
  auto b = __builtin_amdgcn_permlane32_swap(u2, u2, false, false);
  return vmax(b[0], b[1]);
}
DEVI float rows_sum(float x) {
  const unsigned u = __float_as_uint(x);
  auto a = __builtin_amdgcn_permlane16_swap(u, u, false, false);
  x = __uint_as_float(a[0]) + __uint_as_float(a[1]);
  const unsigned u2 = __float_as_uint(x);
  auto b = __builtin_amdgcn_permlane32_swap(u2, u2, false, false);
  return __uint_as_float(b[0]) + __uint_as_float(b[1]);
}

DEVI void req4(u32x4 r, unsigned vo, unsigned so, u32x4* v) {
  asm volatile("buffer_load_dwordx4 %0, %4, %5, %6 offen sc1\n buffer_load_dwordx4 %1, %4, %5, %6 offen offset:1024 sc1\n"
               "buffer_load_dwordx4 %2, %4, %5, %6 offen offset:2048 sc1\n buffer_load_dwordx4 %3, %4, %5, %6 offen offset:3072 sc1"
               : "=&v"(v[0]), "=&v"(v[1]), "=&v"(v[2]), "=&v"(v[3]) : "v"(vo), "s"(r), "s"(so) : "memory");
}
#define ILV 4
DEVI void req4i(u32x4 r, unsigned vo, unsigned so, u32x4* v) {
  asm volatile("buffer_load_dwordx4 %0, %5, %6, %4 offen sc1\n s_xor_b32 %4, %4, 0x400\n s_nop %7\n buffer_load_dwordx4 %1, %5, %6, %4 offen sc1\n s_xor_b32 %4, %4, 0xc00\n s_nop %7\n"
               "buffer_load_dwordx4 %2, %5, %6, %4 offen sc1\n s_xor_b32 %4, %4, 0x400\n s_nop %7\n buffer_load_dwordx4 %3, %5, %6, %4 offen sc1"
               : "=&v"(v[0]), "=&v"(v[1]), "=&v"(v[2]), "=&v"(v[3]), "+s"(so) : "v"(vo), "s"(r), "n"(ILV ? ILV - 1 : 0) : "memory", "scc");
}
DEVI void req4it(u32x4 r, unsigned so, u32x4* v) {
  asm volatile("buffer_load_dwordx4 %0, off, %5, %4 sc1\n s_xor_b32 %4, %4, 0x400\n s_nop %6\n buffer_load_dwordx4 %1, off, %5, %4 sc1\n s_xor_b32 %4, %4, 0xc00\n s_nop %6\n"
               "buffer_load_dwordx4 %2, off, %5, %4 sc1\n s_xor_b32 %4, %4, 0x400\n s_nop %6\n buffer_load_dwordx4 %3, off, %5, %4 sc1"
               : "=&v"(v[0]), "=&v"(v[1]), "=&v"(v[2]), "=&v"(v[3]), "+s"(so) : "s"(r), "n"(ILV ? ILV - 1 : 0) : "memory", "scc");
}
DEVI u32x4 rs_tid(u32x4 r) { return u32x4{r[0], r[1] | (16u << 16), r[2] >> 4, 1u << 23}; }
DEVI void req4t(u32x4 r, unsigned so, u32x4* v) {
  asm volatile("buffer_load_dwordx4 %0, off, %4, %5 sc1\n buffer_load_dwordx4 %1, off, %4, %5 offset:1024 sc1\n"
               "buffer_load_dwordx4 %2, off, %4, %5 offset:2048 sc1\n buffer_load_dwordx4 %3, off, %4, %5 offset:3072 sc1"
               : "=&v"(v[0]), "=&v"(v[1]), "=&v"(v[2]), "=&v"(v[3]) : "s"(r), "s"(so) : "memory");
}
DEVI void req2t(u32x4 r, unsigned so, u32x4* v) {
  asm volatile("buffer_load_dwordx4 %0, off, %2, %3 sc1\n buffer_load_dwordx4 %1, off, %2, %3 offset:1024 sc1" : "=&v"(v[0]), "=&v"(v[1]) : "s"(r), "s"(so) : "memory");
}
DEVI void req2(u32x4 r, unsigned vo, unsigned so, u32x4* v) {
  asm volatile("buffer_load_dwordx4 %0, %2, %3, %4 offen sc1\n buffer_load_dwordx4 %1, %2, %3, %4 offen offset:1024 sc1"
               : "=&v"(v[0]), "=&v"(v[1]) : "v"(vo), "s"(r), "s"(so) : "memory");
}
template <int N>
DEVI void got(u32x4& v) { asm volatile("s_waitcnt vmcnt(%1)" : "+v"(v) : "n"(N)); }
#define A8 ".p2align 3\n"
#define A4 ".p2align 3\n s_nop 0\n"
#define STR_(x) #x
#define STR(x) STR_(x)
#define MF "v_mfma_f32_16x16x32_bf16 "
#define CV "v_cvt_pk_bf16_f32 "
#define SM "v_smfmac_f32_16x16x64_bf16 "
template <int N>
DEVI void got_s4(u32x4& a, f32x4* c, u32x8 b0, u32x8 b1, u32x8 b2, u32x8 b3, int idx) {
  asm volatile(".if %10 == 3 + " STR(LOCK) "\n" A4 ".endif\n s_waitcnt vmcnt(%10)\n" SM "%1, %0, %5, %9\n" SM "%2, %0, %6, %9\n" SM "%3, %0, %7, %9\n" SM "%4, %0, %8, %9"
               : "+v"(a), "+v"(c[0]), "+v"(c[1]), "+v"(c[2]), "+v"(c[3]) : "v"(b0), "v"(b1), "v"(b2), "v"(b3), "v"(idx), "n"(N));
}
template <int N>
DEVI void got_s2(u32x4& a, f32x4* c, u32x8 b0, u32x8 b1, int idx) {
  asm volatile(".if %6 == 1\n" A4 ".endif\n s_waitcnt vmcnt(%6)\n" SM "%1, %0, %3, %5\n" SM "%2, %0, %4, %5" : "+v"(a), "+v"(c[0]), "+v"(c[1]) : "v"(b0), "v"(b1), "v"(idx), "n"(N));
}
template <int N>
DEVI void got_x(u32x4& x, f32x4& sa, f32x4& sb, f32x4& sm, u32x4 ka, u32x4 kb) {
  asm volatile("s_waitcnt vmcnt(%6)\n" MF "%1, %4, %0, %1\n" MF "%2, %5, %0, %2\n" MF "%3, %0, %0, %3" : "+v"(x), "+v"(sa), "+v"(sb), "+v"(sm) : "v"(ka), "v"(kb), "n"(N));
}
template <int N>
DEVI void got_m(u32x4& b, f32x4& d, u32x4 a) { asm volatile("s_waitcnt vmcnt(%3)\n" MF "%1, %2, %0, %1" : "+v"(b), "+v"(d) : "v"(a), "n"(N)); }
DEVI unsigned umax3(unsigned a, unsigned b, unsigned c) { return max(max(a, b), c); }
DEVI unsigned umax5(u32x4 v, unsigned m) { return umax3(v[2], v[3], umax3(v[0], v[1], m)); }
DEVI bool bad(unsigned m) { return __builtin_expect(__builtin_amdgcn_ballot_w64(m == POISON) != 0ull, 0); }
DEVI bool over(unsigned m, unsigned thr) { return __builtin_expect(__builtin_amdgcn_ballot_w64(m > thr) != 0ull, 0); }
DEVI bool bad4(const u32x4* v) { return bad(umax5(v[3], umax5(v[2], umax5(v[1], umax5(v[0], 0u))))); }
DEVI unsigned tag5(u32x4 v, unsigned t, unsigned m) {
  return __builtin_amdgcn_bitop3_b32(v[0], v[1], t, 0x7e) | __builtin_amdgcn_bitop3_b32(v[2], v[3], t, 0x7e) | m;
}
DEVI unsigned rt_now() {
  unsigned long long t;
  asm volatile("s_memrealtime %0\n s_waitcnt lgkmcnt(0)" : "=s"(t) :: "memory");
  return (unsigned)t;
}
#define DLYARG
#define DLYA
#define DLYI(mask_) unsigned dly[16] = {DLYS}; _Pragma("unroll") for (int i = 0; i < 16; ++i) if (((mask_) >> i) & 1) asm volatile("" : "+s"(dly[i]));
#define PAL 6
DEVI void pause(unsigned n) { asm volatile(".p2align %1\n 1: s_sub_u32 %0, %0, 1\n s_cbranch_scc0 1b" : "+s"(n) : "n"(PAL) : "scc", "memory"); }
DEVI unsigned mt_now() {
  unsigned long long t;
  asm volatile("s_memtime %0\n s_waitcnt lgkmcnt(0)" : "=s"(t) :: "memory");
  return (unsigned)t;
}
DEVI unsigned gt_now() {
  unsigned long long b, c, d;
  asm volatile("s_memrealtime %0\n s_nop 6\n s_memrealtime %1\n s_nop 6\n s_memrealtime %2\n s_waitcnt lgkmcnt(0)" : "=&s"(b), "=&s"(c), "=&s"(d) :: "memory");
  return ((unsigned)b + (unsigned)c + (unsigned)d) * 8u;
}
DEVI void clk_pair(unsigned& t, unsigned& gt) {
  unsigned long long a;
  asm volatile("s_memtime %0" : "=s"(a) :: "memory");
  gt = gt_now();
  asm volatile("" : "+s"(a));
  t = (unsigned)a;
}
DEVI unsigned local_of(unsigned tgt, int rq, unsigned t, unsigned g) {
  int d = (int)(tgt - g);
  asm("s_max_i32 %0, %0, 0xffffc000\n s_min_i32 %0, %0, 0x4000" : "+s"(d) :: "scc");
  return t + (unsigned)(d * rq >> 16);
}
DEVI int until(unsigned tgt, int dmin, unsigned now) {
  const int left = (int)(tgt - now);
  int d;
  asm volatile("s_max_i32 %0, %1, %3\n s_min_i32 %0, %0, 4000\n"
               ".p2align %2\n 1: s_sub_u32 %0, %0, 28\n s_cbranch_scc0 1b\n s_cmp_lt_i32 %0, -20\n s_cbranch_scc1 2f\n s_cmp_lt_i32 %0, -12\n s_cbranch_scc1 2f\n"
               "s_cmp_lt_i32 %0, -4\n s_cbranch_scc1 2f\n s_nop 0\n s_branch 2f\n 2:" : "=&s"(d) : "s"(left), "n"(PAL), "s"(dmin) : "scc", "memory");
  return left;
}
DEVI int until(unsigned tgt, int dmin) { return until(tgt, dmin, mt_now()); }
constexpr int TG_OFF = KC_OFF + 16, GP_OFF = XK_OFF + 3072;
#define KTHR 200
#define KFOL 0
#define KPEN 600
#define KLATE 2050

#define TRD
#define TR(i)
#define TRW()
#define TRE(b)
#define TRARGD
#define TRARG
#define SLP(i)
#define TRT()

#define T0(i)
#define TP(i)
#define XM 0
#define TRX(n) do { if (XM == (n)) TR(7); } while (0)
#define TRY(n) do { if (XM == (n)) TR(4); } while (0)
DEVI unsigned kv_want(int s) { return s < XS_ ? 0x404u : 0x40404u; }
DEVI void kv_wait(u32x4 rs, int s, int h, int& budget) {
  const unsigned want = s > 1 ? kv_want(s) : 8u << 24;
  while (true) {
    unsigned c;
    asm volatile("buffer_load_dword %0, off, %1, %2 sc1\n s_waitcnt vmcnt(0)" : "=v"(c) : "s"(rs), "s"(OFF_CKV + (unsigned)(h * 64 + s) * 4u) : "memory");
    if ((__builtin_amdgcn_readfirstlane(c) & want) == want || --budget <= 0) break;
    __builtin_amdgcn_s_sleep(4);
  }
}

DEVI unsigned kv_src(int s, int h, int w, unsigned hb) { return ((w >> 1) ? OFF_VX : OFF_K) + tile8(s, h, hb) + (w & 1) * HALF_; }
DEVI unsigned kv_dst(int s, int w) { return (unsigned)(size_t)LDS3 + KV_OFF + (s & 1) * 16384 + w * 4096; }
DEVI void kv_dma(u32x4 rs, unsigned dvo, unsigned so, unsigned dst) {
  asm volatile("s_mov_b32 m0, %3\n s_nop 0\n buffer_load_dwordx4 %0, %1, %2 offen sc1 lds\n buffer_load_dwordx4 %0, %1, %2 offen offset:1024 sc1 lds\n"
               "buffer_load_dwordx4 %0, %1, %2 offen offset:2048 sc1 lds\n buffer_load_dwordx4 %0, %1, %2 offen offset:3072 sc1 lds"
               :: "v"(dvo), "s"(rs), "s"(so), "s"(dst) : "memory");
}

DEVI unsigned ovoff(unsigned lv) { return (lv & 128u) * 8u + (lv & 0x370u); }
DEVI unsigned osoff(int s, int h, int jj, unsigned hb) { return ooff(s, h, hb) + (jj >> 1) * HALF_ + (jj & 1) * 128u; }
DEVI void ostore(u32x4 rs, const u32x4* oc, unsigned ov, unsigned os) {
  asm volatile("buffer_store_dwordx4 %0, %2, %3, %4 offen sc1\n buffer_store_dwordx4 %1, %2, %3, %4 offen offset:2048 sc1\n s_nop 1" :: "v"(oc[0]), "v"(oc[1]), "v"(ov), "s"(rs), "s"(os) : "memory");
}
DEVI void kv_first(char* lds, u32x4 rs, int sg, int h, int w, unsigned lv, int& budget) {
  u32x4 t[4];
  do {
    req4(rs, lv ^ (unsigned)(w >> 1) * (((lv >> 3) & 32u) + ((lv >> 2) & 128u)), kv_src(sg, h, w, 0u), t);
    asm volatile("s_waitcnt vmcnt(0)" : "+v"(t[0]), "+v"(t[1]), "+v"(t[2]), "+v"(t[3]));
  } while (bad4(t) && --budget > 0);
#pragma unroll
  for (int i = 0; i < 4; ++i) *(u32x4*)(lds + KV_OFF + sg * 16384 + w * 4096 + i * 1024 + lv) = t[i];
}
DEVI float max6(const float* e) {
  float mx, t0, t1;
  asm("v_max3_f32 %0, %3, %4, %5\n v_max3_f32 %1, %6, %7, %8\n v_max_f32 %0, %0, %1\n s_nop 1\n"
      "v_max_f32_dpp %1, %0, %0 row_ror:8 row_mask:0xf bank_mask:0xf\n v_max_f32_dpp %2, %0, %0 row_ror:8 row_mask:0xf bank_mask:0xf\n s_nop 1\n"
      "v_permlane16_swap_b32 %1, %2\n v_max_f32 %0, %1, %2\n v_max_f32 %1, %1, %2\n s_nop 1\n"
      "v_permlane32_swap_b32 %0, %1\n v_max_f32 %0, %0, %1"
      : "=&v"(mx), "=&v"(t0), "=&v"(t1) : "v"(e[0]), "v"(e[1]), "v"(e[2]), "v"(e[3]), "v"(e[4]), "v"(e[5]));
  return mx;
}

DEVI void recur_p1(char* lds, rsrc_t rw, const u32x4 rs, const u16* __restrict__ mem0, const u16* __restrict__ Wq, const u16* __restrict__ Wk,
                   const u16* __restrict__ Wv, u16* __restrict__ out, const char* ws, int wg TRARGD DLYARG) {
  DLYI((LOCK ? 0x7849 : 0x49) | (RSE ? 0x20 : 0) | (SL ? 0x1000 : 0) | (TWO ? 0x800 : 0));
  const int l = threadIdx.x & 63, w = __builtin_amdgcn_readfirstlane(threadIdx.x >> 6) & 3, g = l >> 4, lo = l & 15;
  const int h = wg >> 2, jj = wg & 3;
  int budget = SPIN_CAP;
  unsigned te = 0u, ge = 0u, gpo = 0u, t1g = 0u, t3g = 0u;
  int kq = 0, rq = 0;
  if ((LOCK || SL) && w == 1) clk_pair(te, ge);
  unsigned mz = 0u;
  if (ALG) asm volatile("" : "+s"(mz));
  const int vq = 2 * (~jj & 1) + (jj >> 1);
  u32x8 w1[6][4];
  u32x4 a[4];
  {
    char* wl = lds + w * 24576;
    const char* g1 = (const char*)((jj & 2) ? Wk : Wq) + (size_t)((256 * w + (l >> 3)) * HD_ + h * DH_ + 64 * (jj & 1) + ((l & 7) ^ (2 * g)) * 8) * 2u;
    const char* g2 = (const char*)Wv + (size_t)((256 * w + (l >> 2)) * HD_ + h * DH_ + 32 * vq + (l & 3) * 8) * 2u;
    const unsigned mo = (unsigned)((2 * (lo >> 2) + (lo & 1)) * D_ + 256 * w + 16 * g + 4 * (lo & 2)) * 2u;
    const int kx = ILV ? w : 0;
    asm volatile("buffer_load_dwordx4 %0, %4, %5, %6 offen\n buffer_load_dwordx4 %1, %4, %5, %7 offen\n"
                 "buffer_load_dwordx4 %2, %4, %5, %8 offen\n buffer_load_dwordx4 %3, %4, %5, %9 offen"
                 : "=&v"(a[0]), "=&v"(a[1]), "=&v"(a[2]), "=&v"(a[3]) : "v"(mo), "s"(rsrc4(mem0, MEM_ * D_ * 2u)), "s"(kx * 128), "s"((kx ^ 1) * 128), "s"((kx ^ 2) * 128), "s"((kx ^ 3) * 128) : "memory");
    auto ask = [&](int n) {
#pragma unroll
      for (int i = 0; i < 4; ++i)
        __builtin_amdgcn_global_load_lds((const unsigned*)(n % 3 < 2 ? g1 + (size_t)(64 * ((n / 3) ^ kx) + 32 * (n % 3) + 8 * i) * HD_ * 2 : g2 + (size_t)(64 * ((n / 3) ^ kx) + 16 * i) * HD_ * 2),
                                         (unsigned*)(wl + (n % 6) * 4096 + i * 1024 + l * 16), 16, 0, NTW);
      SCHED();
    };
    const int row = 16 * (g >> 1) + 4 * (g & 1) + (lo >> 2);
    const char* b1 = wl + row * 128 + (lo & 3) * 8;
    const char* b2 = wl + row * 64 + (lo & 3) * 8;
    auto use = [&](int n) {
#pragma unroll
      for (int i = 0; i < 8; ++i) {
        const int u = n % 3 < 2 ? i >> 1 : 4 + (i >> 2), q = n % 3 < 2 ? 2 * (n % 3) + (i & 1) : i & 3;
        const u32x2 v = tr_quad(n % 3 < 2 ? b1 + (n % 6) * 4096 + ((u ^ ((row >> 1) & 3)) * 32) + (q & 1) * 1024
                                          : b2 + (n % 6) * 4096 + (u & 1) * 32 + (32 * (q >> 1) + 8 * (q & 1)) * 64);
        w1[u][n / 3][(q & 1) + 4 * (q >> 1)] = v[0];
        w1[u][n / 3][(q & 1) + 4 * (q >> 1) + 2] = v[1];
      }
      __builtin_amdgcn_s_waitcnt(0xc07f);
      asm volatile("" ::: "memory");
      SCHED();
    };
#pragma unroll
    for (int n = 0; n < 6; ++n) ask(n);
#pragma unroll
    for (int n = 0; n < 12; ++n) {
      if (n == WB1 || n == WB2) __builtin_amdgcn_s_barrier();
      asm volatile("s_waitcnt vmcnt(%0)" :: "n"(n < 7 ? 20 : 44 - 4 * n) : "memory");
      if (n == 0) asm volatile("" : "+v"(a[0]), "+v"(a[1]), "+v"(a[2]), "+v"(a[3]));
      if (n == 0 || n == 11) TP(n == 11);
      use(n);
      if (n < 6) ask(n + 6);
    }
    TP(2);
    if ((LOCK || SL) && w == 1) *(u32x4*)(lds + TG_OFF) = u32x4{te, ge, 61000u, te};
    bar();
  }
  const int idx = (lo & 2) ? 0xeeee : 0x4444;
  const unsigned vo = (unsigned)l * 16u;
  const unsigned pq0 = (unsigned)(size_t)LDS3 + PQK_OFF + (w * 960 + 2 * g * 40 + 8 * (lo >> 2) + (lo & 3)) * 4;
  const unsigned r1w = NearMap::blk(NB1 + jj) + h * 128u;
  unsigned mvo = 0u, r1r = 0u, r3r = 0u, r2b = 0u, hb = 0u;
  unsigned tg = 0u;
  int known = 0;
  unsigned qs = 0u;
  unsigned sc = 0u, sn = SET_;
  u32x4 rt = rs_tid(rs);
  if (TID) asm volatile("" : "+s"(rt));
  unsigned anv = 0x80000000u;
  auto announce = [&](unsigned t, int db) {
    db += (int)dly[KG1];
    const u32x4 an = u32x4{t + (unsigned)(db * kq >> 16), t + (unsigned)((db + (int)dly[KG2]) * kq >> 16), t + (unsigned)((db + (int)dly[KG3]) * kq >> 16), 0u};
    asm volatile("buffer_store_dwordx4 %0, %1, %2, %3 offen sc1\n s_nop 1" :: "v"(an), "v"(anv), "s"(rs), "s"(4u * SET_ + NearMap::blk(NB1) + (unsigned)(wg >> 3) * 128u) : "memory");
  };
  TRD;
  for (int s = 0; s < NSEG_; ++s) {
    f32x4 acc[6];

    TR(0);
    unsigned m, thr, pq1 = pq0;
    asm volatile("" : "+v"(pq1));
    pq1 += 1280u;
    asm volatile("" : "+v"(pq1));
    if (APP) asm volatile("s_mov_b32 m0, 0");
    unsigned kl;
    const int late = 0;
    asm volatile("ds_read_b32 %0, %1" : "=v"(kl) : "v"((unsigned)(size_t)LDS3 + KC_OFF + 8u) : "memory");
    if (s > 0) pause(dly[DLY1]);
    do {
      thr = --budget > 0 && s > 0 ? POISON - 1u : POISON;
      asm volatile("" : "+s"(thr));
      TRY(3);
      if (__builtin_expect(s > 0, 1)) { if (TID & 2) req4it(rt, r1r + sc, a); else req4i(rs, vo, r1r + sc, a); }
      if (LOCK) {
        announce(t1g, late > KTHR ? min(late, 20000) + KPEN : (KFOL ? max(late, 0) : 0));
      }
#pragma unroll
      for (int u = 0; u < 6; ++u) acc[u] = zero4();
      SCHED();
      asm volatile("s_nop 0");
#define G1P(i, al, m0) asm volatile(al "s_waitcnt vmcnt(%15)\n" SM "%1, %0, %6, %10\n" SM "%2, %0, %7, %10\n v_max3_u32 %5, %11, %12, " m0 "\n" SM "%3, %0, %8, %10\n v_max3_u32 %5, %13, %14, %5\n" SM "%4, %0, %9, %10" \
                   : "+v"(a[i]), "+v"(acc[0]), "+v"(acc[1]), "+v"(acc[2]), "+v"(acc[3]), "+v"(m) \
                   : "v"(w1[0][i]), "v"(w1[1][i]), "v"(w1[2][i]), "v"(w1[3][i]), "v"(idx), "v"(a[i][0]), "v"(a[i][1]), "v"(a[i][2]), "v"(a[i][3]), "n"(3 - i + LOCK))
      asm volatile("" : "=v"(m));
      G1P(0, A4, "%12"); SCHED();
      G1P(1, G1N, "%5"); SCHED();
      G1P(2, G1N, "%5"); SCHED();
      G1P(3, G1N, "%5"); SCHED();
    } while (over(m, thr));
    TR(1);
    SCHED();
    asm volatile(G1F "v_pk_add_f32 %2, %2, %3\n v_pk_add_f32 %6, %6, %7\n ds_write2_b32 %0, %4, %5 offset1:40\n v_pk_add_f32 %10, %10, %11\n"
                 "ds_write2_b32 %0, %8, %9 offset0:4 offset1:44\n v_pk_add_f32 %14, %14, %15\n ds_write2_b32 %1, %12, %13 offset1:40\n ds_write2_b32 %1, %16, %17 offset0:4 offset1:44"
                 :: "v"(pq0), "v"(pq1), "v"(LO2(acc[0])), "v"(HI2(acc[0])), "v"(acc[0][0]), "v"(acc[0][1]), "v"(LO2(acc[1])), "v"(HI2(acc[1])), "v"(acc[1][0]), "v"(acc[1][1]),
                    "v"(LO2(acc[2])), "v"(HI2(acc[2])), "v"(acc[2][0]), "v"(acc[2][1]), "v"(LO2(acc[3])), "v"(HI2(acc[3])), "v"(acc[3][0]), "v"(acc[3][1]) : "memory");
    SCHED();
#pragma unroll
    for (int i = 0; i < 7; ++i) {
      acc[4 + (i >> 2)] = smf(a[i & 3], w1[4 + (i >> 2)][i & 3], acc[4 + (i >> 2)], idx);
      SCHED();
      if (i == NV1 - 1) { TR(2); bar(); SCHED(); }
    }
    asm volatile(SM "%9, %10, %11, %12\n v_add_u32 %0, 0x500, %0\n s_nop 0\n v_pk_add_f32 %1, %1, %2\n ds_write2_b32 %0, %3, %4 offset1:40\n s_nop 3\n v_pk_add_f32 %5, %5, %6\n ds_write2_b32 %0, %7, %8 offset0:4 offset1:44\n"
                 ".if " STR(APP) "\n ds_append %0 offset:" STR(CNT_OFF) "\n s_barrier\n .endif"
                 :: "v"(pq1), "v"(LO2(acc[4])), "v"(HI2(acc[4])), "v"(acc[4][0]), "v"(acc[4][1]), "v"(LO2(acc[5])), "v"(HI2(acc[5])), "v"(acc[5][0]), "v"(acc[5][1]),
                    "v"(acc[5]), "v"(a[3]), "v"(w1[5][3]), "v"(idx) : "memory");
    if (APP) asm volatile("s_waitcnt lgkmcnt(0)" :: "v"(pq1) : "memory"); else bar();
    asm volatile("s_cmp_eq_u32 %6, 0\n s_cbranch_scc1 1f\n v_and_b32 %4, 0x370, %7\n"
                 "buffer_load_dwordx4 %0, %4, %5, %6 offen sc1\n buffer_load_dwordx4 %1, %4, %5, %6 offen offset:1024 sc1\n"
                 "buffer_load_dwordx4 %2, %4, %5, %6 offen offset:2048 sc1\n buffer_load_dwordx4 %3, %4, %5, %6 offen offset:3072 sc1\n 1:"
                 : "=&v"(a[0]), "=&v"(a[1]), "=&v"(a[2]), "=&v"(a[3]), "=&v"(m) : "s"(rs), "s"(qs), "v"(vo) : "memory", "scc");
    TR(3);
    SLP(0);
    const bool last = s == NSEG_ - 1;
    asm volatile("" : "+v"(kl));
    if (__builtin_expect(__builtin_amdgcn_readfirstlane(kl) != 0x404u && s > 0 && !last, 0)) {
      unsigned lv = vo;
      asm volatile("" : "+v"(lv));
      __builtin_amdgcn_s_sleep(LSL);
      kv_wait(rs, s + 1, h, budget);
      kv_dma(rs, lv ^ (unsigned)(w >> 1) * (((lv >> 3) & 32u) + ((lv >> 2) & 128u)), kv_src(s + 1, h, w, hb), kv_dst(s + 1, w));
      drain_vm();
    }
    auto reset = [&]() {
      u32x2 p2 = u32x2{POISON, POISON};
      const unsigned sp = 3u * SET_ - sc - sn;
      unsigned lv = vo;
      asm volatile("" : "+v"(lv));
      unsigned po = w ? (lv >> 1) + (w < 3 ? (lv >> 8) * 128u : 0u) : mvo;
      const unsigned ps = sp + (w ? r2b + (w == 3 ? BLK_ + vq * 512u : (2 * (jj & 1) + w - 1) * 1024u + (jj >> 1) * 128u) : r1w);
      asm volatile("s_waitcnt lgkmcnt(0)" : "+v"(po), "+v"(p2) : "s"(ps) : "memory");
      __builtin_amdgcn_s_barrier();
      st_sc1s_64(rw, po, ps, p2);
    };
    unsigned tv_, ov, os, pm;
    u32x4 oc[2], sel;
    auto none3 = [&]() { asm volatile("" : "=v"(tv_), "=v"(sel), "=v"(oc[0]), "=v"(oc[1]), "=v"(ov)); asm volatile("" : "=s"(pm), "=s"(os)); };
    auto prep3 = [&]() {
      tv_ = vo;
      asm volatile("" : "+v"(tv_));
      oc[0] = *(const u32x4*)(lds + OB_OFF + tv_); oc[1] = *(const u32x4*)(lds + OB_OFF + 1024 + tv_);
      ov = ovoff(tv_) | (s > XS_ && !last ? 0u : 0x80000000u); os = osoff(max(s - 1, 0), h, jj, hb);
      const unsigned sel1 = 0x3f80u << (tv_ & 16u);
      sel = u32x4{(tv_ & 96u) == 0u ? sel1 : 0u, (tv_ & 96u) == 32u ? sel1 : 0u, (tv_ & 96u) == 64u ? sel1 : 0u, (tv_ & 96u) == 96u ? sel1 : 0u};
      tv_ = (tv_ >> 7) ^ tg;
      pm = r1w + sn;
      asm volatile("" : "+v"(tv_), "+v"(sel), "+v"(oc[0]), "+v"(oc[1]), "+v"(ov));
      asm volatile("" : "+s"(pm), "+s"(os));
    };
    if (qs) {
      float e[6];
      unsigned lv = vo;
      asm volatile("" : "+v"(lv));
      f32x4 sa, sb;
      u32x4 km[4];
      asm volatile("ds_read_b128 %2, %10\n ds_read_b128 %3, %10 offset:1024\n ds_read_b128 %4, %10 offset:2048\n ds_read_b128 %5, %10 offset:3072\n s_waitcnt vmcnt(0) lgkmcnt(0)\n"
                   MF "%0, %2, %6, 0\n ds_read_b128 %2, %10 offset:4096\n" MF "%0, %3, %7, %0\n ds_read_b128 %3, %10 offset:5120\n"
                   MF "%0, %4, %8, %0\n ds_read_b128 %4, %10 offset:6144\n" MF "%0, %5, %9, %0\n ds_read_b128 %5, %10 offset:7168\n s_waitcnt lgkmcnt(3)\n"
                   MF "%1, %2, %6, 0\n s_waitcnt lgkmcnt(2)\n" MF "%1, %3, %7, %1\n s_waitcnt lgkmcnt(1)\n" MF "%1, %4, %8, %1\n s_waitcnt lgkmcnt(0)\n" MF "%1, %5, %9, %1\n s_nop 7"
                   : "=&v"(sa), "=&v"(sb), "=&v"(km[0]), "=&v"(km[1]), "=&v"(km[2]), "=&v"(km[3]), "+v"(a[0]), "+v"(a[1]), "+v"(a[2]), "+v"(a[3])
                   : "v"((unsigned)(size_t)LDS3 + KV_OFF + (s & 1) * 16384 + lv) : "memory");
      const int qi = 8 * jj + (int)((lv >> 4) & 7u) - 4 * (int)(lv >> 8) - (int)((lv >> 3) & 16u);
#pragma unroll
      for (int r = 0; r < 4; ++r) e[r] = (lv & 128u) ? sb[r] : sa[r];
      asm volatile("" : "+v"(e[0]), "+v"(e[1]), "+v"(e[2]), "+v"(e[3]));
#pragma unroll
      for (int r = 0; r < 4; ++r) e[r] = r <= qi ? e[r] : rbf(-1000000000.0f) / SCALE_;
      asm volatile("" : "+v"(e[0]), "+v"(e[1]), "+v"(e[2]), "+v"(e[3]));
      e[4] = e[5] = e[0];
      float m1 = max6(e);
      asm volatile("" : "+v"(m1));
      TRY(7);
      const unsigned ls = (unsigned)(size_t)LDS3, pb = ls + PS_OFF + 1024u;
      unsigned ka = ls + XK_OFF + (lv >> 8) * 128u + (lv & 112u), fa = ls + XK_OFF + 2048u, fv;
      unsigned w1a = pb + (lv >> 8) * 128u + (lv & 112u) + ((lv >> 4) & 8u), w2a = pb + 512u + (lv & 256u) + (lv & 112u) * 2u + ((lv >> 5) & 4u);
      asm volatile("" : "+v"(ka), "+v"(fa), "+v"(w1a), "+v"(w2a));
      reset();
      TR(5);
      __builtin_amdgcn_s_sleep(KSL);
      do {
        asm volatile("ds_read_b32 %4, %6\n ds_read_b128 %0, %5\n ds_read_b128 %1, %5 offset:1024\n ds_read_b128 %2, %5 offset:512\n ds_read_b128 %3, %5 offset:1536\n s_waitcnt lgkmcnt(0)"
                     : "=&v"(km[0]), "=&v"(km[1]), "=&v"(km[2]), "=&v"(km[3]), "=&v"(fv) : "v"(ka), "v"(fa) : "memory");
      } while (__builtin_amdgcn_readfirstlane(fv) != (unsigned)s);
      const f32x4 sm = mfu(km[3], a[3], mfu(km[2], a[2], mfu(km[1], a[1], mfu(km[0], a[0], f32x4{0.f, 0.f, 0.f, 0.f}))));
      const unsigned m2 = __float_as_uint(fmaxf(fmaxf(sm[0], sm[1]), fmaxf(fmaxf(sm[2], sm[3]), m1)));
      e[4] = (lv & 128u) ? sm[2] : sm[0]; e[5] = (lv & 128u) ? sm[3] : sm[1];
      const auto m3 = __builtin_amdgcn_permlane16_swap(m2, m2, false, false);
      float mx;
      asm("v_max_f32 %0, %1, %2" : "=v"(mx) : "v"(m3[0]), "v"(m3[1]));
#pragma unroll
      for (int i = 0; i < 6; ++i) e[i] = __builtin_amdgcn_exp2f((e[i] - mx) * CS);
      asm volatile("ds_write_b64 %0, %1\n ds_write2_b32 %2, %3, %3 offset1:6" :: "v"(w1a), "v"(u32x2{cvt2(e[0], e[1]), cvt2(e[2], e[3])}), "v"(w2a), "v"(cvt2(e[4], e[5])) : "memory");
      TRY(6);
      none3();
    } else {
    if (__builtin_expect(s == 0, 0)) {
      const u32x2 no = launch_no(ws);
      unsigned lv = vo;
      asm volatile("" : "+v"(lv));
      kv_wait(rs, 1, h, budget);
      __builtin_amdgcn_s_sleep(ESL);
      kv_first(lds, rs, 1, h, w, lv, budget);
      if (w >= 2) rec_wait(rw, no, 63u, 64u + 64u * w);
      const NearMap nm{(unsigned)__builtin_amdgcn_readfirstlane(*(const unsigned*)(lds + KC_OFF + 4))};
      mvo = (lv & 32u) * 2u + ((lv >> 1) & 32u) + (lv & 16u) + ((lv >> 5) & 8u) + nm(0u, ((lv >> 8) & 2u) + ((lv >> 7) & 1u), NB1) - NearMap::blk(NB1);
      r1r = nm(0u, h >> 3, NB1 + w) + (ILV ? w * 1024u : 0u); r3r = nm(0u, h >> 3, NB3 + jj * 8 + (h & 7)); r2b = nm(0u, h >> 3, NB2 + (h & 7) * 2);
      hb = far_bit(nm.k);
      gpo = nm(4u, h >> 3, NB1);
      if (w == 1 && ((wg ^ (wg >> 3)) & 7) == 0 && lv < 64u) anv = nm(0u, lv >> 4, NB1) - NearMap::blk(NB1);
    }
    if (w == 1) *(u32x2*)(lds + KC_OFF + 40) = u32x2{known > s + 2 ? 0x404u : 0u, known > s + 3 ? 1u : 0u};
    if ((LOCK || SL) && w == 1) {
      unsigned t1, g1;
      clk_pair(t1, g1);
      const int err = (int)(g1 - ge) - ((int)(t1 - te) * kq >> 16);
      const int dk = (unsigned)(err + 400) < 800u ? err * 3 : 0;
      kq += dk; rq -= dk * 57 >> 6;
      if (__builtin_expect(s == 0, 0)) {
        const float r = (float)err * __builtin_amdgcn_rcpf((float)(int)(t1 - te));
        kq = __builtin_amdgcn_readfirstlane((int)(r * 65536.f)); rq = __builtin_amdgcn_readfirstlane((int)(__builtin_amdgcn_rcpf(r) * 65536.f));
      }
      te = t1; ge = g1;
      if (s == 0) { t1g = t3g = g1; *(unsigned*)(lds + TG_OFF) = t1; }
    }
    if (PRE3 && w == 0) prep3(); else none3();
    reset();
    TR(5);
    if (w == 3) {
      unsigned now = 1u;
      if (__builtin_expect(jj == 0 && s >= FS_, 0)) now = rt_now() | 1u;
      if (l == 0 && s > 1) st_sc1_32(rw, cooff(h, hb) + (unsigned)((s - 2) * 16 + jj * 4), s < XS_ + 2 ? 2u : now);
      if (__builtin_expect(s < XS_, 0)) {
        unsigned lv = vo;
        asm volatile("" : "+v"(lv));
        const float* p = (const float*)(lds + PQK_OFF + 2560 + ((lv >> 8) & 1u) * 640u + ((lv >> 1) & 96u) + (lv >> 9) * 16u + ((lv >> 2) & 12u));
        float v[4];
#pragma unroll
        for (int r = 0; r < 4; ++r) v[r] = (p[r * 40] + p[960 + r * 40]) + (p[1920 + r * 40] + p[2880 + r * 40]);
        st_sc1s_64(rw, (lv & 511u) + (lv >> 9) * 8u, kmoff(s, h, hb) + 2048u + vq * 512u, u32x2{cvt2(v[0], v[1]), cvt2(v[2], v[3])});
      }
      if (__builtin_expect(s == FS_ - 1, 0)) {
        while (__builtin_amdgcn_readfirstlane(ld_sc1_32(rw, OFF_CF + 256u)) < 32u && --budget > 0) __builtin_amdgcn_s_sleep(8);
      }
    } else if (w == 1) {
      if (__builtin_expect(known < min(s + 4 + TC1, NSEG_), 0)) {
        unsigned lv = vo;
        asm volatile("" : "+v"(lv));
        const unsigned c = ld_sc1_32(rw, OFF_CKV + (unsigned)h * 256u + (lv >> 2)), want = lv < XS_ * 16u ? 0x404u : 0x40404u;
        const unsigned long long miss = __builtin_amdgcn_ballot_w64((c & want) != want);
        known = miss ? __builtin_ctzll(miss) : NSEG_;
        *(unsigned*)(lds + KC_OFF + 8) = known > s + 2 ? 0x404u : 0u;
      }
      if (SL && __builtin_expect(s > 0, 1)) {
        const int lt = min(max(late, 0), 20000);
        t1g += dly[KG1] + (unsigned)((lt > 500 ? lt : lt * GAM >> 2) * kq >> 16);
        unsigned t1, g1;
        clk_pair(t1, g1);
        *(unsigned*)(lds + TG_OFF) = local_of(t1g, rq, t1, g1);
      }
      if (LOCK && __builtin_expect(s > 0, 1)) {
        u32x4 gp;
        unsigned lv = vo;
        asm volatile("" : "+v"(lv));
#define MX(r) "v_max_i32_dpp %1, %1, %1 row_ror:" #r " row_mask:0xf bank_mask:0xf\n v_max_i32_dpp %2, %2, %2 row_ror:" #r " row_mask:0xf bank_mask:0xf\n v_max_i32_dpp %3, %3, %3 row_ror:" #r " row_mask:0xf bank_mask:0xf\n"
        asm volatile("s_waitcnt vmcnt(1)\n ds_read_b128 %0, %1\n s_waitcnt lgkmcnt(0)" : "=&v"(gp) : "v"((unsigned)(size_t)LDS3 + GP_OFF + lv) : "memory");
        asm volatile("v_subrev_u32 %1, %4, %1\n v_subrev_u32 %2, %4, %2\n v_subrev_u32 %3, %4, %3\n" MX(8) MX(4) MX(2) MX(1) "s_nop 0"
                     : "+v"(gp) : "v"(gp[0]), "v"(gp[1]), "v"(gp[2]), "s"(ge) : "memory");
        t1g = ge + __builtin_amdgcn_readfirstlane(gp[0]); t3g = ge + __builtin_amdgcn_readfirstlane(gp[2]);
        unsigned t1, g1;
        clk_pair(t1, g1);
        typedef unsigned u32x3 __attribute__((ext_vector_type(3)));
        *(u32x3*)(lds + TG_OFF) = u32x3{local_of(t1g, rq, t1, g1), ge + gp[1], (unsigned)rq};
      }
    }
    }
    int lf3 = 0;
    float gv;
    u32x2 bw;
    unsigned oa;
    asm volatile("" : "=v"(gv), "=v"(bw), "=v"(oa));
    if (__builtin_expect(last, 0)) {
      unsigned lv = vo;
      asm volatile("" : "+v"(lv));
      const unsigned tk = (NSEG_ - 1) * SEG_ + 8 * w + 4 * (h & 1);
      gv = __uint_as_float(__builtin_amdgcn_raw_buffer_load_b32(rw, (lv >> 2) & 12u, OFF_G + tk * 4u, 16));
      bw = __builtin_amdgcn_raw_buffer_load_b64(rw, (lv & 64u) * 1024u + (lv & 48u) + ((lv >> 5) & 8u), bfoff(jj * 32 + (h & ~1), tk, hb), 16);
      sc = w ? (2 + w) * SET_ : 0u;
      const float omg = rbf(1.0f - gv);
#pragma unroll
      for (int i = 0; i < 2; ++i) bw[i] = cvt2(omg * __uint_as_float(bw[i] << 16), omg * __uint_as_float(bw[i] & 0xffff0000u));
      oa = (lv & 48u) * (D_ / 8) + ((lv >> 2) & 16u) + ((lv >> 5) & 8u);
      asm volatile("" : "+v"(gv), "+v"(bw), "+v"(oa));
      pause(dly[DLYL]);
    }
    if (w == 0 || last) {
      prep3();
      if (S3) __builtin_amdgcn_s_sleep(S3);
      pause(dly[DLY3]);
      __builtin_amdgcn_s_setprio(3);
      f32x4 d;
      u32x4 b[4];
      u32x2 c;
      unsigned t0, t1, r3s = r3r + sc, dlb = dly[DLYB];
      b[0] = oc[0]; b[1] = oc[1];
      asm volatile("" : "=v"(d), "=v"(b[2]), "=v"(b[3]), "=v"(c), "=v"(m), "=v"(t0), "=v"(t1), "=v"(a[0]), "=v"(a[1]), "=v"(a[2]), "=v"(a[3]));
      asm volatile("" : "+s"(r3s), "+s"(dlb), "=s"(thr));
#define RO8 " row_ror:8 row_mask:0xf bank_mask:0xf\n"
#define RQ4 "buffer_load_dwordx4 %0, off, %4, %5 sc1\n buffer_load_dwordx4 %1, off, %4, %5 offset:1024 sc1\n buffer_load_dwordx4 %2, off, %4, %5 offset:2048 sc1\n buffer_load_dwordx4 %3, off, %4, %5 offset:3072 sc1\n"
#define BUD "s_add_i32 %6, %6, -1\n s_cmp_gt_i32 %6, 0\n s_cselect_b32 %7, -2, -1\n v_mov_b64 %8, 0\n v_mov_b64 %9, 0\n"
#define OST "buffer_store_dwordx4 %10, %12, %13, %14 offen sc1\n buffer_store_dwordx4 %11, %12, %13, %14 offen offset:2048 sc1\n"
#define RQO(x) "v"(x[0]), "v"(x[1]), "v"(x[2]), "v"(x[3]), "s"(rt), "s"(r3s), "s"(budget), "s"(thr), "v"(LO2(d)), "v"(HI2(d))
#define BT(d, i, j) "v_bitop3_b32 " d ", %" #i ", %" #j ", %9 bitop3:0x7e\n"
#define SUM(x, to) asm volatile(A4 "s_waitcnt vmcnt(9)\n" MF "%4, %8, %0, %4\n" BT("%5", 10, 11) BT("%6", 12, 13) "s_waitcnt vmcnt(8)\n" MF "%4, %8, %1, %4\n" BT("%7", 14, 15) "v_or3_b32 %5, %5, %6, %7\n" \
                     "s_waitcnt vmcnt(7)\n" MF "%4, %8, %2, %4\n" BT("%6", 16, 17) BT("%7", 18, 19) "s_waitcnt vmcnt(6)\n" MF "%4, %8, %3, %4\n" \
                     "v_or3_b32 %5, %5, %6, %7\n" BT("%6", 20, 21) BT("%7", 22, 23) "v_or3_b32 %5, %5, %6, %7\n" BT("%6", 24, 25) "v_or3_b32 %5, %5, %6, -2\n v_cmp_lt_u32 vcc, %26, %5\n s_cbranch_vccnz " to \
                     :: "v"(x[0]), "v"(x[1]), "v"(x[2]), "v"(x[3]), "v"(d), "v"(m), "v"(t0), "v"(t1), "v"(sel), "v"(tv_), \
                        "v"(x[0][0]), "v"(x[0][1]), "v"(x[0][2]), "v"(x[0][3]), "v"(x[1][0]), "v"(x[1][1]), "v"(x[1][2]), "v"(x[1][3]), \
                        "v"(x[2][0]), "v"(x[2][1]), "v"(x[2][2]), "v"(x[2][3]), "v"(x[3][0]), "v"(x[3][1]), "v"(x[3][2]), "v"(x[3][3]), "s"(thr) : "vcc")
#define PUBM(end, ...) asm volatile("v_add_f32_dpp %1, %1, %1" RO8 "v_add_f32_dpp %2, %2, %2" RO8 "v_add_f32_dpp %3, %3, %3" RO8 "v_add_f32_dpp %4, %4, %4" RO8 \
                   "v_cvt_pk_bf16_f32 %1, %1, %2\n v_cvt_pk_bf16_f32 %2, %3, %4\n v_min_u32 %1, -2, %1\n v_min_u32 %2, -2, %2\n buffer_store_dwordx2 %5, %6, %7, %8 offen sc1\n v_mov_b64 %0, %5\n" end \
                   :: "v"(c), "v"(d[0]), "v"(d[1]), "v"(d[2]), "v"(d[3]), "v"(LO2(d)), "v"(mvo), "s"(rs), "s"(pm) __VA_ARGS__ : "memory")
      SCHED();
      asm volatile(RQ4 OST "s_nop 1\n v_bfrev_b32 %12, 1\n .p2align 6\n 1: s_sub_u32 %15, %15, 1\n s_cbranch_scc0 1b\n s_branch 4f\n"
                   "5:" RQ4 OST BUD :: RQO(a), "v"(b[0]), "v"(b[1]), "v"(ov), "s"(rs), "s"(os), "s"(dlb) : "memory", "scc");
      SCHED();
      SUM(b, "4f");
      SCHED();
      PUBM("s_branch 6f");
      SCHED();
      asm volatile("4:" RQ4 BUD :: RQO(b) : "memory", "scc");
      SCHED();
      SUM(a, "5b");
      SCHED();
      PUBM("6: s_waitcnt vmcnt(1)", , "v"(a[0]), "v"(a[1]), "v"(a[2]), "v"(a[3]), "v"(b[0]), "v"(b[1]), "v"(b[2]), "v"(b[3]), "v"(ov), "s"(os), "s"(r3s), "v"(sel), "v"(tv_), "v"(m), "v"(t0), "v"(t1), "s"(thr));
      asm volatile("" : "+s"(budget));
      asm volatile("" : "+v"(c));
      TR(6);
      T0(3);
      if (__builtin_expect(last, CIN)) {
        u32x2 r;
#pragma unroll
        for (int i = 0; i < 2; ++i) {
          const unsigned x = cvt2(gv * __uint_as_float(c[i] << 16), gv * __uint_as_float(c[i] & 0xffff0000u));
          r[i] = cvt2(__uint_as_float(x << 16) + __uint_as_float(bw[i] << 16), __uint_as_float(x & 0xffff0000u) + __uint_as_float(bw[i] & 0xffff0000u));
        }
        __builtin_amdgcn_raw_buffer_store_b64(r, __builtin_amdgcn_make_buffer_rsrc(out, 0, S_ * D_ * 2, 0x00020000), oa,
                                              (unsigned)(((NSEG_ - 1) * SEG_ + 8 * w + 4 * (h & 1)) * (D_ * 2) + (jj * 32 + (h & ~1)) * 16), OAUX);
        break;
      }
      __builtin_amdgcn_s_setprio(0);
      TR(7);
      SLP(1);
    }
    if (w == 2) qs = s + 1 >= XS_ && s + 1 < NSEG_ - 1 ? OFF_Q + tile8(s + 1, h, hb) + (jj >> 1) * HALF_ + (jj & 1) * 128u : 0u;
    asm volatile("" : "+s"(qs));
    bar();
    TRW();
    asm volatile("" : "=v"(a[0]), "=v"(a[1]), "=v"(a[2]), "=v"(a[3]));
    tg ^= sc >> 23;
    sc = sn; sn = sn == 2u * SET_ ? 0u : sn + SET_;
  }
  TRE(4 + w);
}

DEVI void recur_at(char* lds, rsrc_t rw, const u32x4 rs, const u16* __restrict__ Wo, const char* ws, int wg TRARGD DLYARG) {
  DLYI(LOCK ? 0x8486 : 0x2c87);
  const int l = threadIdx.x & 63, w = __builtin_amdgcn_readfirstlane(threadIdx.x >> 6), g = l >> 4, lo = l & 15;
  const int h = wg >> 2, jj = wg & 3;
  int budget = SPIN_CAP;
  unsigned mz = 0u;
  if (ALG) asm volatile("" : "+s"(mz));
  u32x8 w3[6][2];
  const int er = (lo >> 2 & 1) + 4 * (lo >> 3);
  const unsigned vo = (unsigned)l * 16u;
  const unsigned dvo = vo ^ (unsigned)((w >> 1) * ((g & 1) * 32 + (g & 2) * 64));
  u32x2 no;
  {
    u32x4 p4 = u32x4{POISON, POISON, POISON, POISON};
    asm volatile("" : "+v"(p4));
#pragma unroll
    for (int i = 0; i < 2; ++i) st_sc1s(rw, vo + i * 1024u, ((w & 1) ? OFF_VX : OFF_K) + tile8(w >> 1, h, 0u) + (jj >> 1) * HALF_ + (jj & 1) * 2048u, p4);
    st_sc1s(rw, w < 2 ? vo : 0x80000000u, NearMap{0u}(0u, h >> 3, NB2 + (h & 7) * 2 + (w & 1)) + jj * 1024u, p4);
    const u16* gw = Wo + (size_t)(h * DH_ + (l >> 3)) * D_ + jj * 256 + 64 * w + ((l & 7) ^ (l >> 5)) * 8;
#pragma unroll
    for (int i = 0; i < 16; ++i)
      __builtin_amdgcn_global_load_lds((const unsigned*)(gw + (size_t)i * 8 * D_), (unsigned*)(lds + W3_OFF + w * 16384 + i * 1024 + l * 16), 16, 0, NTO);
    no = launch_no(ws);
    drain_vm();
    asm volatile("" :: "v"(p4));
    TP(0);
    __builtin_amdgcn_s_barrier();
    asm volatile("" ::: "memory");
    if (threadIdx.x == 0) st_sc1_64(rw, OFF_REC + (64u + wg) * 16u, no);
#pragma unroll
    for (int T = 0; T < 6; ++T) {
      const int Tc = T < 4 ? T : (T & 1) + (w & 2);
      const char* base = lds + W3_OFF + (T < 4 ? w * 16384 : 0) + (16 * (g & 1) + 8 * (g >> 1) + er) * 128 + (((lo & 3) * 2 + (T & 1)) ^ (lo >> 3)) * 16 + (Tc >> 1) * 8;
#pragma unroll
      for (int q = 0; q < 8; ++q) {
        const u32x2 v = tr_quad(base + (32 * (q >> 1) + 2 * (q & 1)) * 128);
        w3[T][q >> 2][2 * (q & 3)] = v[0];
        w3[T][q >> 2][2 * (q & 3) + 1] = v[1];
      }
      asm volatile("" : "+v"(w3[T][0]), "+v"(w3[T][1]));
    }
    bar();
    TP(1);
    *(u32x4*)(lds + PS_OFF + threadIdx.x * 16) = u32x4{0u, 0u, 0u, 0u};
    *(unsigned*)(lds + PZ_OFF + threadIdx.x * 4) = 0u;
    *(unsigned*)(lds + KC_OFF + 12) = 0u;
    rec_wait(rw, no, 3u, 64u + 4u * h, 31u);
    if (w == 1) {
      const NearMap nm{near_kind(rw, no)};
      *(unsigned*)(lds + KC_OFF + 4) = nm.k;
      const unsigned pv = (l & 8) ? 0u : POISON;
      u32x4 p3 = u32x4{pv, pv, pv, pv};
      asm volatile("" : "+v"(p3));
#pragma unroll
      for (int i = 0; i < 12; ++i) st_sc1s(rw, vo + (i & 3) * 1024u, nm(i >> 2, h >> 3, NB3 + jj * 8 + (h & 7)), p3);
#pragma unroll
      for (int i = 0; i < 2; ++i) st_sc1s(rw, vo, nm(1u, h >> 3, NB2 + (h & 7) * 2 + i) + jj * 1024u, p4);
      st_sc1s(rw, l < 32 ? vo : 0x80000000u, nm(1u, h >> 3, NB1 + jj) + (h & 7) * 512u, p4);
      drain_vm();
      asm volatile("" :: "v"(p3), "v"(p4));
      if (l == 0) st_sc1_64(rw, OFF_REC + (192u + wg) * 16u, no);
    } else kv_wait(rs, 0, h, budget);
    TP(2);
    kv_first(lds, rs, 0, h, w, vo, budget);
    TP(3);
    bar();
    TP(4);
  }
  const int idx = lo < 8 ? 0x4444 : 0xeeee;
  const int xg = 2 * (w & 1) + (l >> 5), xm = w >> 1;
  const f32x2* pq = (const f32x2*)((const float*)(lds + PQK_OFF) + xm * 320 + ((l >> 2) & 7) * 40 + 8 * xg + 2 * (l & 3));
  const unsigned xvo = (unsigned)((2 * (jj & 1) + xm) * 1024 + xg * 256 + (jj >> 1) * 128 + (l & 31) * 4), kvo = (unsigned)(xg * 128 + (l & 31) * 4);
  unsigned r2b;
  unsigned sc = 0u, sn = SET_;
  unsigned tg = h & 1;
  const unsigned dlyd = LOCK || SL ? dly[DLYD] * 28 : dly[DLY1] + dly[DLYD];
  const unsigned dlyv = (w == 0 ? dly[DLY2] : dly[DLYV] + (w == 1 ? dly[DLYW] : 0u)) * (LOCK && L2 ? 28 : 1), kgv = !LOCK || w == 0 ? 0u : dly[KGV] + (w == 1 ? dly[DLYW] * 28 : 0u);
  unsigned t2 = LOCK ? mt_now() : 0u;
  unsigned xo = vo, xt = POISON - 1u;
  const unsigned vo2 = vo * 2u;
  const unsigned tch = w == TCW && l < 32 ? lo * 128u + (l >> 4) * (OFF_VX - OFF_K) + (jj & 1) * 2048u + (jj >> 1) * HALF_ : 0x80000000u;
  unsigned tcs = 0x80000000u;
  f32x4 ma = zero4(), mb = zero4();
  const int vr = (lo & 3) * 16 + ((8 * (g >> 1) + er) ^ ((lo & 1) * 2 + (lo & 2) * 4));
  unsigned vb = KV_OFF + 8192 + (g & 1) * 1024 + vr * 16, kb = KV_OFF + l * 16;
  const unsigned ls = (unsigned)(size_t)LDS3;
  const unsigned pw1 = ls + PS_OFF + w * 1024 + (g * 8 + (lo & 7)) * 16 + (lo & 8);
  const unsigned pw2 = ls + (g >= 2 ? PS_OFF + w * 1024 + 512 : UM_OFF + 2048) + ((g & 1) * 8 + (lo & 7)) * 32 + (lo >> 3) * 4;
  const unsigned pr0 = ls + PS_OFF + (g * 8 + (lo & 7)) * 16, pr3 = ls + PS_OFF + 512 + ((g & 1) * 8 + (lo & 7)) * 32 + (lo >> 3) * 16;
  const unsigned pr1 = lo < 8 ? pr0 : ls + PZ_OFF, pr2 = lo < 8 ? ls + PZ_OFF : pr0;
  const unsigned oa1 = lo < 8 ? pr0 + 1024u : pr1, oa2 = lo < 8 ? pr2 : pr0 + 1024u, oa3 = g < 2 ? pr3 + 1024u : ls + PZ_OFF;
  const unsigned oam = ls + XV_OFF + (lo & 3) * 128 + ((l >> 2) & 7) * 16;
  const unsigned uma = ls + UM_OFF + max(w - 1, 0) * 1024 + vo;
  unsigned long long w0m;
  asm("s_cmp_eq_u32 %1, 0\n s_cselect_b64 %0, -1, 0" : "=s"(w0m) : "s"(w) : "scc");
  unsigned long long xwm;
  asm("s_cmp_eq_u32 %1, 1\n s_cselect_b64 %0, -1, 0" : "=s"(xwm) : "s"(w) : "scc");
  xwm |= w0m & 0xff00ff00ff00ff00ull;
  const unsigned xw = ls + (w ? XV_OFF + vo : XK_OFF + g * 128 + (lo & 7) * 16);
  const float ninf = g < 2 ? -INFINITY : 0.f;
  u32x4 one = u32x4{0x3f803f80u, 0x3f803f80u, 0x3f803f80u, 0x3f803f80u}, half = u32x4{0x3f003f00u, 0x3f003f00u, 0x3f003f00u, 0x3f003f00u};
  f32x4 nin4 = f32x4{ninf, ninf, ninf, ninf};
  asm volatile("" : "+v"(one), "+v"(half), "+v"(nin4));
  u32x4 kx[8], us[4];
  auto lo4 = [](u32x8 b) { return u32x4{b[0], b[1], b[2], b[3]}; };
  auto hi4 = [](u32x8 b) { return u32x4{b[4], b[5], b[6], b[7]}; };
  u32x4 va[2][4];
  auto useg_a = [&]() {
#pragma unroll
    for (int i = 0; i < 8; ++i) {
      const u32x2 x0 = tr_quad(lds + vb + (i & 3) * 2048 + (i >> 2) * 8), x1 = tr_quad(lds + (vb ^ 32u) + (i & 3) * 2048 + (i >> 2) * 8);
      va[i >> 2][i & 3] = u32x4{x0[0], x0[1], x1[0], x1[1]};
    }
#pragma unroll
    for (int i = 0; i < 8; ++i) kx[i] = *(const u32x4*)(lds + kb + i * 1024);
    vb ^= 16384u; kb ^= 16384u;
  };
  auto useg_b = [&](int T0, int T1) {
#pragma unroll
    for (int T = 0; T < 4; ++T) {
      if (T < T0 || T >= T1) continue;
      f32x4 d[2];
#pragma unroll
      for (int ks = 0; ks < 4; ++ks)
#pragma unroll
        for (int n = 0; n < 2; ++n) d[n] = mfu(va[n][ks], (ks & 1) ? hi4(w3[T][ks >> 1]) : lo4(w3[T][ks >> 1]), ks ? d[n] : f32x4{0.f, 0.f, 0.f, 0.f});
      us[T] = cvt8(d[0], d[1]);
      asm volatile("" : "+v"(us[T]));
    }
    asm volatile("" : "+v"(kx[0]), "+v"(kx[1]), "+v"(kx[2]), "+v"(kx[3]), "+v"(kx[4]), "+v"(kx[5]), "+v"(kx[6]), "+v"(kx[7]));
  };
#define USP 4
  u32x4 rt = rs_tid(rs);
  if (TID) asm volatile("" : "+s"(rt));
  const unsigned qsu = jj < 2 ? __builtin_bit_cast(unsigned, CS) : 0x3f800000u;
  u32x2 qs2 = u32x2{qsu, qsu};
  if (NMX) asm volatile("" : "+s"(qs2));
  unsigned mlo = lo < 8 ? ~0u : 0u, mhi = ~mlo;
  if (PAN) asm volatile("" : "+v"(mlo), "+v"(mhi));
  unsigned c, cnt = 256u, zr = 0u;
  asm volatile("" : "+v"(zr));
  auto pub = [&]() {
    SCHED();
    if (PBA) asm volatile(".p2align %0\n .rept %1\n s_nop 0\n .endr" :: "n"(PBA), "n"(PBN));
    unsigned so1 = r2b + sc, so2 = r2b + BLK_ + (2 * (~jj & 1) + (jj >> 1)) * 512u + sc;
    f32x2 t0, t1, t2, t3;
    asm volatile("" : "+s"(so1), "+s"(so2));
    unsigned t4;
    asm volatile("" : "=v"(t0), "=v"(t1), "=v"(t2), "=v"(t3), "=v"(c), "=v"(t4));
#define PSUM(o0, o1, o2, o3, d, mul) "ds_read_b64 %0, %7 offset:" #o0 "\n ds_read_b64 %1, %7 offset:" #o1 "\n ds_read_b64 %2, %7 offset:" #o2 "\n ds_read_b64 %3, %7 offset:" #o3 "\n" \
      "s_waitcnt lgkmcnt(2)\n v_pk_add_f32 %0, %0, %1\n s_waitcnt lgkmcnt(0)\n v_pk_add_f32 %2, %2, %3\n v_pk_add_f32 %0, %0, %2\n" mul CV d ", %4, %5\n .if " STR(NOMIN) " == 0\n v_min_u32 " d ", -2, " d "\n .endif\n"
    asm volatile("s_waitcnt lgkmcnt(0)\n s_barrier\n" PSUM(0, 3840, 7680, 11520, "%6", ".if " STR(NMX) "\n v_pk_mul_f32 %0, %14, %0\n .endif\n") "buffer_store_dword %6, %8, %10, %11 offen sc1\n s_cmp_eq_u32 %13, 0\n s_cbranch_scc0 2f\n s_barrier\n s_branch 3f\n"
                 "2: s_barrier\n"
                 PSUM(1280, 5120, 8960, 12800, "%4", "")
                 "buffer_store_dword %4, %9, %10, %12 offen sc1\n 3:"
                 :: "v"(t0), "v"(t1), "v"(t2), "v"(t3), "v"(t0[0]), "v"(t0[1]), "v"(c), "v"((unsigned)(size_t)pq), "v"(xvo), "v"(kvo), "s"(rs), "s"(so1), "s"(so2), "s"(xm), "s"(qs2), "v"(t4), "v"(zr), "s"(cnt) : "memory", "scc", "vcc");
    cnt += 256u;
    asm volatile("" : "+v"(c));
    SCHED();
  };
  const NearMap nm{(unsigned)__builtin_amdgcn_readfirstlane(*(const unsigned*)(lds + KC_OFF + 4))};
  const unsigned hb = far_bit(nm.k);
  r2b = NearMap{0u}(0u, h >> 3, NB2 + (h & 7) * 2);
  const unsigned r3o = nm(0u, w, NB3 + jj * 8 + 2 * g + ((lo >> 2) & 1)) + h * 128u + (unsigned)((lo >> 3) * 4 + (lo & 3)) * 16u;
  asm volatile("" ::: "memory");
  *(unsigned*)(lds + CNT_OFF) = 0u;
  useg_a(); useg_b(0, 4);
  if (w == 1) kb = KV_OFF + 8192 + (l ^ ((g & 1) * 2 + (g & 2) * 4)) * 16;
  TP(5);
  pub();
  TP(7);
  TRD;
  for (int s = 0;;) {
    const bool last = s == NSEG_ - 1;
    const unsigned r2c = r2b + sc;
    unsigned m;
    f32x4 d[6], sa, sb, sm;
    u32x4 x[4];
    asm volatile("" : "=v"(d[0]), "=v"(d[1]), "=v"(d[2]), "=v"(d[3]), "=v"(x[0]), "=v"(x[1]), "=v"(x[2]), "=v"(x[3]), "=v"(sa), "=v"(sb), "=v"(sm));
    unsigned dox = w == 0 || last ? 2u : 1u, lim = last ? 5u * SET_ : sc, rlim = last || !NMX ? 0xffffffffu : 0x28000000u;
    asm volatile("" : "+s"(dox), "+s"(lim), "+s"(rlim));
    int lf2 = 0;
    if (LOCK && L2) lf2 = until(t2, dlyv + (s > 1 ? 0u : 84u)) - (int)dlyv; else pause(dlyv);
#define SH8 " row_shr:8 row_mask:0xf bank_mask:0xc\n"
#define SOFT(e4, e5, a1, a2, cs, lo2) "v_max3_f32 %4, %0, %1, %2\n v_max3_f32 %4, %4, " e4 ", " e5 "\n v_max_f32 %4, %4, %3\n s_nop 1\n" \
                   "v_max_f32_dpp %5, %4, %4" RO8 "v_max_f32_dpp %6, %4, %4" RO8 "s_nop 1\n" \
                   "v_permlane16_swap_b32 %5, %6\n v_max_f32 %4, %5, %6\n v_max_f32 %5, %5, %6\n s_nop 1\n" \
                   "v_permlane32_swap_b32 %4, %5\n v_max_f32 %4, %4, %5\n" \
                   "v_sub_f32 %0, %0, %4\n v_sub_f32 %1, %1, %4\n v_sub_f32 %2, %2, %4\n v_sub_f32 %3, %3, %4\n v_sub_f32 " e4 ", " e4 ", %4\n v_sub_f32 " e5 ", " e5 ", %4\n" \
                   "v_mul_f32 %0, " cs ", %0\n v_mul_f32 %1, " cs ", %1\n v_mul_f32 %2, " cs ", %2\n v_exp_f32 %0, %0\n v_exp_f32 %1, %1\n v_mul_f32 %3, " cs ", %3\n" \
                   "v_mul_f32 " e4 ", " cs ", " e4 "\n v_exp_f32 %2, %2\n v_exp_f32 %3, %3\n v_mul_f32 " e5 ", " cs ", " e5 "\n v_cvt_pk_bf16_f32 %0, %0, %1\n v_exp_f32 " e4 ", " e4 "\n" \
                   "v_exp_f32 " e5 ", " e5 "\n v_cvt_pk_bf16_f32 %1, %2, %3\n ds_write_b64 " a1 ", " lo2 "\n v_cvt_pk_bf16_f32 " e4 ", " e4 ", " e5 "\n ds_write2_b32 " a2 ", " e4 ", " e4 " offset1:6"
    if (dox == 2u) {
      TR(0);
      do {
        asm volatile("s_cmp_eq_u32 %11, %12\n s_cbranch_scc0 6f\n buffer_load_dwordx4 %0, off, %4, %5 sc1\n buffer_load_dwordx4 %1, off, %4, %5 offset:1024 sc1\n"
                     "buffer_load_dwordx4 %2, off, %4, %5 offset:2048 sc1\n buffer_load_dwordx4 %3, off, %4, %5 offset:3072 sc1\n s_branch 7f\n"
                     "6: buffer_load_dwordx4 %0, %6, %7, %5 offen sc1\n buffer_load_dwordx4 %1, %6, %7, %5 offen offset:1024 sc1\n"
                     "buffer_load_dwordx4 %2, %6, %7, %5 offen offset:2048 sc1\n buffer_load_dwordx4 %3, %6, %7, %5 offen offset:3072 sc1\n"
                     "s_sleep %13\n buffer_load_dword %8, %9, %7, %10 offen sc1\n 7:"
                     : "=&v"(x[0]), "=&v"(x[1]), "=&v"(x[2]), "=&v"(x[3])
                     : "s"(rt), "s"(r2c), "v"(xo), "s"(rs), "v"(d[0][0]), "v"(vo2), "s"(r2c + BLK_), "s"(lim), "s"(sc), "n"(LVS) : "memory", "scc");
        if (--budget <= 0) xt = POISON;
        SCHED();
#define MX3(a, b, c) "v_max3_u32 %0, %" #a ", %" #b ", %" #c "\n"
        asm volatile(A4 "s_waitcnt vmcnt(3)\n" MF "%2, %5, %1, %7\n" MX3(10, 11, 12) MF "%3, %6, %1, %8\n" MX3(13, 13, 0) MF "%4, %1, %1, %9"
                     : "=&v"(m), "+v"(x[0]), "=&v"(sa), "=&v"(sb), "=&v"(sm) : "v"(kx[0]), "v"(kx[4]), "v"(ma), "v"(mb), "v"(nin4), "v"(x[0][0]), "v"(x[0][1]), "v"(x[0][2]), "v"(x[0][3]));
        SCHED();
        asm volatile("s_waitcnt vmcnt(0)\n" MF "%4, %7, %1, %4\n" MX3(13, 14, 0) MF "%5, %10, %1, %5\n" MX3(15, 16, 0) MF "%6, %1, %1, %6\n" MX3(17, 18, 0)
                     MF "%4, %8, %2, %4\n" MX3(19, 20, 0) MF "%5, %11, %2, %5\n" MX3(21, 22, 0) MF "%6, %2, %2, %6\n" MX3(23, 24, 0)
                     MF "%4, %9, %3, %4\n" MF "%5, %12, %3, %5\n" MF "%6, %3, %3, %6"
                     : "+v"(m), "+v"(x[1]), "+v"(x[2]), "+v"(x[3]), "+v"(sa), "+v"(sb), "+v"(sm)
                     : "v"(kx[1]), "v"(kx[2]), "v"(kx[3]), "v"(kx[5]), "v"(kx[6]), "v"(kx[7]), "v"(x[1][0]), "v"(x[1][1]), "v"(x[1][2]), "v"(x[1][3]),
                       "v"(x[2][0]), "v"(x[2][1]), "v"(x[2][2]), "v"(x[2][3]), "v"(x[3][0]), "v"(x[3][1]), "v"(x[3][2]), "v"(x[3][3]));
        SCHED();
      } while (over(m, xt));
      TR(3);
#define MOV6 A8 "v_mov_b32_dpp %0, %4" SH8 "v_mov_b32_dpp %1, %5" SH8 "v_mov_b32_dpp %2, %6" SH8 "v_mov_b32_dpp %8, %10" SH8 "v_mov_b32_dpp %9, %11" SH8 "v_mov_b32_dpp %3, %7" SH8
#define SC12 "v"(sa[0]), "v"(sa[1]), "v"(sa[2]), "v"(sa[3]), "v"(sb[0]), "v"(sb[1]), "v"(sb[2]), "v"(sb[3]), "v"(sm[0]), "v"(sm[1]), "v"(sm[2]), "v"(sm[3])
      asm volatile("s_cmp_eq_u32 %17, -1\n s_cbranch_scc1 3f\n s_nop " STR(XIL * 2) "\n" MOV6
                   "v_exp_f32 %4, %0\n v_exp_f32 %5, %1\n v_exp_f32 %6, %2\n v_exp_f32 %7, %3\n" CV "%4, %4, %5\n v_exp_f32 %10, %8\n v_exp_f32 %11, %9\n" CV "%5, %6, %7\n"
                   "ds_write_b64 %12, %14\n" CV "%10, %10, %11\n ds_write2_b32 %13, %10, %10 offset1:6\n 9:\n"
                   ".subsection 1\n 3: s_nop 2\n" MOV6 SOFT("%8", "%9", "%12", "%13", "%16", "%15") "\n s_branch 9b\n .subsection 2\n code_to: .fill 64, 4, 0xbf800000\n .subsection 0"
                   :: SC12, "v"(pw1), "v"(pw2), "v"(LO2(sb)), "v"(LO2(sa)), "s"(CS), "s"(rlim) : "memory", "scc");
    }
    if (w != 0) {
      u32x4* vv = x;
      unsigned thr, r2v = r2c + BLK_;
      asm volatile("" : "=s"(thr), "+s"(r2v));
      TR(0);
      asm volatile("5:\n .if " STR(TID) "\n buffer_load_dwordx4 %0, off, %6, %7 sc1\n buffer_load_dwordx4 %1, off, %6, %7 offset:1024 sc1\n .else\n"
                   "buffer_load_dwordx4 %0, %5, %6, %7 offen sc1\n buffer_load_dwordx4 %1, %5, %6, %7 offen offset:1024 sc1\n .endif\n s_add_i32 %8, %8, -1\n s_cmp_gt_i32 %8, 0\n s_cselect_b32 %9, -2, -1"
                   : "=&v"(vv[0]), "=&v"(vv[1]), "=&v"(d[4]), "=&v"(d[5]), "=&v"(m) : "v"(vo), "s"(TID ? rt : rs), "s"(r2v), "s"(budget), "s"(thr) : "memory", "scc");
      if (TCH) asm volatile("s_mov_b32 m0, %0\n s_nop 0\n buffer_load_dword %1, %2, %3 offen sc1 lds" :: "n"(UM_OFF + 2560), "v"(tch), "s"(rs), "s"(tcs) : "memory");
      SCHED();
      asm volatile("s_nop 3\n v_mov_b64 %0, 0\n v_mov_b64 %1, 0\n v_mov_b64 %2, 0\n v_mov_b64 %3, 0\n v_mov_b64 %4, 0\n v_mov_b64 %5, 0\n"
                   "v_mov_b64 %6, 0\n v_mov_b64 %7, 0\n v_mov_b64 %8, 0\n v_mov_b64 %9, 0\n v_mov_b64 %10, 0\n v_mov_b64 %11, 0"
                   :: "v"(LO2(d[0])), "v"(HI2(d[0])), "v"(LO2(d[1])), "v"(HI2(d[1])), "v"(LO2(d[2])), "v"(HI2(d[2])),
                      "v"(LO2(d[3])), "v"(HI2(d[3])), "v"(LO2(d[4])), "v"(HI2(d[4])), "v"(LO2(d[5])), "v"(HI2(d[5])));
      SCHED();
      __builtin_amdgcn_s_setprio(0);
      SCHED();
      asm volatile(A4 "s_waitcnt vmcnt(1 + " STR(TC1) ")\n" SM "%5, %0, %8, %15\n v_max3_u32 %7, %16, %17, %18\n" SM "%6, %0, %9, %15\n s_nop 0\n s_waitcnt vmcnt(" STR(TC1) ")\n"
                   SM "%5, %1, %10, %15\n v_max3_u32 %7, %19, %20, %7\n" SM "%6, %1, %11, %15\n v_max3_u32 %7, %21, %22, %7\n"
                   SM "%2, %0, %12, %15\n v_max3_u32 %7, %23, %23, %7\n" SM "%3, %0, %13, %15\n v_cmp_lt_u32 vcc, %24, %7\n" SM "%4, %0, %14, %15\n s_cbranch_vccnz 5b"
                   :: "v"(vv[0]), "v"(vv[1]), "v"(d[0]), "v"(d[1]), "v"(d[2]), "v"(d[4]), "v"(d[5]), "v"(m),
                      "v"(w3[4][0]), "v"(w3[5][0]), "v"(w3[4][1]), "v"(w3[5][1]), "v"(w3[0][0]), "v"(w3[1][0]), "v"(w3[2][0]), "v"(idx),
                      "v"(vv[0][0]), "v"(vv[0][1]), "v"(vv[0][2]), "v"(vv[0][3]), "v"(vv[1][0]), "v"(vv[1][1]), "v"(vv[1][2]), "v"(vv[1][3]), "s"(thr), "s"(r2v), "v"(d[3]) : "vcc");
      SCHED();
      asm volatile(CV "%8, %8, %9\n" CV "%9, %10, %11\n" CV "%10, %12, %13\n" CV "%11, %14, %15\n ds_write_b128 %6, %7\n"
                   SM "%5, %0, %16, %21\n" SM "%2, %1, %17, %21\n" SM "%3, %1, %18, %21\n" SM "%4, %1, %19, %21\n" SM "%5, %1, %20, %21"
                   :: "v"(vv[0]), "v"(vv[1]), "v"(d[0]), "v"(d[1]), "v"(d[2]), "v"(d[3]), "v"(uma), "v"(d[4]),
                      "v"(d[4][0]), "v"(d[4][1]), "v"(d[4][2]), "v"(d[4][3]), "v"(d[5][0]), "v"(d[5][1]), "v"(d[5][2]), "v"(d[5][3]),
                      "v"(w3[3][0]), "v"(w3[0][1]), "v"(w3[1][1]), "v"(w3[2][1]), "v"(w3[3][1]), "v"(idx) : "memory");
      SCHED();
    }
    TR(4);
    T0(8);
    f32x4 su, c0, c1;
    u32x4 p0, p1, p2, p3;
    SCHED();
    asm volatile("s_waitcnt lgkmcnt(0)" : "=v"(su), "=v"(c0), "=v"(c1), "=v"(p0), "=v"(p1), "=v"(p2), "=v"(p3) :: "memory");
    __builtin_amdgcn_s_barrier();
    TR(5);
#define RDP "ds_read_b128 %0, %4\n ds_read_b128 %1, %5\n ds_read_b128 %2, %6\n ds_read_b128 %3, %7\n"
    asm volatile("ds_read_b128 %0, %2\n ds_read_b128 %1, %3\n s_nop 0\n s_nop 0\n" CV "%4, %4, %5\n" CV "%5, %6, %7\n" CV "%6, %8, %9\n" CV "%7, %10, %11\n" CV "%12, %12, %13\n" CV "%13, %14, %15\n" CV "%14, %16, %17\n" CV "%15, %18, %19\n"
                 "s_mov_b64 exec, %23\n ds_read_b128 %20, %22\n ds_read_b128 %21, %22 offset:1024\n s_mov_b64 exec, -1"
                 :: "v"(p0), "v"(p3), "v"(pr0), "v"(pr3),
                   "v"(d[0][0]), "v"(d[0][1]), "v"(d[0][2]), "v"(d[0][3]), "v"(d[1][0]), "v"(d[1][1]), "v"(d[1][2]), "v"(d[1][3]),
                   "v"(d[2][0]), "v"(d[2][1]), "v"(d[2][2]), "v"(d[2][3]), "v"(d[3][0]), "v"(d[3][1]), "v"(d[3][2]), "v"(d[3][3]), "v"(d[0]), "v"(d[2]), "v"(uma), "s"(w0m) : "memory");
    SCHED();
#define AN(d, s, m) "v_and_b32 %" #d ", %" #m ", %" #s "\n"
    asm volatile(A8 "s_waitcnt lgkmcnt(2)\n" AN(17, 13, 25) AN(18, 14, 25) AN(19, 15, 25) MF "%0, %8, %3, 0\n" AN(20, 16, 25) AN(21, 13, 26) MF "%0, %9, %6, %0\n"
                 AN(22, 14, 26) AN(23, 15, 26) MF "%1, %10, %4, 0\n" AN(24, 16, 26) "s_waitcnt lgkmcnt(1)\n" MF "%2, %12, %4, 0\n"
                 MF "%1, %11, %5, %1\n" MF "%1, %7, %6, %1\n" MF "%2, %27, %5, %2\n"
                 :: "v"(su), "v"(c0), "v"(c1), "v"(p0), "v"(p1), "v"(p2), "v"(p3), "v"(d[0]), "v"(one), "v"(half), "v"(us[0]), "v"(us[1]), "v"(us[2]),
                    "v"(p0[0]), "v"(p0[1]), "v"(p0[2]), "v"(p0[3]), "v"(p1[0]), "v"(p1[1]), "v"(p1[2]), "v"(p1[3]), "v"(p2[0]), "v"(p2[1]), "v"(p2[2]), "v"(p2[3]),
                    "v"(mlo), "v"(mhi), "v"(us[3]) : "memory");
    SCHED();
    unsigned so;
#define TAIL "v_pk_mul_f32 %2, %2, %17 op_sel_hi:[1,0]\n v_pk_mul_f32 %3, %3, %17 op_sel_hi:[1,0]\n v_cvt_pk_bf16_f32 %6, %6, %7\n v_cvt_pk_bf16_f32 %7, %8, %9\n" \
             "v_pk_mul_f32 %4, %4, %17 op_sel_hi:[1,0]\n v_pk_mul_f32 %5, %5, %17 op_sel_hi:[1,0]\n v_and_or_b32 %6, %6, -2, %14\n v_cvt_pk_bf16_f32 %8, %10, %11\n" \
             "v_cvt_pk_bf16_f32 %9, %12, %13\n v_and_or_b32 %7, %7, -2, %14\n v_and_or_b32 %8, %8, -2, %14\n v_and_or_b32 %9, %9, -2, %14\n" \
             "buffer_store_dwordx4 %1, %15, %16, %0 offen sc1\n s_nop 1"
#define TOPS "v"(c0), "v"(LO2(c0)), "v"(HI2(c0)), "v"(LO2(c1)), "v"(HI2(c1)), "v"(c0[0]), "v"(c0[1]), "v"(c0[2]), "v"(c0[3]), "v"(c1[0]), "v"(c1[1]), "v"(c1[2]), "v"(c1[3]), \
             "s"(tg), "v"(r3o), "s"(rs), "v"(LO2(su))
    asm volatile(".if " STR(PAN) "\n s_waitcnt lgkmcnt(0)\n v_rcp_f32 %21, %21\n .else\n v_rcp_f32_e64 %21, %21\n .endif\n" MF "%18, %19, %20, %18\n"
                 "v_add_u32 %22, 0xd4800000, %21\n .if " STR(PAN) "\n v_cmp_lt_u32 vcc, %23, %22\n .else\n v_cmp_lt_u32_e64 vcc, %23, %22\n .endif\n s_cmp_lg_u64 vcc, 0\n s_cselect_b32 %0, 0x80000000, %24\n .if " STR(PAN) " == 0\n s_nop 0\n .endif\n" TAIL
                 : "=&s"(so) : TOPS, "v"(c1), "v"(d[2]), "v"(p3), "v"(su[0]), "v"(su[2]), "s"(rlim), "s"(sc) : "memory", "vcc", "scc");
    SCHED();
    asm volatile("" : "+s"(budget));
    if (__builtin_expect(so != lim, CIN)) {
      if (so == 0x80000000u) {
        if (w == 0) {
          asm volatile(SOFT("%7", "%8", "%9", "%10", "1.0", "%11") "\n ds_write_b32 %12, %13"
                       :: "v"(sa[0]), "v"(sa[1]), "v"(sa[2]), "v"(sa[3]), "v"(sb[0]), "v"(sb[1]), "v"(sb[2]), "v"(sm[0]), "v"(sm[1]),
                          "v"(pw1 + 2048u), "v"(g >= 2 ? pw2 + 2048u : pw2), "v"(LO2(sa)), "v"(ls + KC_OFF + 12u), "v"(s + 1) : "memory");
        }
        unsigned fv;
        do asm volatile("ds_read_b32 %0, %1\n s_waitcnt lgkmcnt(0)" : "=&v"(fv) : "v"(ls + KC_OFF + 12u) : "memory");
        while (__builtin_amdgcn_readfirstlane(fv) != (unsigned)(s + 1) && --budget > 0);
      }
      asm volatile("" : "+v"(d[0]), "+v"(d[2]));
      unsigned lv = vo;
      asm volatile("" : "+v"(lv));
      const unsigned z1 = (lv & 128u) ? PZ_OFF : pr0 - ls, z2 = (lv & 128u) ? pr0 - ls : PZ_OFF, g1 = (lv & 128u) ? 0u : 1024u;
      u32x4 r[3];
#pragma unroll
      for (int j = 1; j < 4; ++j) {
        const u32x4 q0 = *(const u32x4*)(lds + (pr0 - ls) + j * 1024), q3 = *(const u32x4*)(lds + (pr3 - ls) + j * 1024);
        const u32x4 q1 = *(const u32x4*)(lds + z1 + j * g1), q2 = *(const u32x4*)(lds + z2 + j * (1024u - g1));
        const f32x4 z = f32x4{0.f, 0.f, 0.f, 0.f};
        const float ir = __builtin_amdgcn_rcpf(mfu(half, q3, mfu(one, q0, z))[0]);
        const f32x4 e0 = mfu(__builtin_bit_cast(u32x4, d[0]), q3, mfu(us[1], q2, mfu(us[0], q1, z))) * ir, e1 = mfu(__builtin_bit_cast(u32x4, d[2]), q3, mfu(us[3], q2, mfu(us[2], q1, z))) * ir;
        r[j - 1] = (cvt8(e0, e1) & ~1u) | tg;
        asm volatile("" : "+v"(r[j - 1]));
        st_sc1s(rw, last || j == 2 ? r3o : 0x80000000u, last ? (2 + j) * SET_ : sc, r[j - 1]);
      }
      asm volatile("s_nop 1" :: "v"(r[0]), "v"(r[1]), "v"(r[2]));
    }
    TR(6);
    T0(9);
    SLP(0);
    asm volatile("" : "=v"(kx[0]), "=v"(kx[1]), "=v"(kx[2]), "=v"(kx[3]), "=v"(kx[4]), "=v"(kx[5]), "=v"(kx[6]), "=v"(kx[7]));
    if (last) break;
    asm volatile("s_mov_b64 exec, %5\n ds_write_b128 %4, %0\n ds_write_b128 %4, %1 offset:1024\n s_and_b64 exec, exec, %6\n"
                 "ds_write_b128 %4, %2 offset:512\n ds_write_b128 %4, %3 offset:1536\n ds_write_b32 %7, %8\n s_mov_b64 exec, -1"
                 :: "v"(x[0]), "v"(x[1]), "v"(x[2]), "v"(x[3]), "v"(xw), "s"(xwm), "s"(w0m), "v"(ls + XK_OFF + 2048u), "v"(s) : "memory", "scc");
    if (__builtin_expect(s < XS_, 0))
      if (jj & 2) st_sc1s_32(rw, kvo, kmoff(s, h, hb) + (2 * (jj & 1) + xm) * 512u, c);
    const u32x2 kc2 = *(const u32x2*)(lds + KC_OFF + 40);
    const unsigned kc = kc2[0];
    useg_a(); useg_b(0, USP);
    if (USP < 4) asm volatile("" : "+v"(va[0][0]), "+v"(va[0][1]), "+v"(va[0][2]), "+v"(va[0][3]), "+v"(va[1][0]), "+v"(va[1][1]), "+v"(va[1][2]), "+v"(va[1][3]));
    TRX(0);
    const bool have = __builtin_amdgcn_readfirstlane(kc) == 0x404u;
    unsigned ks = kv_src(s + 2, h, w, hb), kd = kv_dst(s, w), kv = have && s < NSEG_ - 2 ? dvo : 0x80000000u;
    if (TCH) {
      tcs = __builtin_amdgcn_readfirstlane(kc2[1]) && s < NSEG_ - 3 ? OFF_K + tile8(s + 3, h, hb) : 0x80000000u;
      asm volatile("" : "+s"(tcs));
    }
    asm volatile("" : "+s"(ks), "+s"(kd), "+v"(kv));
    if (D4) pause(dly[DLY4]);
    bar();
#define KVE 2
    if (w == KVE || KVE == 8) kv_dma(rs, kv, ks, kd);
    r2b = nm(0u, h >> 3, NB2 + (h & 7) * 2);
    tg ^= sc >> 23;
    sc = sn; sn = sn == 2u * SET_ ? 0u : sn + SET_;
    TRW();
    useg_b(USP, 4);
    if (w == 1 && s >= XS_) {
      u32x4 p1, p2, p3;
      u32x2 vm[8];
      asm volatile("ds_read_b128 %0, %11\n ds_read_b128 %1, %12\n ds_read_b128 %2, %13\n"
                   "ds_read_b64_tr_b16 %3, %14\n ds_read_b64_tr_b16 %4, %14 offset:512\n ds_read_b64_tr_b16 %5, %14 offset:8\n ds_read_b64_tr_b16 %6, %14 offset:520\n"
                   "ds_read_b64_tr_b16 %7, %14 offset:1024\n ds_read_b64_tr_b16 %8, %14 offset:1536\n ds_read_b64_tr_b16 %9, %14 offset:1032\n ds_read_b64_tr_b16 %10, %14 offset:1544\n"
                   "s_waitcnt lgkmcnt(0)"
                   : "=&v"(p1), "=&v"(p2), "=&v"(p3), "=&v"(vm[0]), "=&v"(vm[1]), "=&v"(vm[2]), "=&v"(vm[3]), "=&v"(vm[4]), "=&v"(vm[5]), "=&v"(vm[6]), "=&v"(vm[7])
                   : "v"(oa1), "v"(oa2), "v"(oa3), "v"(oam) : "memory");
      const f32x4 su = mfu(one, p3, mfu(one, p2, mfu(one, p1, f32x4{0.f, 0.f, 0.f, 0.f})));
      f32x4 o[4];
#pragma unroll
      for (int i = 0; i < 4; ++i) o[i] = mfu(kx[4 * (i >> 1) + (i & 1)], p1, f32x4{0.f, 0.f, 0.f, 0.f});
#pragma unroll
      for (int i = 0; i < 4; ++i) o[i] = mfu(kx[4 * (i >> 1) + 2 + (i & 1)], p2, o[i]);
#pragma unroll
      for (int i = 0; i < 4; ++i) o[i] = mfu(u32x4{vm[2 * i][0], vm[2 * i][1], vm[2 * i + 1][0], vm[2 * i + 1][1]}, p3, o[i]);
      SCHED();
      const float ir = __builtin_amdgcn_rcpf(su[0]);
      u32x4 oc[2];
#pragma unroll
      for (int i = 0; i < 8; ++i) oc[i >> 2][i & 3] = cvt2x(o[i >> 1][2 * (i & 1)] * ir, o[i >> 1][2 * (i & 1) + 1] * ir);
      *(u32x4*)(lds + OB_OFF + vo) = oc[0];
      *(u32x4*)(lds + OB_OFF + 1024 + vo) = oc[1];
      if (__builtin_expect(s == NL_ - 1, 0)) {
        unsigned lv = vo;
        asm volatile("" : "+v"(lv));
        ostore(rs, oc, ovoff(lv), osoff(s, h, jj, hb));
      }
      TRX(6);
    }
    if (w != KVE && KVE != 8) {
      pause(max((int)dlyd - (w == 1 && s >= XS_ ? 19 : 0) - 5 * (4 - USP), 0));
      kv_dma(rs, kv, ks, kd);
    }
    ++s;
    if (__builtin_expect(!have && s < NSEG_ - 1, 0))
      if (__builtin_amdgcn_readfirstlane(*(const unsigned*)(lds + KC_OFF + 8)) == 0x404u) kv_dma(rs, dvo, ks, kd);
    if (__builtin_expect(s == NSEG_ - 1, 0)) {
      static_assert((NSEG_ - 1) % 3 == 0, "set 0");
      asm volatile("" : "+v"(xo));
      const int qi = 8 * w + (int)((xo >> 4) & 7u) - 4 * (int)(xo >> 8);
#pragma unroll
      for (int r = 0; r < 4; ++r) {
        ma[r] = r <= qi ? 0.f : rbf(-1000000000.0f) / SCALE_;
        mb[r] = 16 + r <= qi ? 0.f : rbf(-1000000000.0f) / SCALE_;
      }
      if (w == 1) {
#pragma unroll
        for (int i = 0; i < 8; ++i) kx[i] = *(const u32x4*)(lds + KV_OFF + 16384 + xo + i * 1024);
        asm volatile("" : "+v"(kx[0]), "+v"(kx[1]), "+v"(kx[2]), "+v"(kx[3]), "+v"(kx[4]), "+v"(kx[5]), "+v"(kx[6]), "+v"(kx[7]));
      }
      xt = (xo & 128u) ? xt : POISON;
      xo += (xo & 128u) ? 0u : OFF_Q + tile8(NSEG_ - 1, h, hb) + (w >> 1) * HALF_ + (w & 1) * 128u - r2b;
    }
    pub();
    TR(2);
  }
  asm volatile("" : "+s"(no));
  if (wg == 0 && w == 0 && vo == 0u) st_sc1_64(rw, OFF_EP, no);
  TRE(w);
}

DEVI void recur_role(char* lds, char* ws, rsrc_t rw, const u16* __restrict__ mem0, const u16* __restrict__ Wq, const u16* __restrict__ Wk,
                     const u16* __restrict__ Wv, const u16* __restrict__ Wo, u16* __restrict__ out, int wg TRARGD) {
  const u32x4 rs = u32x4{(unsigned)(size_t)ws, (unsigned)((size_t)ws >> 32) & 0xffffu, WS_USED, 0x00020000u};
  if (__builtin_amdgcn_readfirstlane(threadIdx.x) >= 256) recur_p1(lds, rw, rs, mem0, Wq, Wk, Wv, out, ws, wg TRARG DLYA);
  else recur_at(lds, rw, rs, Wo, ws, wg TRARG DLYA);
}

#define HTR(i)
#define XSLP 16
#define PD1 8
#define ODS 3
#define OB0 17
#define OB1 20
#define OB2 14
#define OPR 2
#define OLIM 16
#define ON0 4
#define CPF 1
#define OFA 11
#define FSLP 16
template <int SLP>
DEVI unsigned poll_flags(rsrc_t rw, unsigned off, unsigned f) {
#pragma nounroll
  for (int it = 0; it < HSPIN_CAP; ++it) {
    if (__all(f != 0u)) break;
    __builtin_amdgcn_s_sleep(SLP);
    f = ld_sc1_32(rw, off);
  }
  return f;
}
DEVI void after(unsigned f, int d) {
  const unsigned t = __builtin_amdgcn_readfirstlane(f) + (unsigned)d;
#pragma nounroll
  for (int k = 0; k < 4 && t != 1u + (unsigned)d; ++k) {
    const int r = min((int)(t - rt_now()), 4096);
    if (r < 4) break;
#pragma nounroll
    for (int i = r * 9 >> 5; i > 0; --i) __builtin_amdgcn_s_sleep(1);
    if (r < 40) break;
  }
}
DEVI void xattn_wave(rsrc_t rw, int s, int hq, unsigned hb) {
  int tx = threadIdx.x;
  asm volatile("" : "+v"(tx));
  const int l = tx & 63, g = l >> 4, lo = l & 15, h = hq * 8 + __builtin_amdgcn_readfirstlane(tx >> 6);
#pragma nounroll
  for (int it = 0; it < HSPIN_CAP; ++it) {
    const unsigned f = ld_sc1_32(rw, l < 32 ? OFF_CKV + (unsigned)(h * 64 + s) * 4u : cooff(h, hb) + (unsigned)(s * 16 + (l & 3) * 4));
    if (__all((f & 0xffffffu) == (l < 32 ? 0x40404u : 2u))) break;
    __builtin_amdgcn_s_sleep(XSLP);
  }
  asm volatile("" ::: "memory");
  const unsigned lv = (unsigned)l * 16u, so = tile8(s, h, hb);
  u32x4 bq[4][2], kx[8], vx[8];
#pragma unroll
  for (int ks = 0; ks < 4; ++ks) {
#pragma unroll
    for (int n = 0; n < 2; ++n) bq[ks][n] = ld_sc1s(rw, lv + ks * 1024u, OFF_Q + so + n * HALF_);
    kx[ks] = ld_sc1s(rw, lv + ks * 1024u, OFF_K + so);
    kx[4 + ks] = ld_sc1s(rw, lv + ks * 1024u, OFF_K + so + HALF_);
  }
#pragma unroll
  for (int te = 0; te < 8; ++te) vx[te] = ld_sc1s(rw, lv + (te & 3) * 1024u, OFF_VX + so + (te >> 2) * HALF_);
  constexpr float L2E = 1.4426950408889634f;
  float m1[2], s1[2];
  f32x4 os[8][2];
  auto seg_part = [&]() {
    f32x4 st[2][2];
#pragma unroll
    for (int i = 0; i < 4; ++i) st[i >> 1][i & 1] = f32x4{0.f, 0.f, 0.f, 0.f};
#pragma unroll
    for (int ks = 0; ks < 4; ++ks) {
      st[0][0] = mfu(kx[ks], bq[ks][0], st[0][0]);
      st[0][1] = mfu(kx[ks], bq[ks][1], st[0][1]);
      st[1][1] = mfu(kx[4 + ks], bq[ks][1], st[1][1]);
    }
#pragma unroll
    for (int n = 0; n < 2; ++n) {
      float sv[8], mx = -3.0e38f, sum = 0.f;
#pragma unroll
      for (int i = 0; i < 8; ++i) {
        sv[i] = 16 * (i >> 2) + 4 * g + (i & 3) <= 16 * n + lo ? rbf(rbf(st[i >> 2][n][i & 3]) * SCALE_) : rbf(-1000000000.0f);
        mx = fmaxf(mx, sv[i]);
      }
      mx = rows_max(mx);
#pragma unroll
      for (int i = 0; i < 8; ++i) { sv[i] = __builtin_amdgcn_exp2f((sv[i] - mx) * L2E); sum += sv[i]; }
      m1[n] = mx;
      s1[n] = rows_sum(sum);
      const u32x4 pb = u32x4{cvt2(sv[0], sv[1]), cvt2(sv[2], sv[3]), cvt2(sv[4], sv[5]), cvt2(sv[6], sv[7])};
#pragma unroll
      for (int te = 0; te < 8; ++te) os[te][n] = mfu(vx[te], pb, f32x4{0.f, 0.f, 0.f, 0.f});
    }
  };
  u32x4 km[4], vm[4];
#pragma unroll
  for (int i = 0; i < 4; ++i) km[i] = ld_sc1(rw, kmoff(s, h, hb) + (unsigned)(i * 512 + g * 128 + (lo & 7) * 16));
#pragma unroll
  for (int i = 0; i < 4; ++i) vm[i] = ld_sc1(rw, kmoff(s, h, hb) + 2048u + (unsigned)(i * 512 + (l & 31) * 16));
  seg_part();
  f32x4 sm[2];
#pragma unroll
  for (int ks = 0; ks < 4; ++ks)
#pragma unroll
    for (int n = 0; n < 2; ++n) sm[n] = mfu(km[ks], bq[ks][n], ks ? sm[n] : f32x4{0.f, 0.f, 0.f, 0.f});
  s16x4 pm[2];
  float cs[2];
#pragma unroll
  for (int n = 0; n < 2; ++n) {
    float sv[4], mx = m1[n], sum = 0.f;
#pragma unroll
    for (int r = 0; r < 4; ++r) {
      sv[r] = g < 2 ? rbf(rbf(sm[n][r]) * SCALE_) : -INFINITY;
      mx = fmaxf(mx, sv[r]);
    }
    mx = rows_max(mx);
#pragma unroll
    for (int r = 0; r < 4; ++r) { sv[r] = __builtin_amdgcn_exp2f((sv[r] - mx) * L2E); sum += sv[r]; }
    const float cr = __builtin_amdgcn_exp2f((m1[n] - mx) * L2E);
    const float inv = __builtin_amdgcn_rcpf(__builtin_fmaf(s1[n], cr, rows_sum(sum)));
    cs[n] = cr * inv;
    pm[n] = __builtin_bit_cast(s16x4, u32x2{cvt2(sv[0] * inv, sv[1] * inv), cvt2(sv[2] * inv, sv[3] * inv)});
  }
#pragma unroll
  for (int n = 0; n < 2; ++n)
#pragma unroll
    for (int a = 0; a < 4; ++a) {
      f32x4 o[2];
#pragma unroll
      for (int i = 0; i < 2; ++i)
        o[i] = __builtin_amdgcn_mfma_f32_16x16x16bf16_1k(__builtin_bit_cast(s16x4, u32x2{vm[a][2 * i], vm[a][2 * i + 1]}), pm[n], os[2 * a + i][n] * cs[n], 0, 0, 0);
      st_sc1(rw, ooff(s, h, hb) + n * HALF_ + (unsigned)(a * 1024 + l * 16),
             u32x4{cvt2x(o[0][0], o[0][1]), cvt2x(o[0][2], o[0][3]), cvt2x(o[1][0], o[1][1]), cvt2x(o[1][2], o[1][3])});
    }
  drain_vm();
  if (l < 4) st_sc1_32(rw, cooff(h, hb) + (unsigned)(s * 16 + l * 4), 1u);
}

typedef f32x2 cf;
DEVI cf cmul(cf a, cf b) { return __builtin_elementwise_fma(cf{a.y, a.y}, cf{-b.y, b.x}, cf{a.x, a.x} * b); }
DEVI cf mni(cf a) { return cf{a.y, -a.x}; }
DEVI cf wexp(float r) {
  float sn, cs;
  sincospif(r, &sn, &cs);
  return cf{cs, -sn};
}
DEVI void dft4(cf& a0, cf& a1, cf& a2, cf& a3) {
  const cf t0 = a0 + a2, t1 = a0 - a2, t2 = a1 + a3, t3 = mni(a1 - a3);
  a0 = t0 + t2; a1 = t1 + t3; a2 = t0 - t2; a3 = t1 - t3;
}
constexpr float RH_ = 0.70710678118654752f, C8_ = 0.92387953251128674f, S8_ = 0.38268343236508977f;
DEVI cf w8_1(cf a) { return (a + mni(a)) * RH_; }
DEVI cf w8_3(cf a) { return (mni(a) - a) * RH_; }
template <int ST>
DEVI void dft8(cf* v) {
  dft4(v[0], v[2 * ST], v[4 * ST], v[6 * ST]);
  dft4(v[ST], v[3 * ST], v[5 * ST], v[7 * ST]);
  const cf e[4] = {v[0], v[2 * ST], v[4 * ST], v[6 * ST]}, o[4] = {v[ST], w8_1(v[3 * ST]), mni(v[5 * ST]), w8_3(v[7 * ST])};
#pragma unroll
  for (int c = 0; c < 4; ++c) { v[c * ST] = e[c] + o[c]; v[(c + 4) * ST] = e[c] - o[c]; }
}
DEVI void dft16(cf* v) {
#pragma unroll
  for (int b = 0; b < 4; ++b) dft4(v[b], v[4 + b], v[8 + b], v[12 + b]);
  v[5] = cmul(v[5], cf{C8_, -S8_}); v[6] = w8_1(v[6]); v[7] = cmul(v[7], cf{S8_, -C8_});
  v[9] = w8_1(v[9]); v[10] = mni(v[10]); v[11] = w8_3(v[11]);
  v[13] = cmul(v[13], cf{S8_, -C8_}); v[14] = w8_3(v[14]); v[15] = cmul(v[15], cf{-C8_, S8_});
  cf y[16];
#pragma unroll
  for (int c = 0; c < 4; ++c) {
    dft4(v[4 * c], v[4 * c + 1], v[4 * c + 2], v[4 * c + 3]);
#pragma unroll
    for (int d = 0; d < 4; ++d) y[c + 4 * d] = v[4 * c + d];
  }
#pragma unroll
  for (int i = 0; i < 16; ++i) v[i] = y[i];
}
template <int N, int ST>
DEVI void twiddle(cf* v, const cf* b) {
  cf w[8];
  w[1] = b[0]; w[2] = b[1]; w[4] = b[2];
  w[3] = cmul(w[1], w[2]); w[5] = cmul(w[1], w[4]); w[6] = cmul(w[2], w[4]); w[7] = cmul(w[3], w[4]);
#pragma unroll
  for (int i = 1; i < 8; ++i) v[i * ST] = cmul(v[i * ST], w[i]);
  if (N == 16) {
    v[8 * ST] = cmul(v[8 * ST], b[3]);
#pragma unroll
    for (int i = 1; i < 8; ++i) v[(8 + i) * ST] = cmul(v[(8 + i) * ST], cmul(w[i], b[3]));
  }
}
template <int N>
DEVI void wpow(cf* b, float r) {
#pragma unroll
  for (int i = 0; i < N; ++i) b[i] = wexp(r * (float)(1 << i));
}
DEVI u32x4 cc2u(cf a, cf b) { return u32x4{__float_as_uint(a.x), __float_as_uint(a.y), __float_as_uint(b.x), __float_as_uint(b.y)}; }

DEVI void fft2048(int t, char* lds, cf* v, const cf* b1, const cf* b2) {
  const int p = t & 3, j = t >> 2;
  dft16(v);
  twiddle<16, 1>(v, b1);
#pragma unroll
  for (int i = 0; i < 16; ++i) *(cf*)(lds + i * 4096 + t * 8) = v[i];
  bar();
#pragma unroll
  for (int i = 0; i < 16; ++i) v[i] = *(const cf*)(lds + (j >> 3) * 4096 + i * 256 + (t & 31) * 8);
  dft16(v);
  twiddle<16, 1>(v, b2);
  char* l2 = lds + 65536;
#pragma unroll
  for (int i = 0; i < 16; ++i) *(cf*)(l2 + (j & 7) * 8224 + i * 512 + (j >> 3) * 32 + p * 8) = v[i];
  bar();
#pragma unroll
  for (int i = 0; i < 16; ++i) v[i] = *(const cf*)(l2 + (i >> 1) * 8224 + (i & 1) * 4096 + t * 8);
  dft8<2>(v);
  dft8<2>(v + 1);
}
DEVI void fft_cols_fwd(char* lds, rsrc_t rw, int blk, unsigned hb) {
  int t = threadIdx.x;
  asm volatile("" : "+v"(t));
  const int p = t & 3, j = t >> 2;
  u32x4 xv[16];
#pragma unroll
  for (int i = 0; i < 16; ++i) xv[i] = ld_sc1s(rw, (j >> 4) * 65536 + p * 256 + (j & 15) * 16, OFF_XF + far8(32 * i + (blk >> 3), hb) + (blk & 7) * 1024u);
  cf b1[4], b2[4];
  wpow<4>(b1, (float)j * (1.f / 1024.f));
  wpow<4>(b2, (float)(j & 7) * (1.f / 64.f));
#pragma nounroll
  for (int it = 0; it < 4; ++it) {
    asm volatile("" : "+v"(t), "+v"(b1[0]), "+v"(b1[1]), "+v"(b1[2]), "+v"(b1[3]), "+v"(b2[0]), "+v"(b2[1]), "+v"(b2[2]), "+v"(b2[3]));
    cf v[16];
#pragma unroll
    for (int i = 0; i < 16; ++i) {
      v[i] = cf{__uint_as_float(xv[i][0] << 16), __uint_as_float(xv[i][0] & 0xffff0000u)};
      xv[i] = u32x4{xv[i][1], xv[i][2], xv[i][3], 0u};
    }
    bar();
    fft2048(t, lds, v, b1, b2);
#pragma unroll
    for (int i = 8; i < 16; ++i) *(cf*)(lds + (i - 8) * 4096 + t * 8) = v[i];
    bar();
#pragma unroll
    for (int i = 0; i < 8; ++i) {
      const int k = (t >> 2) + 128 * i;
      const cf z = v[i], c = *(const cf*)(lds + ((1024 - k) & 1023) * 32 + (t & 3) * 8);
      cf A = cf{z.x + c.x, z.y - c.y}, B = cf{z.y + c.y, c.x - z.x};
      if (k == 0) { A = cf{2.f * z.x, 2.f * c.x}; B = cf{2.f * z.y, 2.f * c.y}; }
      st_sc1(rw, OFF_T + far8a(128 * i, hb) + (unsigned)((t >> 2) * 16384 + blk * 256 + it * 64 + (t & 3) * 16), cc2u(A, B));
    }
  }
}
#define RPF 1
#define NTF 1
DEVI unsigned wpair(const u16* __restrict__ wr, const u16* __restrict__ wi, int o) {
  return NTF ? pack2(__builtin_nontemporal_load(wr + o), __builtin_nontemporal_load(wi + o)) : pack2(wr[o], wi[o]);
}
typedef unsigned u32x4u __attribute__((ext_vector_type(4), aligned(2)));
DEVI void fft_rows_ask(const u16* __restrict__ wr, const u16* __restrict__ wi, int row, int l, u32x4* raw, unsigned* wv) {
  const int rm = (2048 - row) & 2047;
#pragma unroll
  for (int i = 0; i < 4; ++i) raw[i] = __builtin_nontemporal_load((const u32x4u*)(((i & 1) ? wi : wr) + ((i & 2) ? rm * FB_ + 1 : row * FB_) + 8 * l));
  wv[16] = wpair(wr, wi, (l & 1) ? row * FB_ + 512 : rm * FB_);
}
DEVI void fft_rows_w(char* wl, int l, const u32x4* raw, unsigned* wv) {
#pragma unroll
  for (int i = 0; i < 4; ++i) {
    const u32x4 a = raw[i & 2], b = raw[(i & 2) + 1];
    const int k = 2 * (i & 1);
    *(u32x4*)(wl + (i >> 1) * 2048 + l * 32 + (i & 1) * 16) = u32x4{__builtin_amdgcn_perm(b[k], a[k], 0x05040100u), __builtin_amdgcn_perm(b[k], a[k], 0x07060302u),
                                                                   __builtin_amdgcn_perm(b[k + 1], a[k + 1], 0x05040100u), __builtin_amdgcn_perm(b[k + 1], a[k + 1], 0x07060302u)};
  }
#pragma unroll
  for (int i = 0; i < 16; ++i) wv[i] = *(const unsigned*)(wl + (i < 8 ? (l + 64 * i) * 4 : 2048 + (1023 - l - 64 * i) * 4));
  wv[17] = __builtin_amdgcn_update_dpp(0u, wv[16], 0x101, 0xf, 0xf, true);
}
DEVI cf wval(const unsigned* wv, int i, int l) {
  cf w = cf{__uint_as_float(wv[i] << 16), __uint_as_float(wv[i] & 0xffff0000u)};
  if (i >= 8) w.y = -w.y;
  if ((i & 7) == 0) {
    const unsigned e = wv[16 + (i >> 3)];
    const cf m = (w + cf{__uint_as_float(e << 16), __uint_as_float((e & 0xffff0000u) ^ (i ? 0u : 0x80000000u))}) * 0.5f;
    if (l == 0) w = m;
  }
  return w;
}
DEVI void fft_rows(char* lds, rsrc_t rw, const u16* __restrict__ wr, const u16* __restrict__ wi, int blk, unsigned hb) {
  int t = threadIdx.x;
  asm volatile("" : "+v"(t));
  int l = t & 63, row = blk * 8 + __builtin_amdgcn_readfirstlane(t >> 6);
  char* wl = lds + __builtin_amdgcn_readfirstlane(t >> 6) * 8704;
  unsigned wv[18];
  u32x4 in[8], raw[4];
  auto get = [&](int r) {
#pragma unroll
    for (int i = 0; i < 8; ++i) in[i] = ld_sc1s(rw, (l & 48) * 16 + (l & 3) * 64 + (l & 12) * 4 + (i & 3) * 1024, OFF_T + far8a(r, hb) + (i >> 2) * 4096u);
    fft_rows_ask(wr, wi, r, l, raw, wv);
  };
  get(row);
  cf b1[2][3], b2[4];
  wpow<3>(b1[0], (float)(2 * l) * (1.f / 512.f));
  wpow<3>(b1[1], (float)(2 * l + 1) * (1.f / 512.f));
  wpow<4>(b2, (float)(l & 7) * (1.f / 64.f));
#pragma nounroll
  for (int it = 0; it < 4; ++it, row += 256) {
  auto fresh = [&]() {
    asm volatile("" : "+v"(l), "+v"(b1[0][0]), "+v"(b1[0][1]), "+v"(b1[0][2]), "+v"(b1[1][0]), "+v"(b1[1][1]), "+v"(b1[1][2]), "+v"(b2[0]), "+v"(b2[1]), "+v"(b2[2]), "+v"(b2[3]));
  };
  fresh();
  const unsigned go = far8a(row, hb) + (unsigned)(l * 16);
  cf v[16];
#pragma unroll
  for (int i = 0; i < 8; ++i) {
    v[2 * i] = cf{__uint_as_float(in[i][0]), __uint_as_float(in[i][1])};
    v[2 * i + 1] = cf{__uint_as_float(in[i][2]), __uint_as_float(in[i][3])};
  }
  dft8<2>(v); dft8<2>(v + 1);
  twiddle<8, 2>(v, b1[0]); twiddle<8, 2>(v + 1, b1[1]);
#pragma unroll
  for (int i = 0; i < 8; ++i) *(u32x4*)(wl + i * 1088 + l * 16) = cc2u(v[2 * i], v[2 * i + 1]);
#pragma unroll
  for (int i = 0; i < 16; ++i) v[i] = *(const cf*)(wl + (l >> 3) * 1088 + i * 64 + (l & 7) * 8);
  dft16(v);
  twiddle<16, 1>(v, b2);
#pragma unroll
  for (int i = 0; i < 16; ++i) *(cf*)(wl + (l & 7) * 1040 + i * 64 + (l >> 3) * 8) = v[i];
#pragma unroll
  for (int i = 0; i < 16; ++i) v[i] = *(const cf*)(wl + (i >> 1) * 1040 + (i & 1) * 512 + l * 8);
  dft8<2>(v); dft8<2>(v + 1);
  fft_rows_w(wl, l, raw, wv);
  if (row == 0) {
    unsigned w1[18];
    fft_rows_ask(wr, wi, 1024, l, raw, w1);
    fft_rows_w(wl, l, raw, w1);
#pragma unroll
    for (int i = 0; i < 16; ++i) *(cf*)(wl + i * 512 + l * 8) = v[i];
#pragma unroll
    for (int i = 0; i < 16; ++i) {
      const cf c = *(const cf*)(wl + ((1024 - l - 64 * i) & 1023) * 8), wa = wval(wv, i, l), wb = wval(w1, i, l);
      v[i] = cmul(v[i], (wa + wb) * 0.5f) + cmul(cf{c.x, -c.y}, (wa - wb) * 0.5f);
    }
  } else {
#pragma unroll
    for (int i = 0; i < 16; ++i) v[i] = cmul(v[i], wval(wv, i, l));
  }
#pragma unroll
  for (int i = 0; i < 16; ++i) v[i] = cf{v[i].y, v[i].x};
  asm volatile("" : "+v"(v[0]), "+v"(v[8]), "+v"(v[15]) :: "memory");
  if (it < 3) get(row + 256);
  asm volatile("" ::: "memory");
  fresh();
  dft8<2>(v); dft8<2>(v + 1);
#pragma unroll
  for (int i = 0; i < 16; ++i) *(cf*)(wl + (i >> 1) * 1056 + (i & 1) * 512 + l * 8) = v[i];
#pragma unroll
  for (int i = 0; i < 16; ++i) v[i] = *(const cf*)(wl + (l & 7) * 1056 + i * 64 + (l >> 3) * 8);
  twiddle<16, 1>(v, b2);
  dft16(v);
#pragma unroll
  for (int i = 0; i < 16; ++i) *(cf*)(wl + (l >> 3) * 1088 + i * 64 + (l & 7) * 8) = v[i];
#pragma unroll
  for (int i = 0; i < 8; ++i) {
    const f32x4 a = *(const f32x4*)(wl + i * 1088 + l * 16);
    v[2 * i] = cf{a[0], a[1]}; v[2 * i + 1] = cf{a[2], a[3]};
  }
  twiddle<8, 2>(v, b1[0]); twiddle<8, 2>(v + 1, b1[1]);
  dft8<2>(v); dft8<2>(v + 1);
#pragma unroll
  for (int i = 0; i < 8; ++i) st_sc1(rw, OFF_T + go + i * 1024u, cc2u(cf{v[2 * i].y, v[2 * i].x}, cf{v[2 * i + 1].y, v[2 * i + 1].x}));
  }
}
DEVI void fft_cols_inv(char* lds, rsrc_t rw, int blk, unsigned hb) {
  int t = threadIdx.x;
  asm volatile("" : "+v"(t));
  const int j = t >> 2;
  u32x4 in[8];
  auto get = [&](int b) {
#pragma unroll
    for (int i = 0; i < 8; ++i) in[i] = ld_sc1s(rw, (unsigned)((t >> 2) * 16384 + (t & 3) * 16), OFF_T + far8a(128 * i, hb) + (unsigned)b * 64u);
  };
  get(blk);
  cf b1[4], b2[4];
  wpow<4>(b1, (float)j * (1.f / 1024.f));
  wpow<4>(b2, (float)(j & 7) * (1.f / 64.f));
#pragma nounroll
  for (int it = 0; it < 4; ++it, blk += 8) {
    asm volatile("" : "+v"(t), "+v"(b1[0]), "+v"(b1[1]), "+v"(b1[2]), "+v"(b1[3]), "+v"(b2[0]), "+v"(b2[1]), "+v"(b2[2]), "+v"(b2[3]));
    cf v[16];
#pragma unroll
    for (int i = 0; i < 8; ++i) {
      const f32x4 a = __builtin_bit_cast(f32x4, in[i]);
      cf m = cf{a[2] - a[1], a[0] + a[3]};
      v[i] = cf{a[1] + a[2], a[0] - a[3]};
      if (i == 0 && t < 4) { v[0] = cf{a[2], a[0]}; m = cf{a[3], a[1]}; }
      *(cf*)(lds + 65536 + i * 4096 + t * 8) = m;
    }
    asm volatile("" : "+v"(v[0]), "+v"(v[1]), "+v"(v[2]), "+v"(v[3]), "+v"(v[4]), "+v"(v[5]), "+v"(v[6]), "+v"(v[7]) :: "memory");
    if (it < 3) get(blk + 8);
    bar();
#pragma unroll
    for (int i = 8; i < 16; ++i) v[i] = *(const cf*)(lds + 65536 + ((2048 - 128 * i - (t >> 2)) & 1023) * 32 + (t & 3) * 8);
    fft2048(t, lds, v, b1, b2);
    const float sc = 0.5f / ((float)S_ * (float)D_);
#pragma unroll
    for (int i = 0; i < 16; ++i) *(unsigned*)(lds + i * 2048 + t * 4) = cvt2(v[i].y * sc, v[i].x * sc);
    bar();
#pragma unroll
    for (int i = 0; i < 4; ++i) st_sc1(rw, bfoff(blk, i * 512, hb) + (unsigned)t * 16u, *(const u32x4*)(lds + i * 8192 + t * 16));
  }
}

DEVI void oproj_worker(char* lds, rsrc_t rw, const u16* __restrict__ Wo, int set, int c, int q, u16* __restrict__ out, unsigned hb DLYARG
) {
  DLYI(0x300);
  int t = threadIdx.x;
  asm volatile("" : "+v"(t));
  const int l = t & 63, w = __builtin_amdgcn_readfirstlane(t >> 6), g = l >> 4, lo = l & 15, hh = 4 * q + (w >> 1);
  constexpr int sstep = 4;
  const int s0 = set < 3 ? set - ON0 : 3 * ON0 + 3;
  auto seg = [&](int s) { return s >= 3 * ON0 ? s : (s < s0 ? 0 : ((s - s0) >> 2) * 3 + set); };
  u32x4 a[4][4][2];
  __syncthreads();
  {
    const u16* gp = Wo + (size_t)(hh * DH_ + g) * D_ + c * 256 + (w & 1) * 128 + (lo ^ (4 * g)) * 8;
    auto ask = [&](int p) {
#pragma unroll
      for (int i = 0; i < 4; ++i)
        __builtin_amdgcn_global_load_lds((const unsigned*)(gp + (size_t)(16 * p + 4 * i) * D_), (unsigned*)(lds + p % 5 * 32768 + w * 4096 + i * 1024 + l * 16), 16, 0, NTO);
      SCHED();
    };
#pragma unroll
    for (int p = 0; p < 5; ++p) ask(p);
    const char* pp = lds + (w >> 2) * 4096 + (4 * g + (lo >> 2)) * 256 + ((4 * (w & 3)) ^ lo) * 16;
#pragma unroll
    for (int n = 0; n < 8; ++n) {
      asm volatile("s_waitcnt vmcnt(%0) lgkmcnt(0)\n s_barrier" :: "n"(4 * (n == 0 ? 4 : (n < 5 ? 3 : 7 - n))) : "memory");
      if (n >= 1 && n < 4) ask(n + 4);
#pragma unroll
      for (int i = 0; i < 8; ++i) {
        const u32x2 x0 = tr_quad(pp + n % 5 * 32768 + (i >> 1) * 8192 + (i & 1) * 8);
        a[i >> 1][n >> 1][i & 1][2 * (n & 1)] = x0[0];
        a[i >> 1][n >> 1][i & 1][2 * (n & 1) + 1] = x0[1];
      }
      SCHED();
    }
    bar();
  }
  const unsigned vo = (unsigned)l * 16u;
  const unsigned fo = cooff(4 * q, hb) + (unsigned)(((l >> 2) & 3) * 1024 + (l & 3) * 4);
  auto flags = [&](int s) { return ld_sc1_32(rw, fo + (unsigned)((l & 16) ? seg(s + sstep) : seg(s)) * 16u); };
  auto fbits = [&](int s, unsigned f) {
    const unsigned m = (unsigned)__builtin_amdgcn_ballot_w64((f & 1u) != 0u);
    return ((m & 0xffffu) == 0xffffu && s < NL_ ? 1u : 0u) | (m == 0xffffffffu && s + sstep < NL_ ? 2u : 0u);
  };
  auto wait_b = [&](int s) {
#pragma nounroll
    for (int it = 0; it < HSPIN_CAP; ++it) {
      if (__all((ld_sc1_32(rw, cooff(hh, hb) + (unsigned)(seg(s) * 16 + (l & 3) * 4)) & 1u) != 0u)) break;
      __builtin_amdgcn_s_sleep(XSLP);
    }
    asm volatile("" ::: "memory");
  };
  auto load_b = [&](int s, int par, int i, unsigned vd) {
    auto* dst = (__attribute__((address_space(3))) void*)(LDS3 + par * 32768 + w * 4096);
    const unsigned so = ooff(seg(s), hh, hb) + (w & 1) * HALF_;
    if (i == 0) __builtin_amdgcn_raw_ptr_buffer_load_lds(rw, dst, 16, vd, so, 0, 16);
    if (i == 1) __builtin_amdgcn_raw_ptr_buffer_load_lds(rw, dst, 16, vd, so, 1024, 16);
    if (i == 2) __builtin_amdgcn_raw_ptr_buffer_load_lds(rw, dst, 16, vd, so, 2048, 16);
    if (i == 3) __builtin_amdgcn_raw_ptr_buffer_load_lds(rw, dst, 16, vd, so, 3072, 16);
  };
  auto pxs = [&](int slot) { return OFF_PX + far8a((slot * 4 + c) * 16 + (w >> 2), hb) + (unsigned)(w & 3) * 2048u; };
  auto pxo = [&](int s) { return pxs(s < FS_ ? s & 31 : s - FS_ + 32); };
  for (int s = FS_ + (set + sstep - FS_ % sstep) % sstep; s < NL_; s += sstep) {
    st_sc1s(rw, vo, pxo(s) + q * 32768u, u32x4{POISON, POISON, POISON, POISON});
    st_sc1s(rw, vo + 1024u, pxo(s) + q * 32768u, u32x4{POISON, POISON, POISON, POISON});
    SCHED();
    asm volatile("s_nop 1");
    SCHED();
  }
  const unsigned sv = (unsigned)((lo >> 2) * 256 + g * 64 + (lo & 3) * 16);
  auto flag = [&](int s) { if (t == 64) st_sc1_32(rw, OFF_CPX + (unsigned)(seg(s) * 4 + c) * 128u + q * 4u, 1u); };
  float* gl = (float*)(lds + 132096);
  u32x4 pv[2];
  float gv;
  unsigned bb;
  auto pf = [&](int s) { return ld_sc1_32(rw, OFF_CPX + (unsigned)(seg(s) * 4 + c) * 128u + (l & 7) * 4u); };
  auto fin_a = [&](int s, bool ok) {
    if (!ok && s < FS_) {
#pragma nounroll
      for (int it = 0; it < HSPIN_CAP; ++it) {
        if (__all(pf(s) == 1u)) break;
        __builtin_amdgcn_s_sleep(2);
      }
    }
    int ll = l;
    asm volatile("" : "+v"(ll));
    gv = gl[seg(s) * 4 + (ll & 3)];
    bb = __builtin_amdgcn_raw_buffer_load_b32(rw, (ll & 12) * 16384 + (ll & 3) * 16 + (ll >> 4) * 4, bfoff(32 * c + 4 * w, seg(s) * SEG_ + 4 * q, hb), 16);
    if (s >= FS_) __builtin_amdgcn_s_sleep(PD1);
#pragma nounroll
    for (int it = 0; it < HSPIN_CAP; ++it) {
      asm volatile("" ::: "memory");
#pragma unroll
      for (int k = 0; k < 2; ++k) pv[k] = ld_sc1s(rw, (ll >> 4) * 32768 + (ll & 15) * 16, pxo(s) + k * 131072u + q * 256u);
      if (s < FS_ || !bad(umax5(pv[1], umax5(pv[0], 0u)))) break;
      __builtin_amdgcn_s_sleep(1);
    }
  };
  auto fin_b = [&](int s) {
    int ll = l;
    asm volatile("" : "+v"(ll));
    const unsigned s1 = (ll & 2) ? 0u : 0x3f80u << (16 * (ll & 1));
    const u32x4 sel = u32x4{(ll & 12) == 0 ? s1 : 0u, (ll & 12) == 4 ? s1 : 0u, (ll & 12) == 8 ? s1 : 0u, (ll & 12) == 12 ? s1 : 0u};
    const f32x4 v = mfu(sel, pv[1], mfu(sel, pv[0], f32x4{0.f, 0.f, 0.f, 0.f}));
    const float omg = rbf(1.0f - gv);
    const unsigned y = cvt2(omg * __uint_as_float(bb << 16), omg * __uint_as_float(bb & 0xffff0000u));
    const unsigned av = cvt2(v[0], v[1]);
    const unsigned x = cvt2(gv * __uint_as_float(av << 16), gv * __uint_as_float(av & 0xffff0000u));
    const unsigned r = cvt2(__uint_as_float(x << 16) + __uint_as_float(y << 16), __uint_as_float(x & 0xffff0000u) + __uint_as_float(y & 0xffff0000u));
    __builtin_amdgcn_raw_buffer_store_b32(r, __builtin_amdgcn_make_buffer_rsrc(out, 0, S_ * D_ * 2, 0x00020000), (ll & 3) * (D_ * 2) + (ll & 12) * 4 + (ll >> 4) * 4,
                                          (unsigned)((seg(s) * SEG_ + 4 * q) * (D_ * 2) + (256 * c + 32 * w) * 2), OAUX);
  };
  f32x4 acc[2][2];
  auto send = [&](unsigned po) {
    u32x4 u[2];
#pragma unroll
    for (int i = 0; i < 8; ++i) u[i >> 2][i & 3] = cvt2x(acc[i >> 2][(i >> 1) & 1][2 * (i & 1)], acc[i >> 2][(i >> 1) & 1][2 * (i & 1) + 1]);
    asm volatile("" : "+v"(u[0]), "+v"(u[1]));
    st_sc1s(rw, sv, po + q * 32768u, u[0]);
    st_sc1s(rw, sv + 1024u, po + q * 32768u, u[1]);
  };
  auto touch = [&](int s, bool on) {
    if (w == 5)
      __builtin_amdgcn_raw_ptr_buffer_load_lds(rw, (__attribute__((address_space(3))) void*)(LDS3 + 131328), 4, on ? (l >> 5) * HALF_ + (l & 31) * 128u : 0x80000000u,
                                               ooff(s, 4 * q + c, hb), 0, 16);
  };
  auto rdo = [](int j) { return (j >> 3) * 8192 + ((j & 1) * 4 + ((j >> 1) & 3)) * 1024; };
#pragma unroll
  for (int k = 0; k < 2; ++k) {
    wait_b(s0 + k * sstep);
#pragma unroll
    for (int i = 0; i < 4; ++i) load_b(s0 + k * sstep, k, i, vo);
  }
  unsigned* vote = (unsigned*)(lds + 131072);
  unsigned fl = flags(s0 + 2 * sstep);
  drain_vm();
  wg_wait_s(rw, OFF_CF + 256u, 32u);
  if (w == 0) *(u32x4*)(gl + 4 * l) = ld_sc1(rw, OFF_G + (unsigned)(l * SEG_ + 4 * q) * 4u);
  HTR(10);
  int par = 0, s1 = s0;
  {
    constexpr unsigned NOW = 0x80000000u;
    const unsigned s1_ = (l & 2) ? 0u : 0x3f80u << (16 * (l & 1));
    const u32x4 sel = u32x4{(l & 12) == 0 ? s1_ : 0u, (l & 12) == 4 ? s1_ : 0u, (l & 12) == 8 ? s1_ : 0u, (l & 12) == 12 ? s1_ : 0u};
    const unsigned ob = (l & 12) * 16384 + (l & 3) * 16 + g * 4, op = g * 32768 + lo * 16, oo = (l & 3) * (D_ * 2) + (l & 12) * 4 + g * 4;
    const unsigned ofx = l < 32 ? fo : OFF_CPX + (unsigned)(c * 128 + (l & 7) * 4);
    const rsrc_t ro = __builtin_amdgcn_make_buffer_rsrc(out, 0, S_ * D_ * 2, 0x00020000);
    u32x4 b[8];
#pragma unroll
    for (int j = 0; j < 8; ++j) b[j] = *(const u32x4*)(lds + l * 16 + rdo(j));
    unsigned vt, px[4] = {0u, 0u, 0u, 0u};
    pv[0] = pv[1] = u32x4{0u, 0u, 0u, 0u};
    gv = 0.f; bb = 0u;
    int rn = 0, sg[9];
#pragma unroll
    for (int i = 0; i < 9; ++i) sg[i] = seg(s0 + (i - 4) * sstep);
    if (OPR == 1 && w >= 4) __builtin_amdgcn_s_setprio(1);
    if (OPR == 2 && w < 4) __builtin_amdgcn_s_setprio(1);
#pragma nounroll
    do {
      const int s = s1;
      const char* bp = lds + par * 32768 + l * 16;
      const char* bn = lds + ((par + 1) & 3) * 32768 + l * 16;
      unsigned vd, so, x, y;
      f32x4 v;
      SCHED();
#pragma unroll
      for (int j = 0; j < 32; ++j) {
        if (j >= OB1 && j < OB1 + 4 * ODS && (j - OB1) % ODS == 0) {
          auto* dst = (__attribute__((address_space(3))) void*)(LDS3 + (par ^ 2) * 32768 + w * 4096);
          if (j == OB1) __builtin_amdgcn_raw_ptr_buffer_load_lds(rw, dst, 16, vd, so, 0, 16);
          if (j == OB1 + ODS) __builtin_amdgcn_raw_ptr_buffer_load_lds(rw, dst, 16, vd, so, 1024, 16);
          if (j == OB1 + 2 * ODS) __builtin_amdgcn_raw_ptr_buffer_load_lds(rw, dst, 16, vd, so, 2048, 16);
          if (j == OB1 + 3 * ODS) __builtin_amdgcn_raw_ptr_buffer_load_lds(rw, dst, 16, vd, so, 3072, 16);
        }
#pragma unroll
        for (int ct = 0; ct < 2; ++ct) acc[j & 1][ct] = mfu(a[j >> 3][(j >> 1) & 3][ct], b[j & 7], j >= 2 ? acc[j & 1][ct] : f32x4{0.f, 0.f, 0.f, 0.f});
        b[j & 7] = *(const u32x4*)((j < 24 ? bp : bn) + rdo((j + 8) & 31));
        if (j == 2) { px[3] = px[2]; px[2] = px[1]; px[1] = px[0]; px[0] = pxo(s); asm volatile("" : "+s"(px[0])); }
        if (j == 5) { so = ooff(sg[6], hh, hb) + (w & 1) * HALF_; asm volatile("" : "+s"(so)); }
        if (j == OB2 - 5) {
          v = mfu(sel, pv[1], mfu(sel, pv[0], f32x4{0.f, 0.f, 0.f, 0.f}));
          const float omg = rbf(1.0f - gv);
          y = cvt2(omg * __uint_as_float(bb << 16), omg * __uint_as_float(bb & 0xffff0000u));
          asm volatile("" : "+v"(y));
        }
        if (j == OB2 - 3) {
          const unsigned av = cvt2(v[0], v[1]);
          x = cvt2(gv * __uint_as_float(av << 16), gv * __uint_as_float(av & 0xffff0000u));
          asm volatile("" : "+v"(x));
        }
        if (j == OB2 - 2)
          __builtin_amdgcn_raw_buffer_store_b32(cvt2(__uint_as_float(x << 16) + __uint_as_float(y << 16), __uint_as_float(x & 0xffff0000u) + __uint_as_float(y & 0xffff0000u)),
                                                ro, oo | (rn >= 4 ? 0u : NOW), (unsigned)((sg[0] * SEG_ + 4 * q) * (D_ * 2) + (256 * c + 32 * w) * 2), OAUX);
        SCHED();
        if (j == OB0 - 9 && w == 0) {
          unsigned fb = fbits(s + 2 * sstep, fl);
          if (__builtin_expect(!(fb & 1u) && s < OLIM, 0)) {
#pragma nounroll
            for (int it = 0; it < HSPIN_CAP && !(fb & 1u); ++it) {
              __builtin_amdgcn_s_sleep(8);
              fb = fbits(s + 2 * sstep, flags(s + 2 * sstep));
            }
          }
          vote[par] = fb;
          if (__builtin_expect((__builtin_amdgcn_ballot_w64(fl == 1u) >> 32) != 0xffffffffu && rn >= 3, 0)) {
#pragma nounroll
            for (int it = 0; it < HSPIN_CAP; ++it) {
              if (__all(pf(s - 3 * sstep) == 1u)) break;
              __builtin_amdgcn_s_sleep(2);
            }
          }
        }
        if (j == OB0 + 1) {
#pragma unroll
          for (int k = 0; k < 2; ++k) pv[k] = ld_sc1s(rw, op | (rn >= 3 ? 0u : NOW), px[3] + k * 131072u + q * 256u);
        }
        if (j == OB1 + 1) {
          gv = gl[sg[1] * 4 + (l & 3)];
          bb = __builtin_amdgcn_raw_buffer_load_b32(rw, ob, bfoff(32 * c + 4 * w, sg[1] * SEG_ + 4 * q, hb), 16);
        }
        if (j == OB0) {
          wait_vm<1>();
          __builtin_amdgcn_s_barrier();
          asm volatile("" ::: "memory");
          vt = vote[par];
          if (rn >= 1 && t == 64) st_sc1_32(rw, OFF_CPX + (unsigned)(sg[3] * 4 + c) * 128u + q * 4u, 1u);
        }
        if (j == OB0 && w == 0) fl = ld_sc1_32(rw, ofx + (l < 32 ? (unsigned)((l & 16) ? sg[8] : sg[7]) * 16u : (unsigned)sg[2] * 512u));
        if (j == OB1 - 1) {
          vt = __builtin_amdgcn_readfirstlane(vt);
          vd = (vt & 1u) ? vo : NOW;
        }
        if (j == OB1 + 3 * ODS + 1) touch(sg[7], vt & 2u);
      }
      send(px[0]);
      s1 += sstep;
#pragma unroll
      for (int i = 0; i < 8; ++i) sg[i] = sg[i + 1];
      sg[8] += sg[8] < 3 * ON0 ? 3 : 4;
      par = (par + 1) & 3;
      ++rn;
    } while (vt & 1u);
    if (w == 0) vote[par] = fbits(s1 + sstep, flags(s1 + sstep));
    asm volatile("" ::: "memory");
    __builtin_amdgcn_s_waitcnt(0x0f72);
    bar();
  }
  int np = min((s1 - s0) / sstep, 4);
#pragma nounroll
  for (int s = s1; s < NL_; s += sstep, par ^= 1) {
    const int nxt = s + sstep;
    const unsigned vt = __builtin_amdgcn_readfirstlane(vote[par]);
    const bool pre = (vt & 1u) != 0u;
    const unsigned vd = pre ? vo : 0x80000000u;
    const char* bp = lds + par * 32768 + l * 16;
    auto rd = [&](int j) { return *(const u32x4*)(bp + rdo(j)); };
    unsigned f;
    if (np >= 3) f = pf(s - 3 * sstep);
    u32x4 b[8];
#pragma unroll
    for (int j = 0; j < 8; ++j) b[j] = rd(j);
    if (np >= 2) flag(s - 2 * sstep);
    if (np >= 4) fin_b(s - 4 * sstep);
    SCHED();
#pragma unroll
    for (int j = 0; j < 32; ++j) {
      if (j < 4 * ODS && j % ODS == 0) load_b(nxt, par ^ 1, j / ODS, vd);
#pragma unroll
      for (int ct = 0; ct < 2; ++ct) acc[j & 1][ct] = mfu(a[j >> 3][(j >> 1) & 3][ct], b[j & 7], j >= 2 ? acc[j & 1][ct] : f32x4{0.f, 0.f, 0.f, 0.f});
      if (j + 8 < 32) b[j & 7] = rd(j + 8);
      SCHED();
      if (j == OFA) {
        if (np >= 3) fin_a(s - 3 * sstep, __all(f == 1u));
        if (w == 0) fl = flags(nxt + sstep);
        touch(seg(nxt + sstep), vt & 2u);
        SCHED();
      }
    }
    send(pxo(s));
    asm volatile("" ::: "memory");
    __builtin_amdgcn_s_waitcnt(0x0f72);
    asm volatile("" ::: "memory");
    if (w == 0) vote[par ^ 1] = fbits(nxt + sstep, fl);
    np = min(np + 1, 4);
    if (!pre) {
      if (np >= 4) fin_b(s - 3 * sstep);
      if (s - sstep < FS_) {
        if (s < FS_) drain_vm();
        bar();
        if (np >= 2) flag(s - sstep);
        if (s < FS_) flag(s);
      }
#pragma nounroll
      for (int k = min(np, 3) - 1; k >= 0; --k) { fin_a(s - k * sstep, false); fin_b(s - k * sstep); }
      np = 0;
      if (nxt >= NL_) break;
      if (nxt < FS_) {
        wait_b(nxt);
#pragma unroll
        for (int i = 0; i < 4; ++i) load_b(nxt, par ^ 1, i, vo);
        drain_vm();
      } else {
        const unsigned so = ooff(nxt, hh, hb) + (w & 1) * HALF_;
        const unsigned cm = cooff(hh, hb) + (unsigned)((nxt - 2) * 16 + (l & 3) * 4);
        after(poll_flags<FSLP>(rw, cm, ld_sc1_32(rw, cm)), (int)(nxt == NL_ - 1 ? dly[XTL] : dly[XT1]));
#pragma nounroll
        for (int it = 0; it < HSPIN_CAP; ++it) {
          asm volatile("" ::: "memory");
#pragma unroll
          for (int j = 0; j < 4; ++j) b[j] = ld_sc1s(rw, vo + j * 1024u, so);
          if (!bad4(b)) break;
          __builtin_amdgcn_s_sleep(1);
        }
#pragma unroll
        for (int j = 0; j < 4; ++j) *(u32x4*)(lds + (par ^ 1) * 32768 + w * 4096 + j * 1024 + l * 16) = b[j];
      }
    }
    bar();
  }
}

DEVI void helper_role(char* lds, char* ws, rsrc_t rw, const u16* __restrict__ x, const u16* __restrict__ Wq,
                      const u16* __restrict__ Wk, const u16* __restrict__ Wv, const u16* __restrict__ Wo,
                      const u16* __restrict__ wr, const u16* __restrict__ wi, const u16* __restrict__ gw, u16* __restrict__ out, int hw
                      ) {
  const int xg = hw & 7, loc = hw >> 3;
  const int q = xg >> 1, m = loc * 2 + (hw & 1);
  DLYI(0x30);
  HTR(0);
  const int xi = (m & 7) * 4 + q, l = threadIdx.x & 63, w = __builtin_amdgcn_readfirstlane(threadIdx.x >> 6);
  auto xld = [&](int i) {
    return __builtin_amdgcn_raw_buffer_load_b128(__builtin_amdgcn_make_buffer_rsrc((void*)x, 0, S_ * D_ * 2, 0x00020000),
                                                 (l & 7) * 2048 + w * 256 + (l >> 3) * 16 + (i & 2) * 64, (unsigned)(32 * (i >> 2) + xi) * 32768u + (i & 1) * 16384u, NTX);
  };
  auto xst = [&](int i, unsigned hb, u32x4 v) { st_sc1s(rw, (l >> 3) * 256 + (l & 7) * 16 + (i & 2) * 1024 + (i & 1) * 128, OFF_XF + far8((32 * (i >> 2) + xi) * 4 + (w >> 1), hb) + (w & 1) * 4096u, v); };
  unsigned hb;
  if (m < 24) {
    if (m >= 16) {
      static_assert((OFF_REC - OFF_CKV) % 512u == 0 && (OFF_REC - OFF_CKV) / 512u <= 512u, "shares");
      if (threadIdx.x < (OFF_REC - OFF_CKV) / 512u) st_sc1(rw, OFF_CKV + xi * ((OFF_REC - OFF_CKV) / 32u) + threadIdx.x * 16u, u32x4{0u, 0u, 0u, 0u});
      u32x4 xs[4];
#pragma unroll
      for (int i = 0; i < 4; ++i) xs[i] = xld(i);
      const u32x2 no = launch_no(ws);
#pragma unroll
      for (int i = 0; i < 4; ++i) xst(i, 0u, xs[i]);
      rec_put(rw, xi, no);
      asm volatile("" :: "v"(xs[3]));
      HTR(4);
      for (unsigned i = 0; i < (RSE ? 7u : dly[QDL]); ++i) __builtin_amdgcn_s_sleep(32);
    }
    if ((m >> 3) == 1) hb = proj_reg<false>(lds, ws, rw, Wv, 8 * q + (m & 7), 2, hw);
    else hb = proj_reg<true>(lds, ws, rw, m < 8 ? Wk : Wq, 8 * q + (m & 7), m < 8 ? 1 : 0, hw);
    HTR(2);
  } else {
    probe(ws, rw, 8 * q + m - 24);
    const u32x2 no = launch_no(ws);
    rec_put(rw, 32 + 8 * q + m - 24, no);
    HTR(4);
    for (unsigned i = 0; i < dly[XDL]; ++i) __builtin_amdgcn_s_sleep(32);
    NearMap nm;
    {
      u32x4 wv[2], xs[16];
#pragma unroll
      for (int i = 0; i < 16; ++i)
        xs[(i + 4) & 15] = __builtin_amdgcn_raw_buffer_load_b128(__builtin_amdgcn_make_buffer_rsrc((void*)x, 0, S_ * D_ * 2, 0x00020000), l * 16 + (i & 3) * 1024,
                                                                 (unsigned)(32 * (((i + 4) & 15) >> 2) + xi) * 32768u + w * 4096u, NTX);
#pragma unroll
      for (int i = 0; i < 2; ++i) wv[i] = *(const u32x4*)(gw + i * 512 + l * 8);
      nm.k = near_kind(rw, no);
      hb = far_bit(nm.k);
#pragma unroll
      for (int i = 4; i < 16; ++i) *(u32x4*)(lds + (i >> 2) * 32768 + (i & 1) * 16384 + l * 256 + ((2 * w + ((i >> 1) & 1)) ^ (l & 15)) * 16) = xs[i];
      __syncthreads();
      u32x4 xf[12];
#pragma unroll
      for (int i = 0; i < 12; ++i) xf[i] = *(const u32x4*)(lds + ((i >> 2) + 1) * 32768 + w * 4096 + (i & 3) * 1024 + (l >> 4) * 256 + ((l & 15) ^ ((i & 3) * 4 + (l >> 4))) * 16);
#pragma unroll
      for (int i = 0; i < 12; ++i) st_sc1s(rw, l * 16 + (i & 3) * 1024, OFF_XF + far8((32 * ((i >> 2) + 1) + xi) * 4 + (w >> 1), hb) + (w & 1) * 4096u, xf[i]);
      float gs = 0.f;
#pragma unroll
      for (int n = 0; n < 8; ++n) {
        float acc = 0.f;
#pragma unroll
        for (int i = 0; i < 8; ++i) {
          const unsigned a = xs[2 * n + (i >> 2)][i & 3], b = wv[i >> 2][i & 3];
          const unsigned pr = cvt2(__uint_as_float(a << 16) * __uint_as_float(b << 16), __uint_as_float(a & 0xffff0000u) * __uint_as_float(b & 0xffff0000u));
          acc += __uint_as_float(pr << 16) + __uint_as_float(pr & 0xffff0000u);
        }
#pragma unroll
        for (int o = 1; o < 8; o *= 2) acc += __shfl_xor(acc, o);
        acc = rows_sum(fold8(acc));
        if (l == n) gs = acc;
      }
      asm volatile("" :: "v"(xf[11]));
      wg_signal(ws, OFF_CF + 384u);
      if (l < 8) st_sc1_32(rw, OFF_G + (unsigned)((32 * (l >> 1) + xi) * 16 + 2 * w + (l & 1)) * 4u, __float_as_uint(rbf(1.0f / (1.0f + expf(-rbf(gs))))));
    }
    wg_wait_s(rw, OFF_CF + 384u, 32u);
    HTR(1);
    fft_cols_fwd(lds, rw, 8 * q + m - 24, hb);
    wg_signal(ws, OFF_CF);
    HTR(3);
    {
      unsigned tx = threadIdx.x;
      asm volatile("" : "+v"(tx));
      const unsigned tv = (tx >> 3) & 1;
      u32x4 p3 = u32x4{tv, tv, tv, tv}, p4 = u32x4{POISON, POISON, POISON, POISON};
      asm volatile("" : "+v"(p3), "+v"(p4));
#pragma unroll
      for (int i = 0; i < 6; ++i) st_sc1s(rw, (tx & 255u) * 16u, nm(3 + (i >> 1), 2 * (i & 1) + (w >> 2), NB3 + xi), p3);
#pragma unroll
      for (int i = FS_; i < NL_; ++i) st_sc1s(rw, (w >> 2) * HALF_ + (tx & 255u) * 16u, ooff(i, xi, hb), p4);
      drain_vm();
      asm volatile("" :: "v"(p3), "v"(p4));
    }
    wg_wait_s(rw, OFF_CF, 32u);
    fft_rows(lds, rw, wr, wi, xi, hb);
    wg_signal(ws, OFF_CF + 128u);
    HTR(5);
    xattn_wave(rw, m & 7, q, hb);
    HTR(11);
    wg_wait_s(rw, OFF_CF + 128u, 32u);
    fft_cols_inv(lds, rw, 32 * q + m - 24, hb);
    wg_signal(ws, OFF_CF + 256u);
    HTR(7);
  }
  oproj_worker(lds, rw, Wo, (m >> 3) ^ (m >> 4), m & 3, 2 * q + ((m >> 2) & 1), out, hb DLYA
);
  HTR(9);
}

static_assert(W3_OFF + 65536 <= SMEM_BYTES, "LDS");
__global__ __launch_bounds__(512, 1) void mega_kernel(const u16* __restrict__ x, const u16* __restrict__ mem0,
                                                      const u16* __restrict__ Wq, const u16* __restrict__ Wk,
                                                      const u16* __restrict__ Wv, const u16* __restrict__ Wo,
                                                      const u16* __restrict__ wr, const u16* __restrict__ wi,
                                                      const u16* __restrict__ gw, u16* __restrict__ out, char* ws) {
  char* lds = lds_;
  ws += (0u - (unsigned)(size_t)ws) & (SET_ - 1u);
  const rsrc_t rw = __builtin_amdgcn_make_buffer_rsrc(ws, 0, WS_USED, 0x00020000);
  const int idx = (blockIdx.x >> 3) * 4 + (blockIdx.x & 3);
  const bool isrec = (blockIdx.x & 4) == 0;
  const int rid = (blockIdx.x & 3) * 32 + (blockIdx.x >> 3), hid = (idx & 3) * 2 + ((idx >> 2) & 1) + (idx >> 3) * 8;
  if (__builtin_amdgcn_readfirstlane(threadIdx.x >> 6) == (isrec ? 7 : 3) && (blockIdx.x >> 3) >= 16) {
    u32x2 pc;
    unsigned n;
    asm volatile("s_getpc_b64 %0\n code_from: s_mov_b32 %1, code_to - code_from" : "=s"(pc), "=s"(n));
    asm volatile("s_mov_b32 m0, %2\n s_nop 0\n buffer_load_dword %0, %1, 0 offen lds"
                 :: "v"((((blockIdx.x >> 3) - 16) * 64 + (threadIdx.x & 63)) * 128), "s"(u32x4{pc[0], pc[1] & 0xffffu, n, 0x00020000u}),
                    "s"((unsigned)(size_t)LDS3 + (isrec ? 3u * 24576u : 131072u + 3u * 4096u)) : "memory");
  }
  if (isrec) {
    recur_role(lds, ws, rw, mem0, Wq, Wk, Wv, Wo, out, rid TRARG);
  } else helper_role(lds, ws, rw, x, Wq, Wk, Wv, Wo, wr, wi, gw, out, hid TRARG);
}

extern "C" void kernel_launch(void* const* d_in, const int* in_sizes, int n_in, void* d_out, int out_size, void* d_ws,
                              size_t ws_size, hipStream_t stream) {
  const u16* x = (const u16*)d_in[0];
  const u16* mem0 = (const u16*)d_in[1];
  const u16* Wq = (const u16*)d_in[2];
  const u16* Wk = (const u16*)d_in[3];
  const u16* Wv = (const u16*)d_in[4];
  const u16* Wo = (const u16*)d_in[5];
  const u16* wr = (const u16*)d_in[8];
  const u16* wi = (const u16*)d_in[9];
  const u16* gw = (const u16*)d_in[10];
  u16* out = (u16*)d_out;
  char* ws = (char*)d_ws;
  mega_kernel<<<256, 512, 0, stream>>>(x, mem0, Wq, Wk, Wv, Wo, wr, wi, gw, out, ws);
}
```

```cpp
#define DLYS 3, 7, 9, 9, 3, 6, 8, 6, 300, 118, 13, 6, 1, 0, 120
#include <hip/hip_runtime.h>
#include <hip/hip_bf16.h>
#include <stdint.h>
#include <type_traits>
#include <utility>

typedef short bf16x8 __attribute__((ext_vector_type(8)));
typedef short s16x4 __attribute__((ext_vector_type(4)));
typedef float f32x4 __attribute__((ext_vector_type(4)));
typedef unsigned u32x4 __attribute__((ext_vector_type(4)));
typedef unsigned u32x2 __attribute__((ext_vector_type(2)));
typedef unsigned short u16;

#define DEVI __device__ __forceinline__

template <class F, int... I>
DEVI void sfor_(F&& f, std::integer_sequence<int, I...>) { (f(std::integral_constant<int, I>{}), ...); }
template <int N, class F>
DEVI void sfor(F&& f) { sfor_(f, std::make_integer_sequence<int, N>{}); }
DEVI float bf2f(u16 v) { return __uint_as_float(((unsigned)v) << 16); }
DEVI u16 f2bf(float f) { return __builtin_bit_cast(u16, (__bf16)f); }
DEVI float rbf(float f) { return bf2f(f2bf(f)); }
DEVI unsigned pack2(u16 a, u16 b) { return (unsigned)a | ((unsigned)b << 16); }
DEVI f32x4 mfma16(bf16x8 a, bf16x8 b, f32x4 c) { return __builtin_amdgcn_mfma_f32_16x16x32_bf16(a, b, c, 0, 0, 0); }

constexpr int S_ = 2048, D_ = 1024, H_ = 32, DH_ = 128, MEM_ = 8, SEG_ = 32, NSEG_ = 64, LK_ = 40;
constexpr int HD_ = H_ * DH_;
#define FSV 56
constexpr int NL_ = NSEG_ - 1;
constexpr int FS_ = FSV;
constexpr int XS_ = 8;
constexpr int FB_ = 513;
constexpr float SCALE_ = 0.08838834764831845f;

constexpr unsigned POISON = 0xffffffffu;
constexpr unsigned SET_ = 4u << 20, BLK_ = 32768u, NB3 = 0, NB2 = 32, NB1 = 48, NBLK = 52;
constexpr unsigned OFF_CKV = 24u << 20;
constexpr unsigned OFF_CO = OFF_CKV + 16384u;
constexpr unsigned OFF_CPX = OFF_CO + 65536u;
constexpr unsigned OFF_CF = OFF_CPX + 32768u;
constexpr unsigned OFF_REC = OFF_CF + 1024u;
constexpr unsigned OFF_EP = OFF_REC + 5120u;
constexpr unsigned COMM_BYTES = OFF_EP + 1024u;
struct NearMap {
  unsigned k;
  __device__ static unsigned pg(unsigned c) { return ((c & 1u) << 14) | ((c & 6u) << 11); }
#define BLAY 0
  __device__ static constexpr unsigned blk(unsigned n) { return BLAY ? ((n & 3u) << 15) | ((n & 28u) << 17) | ((n >> 5) << 17) : n * BLK_; }
  __device__ unsigned operator()(unsigned set, unsigned g, unsigned n) const { return set * SET_ + blk(n) + pg(k ^ g ^ (blk(n) >> 19)); }
};
DEVI unsigned far8a(unsigned u, unsigned hb) { return u * 16384u + (((u >> 7) ^ hb) & 1u) * 8192u; }
DEVI unsigned far8(unsigned u, unsigned hb) { return u * 16384u + (((u >> 7) ^ (u < 128u ? 0u : hb)) & 1u) * 8192u; }
#define NPROBE 8
DEVI unsigned long long clk_now() {
  unsigned long long t;
  asm volatile("s_waitcnt vmcnt(0) lgkmcnt(0)\n s_memtime %0\n s_waitcnt lgkmcnt(0)" : "=s"(t) :: "memory");
  return t;
}
constexpr unsigned OFF_G = COMM_BYTES;
constexpr unsigned OFF_Q = 28u << 20, OFF_K = 60u << 20, OFF_VX = 92u << 20, OFF_OT = 124u << 20;
constexpr unsigned OFF_T = 128u << 20;
constexpr unsigned OFF_PX = 144u << 20;
constexpr unsigned OFF_BF = 184u << 20;
constexpr unsigned OFF_XF = 192u << 20;
constexpr unsigned WS_USED = 200u << 20;
static_assert(FS_ >= NSEG_ - 8 && 32 + NL_ - FS_ <= 40 && XS_ == 8, "tails");
constexpr unsigned HALF_ = BLK_;
DEVI unsigned tile8(int s, int h, unsigned hb) { return (unsigned)(s * 8 + (h & 7)) * (2u * BLK_) + NearMap::pg((s < 2 ? 0u : hb >> 1) ^ (unsigned)(h >> 3) ^ (unsigned)s); }
DEVI unsigned ooff(int s, int h, unsigned hb) { return (s < FS_ ? OFF_Q : OFF_OT - (NSEG_ - 8) * H_ * 16384u) + tile8(s, h, hb); }
DEVI unsigned bfoff(int blk, int tok, unsigned hb) { return OFF_BF + far8a(blk * 4 + (tok >> 9), hb) + (unsigned)(tok & 511) * 16u; }
DEVI unsigned kmoff(int s, int h, unsigned hb) { return NearMap{hb >> 1}(3u, h >> 3, 64 + s * 8 + (h & 7)); }
DEVI unsigned cooff(int h, unsigned hb) { return OFF_CO + far8a(h >> 3, hb ^ 1u) + (unsigned)(h & 7) * 1024u; }

constexpr int SMEM_BYTES = 163840;
__shared__ __attribute__((aligned(16))) char lds_[SMEM_BYTES];
#define LDS3 ((__attribute__((address_space(3))) char*)lds_)

typedef __amdgpu_buffer_rsrc_t rsrc_t;
DEVI u32x4 ld_sc1(rsrc_t r, unsigned off) { return __builtin_amdgcn_raw_buffer_load_b128(r, off, 0, 16); }
DEVI u32x2 ld_sc1_64(rsrc_t r, unsigned off) { return __builtin_amdgcn_raw_buffer_load_b64(r, off, 0, 16); }
DEVI unsigned ld_sc1_32(rsrc_t r, unsigned off) { return __builtin_amdgcn_raw_buffer_load_b32(r, off, 0, 16); }
DEVI void st_sc1(rsrc_t r, unsigned off, u32x4 v) { __builtin_amdgcn_raw_buffer_store_b128(v, r, off, 0, 16); }
DEVI void st_sc1_64(rsrc_t r, unsigned off, u32x2 v) { __builtin_amdgcn_raw_buffer_store_b64(v, r, off, 0, 16); }
DEVI void st_sc1_32(rsrc_t r, unsigned off, unsigned v) { __builtin_amdgcn_raw_buffer_store_b32(v, r, off, 0, 16); }
DEVI void st_sc1_16(rsrc_t r, unsigned off, u16 v) { __builtin_amdgcn_raw_buffer_store_b16(v, r, off, 0, 16); }
DEVI u32x4 ld_sc1s(rsrc_t r, unsigned off, unsigned so) { return __builtin_amdgcn_raw_buffer_load_b128(r, off, so, 16); }
DEVI void st_sc1s(rsrc_t r, unsigned off, unsigned so, u32x4 v) { __builtin_amdgcn_raw_buffer_store_b128(v, r, off, so, 16); }
DEVI void st_sc1s_64(rsrc_t r, unsigned off, unsigned so, u32x2 v) { __builtin_amdgcn_raw_buffer_store_b64(v, r, off, so, 16); }
DEVI void st_sc1s_32(rsrc_t r, unsigned off, unsigned so, unsigned v) { __builtin_amdgcn_raw_buffer_store_b32(v, r, off, so, 16); }
#define HAUX 16
DEVI void hst(rsrc_t r, unsigned off, u32x4 v) { __builtin_amdgcn_raw_buffer_store_b128(v, r, off, 0, HAUX); }
DEVI void hst64(rsrc_t r, unsigned off, u32x2 v) { __builtin_amdgcn_raw_buffer_store_b64(v, r, off, 0, HAUX); }
DEVI void drain_vm() { asm volatile("s_waitcnt vmcnt(0)" ::: "memory"); }
DEVI void cnt_add(char* ws, unsigned off, unsigned v = 1u) { __hip_atomic_fetch_add((unsigned*)(ws + off), v, __ATOMIC_RELAXED, __HIP_MEMORY_SCOPE_AGENT); }

constexpr int HSPIN_CAP = 1 << 18;
DEVI void wg_wait_s(rsrc_t r, unsigned off, unsigned n) {
  if (threadIdx.x == 0) {
#pragma nounroll
    for (int it = 0; it < HSPIN_CAP; ++it) {
      if (ld_sc1_32(r, off) >= n) break;
      __builtin_amdgcn_s_sleep(8);
    }
  }
  asm volatile("" ::: "memory");
  __syncthreads();
}
DEVI void wg_signal(char* ws, unsigned off) {
  drain_vm();
  __syncthreads();
  if (threadIdx.x == 0) cnt_add(ws, off);
}

DEVI u32x2 launch_no(const char* ws) {
  u32x2 e;
  asm volatile("s_load_dwordx2 %0, %1, 0x0\n s_waitcnt lgkmcnt(0)" : "=s"(e) : "s"(ws + OFF_EP) : "memory");
  return u32x2{e[0] + 1u, e[1] + 0x9e3779b1u};
}
DEVI void rec_put(rsrc_t rw, int i, u32x2 no) {
  drain_vm();
  __syncthreads();
  if (threadIdx.x == 0) st_sc1_64(rw, OFF_REC + (unsigned)i * 16u, no);
}
DEVI u32x4 rec_wait(rsrc_t rw, u32x2 no, unsigned mask, unsigned lo = 0u, unsigned own = 0u) {
  u32x4 r;
#pragma nounroll
  for (int it = 0; it < HSPIN_CAP; ++it) {
    asm volatile("" ::: "memory");
    r = ld_sc1(rw, OFF_REC + (own && __lane_id() <= own ? __lane_id() : (__lane_id() & mask) + lo) * 16u);
    if (__all(r[0] == no[0] && r[1] == no[1])) break;
    __builtin_amdgcn_s_sleep(8);
  }
  asm volatile("" ::: "memory");
  return r;
}
DEVI unsigned near_kind(rsrc_t rw, u32x2 no) {
  const unsigned lat = rec_wait(rw, no, 63u)[2], ls2 = (lat & 0xffffu) + (lat >> 16), l = __lane_id() & 7u;
  unsigned cost = 0u;
#pragma unroll
  for (int i = 0; i < 4; ++i) cost += __shfl(ls2, 32 + 8 * i + (l ^ i));
  cost = cost * 8u + l;
#pragma unroll
  for (int i = 1; i < 8; i *= 2) cost = min(cost, (unsigned)__shfl_xor(cost, i));
  return (__builtin_amdgcn_readfirstlane(cost) & 7u) ^ 4u;
}
DEVI unsigned far_bit(unsigned kind) { return ((kind >> 2) ^ 1u) | (kind << 1); }
DEVI void probe(char* ws, rsrc_t rw, int i) {
  const NearMap nm{0u};
  if (threadIdx.x == 0) {
    char* p0 = ws + nm(4u, i & 7, 40u + (i >> 3));
    unsigned mn[2] = {0xffffu, 0xffffu};
#pragma unroll 1
    for (int k = 0; k < NPROBE; ++k) {
#pragma unroll
      for (int hf = 0; hf < 2; ++hf) {
        const unsigned long long t0 = clk_now();
        const unsigned v = __hip_atomic_fetch_add((unsigned*)(p0 + hf * 2048), 1u, __ATOMIC_RELAXED, __HIP_MEMORY_SCOPE_AGENT);
        asm volatile("" :: "v"(v));
        const unsigned long long t1 = clk_now();
        mn[hf] = min(mn[hf], (unsigned)(t1 - t0));
      }
    }
    st_sc1_32(rw, OFF_REC + (unsigned)(32 + i) * 16u + 8u, mn[0] | (mn[1] << 16));
  }
}

template <int N>
DEVI void wait_vm() { asm volatile("s_waitcnt vmcnt(%0)" :: "n"(N) : "memory"); }
#define PSP 0
#define BPR 2
#define ISG 3
#define ESG 4
#define NTW 0
#define RN 1
#define NTO 0
#define OAUX 0
#define NTX 2
#define SCHED() __builtin_amdgcn_sched_barrier(0)
DEVI u32x4 rsrc4(const void* p, unsigned bytes) { return u32x4{(unsigned)(size_t)p, (unsigned)((size_t)p >> 32) & 0xffffu, bytes, 0x00020000u}; }
DEVI void bar() { asm volatile("s_waitcnt lgkmcnt(0)\n s_barrier" ::: "memory"); }
DEVI u32x2 tr_quad(const char* p) {
  return __builtin_bit_cast(u32x2, __builtin_amdgcn_ds_read_tr16_b64_v4i16((__attribute__((address_space(3))) s16x4*)(p)));
}
DEVI unsigned cvt2(float a, float b) {
  typedef float f32x2_ __attribute__((ext_vector_type(2)));
  typedef __bf16 bf16x2_ __attribute__((ext_vector_type(2)));
  return __builtin_bit_cast(unsigned, __builtin_convertvector(f32x2_{a, b}, bf16x2_));
}
DEVI unsigned cvt2x(float a, float b) { return min(cvt2(a, b), POISON - 1u); }
DEVI f32x4 mfu(u32x4 a, u32x4 b, f32x4 c) { return mfma16(__builtin_bit_cast(bf16x8, a), __builtin_bit_cast(bf16x8, b), c); }
#define PTR(i)
#define PTB(i)
template <bool swap>
DEVI unsigned proj_reg(char* lds, char* ws, rsrc_t rw, const u16* __restrict__ W, int h, int mat, int hw) {
  constexpr int nseg = NSEG_;
  const int rot = mat ? 0 : XS_;
  auto sgm = [&](int r) { return (r & 32) | ((r + ((r & 32) ? 0 : rot)) & 31); };
  int t = threadIdx.x;
  asm volatile("" : "+v"(t));
  const int l = t & 63, w = __builtin_amdgcn_readfirstlane(t >> 6), g = l >> 4, lo = l & 15, wn = w & 3, wk = w >> 2;
  const u32x4 rs = rsrc4(ws, WS_USED);
  const unsigned vo = (unsigned)l * 16u;
  auto wswz = [](int row) { return ((row & 3) | (((row >> 3) & 1) << 2)) << 1; };
  u32x4 b[16][2];
  const unsigned go = (unsigned)((512 * wk + 16 * wn + g) * HD_ + h * DH_) * 2u;
  const unsigned gv[2] = {go + (unsigned)((lo ^ wswz(g)) * 16), go + (unsigned)((lo ^ wswz(8 + g)) * 16)};
  auto ask = [&](int p) {
#pragma unroll
    for (int i = 0; i < 4; ++i)
      __builtin_amdgcn_global_load_lds((const unsigned*)((const char*)W + gv[i >> 1] + (size_t)(64 * p + 4 * i) * HD_ * 2), (unsigned*)(lds + (p + 4) % 5 * 32768 + w * 4096 + i * 1024 + l * 16), 16, 0, NTW);
    SCHED();
  };
#pragma unroll
  for (int p = 0; p < 5; ++p) ask(p);
  const u32x2 no = launch_no(ws);
  const char* bp = lds + (64 * wk + 8 * g + (lo >> 2)) * 256 + (l & 1) * 8;
  const int fq2 = wswz(8 * g + (lo >> 2));
  auto take = [&](int p) {
#pragma unroll
    for (int ks = 0; ks < 2; ++ks)
#pragma unroll
      for (int ct = 0; ct < 2; ++ct) {
        const char* p0 = bp + (p + 4) % 5 * 32768 + ks * 8192 + (((4 * wn + 2 * ct + ((l & 3) >> 1)) ^ fq2) * 16);
        const u32x2 x0 = tr_quad(p0), x1 = tr_quad(p0 + 1024);
        b[2 * p + ks][ct] = u32x4{x0[0], x0[1], x1[0], x1[1]};
      }
    SCHED();
  };
  unsigned hb = 0u;
  auto dma = [&](int i, int pc, auto&& mid) {
    auto* dst = (__attribute__((address_space(3))) void*)(LDS3 + ((i + 2) & 3) * 32768 + pc * 4096);
    const unsigned so = OFF_XF + far8(sgm(i >> 1) * 8 + (i & 1) * 4 + (pc >> 1), hb) + (unsigned)(pc & 1) * 4096u;
    const unsigned vd = i < 2 * nseg ? vo : 0x80000000u;
    __builtin_amdgcn_raw_ptr_buffer_load_lds(rw, dst, 16, vd, so, 0, 16);
    mid(0);
    __builtin_amdgcn_raw_ptr_buffer_load_lds(rw, dst, 16, vd, so, 1024, 16);
    mid(1);
    __builtin_amdgcn_raw_ptr_buffer_load_lds(rw, dst, 16, vd, so, 2048, 16);
    mid(2);
    __builtin_amdgcn_raw_ptr_buffer_load_lds(rw, dst, 16, vd, so, 3072, 16);
    mid(3);
  };
  auto none = [](int) {};
  auto here = [](int v) { asm volatile("" : "+s"(v)); return v; };
  unsigned du, dso, dvd, dm0;
  auto dprep = [&](int j, int i, int pc) {
    if (j == 0) { du = sgm(here(i) >> 1); asm volatile("" : "+s"(du)); }
    if (j == 1) { du = here(du) * 8 + (here(i) & 1) * 4 + (pc >> 1); dm0 = (unsigned)(size_t)LDS3 + ((here(i) + 2) & 3) * 32768 + pc * 4096; asm volatile("" : "+s"(du), "+s"(dm0)); }
    if (j == 2) { dso = OFF_XF + far8(here(du), hb) + (unsigned)(pc & 1) * 4096u; asm volatile("" : "+s"(dso)); }
    if (j == 3) { dvd = here(i) < 2 * nseg ? vo : 0x80000000u; asm volatile("" : "+v"(dvd)); }
  };
  auto dgo = [&](int q) {
    if (q == 0) asm volatile("s_mov_b32 m0, %3\n s_nop 0\n buffer_load_dwordx4 %0, %1, %2 offen sc1 lds" :: "v"(dvd), "s"(rs), "s"(dso), "s"(dm0) : "memory");
    else asm volatile("buffer_load_dwordx4 %0, %1, %2 offen offset:%3 sc1 lds" :: "v"(dvd), "s"(rs), "s"(dso), "n"(q * 1024) : "memory");
  };
  u32x4 f[1];
#pragma unroll
  for (int n = 0; n < 8; ++n) {
    asm volatile("s_waitcnt vmcnt(%0) lgkmcnt(0)\n s_barrier" :: "n"(4 * (n == 0 ? 4 : (n < 5 ? 3 : 7 - n)) + (n > RN && n < RN + 4 ? 1 : 0) + (n > RN + 4 ? 8 : 0)) : "memory");
    if (n == RN) asm volatile("buffer_load_dwordx4 %0, %1, %2, %3 offen sc1" : "=&v"(f[0]) : "v"(vo & 511u), "s"(rs), "s"(OFF_REC) : "memory");
    if (n >= 1 && n < 4) ask(n + 4);
    if (n == RN + 4) {
      asm volatile("" : "+v"(f[0]));
      if (!__all(f[0][0] == no[0] && f[0][1] == no[1])) rec_wait(rw, no, 31u);
      dma(0, w, none); dma(1, w, none);
      SCHED();
    }
    if (n == 7) PTR(17);
    take(n);
  }
  bar();
  const u32x4 zero4 = u32x4{0u, 0u, 0u, 0u};
  if (wk) {
    dma(2, wn, none);
    st_sc1(rw, 0x80000000u, zero4);
    if (!swap) st_sc1(rw, 0x80000010u, zero4);
    wait_vm<swap ? 5 : 6>();
  } else wait_vm<0>();
  __builtin_amdgcn_s_waitcnt(0xc07f);
  __builtin_amdgcn_s_barrier();
  PTR(19);
  const unsigned lb = (unsigned)(size_t)LDS3 + (unsigned)(l * 16);
  const unsigned ab = lb + (unsigned)(wk * 16384), sc = lb + 131072u + (unsigned)(wn * 2048);
  u32x4 a[8];
  auto rd = [&](int j, unsigned ad) { asm volatile("ds_read_b128 %0, %1 offset:%2" : "=v"(a[j & 7]) : "v"(ad), "n"((j & 15) * 1024) : "memory"); };
  if (wk == 0) {
#pragma unroll
    for (int j = 0; j < 8; ++j) rd(j, ab + 65536u);
    asm volatile("s_waitcnt lgkmcnt(0)" : "+v"(a[0]), "+v"(a[1]), "+v"(a[2]), "+v"(a[3]), "+v"(a[4]), "+v"(a[5]), "+v"(a[6]), "+v"(a[7]));
#pragma nounroll
    for (int k = 0; k < 2 * nseg; ++k) {
      const unsigned a0 = ab + (unsigned)(((k + 2) & 3) * 32768), a1 = ab + (unsigned)(((k + 3) & 3) * 32768);
      asm volatile("" :: "v"(a0), "v"(a1));
      wait_vm<8>();
      __builtin_amdgcn_s_setprio(1);
      __builtin_amdgcn_s_barrier();
      f32x4 c[2];
      SCHED();
#pragma unroll
      for (int j = 0; j < 16; ++j) {
#pragma unroll
        for (int ct = 0; ct < 2; ++ct) {
          const f32x4 z = j ? c[ct] : f32x4{0.f, 0.f, 0.f, 0.f};
          c[ct] = swap ? mfu(b[j][ct], a[j & 7], z) : mfu(a[j & 7], b[j][ct], z);
        }
        SCHED();
        rd(j + 8, j < 8 ? a0 : a1);
        if (PSP && j >= 2 && !(j & 1)) dprep((j >> 1) - 1, k + 2, 4 + wn);
        if (j & 1) asm volatile("s_waitcnt lgkmcnt(5)" : "+v"(a[(j + 2) & 7]), "+v"(a[(j + 3) & 7]));
        SCHED();
      }
      __builtin_amdgcn_s_setprio(2);
      if (PSP) { dgo(0); dgo(1); dgo(2); dgo(3); } else dma(k + 2, 4 + wn, none);
      SCHED();
      asm volatile("s_nop 7\n ds_write_b128 %0, %1\n ds_write_b128 %0, %2 offset:1024\n s_waitcnt lgkmcnt(0)" :: "v"(sc + (unsigned)((k & 1) * 8192)), "v"(c[0]), "v"(c[1]) : "memory");
      if (__builtin_expect(k == 8, 0)) hb = far_bit(near_kind(rw, no));
    }
    asm volatile("" : "+v"(a[0]), "+v"(a[1]), "+v"(a[2]), "+v"(a[3]), "+v"(a[4]), "+v"(a[5]), "+v"(a[6]), "+v"(a[7]));
    wait_vm<8>();
    __builtin_amdgcn_s_barrier();
  } else {
    const unsigned oo = (mat == 0 ? OFF_Q : (mat == 1 ? OFF_K : OFF_VX)) + (swap ? wn * 1024u : (wn >> 1) * HALF_ + (wn & 1) * 2048u);
    const unsigned v0 = l == 0 ? 0u : 0x80000000u;
    auto signal = [&](int r, unsigned va) {
      __builtin_amdgcn_raw_ptr_buffer_atomic_add_i32(swap ? (mat ? 1 : 65536) : 256, rw, va, OFF_CKV + (unsigned)(h * 64 + sgm(r)) * 4u, 0);
    };
    f32x4 c[2];
    u32x2 v[2];
    c[0] = c[1] = f32x4{0.f, 0.f, 0.f, 0.f};
    v[0] = v[1] = u32x2{0u, 0u};
    unsigned pso = oo, sgo = 0u, sgv = 0x80000000u, pn;
    auto put = [&](int r, int tt, unsigned vs) {
      const unsigned so = PSP ? pso : oo + tile8(sgm(r), h, hb);
      const u32x2 p0 = u32x2{cvt2x(c[0][0], c[0][1]), cvt2x(c[0][2], c[0][3])}, p1 = u32x2{cvt2x(c[1][0], c[1][1]), cvt2x(c[1][2], c[1][3])};
      if (swap) st_sc1s(rw, vs, so + (unsigned)tt * HALF_, u32x4{p0[0], p0[1], p1[0], p1[1]});
      else if (tt == 0) { v[0] = p0; v[1] = p1; }
      else {
        st_sc1s(rw, vs, so, u32x4{v[0][0], v[0][1], p0[0], p0[1]});
        st_sc1s(rw, vs, so + 1024u, u32x4{v[1][0], v[1][1], p1[0], p1[1]});
      }
    };
    __builtin_amdgcn_s_setprio(3);
    __builtin_amdgcn_s_barrier();
    rec_wait(rw, no, 3u, 64u + 4u * h);
    dma(3, wn, none);
    if (swap) st_sc1(rw, 0x80000000u, zero4);
    if (PSP) {
      if (rot) hb = far_bit(near_kind(rw, no));
#pragma unroll
      for (int j = 0; j < 4; ++j) dprep(j, 4, wn);
    }
#pragma nounroll
    for (int r = 0; r < nseg; ++r) {
#pragma unroll
      for (int tt = 0; tt < 2; ++tt) {
        const int i = 2 * r + tt;
        const unsigned a0 = ab + (unsigned)(((i + 2) & 3) * 32768);
        asm volatile("" :: "v"(a0));
        if (tt == 0) wait_vm<swap ? 5 : 4>(); else wait_vm<swap ? 6 : 7>();
        __builtin_amdgcn_s_barrier();
        if (tt == 1 && __builtin_expect(r < 2 && mat, 0)) __builtin_amdgcn_raw_ptr_buffer_atomic_add_i32(1 << 24, rw, v0, OFF_CKV + (unsigned)(h * 64 + r) * 4u, 0);
        if (__builtin_expect(i == (rot ? (PSP ? -1 : 1) : 4), 0)) hb = far_bit(near_kind(rw, no));
        if (tt == 0 && __builtin_expect(r == 14 - rot, 0)) {
#pragma nounroll
          for (int it = 0; it < HSPIN_CAP; ++it) {
            if (__builtin_amdgcn_readfirstlane(ld_sc1_32(rw, OFF_CF + 384u)) >= 32u) break;
            __builtin_amdgcn_s_sleep(16);
          }
          asm volatile("" ::: "memory");
        }
        if (tt == 1 && __builtin_expect(r > ISG && r <= ESG, 0)) { drain_vm(); signal(r - 1, v0); }
        f32x4 n[2];
        asm volatile("ds_read_b128 %0, %2\n ds_read_b128 %1, %2 offset:1024" : "=&v"(n[0]), "=&v"(n[1]) : "v"(sc + (unsigned)(tt * 8192)) : "memory");
        if (PSP) {
#pragma unroll
          for (int q = 0; q < 4; ++q) { dgo(q); rd(q, a0); }
        } else dma(i + 4, wn, [&](int q) { rd(q, a0); });
        if (tt == 0) put(r - 1, 1, r > 0 ? vo : 0x80000000u); else put(r, 0, vo);
        rd(4, a0);
        if (tt == 0) {
          if (PSP) __builtin_amdgcn_raw_ptr_buffer_atomic_add_i32(swap ? (mat ? 1 : 65536) : 256, rw, sgv, sgo, 0);
          else signal(r - 2, r >= ESG + 2 ? v0 : 0x80000000u);
        }
        rd(5, a0); rd(6, a0); rd(7, a0);
        if (tt == 0 || __builtin_expect(r >= BPR, 1)) __builtin_amdgcn_s_setprio(0);
        SCHED();
        asm volatile("s_waitcnt lgkmcnt(5)" : "+v"(n[0]), "+v"(n[1]), "+v"(a[0]), "+v"(a[1]), "+v"(a[2]));
        c[0] = n[0]; c[1] = n[1];
#pragma unroll
        for (int j = 0; j < 16; ++j) {
#pragma unroll
          for (int ct = 0; ct < 2; ++ct) c[ct] = swap ? mfu(b[j][ct], a[j & 7], c[ct]) : mfu(a[j & 7], b[j][ct], c[ct]);
          SCHED();
          if (j < 8) rd(j + 8, a0);
          if (PSP && j >= 8 && j < 12) dprep(j - 8, i + 5, wn);
          if (PSP && tt == 0 && j == 12) { pn = sgm(here(r)); asm volatile("" : "+s"(pn)); }
          if (PSP && tt == 0 && j == 13) { pso = oo + (unsigned)(here(pn) * 8 + (h & 7)) * (2u * BLK_); pn = (here(pn) < 2 ? 0u : hb >> 1) ^ (unsigned)(h >> 3) ^ pn; asm volatile("" : "+s"(pso), "+s"(pn)); }
          if (PSP && tt == 0 && j == 14) { pso += NearMap::pg(here(pn)); asm volatile("" : "+s"(pso)); }
          if (PSP && tt == 1 && j == 12) { sgo = OFF_CKV + (unsigned)(h * 64 + sgm(max(here(r) - 1, 0))) * 4u; asm volatile("" : "+s"(sgo)); }
          if (PSP && tt == 1 && j == 13) { sgv = here(r) + 1 >= ESG + 2 ? v0 : 0x80000000u; asm volatile("" : "+v"(sgv)); }
          if ((j & 1) && j < 14) asm volatile("s_waitcnt lgkmcnt(%2)" : "+v"(a[(j + 2) & 7]), "+v"(a[(j + 3) & 7]) : "n"(j < 8 ? 5 : (j < 13 ? 12 - j : 0)));
          SCHED();
        }
        __builtin_amdgcn_s_setprio(3);
      }
      if (__builtin_expect(r < ISG, 0)) {
        put(r, 1, vo);
        if (r < 2) PTB(r ? 20 : 8);
        if (r >= 2) {
          drain_vm();
          if (r == 2) { signal(0, v0); signal(1, v0); }
          signal(r, v0);
        }
      }
    }
    put(nseg - 1, 1, vo);
    drain_vm();
    signal(nseg - 2, v0); signal(nseg - 1, v0);
  }
  __builtin_amdgcn_s_setprio(0);
  drain_vm();
  __syncthreads();
  return hb;
}

constexpr int SPIN_CAP = 1 << 19;
#define LOCK 0
#define SL 0
#define GAM 1
enum { DLY1, DLY2, DLYV, DLY3, XDL, QDL, DLYL, DLYW, XT1, XTL, DLYD, KG3, KG1, DLYG, KG2, KGV, DLYR = QDL, DLYX = KG3, DLY4 = DLYG, DLYB = KG3, DL7A = KG2, DL7B = KGV, CAB = KG1, CAK = KG2 };
#define D4 1
#define NMX 1
#define S3 0
#define NOMIN 0
#define TID 3
#define EARLY 0
#define L2 1
#define L3 1
#define PRE3 LOCK
#define RSE 0

#ifndef NV1
#define NV1 4
#endif
#define VOR 1
#define LVS 1
#define CIN 1
#define GIL 1
#define G1N ""
#define G1F "s_nop 0\n"
#define G3N ""
#define XIL 1
#define VAS 1
#define TCH 0
#define TC1 0
#define TCW 1
#define APP 0
#define CNT_OFF 15872
#define PAN 1
#define TWO 1
#define W7 0
#define LWT EARLY
#define S0A 0
#define S0C 0
#define S0D 0
#define B5 0
#define CAD 1
#define CA0 2
#define CA1 6
#define CA2 8
#define G3I 1
#define WB1 1
#define WB2 3
#define ALG 1
#define PBA 6
#define PBN 7
#define ESL 2
#define LSL 0
#define KSL 8
constexpr int PQK_OFF = 20480;
constexpr int W3_OFF = 98304;
constexpr int KV_OFF = 98304;
constexpr int PS_OFF = 131072;
constexpr int PZ_OFF = PS_OFF + 4096;
constexpr int UM_OFF = PZ_OFF + 1024;
constexpr int KC_OFF = UM_OFF + 3072;
constexpr int XV_OFF = KC_OFF + 1024;
constexpr int OB_OFF = XV_OFF + 2048;
constexpr int XK_OFF = OB_OFF + 2048;
constexpr float CS = SCALE_ * 1.4426950408889634f;
typedef __bf16 bfv8 __attribute__((ext_vector_type(8)));
typedef __bf16 bfv16 __attribute__((ext_vector_type(16)));
typedef float f32x2 __attribute__((ext_vector_type(2)));
typedef unsigned u32x8 __attribute__((ext_vector_type(8)));
DEVI f32x4 smf(u32x4 a, u32x8 b, f32x4 c, int idx) {
  return __builtin_amdgcn_smfmac_f32_16x16x64_bf16(__builtin_bit_cast(bfv8, a), __builtin_bit_cast(bfv16, b), c, idx, 0, 0);
}
DEVI float fold8(float a) { return a + __uint_as_float(__builtin_amdgcn_update_dpp(0u, __float_as_uint(a), 0x128, 0xf, 0xf, true)); }
#define LO2(c) f32x2{c[0], c[1]}
#define HI2(c) f32x2{c[2], c[3]}
DEVI f32x4 zero4() { f32x4 z = f32x4{0.f, 0.f, 0.f, 0.f}; asm volatile("" : "+v"(z)); return z; }
DEVI u32x4 cvt8(f32x4 a, f32x4 b) { return u32x4{cvt2(a[0], a[1]), cvt2(a[2], a[3]), cvt2(b[0], b[1]), cvt2(b[2], b[3])}; }

DEVI float vmax(unsigned a, unsigned b) { float r; asm("v_max_f32 %0, %1, %2" : "=v"(r) : "v"(a), "v"(b)); return r; }
DEVI float rows_max(float x) {
  const unsigned u = __float_as_uint(x);
  auto a = __builtin_amdgcn_permlane16_swap(u, u, false, false);
  const unsigned u2 = __float_as_uint(vmax(a[0], a[1]));
  auto b = __builtin_amdgcn_permlane32_swap(u2, u2, false, false);
  return vmax(b[0], b[1]);
}
DEVI float rows_sum(float x) {
  const unsigned u = __float_as_uint(x);
  auto a = __builtin_amdgcn_permlane16_swap(u, u, false, false);
  x = __uint_as_float(a[0]) + __uint_as_float(a[1]);
  const unsigned u2 = __float_as_uint(x);
  auto b = __builtin_amdgcn_permlane32_swap(u2, u2, false, false);
  return __uint_as_float(b[0]) + __uint_as_float(b[1]);
}

DEVI void req4(u32x4 r, unsigned vo, unsigned so, u32x4* v) {
  asm volatile("buffer_load_dwordx4 %0, %4, %5, %6 offen sc1\n buffer_load_dwordx4 %1, %4, %5, %6 offen offset:1024 sc1\n"
               "buffer_load_dwordx4 %2, %4, %5, %6 offen offset:2048 sc1\n buffer_load_dwordx4 %3, %4, %5, %6 offen offset:3072 sc1"
               : "=&v"(v[0]), "=&v"(v[1]), "=&v"(v[2]), "=&v"(v[3]) : "v"(vo), "s"(r), "s"(so) : "memory");
}
#define ILV 4
DEVI void req4i(u32x4 r, unsigned vo, unsigned so, u32x4* v) {
  asm volatile("buffer_load_dwordx4 %0, %5, %6, %4 offen sc1\n s_xor_b32 %4, %4, 0x400\n s_nop %7\n buffer_load_dwordx4 %1, %5, %6, %4 offen sc1\n s_xor_b32 %4, %4, 0xc00\n s_nop %7\n"
               "buffer_load_dwordx4 %2, %5, %6, %4 offen sc1\n s_xor_b32 %4, %4, 0x400\n s_nop %7\n buffer_load_dwordx4 %3, %5, %6, %4 offen sc1"
               : "=&v"(v[0]), "=&v"(v[1]), "=&v"(v[2]), "=&v"(v[3]), "+s"(so) : "v"(vo), "s"(r), "n"(ILV ? ILV - 1 : 0) : "memory", "scc");
}
DEVI void req4it(u32x4 r, unsigned so, u32x4* v) {
  asm volatile("buffer_load_dwordx4 %0, off, %5, %4 sc1\n s_xor_b32 %4, %4, 0x400\n s_nop %6\n buffer_load_dwordx4 %1, off, %5, %4 sc1\n s_xor_b32 %4, %4, 0xc00\n s_nop %6\n"
               "buffer_load_dwordx4 %2, off, %5, %4 sc1\n s_xor_b32 %4, %4, 0x400\n s_nop %6\n buffer_load_dwordx4 %3, off, %5, %4 sc1"
               : "=&v"(v[0]), "=&v"(v[1]), "=&v"(v[2]), "=&v"(v[3]), "+s"(so) : "s"(r), "n"(ILV ? ILV - 1 : 0) : "memory", "scc");
}
DEVI u32x4 rs_tid(u32x4 r) { return u32x4{r[0], r[1] | (16u << 16), r[2] >> 4, 1u << 23}; }
DEVI void req4t(u32x4 r, unsigned so, u32x4* v) {
  asm volatile("buffer_load_dwordx4 %0, off, %4, %5 sc1\n buffer_load_dwordx4 %1, off, %4, %5 offset:1024 sc1\n"
               "buffer_load_dwordx4 %2, off, %4, %5 offset:2048 sc1\n buffer_load_dwordx4 %3, off, %4, %5 offset:3072 sc1"
               : "=&v"(v[0]), "=&v"(v[1]), "=&v"(v[2]), "=&v"(v[3]) : "s"(r), "s"(so) : "memory");
}
DEVI void req2t(u32x4 r, unsigned so, u32x4* v) {
  asm volatile("buffer_load_dwordx4 %0, off, %2, %3 sc1\n buffer_load_dwordx4 %1, off, %2, %3 offset:1024 sc1" : "=&v"(v[0]), "=&v"(v[1]) : "s"(r), "s"(so) : "memory");
}
DEVI void req2(u32x4 r, unsigned vo, unsigned so, u32x4* v) {
  asm volatile("buffer_load_dwordx4 %0, %2, %3, %4 offen sc1\n buffer_load_dwordx4 %1, %2, %3, %4 offen offset:1024 sc1"
               : "=&v"(v[0]), "=&v"(v[1]) : "v"(vo), "s"(r), "s"(so) : "memory");
}
template <int N>
DEVI void got(u32x4& v) { asm volatile("s_waitcnt vmcnt(%1)" : "+v"(v) : "n"(N)); }
#define A8 ".p2align 3\n"
#define A4 ".p2align 3\n s_nop 0\n"
#define STR_(x) #x
#define STR(x) STR_(x)
#define MF "v_mfma_f32_16x16x32_bf16 "
#define CV "v_cvt_pk_bf16_f32 "
#define SM "v_smfmac_f32_16x16x64_bf16 "
template <int N>
DEVI void got_s4(u32x4& a, f32x4* c, u32x8 b0, u32x8 b1, u32x8 b2, u32x8 b3, int idx) {
  asm volatile(".if %10 == 3 + " STR(LOCK) "\n" A4 ".endif\n s_waitcnt vmcnt(%10)\n" SM "%1, %0, %5, %9\n" SM "%2, %0, %6, %9\n" SM "%3, %0, %7, %9\n" SM "%4, %0, %8, %9"
               : "+v"(a), "+v"(c[0]), "+v"(c[1]), "+v"(c[2]), "+v"(c[3]) : "v"(b0), "v"(b1), "v"(b2), "v"(b3), "v"(idx), "n"(N));
}
template <int N>
DEVI void got_s2(u32x4& a, f32x4* c, u32x8 b0, u32x8 b1, int idx) {
  asm volatile(".if %6 == 1\n" A4 ".endif\n s_waitcnt vmcnt(%6)\n" SM "%1, %0, %3, %5\n" SM "%2, %0, %4, %5" : "+v"(a), "+v"(c[0]), "+v"(c[1]) : "v"(b0), "v"(b1), "v"(idx), "n"(N));
}
template <int N>
DEVI void got_x(u32x4& x, f32x4& sa, f32x4& sb, f32x4& sm, u32x4 ka, u32x4 kb) {
  asm volatile("s_waitcnt vmcnt(%6)\n" MF "%1, %4, %0, %1\n" MF "%2, %5, %0, %2\n" MF "%3, %0, %0, %3" : "+v"(x), "+v"(sa), "+v"(sb), "+v"(sm) : "v"(ka), "v"(kb), "n"(N));
}
template <int N>
DEVI void got_m(u32x4& b, f32x4& d, u32x4 a) { asm volatile("s_waitcnt vmcnt(%3)\n" MF "%1, %2, %0, %1" : "+v"(b), "+v"(d) : "v"(a), "n"(N)); }
DEVI unsigned umax3(unsigned a, unsigned b, unsigned c) { return max(max(a, b), c); }
DEVI unsigned umax5(u32x4 v, unsigned m) { return umax3(v[2], v[3], umax3(v[0], v[1], m)); }
DEVI bool bad(unsigned m) { return __builtin_expect(__builtin_amdgcn_ballot_w64(m == POISON) != 0ull, 0); }
DEVI bool over(unsigned m, unsigned thr) { return __builtin_expect(__builtin_amdgcn_ballot_w64(m > thr) != 0ull, 0); }
DEVI bool bad4(const u32x4* v) { return bad(umax5(v[3], umax5(v[2], umax5(v[1], umax5(v[0], 0u))))); }
DEVI unsigned tag5(u32x4 v, unsigned t, unsigned m) {
  return __builtin_amdgcn_bitop3_b32(v[0], v[1], t, 0x7e) | __builtin_amdgcn_bitop3_b32(v[2], v[3], t, 0x7e) | m;
}
DEVI unsigned rt_now() {
  unsigned long long t;
  asm volatile("s_memrealtime %0\n s_waitcnt lgkmcnt(0)" : "=s"(t) :: "memory");
  return (unsigned)t;
}
#define DLYARG
#define DLYA
#define DLYC(i) dly[i]
#define DLYI(mask_) unsigned dly[16] = {DLYS}; _Pragma("unroll") for (int i = 0; i < 16; ++i) if (((mask_) >> i) & 1) asm volatile("" : "+s"(dly[i]));
#define PAL 6
DEVI void pause(unsigned n) { asm volatile(".p2align %1\n 1: s_sub_u32 %0, %0, 1\n s_cbranch_scc0 1b" : "+s"(n) : "n"(PAL) : "scc", "memory"); }
DEVI unsigned mt_now() {
  unsigned long long t;
  asm volatile("s_memtime %0\n s_waitcnt lgkmcnt(0)" : "=s"(t) :: "memory");
  return (unsigned)t;
}
DEVI unsigned gt_now() {
  unsigned long long b, c, d;
  asm volatile("s_memrealtime %0\n s_nop 6\n s_memrealtime %1\n s_nop 6\n s_memrealtime %2\n s_waitcnt lgkmcnt(0)" : "=&s"(b), "=&s"(c), "=&s"(d) :: "memory");
  return ((unsigned)b + (unsigned)c + (unsigned)d) * 8u;
}
DEVI void clk_pair(unsigned& t, unsigned& gt) {
  unsigned long long a, b, c, d;
  asm volatile("s_memtime %0\n s_memrealtime %1\n s_nop 6\n s_memrealtime %2\n s_nop 6\n s_memrealtime %3\n s_waitcnt lgkmcnt(0)" : "=&s"(a), "=&s"(b), "=&s"(c), "=&s"(d) :: "memory");
  t = (unsigned)a;
  gt = ((unsigned)b + (unsigned)c + (unsigned)d) * 8u;
}
DEVI unsigned local_of(unsigned tgt, int rq, unsigned t, unsigned g) {
  int d = (int)(tgt - g);
  asm("s_max_i32 %0, %0, 0xffffc000\n s_min_i32 %0, %0, 0x4000" : "+s"(d) :: "scc");
  return t + (unsigned)(d * rq >> 16);
}
DEVI int until(unsigned tgt, int dmin, unsigned now) {
  const int left = (int)(tgt - now);
  int d;
  asm volatile("s_max_i32 %0, %1, %3\n s_min_i32 %0, %0, 4000\n"
               ".p2align %2\n 1: s_sub_u32 %0, %0, 28\n s_cbranch_scc0 1b\n s_cmp_lt_i32 %0, -20\n s_cbranch_scc1 2f\n s_cmp_lt_i32 %0, -12\n s_cbranch_scc1 2f\n"
               "s_cmp_lt_i32 %0, -4\n s_cbranch_scc1 2f\n s_nop 0\n s_branch 2f\n 2:" : "=&s"(d) : "s"(left), "n"(PAL), "s"(dmin) : "scc", "memory");
  return left;
}
DEVI int until(unsigned tgt, int dmin) { return until(tgt, dmin, mt_now()); }
constexpr int TG_OFF = KC_OFF + 16, GP_OFF = XK_OFF + 3072;
#define KTHR 200
#define KFOL 0
#define KPEN 600
#define KLATE 2050

#define TRD
#define TR(i)
#define TRW()
#define TRE(b)
#define TRARGD
#define TRARG
#define SLP(i)
#define TRT()

#define T0(i)
#define TP(i)
#define XM 0
#define TRX(n) do { if (XM == (n)) TR(7); } while (0)
#define TRY(n) do { if (XM == (n)) TR(4); } while (0)
DEVI unsigned kv_want(int s) { return s < XS_ ? 0x404u : 0x40404u; }
DEVI void kv_wait(u32x4 rs, int s, int h, int& budget) {
  const unsigned want = s > 1 ? kv_want(s) : 8u << 24;
  while (true) {
    unsigned c;
    asm volatile("buffer_load_dword %0, off, %1, %2 sc1\n s_waitcnt vmcnt(0)" : "=v"(c) : "s"(rs), "s"(OFF_CKV + (unsigned)(h * 64 + s) * 4u) : "memory");
    if ((__builtin_amdgcn_readfirstlane(c) & want) == want || --budget <= 0) break;
    __builtin_amdgcn_s_sleep(4);
  }
}

DEVI unsigned kv_src(int s, int h, int w, unsigned hb) { return ((w >> 1) ? OFF_VX : OFF_K) + tile8(s, h, hb) + (w & 1) * HALF_; }
DEVI unsigned kv_dst(int s, int w) { return (unsigned)(size_t)LDS3 + KV_OFF + (s & 1) * 16384 + w * 4096; }
DEVI void kv_dma(u32x4 rs, unsigned dvo, unsigned so, unsigned dst) {
  asm volatile("s_mov_b32 m0, %3\n s_nop 0\n buffer_load_dwordx4 %0, %1, %2 offen sc1 lds\n buffer_load_dwordx4 %0, %1, %2 offen offset:1024 sc1 lds\n"
               "buffer_load_dwordx4 %0, %1, %2 offen offset:2048 sc1 lds\n buffer_load_dwordx4 %0, %1, %2 offen offset:3072 sc1 lds"
               :: "v"(dvo), "s"(rs), "s"(so), "s"(dst) : "memory");
}

DEVI unsigned ovoff(unsigned lv) { return (lv & 128u) * 8u + (lv & 0x370u); }
DEVI unsigned osoff(int s, int h, int jj, unsigned hb) { return ooff(s, h, hb) + (jj >> 1) * HALF_ + (jj & 1) * 128u; }
DEVI void ostore(u32x4 rs, const u32x4* oc, unsigned ov, unsigned os) {
  asm volatile("buffer_store_dwordx4 %0, %2, %3, %4 offen sc1\n buffer_store_dwordx4 %1, %2, %3, %4 offen offset:2048 sc1\n s_nop 1" :: "v"(oc[0]), "v"(oc[1]), "v"(ov), "s"(rs), "s"(os) : "memory");
}
DEVI void kv_first(char* lds, u32x4 rs, int sg, int h, int w, unsigned lv, int& budget) {
  u32x4 t[4];
  do {
    req4(rs, lv ^ (unsigned)(w >> 1) * (((lv >> 3) & 32u) + ((lv >> 2) & 128u)), kv_src(sg, h, w, 0u), t);
    asm volatile("s_waitcnt vmcnt(0)" : "+v"(t[0]), "+v"(t[1]), "+v"(t[2]), "+v"(t[3]));
  } while (bad4(t) && --budget > 0);
#pragma unroll
  for (int i = 0; i < 4; ++i) *(u32x4*)(lds + KV_OFF + sg * 16384 + w * 4096 + i * 1024 + lv) = t[i];
}
DEVI float max6(const float* e) {
  float mx, t0, t1;
  asm("v_max3_f32 %0, %3, %4, %5\n v_max3_f32 %1, %6, %7, %8\n v_max_f32 %0, %0, %1\n s_nop 1\n"
      "v_max_f32_dpp %1, %0, %0 row_ror:8 row_mask:0xf bank_mask:0xf\n v_max_f32_dpp %2, %0, %0 row_ror:8 row_mask:0xf bank_mask:0xf\n s_nop 1\n"
      "v_permlane16_swap_b32 %1, %2\n v_max_f32 %0, %1, %2\n v_max_f32 %1, %1, %2\n s_nop 1\n"
      "v_permlane32_swap_b32 %0, %1\n v_max_f32 %0, %0, %1"
      : "=&v"(mx), "=&v"(t0), "=&v"(t1) : "v"(e[0]), "v"(e[1]), "v"(e[2]), "v"(e[3]), "v"(e[4]), "v"(e[5]));
  return mx;
}

DEVI void recur_p1(char* lds, rsrc_t rw, const u32x4 rs, const u16* __restrict__ mem0, const u16* __restrict__ Wq, const u16* __restrict__ Wk,
                   const u16* __restrict__ Wv, u16* __restrict__ out, const char* ws, int wg TRARGD DLYARG) {
  DLYI((LOCK ? 0x7849 : 0x49) | (RSE ? 0x20 : 0) | (SL ? 0x1000 : 0) | (TWO ? 0x800 : 0) | (W7 ? 0xc000 : 0));
  const int l = threadIdx.x & 63, w = __builtin_amdgcn_readfirstlane(threadIdx.x >> 6) & 3, g = l >> 4, lo = l & 15;
  const int h = wg >> 2, jj = wg & 3;
  int budget = SPIN_CAP;
  unsigned te = 0u, ge = 0u, gpo = 0u, t1g = 0u, t3g = 0u;
  int kq = 0, rq = 0;
  if ((LOCK || SL) && w == 1) clk_pair(te, ge);
  unsigned mz = 0u;
  if (ALG) asm volatile("" : "+s"(mz));
  const int vq = 2 * (~jj & 1) + (jj >> 1);
  u32x8 w1[6][4];
  u32x4 a[4];
  {
    char* wl = lds + w * 24576;
    const char* g1 = (const char*)((jj & 2) ? Wk : Wq) + (size_t)((256 * w + (l >> 3)) * HD_ + h * DH_ + 64 * (jj & 1) + ((l & 7) ^ (2 * g)) * 8) * 2u;
    const char* g2 = (const char*)Wv + (size_t)((256 * w + (l >> 2)) * HD_ + h * DH_ + 32 * vq + (l & 3) * 8) * 2u;
    const unsigned mo = (unsigned)((2 * (lo >> 2) + (lo & 1)) * D_ + 256 * w + 16 * g + 4 * (lo & 2)) * 2u;
    const int kx = ILV ? w : 0;
    asm volatile("buffer_load_dwordx4 %0, %4, %5, %6 offen\n buffer_load_dwordx4 %1, %4, %5, %7 offen\n"
                 "buffer_load_dwordx4 %2, %4, %5, %8 offen\n buffer_load_dwordx4 %3, %4, %5, %9 offen"
                 : "=&v"(a[0]), "=&v"(a[1]), "=&v"(a[2]), "=&v"(a[3]) : "v"(mo), "s"(rsrc4(mem0, MEM_ * D_ * 2u)), "s"(kx * 128), "s"((kx ^ 1) * 128), "s"((kx ^ 2) * 128), "s"((kx ^ 3) * 128) : "memory");
    auto ask = [&](int n) {
#pragma unroll
      for (int i = 0; i < 4; ++i)
        __builtin_amdgcn_global_load_lds((const unsigned*)(n % 3 < 2 ? g1 + (size_t)(64 * ((n / 3) ^ kx) + 32 * (n % 3) + 8 * i) * HD_ * 2 : g2 + (size_t)(64 * ((n / 3) ^ kx) + 16 * i) * HD_ * 2),
                                         (unsigned*)(wl + (n % 6) * 4096 + i * 1024 + l * 16), 16, 0, NTW);
      SCHED();
    };
    const int row = 16 * (g >> 1) + 4 * (g & 1) + (lo >> 2);
    const char* b1 = wl + row * 128 + (lo & 3) * 8;
    const char* b2 = wl + row * 64 + (lo & 3) * 8;
    auto use = [&](int n) {
#pragma unroll
      for (int i = 0; i < 8; ++i) {
        const int u = n % 3 < 2 ? i >> 1 : 4 + (i >> 2), q = n % 3 < 2 ? 2 * (n % 3) + (i & 1) : i & 3;
        const u32x2 v = tr_quad(n % 3 < 2 ? b1 + (n % 6) * 4096 + ((u ^ ((row >> 1) & 3)) * 32) + (q & 1) * 1024
                                          : b2 + (n % 6) * 4096 + (u & 1) * 32 + (32 * (q >> 1) + 8 * (q & 1)) * 64);
        w1[u][n / 3][(q & 1) + 4 * (q >> 1)] = v[0];
        w1[u][n / 3][(q & 1) + 4 * (q >> 1) + 2] = v[1];
      }
      __builtin_amdgcn_s_waitcnt(0xc07f);
      asm volatile("" ::: "memory");
      SCHED();
    };
#pragma unroll
    for (int n = 0; n < 6; ++n) ask(n);
#pragma unroll
    for (int n = 0; n < 12; ++n) {
      if (n == WB1 || n == WB2) __builtin_amdgcn_s_barrier();
      asm volatile("s_waitcnt vmcnt(%0)" :: "n"(n < 7 ? 20 : 44 - 4 * n) : "memory");
      if (n == 0) asm volatile("" : "+v"(a[0]), "+v"(a[1]), "+v"(a[2]), "+v"(a[3]));
      if (n == 0 || n == 11) TP(n == 11);
      use(n);
      if (n < 6) ask(n + 6);
    }
    TP(2);
    if ((LOCK || SL) && w == 1) *(u32x4*)(lds + TG_OFF) = u32x4{te, ge, 61000u, te};
    if (!S0C) bar();
  }
  const int idx = (lo & 2) ? 0xeeee : 0x4444;
  const unsigned vo = (unsigned)l * 16u;
  const unsigned pq0 = (unsigned)(size_t)LDS3 + PQK_OFF + (w * 5120 + 2 * g * 40 + 8 * (lo >> 2) + (lo & 3)) * 4;
  const unsigned r1w = NearMap::blk(NB1 + jj) + h * 128u;
  unsigned mvo = 0u, r1r = 0u, r3r = 0u, r2b = 0u, hb = 0u;
  unsigned tg = 0u;
  int known = 0;
  unsigned qs = 0u;
  unsigned sc = 0u, sn = SET_;
  u32x4 rt = rs_tid(rs);
  if (TID) asm volatile("" : "+s"(rt));
  const unsigned dl3 = W7 && w == 3 ? dly[DL7A] : dly[DLY3], dlb0 = W7 && w == 3 ? dly[DL7B] : dly[DLYB];
  u32x4 b[4];
  asm volatile("" : "=v"(b[0]), "=v"(b[1]), "=v"(b[2]), "=v"(b[3]));
  unsigned anv = 0x80000000u;
  auto announce = [&](unsigned t, int db) {
    db += (int)dly[KG1];
    const u32x4 an = u32x4{t + (unsigned)(db * kq >> 16), t + (unsigned)((db + (int)dly[KG2]) * kq >> 16), t + (unsigned)((db + (int)dly[KG3]) * kq >> 16), 0u};
    asm volatile("buffer_store_dwordx4 %0, %1, %2, %3 offen sc1\n s_nop 1" :: "v"(an), "v"(anv), "s"(rs), "s"(4u * SET_ + NearMap::blk(NB1) + (unsigned)(wg >> 3) * 128u) : "memory");
  };
  TRD;
  for (int s = 0; s < NSEG_; ++s) {
    f32x4 acc[6];

    TR(0);
    unsigned m, thr, pq1 = pq0;
    asm volatile("" : "+v"(pq1));
    pq1 += 1280u;
    asm volatile("" : "+v"(pq1));
    if (APP) asm volatile("s_mov_b32 m0, 0");
    unsigned kl;
    const int late = 0;
    asm volatile("ds_read_b32 %0, %1" : "=v"(kl) : "v"((unsigned)(size_t)LDS3 + KC_OFF + 8u) : "memory");
    if (s > 0) pause(dly[DLY1]);
    unsigned fb5 = s;
    do {
      thr = --budget > 0 && s > 0 ? POISON - 1u : POISON;
      asm volatile("" : "+s"(thr));
      TRY(3);
      if (__builtin_expect(s > 0, 1)) { if (TID & 2) req4it(rt, r1r + sc, a); else req4i(rs, vo, r1r + sc, a); }
      if (B5) asm volatile("s_cmp_eq_u32 %0, 0\n s_cbranch_scc1 9f\n s_barrier\n s_mov_b32 %0, 0\n 9:" : "+s"(fb5) :: "scc", "memory");
      if (LOCK) {
        announce(t1g, late > KTHR ? min(late, 20000) + KPEN : (KFOL ? max(late, 0) : 0));
      }
      asm volatile("" : "=v"(acc[0]), "=v"(acc[1]), "=v"(acc[2]), "=v"(acc[3]), "=v"(acc[4]), "=v"(acc[5]));
      asm volatile("v_mov_b64 %0, 0\n v_mov_b64 %1, 0\n v_mov_b64 %2, 0\n v_mov_b64 %3, 0\n v_mov_b64 %4, 0\n v_mov_b64 %5, 0\n"
                   "v_mov_b64 %6, 0\n v_mov_b64 %7, 0\n v_mov_b64 %8, 0\n v_mov_b64 %9, 0\n v_mov_b64 %10, 0\n v_mov_b64 %11, 0"
                   :: "v"(LO2(acc[0])), "v"(HI2(acc[0])), "v"(LO2(acc[1])), "v"(HI2(acc[1])), "v"(LO2(acc[2])), "v"(HI2(acc[2])),
                      "v"(LO2(acc[3])), "v"(HI2(acc[3])), "v"(LO2(acc[4])), "v"(HI2(acc[4])), "v"(LO2(acc[5])), "v"(HI2(acc[5])));
      SCHED();
#define G1P(i, al, m0) asm volatile(al "s_waitcnt vmcnt(%15)\n" SM "%1, %0, %6, %10\n" SM "%2, %0, %7, %10\n v_max3_u32 %5, %11, %12, " m0 "\n" SM "%3, %0, %8, %10\n v_max3_u32 %5, %13, %14, %5\n" SM "%4, %0, %9, %10" \
                   : "+v"(a[i]), "+v"(acc[0]), "+v"(acc[1]), "+v"(acc[2]), "+v"(acc[3]), "+v"(m) \
                   : "v"(w1[0][i]), "v"(w1[1][i]), "v"(w1[2][i]), "v"(w1[3][i]), "v"(idx), "v"(a[i][0]), "v"(a[i][1]), "v"(a[i][2]), "v"(a[i][3]), "n"(3 - i + LOCK))
      asm volatile("" : "=v"(m));
      G1P(0, A4, "%12"); SCHED();
      G1P(1, G1N, "%5"); SCHED();
      G1P(2, G1N, "%5"); SCHED();
      G1P(3, G1N, "%5"); SCHED();
    } while (over(m, thr));
    TR(1);
    SCHED();
    asm volatile(G1F "v_pk_add_f32 %2, %2, %3\n v_pk_add_f32 %6, %6, %7\n ds_write2_b32 %0, %4, %5 offset1:40\n v_pk_add_f32 %10, %10, %11\n"
                 "ds_write2_b32 %0, %8, %9 offset0:4 offset1:44\n v_pk_add_f32 %14, %14, %15\n ds_write2_b32 %1, %12, %13 offset1:40\n ds_write2_b32 %1, %16, %17 offset0:4 offset1:44"
                 :: "v"(pq0), "v"(pq1), "v"(LO2(acc[0])), "v"(HI2(acc[0])), "v"(acc[0][0]), "v"(acc[0][1]), "v"(LO2(acc[1])), "v"(HI2(acc[1])), "v"(acc[1][0]), "v"(acc[1][1]),
                    "v"(LO2(acc[2])), "v"(HI2(acc[2])), "v"(acc[2][0]), "v"(acc[2][1]), "v"(LO2(acc[3])), "v"(HI2(acc[3])), "v"(acc[3][0]), "v"(acc[3][1]) : "memory");
    SCHED();
#pragma unroll
    for (int i = 0; i < 7; ++i) {
      acc[4 + (i >> 2)] = smf(a[i & 3], w1[4 + (i >> 2)][i & 3], acc[4 + (i >> 2)], idx);
      SCHED();
      if (i == NV1 - 1) { TR(2); bar(); SCHED(); }
    }
    asm volatile(SM "%9, %10, %11, %12\n v_add_u32 %0, 0x500, %0\n s_nop 0\n v_pk_add_f32 %1, %1, %2\n ds_write2_b32 %0, %3, %4 offset1:40\n s_nop 3\n v_pk_add_f32 %5, %5, %6\n ds_write2_b32 %0, %7, %8 offset0:4 offset1:44\n"
                 ".if " STR(APP) "\n ds_append %0 offset:" STR(CNT_OFF) "\n s_barrier\n .endif"
                 :: "v"(pq1), "v"(LO2(acc[4])), "v"(HI2(acc[4])), "v"(acc[4][0]), "v"(acc[4][1]), "v"(LO2(acc[5])), "v"(HI2(acc[5])), "v"(acc[5][0]), "v"(acc[5][1]),
                    "v"(acc[5]), "v"(a[3]), "v"(w1[5][3]), "v"(idx) : "memory");
    if (APP) asm volatile("s_waitcnt lgkmcnt(0)" :: "v"(pq1) : "memory"); else bar();
    asm volatile("s_cmp_eq_u32 %6, 0\n s_cbranch_scc1 1f\n v_and_b32 %4, 0x370, %7\n"
                 "buffer_load_dwordx4 %0, %4, %5, %6 offen sc1\n buffer_load_dwordx4 %1, %4, %5, %6 offen offset:1024 sc1\n"
                 "buffer_load_dwordx4 %2, %4, %5, %6 offen offset:2048 sc1\n buffer_load_dwordx4 %3, %4, %5, %6 offen offset:3072 sc1\n 1:"
                 : "=&v"(a[0]), "=&v"(a[1]), "=&v"(a[2]), "=&v"(a[3]), "=&v"(m) : "s"(rs), "s"(qs), "v"(vo) : "memory", "scc");
    TR(3);
    SLP(0);
    const bool last = s == NSEG_ - 1;
    asm volatile("" : "+v"(kl));
    if (__builtin_expect(__builtin_amdgcn_readfirstlane(kl) != 0x404u && s > 0 && !last, 0)) {
      unsigned lv = vo;
      asm volatile("" : "+v"(lv));
      __builtin_amdgcn_s_sleep(LSL);
      kv_wait(rs, s + 1, h, budget);
      kv_dma(rs, lv ^ (unsigned)(w >> 1) * (((lv >> 3) & 32u) + ((lv >> 2) & 128u)), kv_src(s + 1, h, w, hb), kv_dst(s + 1, w));
      drain_vm();
    }
    auto reset = [&]() {
      u32x2 p2 = u32x2{POISON, POISON};
      const unsigned sp = 3u * SET_ - sc - sn;
      unsigned lv = vo;
      asm volatile("" : "+v"(lv));
      unsigned po = w ? (lv >> 1) + (w < 3 ? (lv >> 8) * 128u : 0u) : mvo;
      const unsigned ps = sp + (w ? r2b + (w == 3 ? BLK_ + vq * 512u : (2 * (jj & 1) + w - 1) * 1024u + (jj >> 1) * 128u) : r1w);
      asm volatile("s_waitcnt lgkmcnt(0)" : "+v"(po), "+v"(p2) : "s"(ps) : "memory");
      __builtin_amdgcn_s_barrier();
      st_sc1s_64(rw, po, ps, p2);
    };
    unsigned tv_, ov, os, pm;
    u32x4 oc[2], sel;
    auto none3 = [&]() { asm volatile("" : "=v"(tv_), "=v"(sel), "=v"(oc[0]), "=v"(oc[1]), "=v"(ov)); asm volatile("" : "=s"(pm), "=s"(os)); };
    auto prep3 = [&]() {
      tv_ = vo;
      asm volatile("" : "+v"(tv_));
      oc[0] = *(const u32x4*)(lds + OB_OFF + tv_); oc[1] = *(const u32x4*)(lds + OB_OFF + 1024 + tv_);
      ov = ovoff(tv_) | (s > XS_ && !last && w == 0 ? 0u : 0x80000000u); os = osoff(max(s - 1, 0), h, jj, hb);
      const unsigned sel1 = 0x3f80u << (tv_ & 16u);
      sel = u32x4{(tv_ & 96u) == 0u ? sel1 : 0u, (tv_ & 96u) == 32u ? sel1 : 0u, (tv_ & 96u) == 64u ? sel1 : 0u, (tv_ & 96u) == 96u ? sel1 : 0u};
      tv_ = (tv_ >> 7) ^ tg;
      pm = r1w + sn;
      asm volatile("" : "+v"(tv_), "+v"(sel), "+v"(oc[0]), "+v"(oc[1]), "+v"(ov));
      asm volatile("" : "+s"(pm), "+s"(os));
    };
    if (qs) {
      float e[6];
      unsigned lv = vo;
      asm volatile("" : "+v"(lv));
      f32x4 sa, sb;
      u32x4 km[4];
      asm volatile("ds_read_b128 %2, %10\n ds_read_b128 %3, %10 offset:1024\n ds_read_b128 %4, %10 offset:2048\n ds_read_b128 %5, %10 offset:3072\n s_waitcnt vmcnt(0) lgkmcnt(0)\n"
                   MF "%0, %2, %6, 0\n ds_read_b128 %2, %10 offset:4096\n" MF "%0, %3, %7, %0\n ds_read_b128 %3, %10 offset:5120\n"
                   MF "%0, %4, %8, %0\n ds_read_b128 %4, %10 offset:6144\n" MF "%0, %5, %9, %0\n ds_read_b128 %5, %10 offset:7168\n s_waitcnt lgkmcnt(3)\n"
                   MF "%1, %2, %6, 0\n s_waitcnt lgkmcnt(2)\n" MF "%1, %3, %7, %1\n s_waitcnt lgkmcnt(1)\n" MF "%1, %4, %8, %1\n s_waitcnt lgkmcnt(0)\n" MF "%1, %5, %9, %1\n s_nop 7"
                   : "=&v"(sa), "=&v"(sb), "=&v"(km[0]), "=&v"(km[1]), "=&v"(km[2]), "=&v"(km[3]), "+v"(a[0]), "+v"(a[1]), "+v"(a[2]), "+v"(a[3])
                   : "v"((unsigned)(size_t)LDS3 + KV_OFF + (s & 1) * 16384 + lv) : "memory");
      const int qi = 8 * jj + (int)((lv >> 4) & 7u) - 4 * (int)(lv >> 8) - (int)((lv >> 3) & 16u);
#pragma unroll
      for (int r = 0; r < 4; ++r) e[r] = (lv & 128u) ? sb[r] : sa[r];
      asm volatile("" : "+v"(e[0]), "+v"(e[1]), "+v"(e[2]), "+v"(e[3]));
#pragma unroll
      for (int r = 0; r < 4; ++r) e[r] = r <= qi ? e[r] : rbf(-1000000000.0f) / SCALE_;
      asm volatile("" : "+v"(e[0]), "+v"(e[1]), "+v"(e[2]), "+v"(e[3]));
      e[4] = e[5] = e[0];
      float m1 = max6(e);
      asm volatile("" : "+v"(m1));
      TRY(7);
      const unsigned ls = (unsigned)(size_t)LDS3, pb = ls + PS_OFF + 1024u;
      unsigned ka = ls + XK_OFF + (lv >> 8) * 128u + (lv & 112u), fa = ls + XK_OFF + 2048u, fv;
      unsigned w1a = pb + (lv >> 8) * 128u + (lv & 112u) + ((lv >> 4) & 8u), w2a = pb + 512u + (lv & 256u) + (lv & 112u) * 2u + ((lv >> 5) & 4u);
      asm volatile("" : "+v"(ka), "+v"(fa), "+v"(w1a), "+v"(w2a));
      reset();
      TR(5);
      __builtin_amdgcn_s_sleep(KSL);
      do {
        asm volatile("ds_read_b32 %4, %6\n ds_read_b128 %0, %5\n ds_read_b128 %1, %5 offset:1024\n ds_read_b128 %2, %5 offset:512\n ds_read_b128 %3, %5 offset:1536\n s_waitcnt lgkmcnt(0)"
                     : "=&v"(km[0]), "=&v"(km[1]), "=&v"(km[2]), "=&v"(km[3]), "=&v"(fv) : "v"(ka), "v"(fa) : "memory");
      } while (__builtin_amdgcn_readfirstlane(fv) != (unsigned)s);
      const f32x4 sm = mfu(km[3], a[3], mfu(km[2], a[2], mfu(km[1], a[1], mfu(km[0], a[0], f32x4{0.f, 0.f, 0.f, 0.f}))));
      const unsigned m2 = __float_as_uint(fmaxf(fmaxf(sm[0], sm[1]), fmaxf(fmaxf(sm[2], sm[3]), m1)));
      e[4] = (lv & 128u) ? sm[2] : sm[0]; e[5] = (lv & 128u) ? sm[3] : sm[1];
      const auto m3 = __builtin_amdgcn_permlane16_swap(m2, m2, false, false);
      float mx;
      asm("v_max_f32 %0, %1, %2" : "=v"(mx) : "v"(m3[0]), "v"(m3[1]));
#pragma unroll
      for (int i = 0; i < 6; ++i) e[i] = __builtin_amdgcn_exp2f((e[i] - mx) * CS);
      asm volatile("ds_write_b64 %0, %1\n ds_write2_b32 %2, %3, %3 offset1:6" :: "v"(w1a), "v"(u32x2{cvt2(e[0], e[1]), cvt2(e[2], e[3])}), "v"(w2a), "v"(cvt2(e[4], e[5])) : "memory");
      TRY(6);
      none3();
    } else {
    if (__builtin_expect(s == 0, 0)) {
      const u32x2 no = launch_no(ws);
      unsigned lv = vo;
      asm volatile("" : "+v"(lv));
      if (!S0D) {
        kv_wait(rs, 1, h, budget);
        __builtin_amdgcn_s_sleep(ESL);
        kv_first(lds, rs, 1, h, w, lv, budget);
      }
      if (w >= 2) rec_wait(rw, no, 63u, 64u + 64u * w);
      const NearMap nm{(unsigned)__builtin_amdgcn_readfirstlane(*(const unsigned*)(lds + KC_OFF + 4))};
      mvo = (lv & 32u) * 2u + ((lv >> 1) & 32u) + (lv & 16u) + ((lv >> 5) & 8u) + nm(0u, ((lv >> 8) & 2u) + ((lv >> 7) & 1u), NB1) - NearMap::blk(NB1);
      r1r = nm(0u, h >> 3, NB1 + w) + (ILV ? w * 1024u : 0u); r3r = nm(0u, h >> 3, NB3 + jj * 8 + (h & 7)); r2b = nm(0u, h >> 3, NB2 + (h & 7) * 2);
      hb = far_bit(nm.k);
      gpo = nm(4u, h >> 3, NB1);
      if (w == 1 && ((wg ^ (wg >> 3)) & 7) == 0 && lv < 64u) anv = nm(0u, lv >> 4, NB1) - NearMap::blk(NB1);
    }
    if (w == 1) *(u32x2*)(lds + KC_OFF + 40) = u32x2{known > s + 2 ? 0x404u : 0u, known > s + 3 ? 1u : 0u};
    if (CAD && w == 1 && __builtin_expect(s <= CA2, 0)) {
      u32x2* st = (u32x2*)(lds + KC_OFF + 56);
      if (s == CA0 || s == CA1) {
        const u32x2 o = *st;
        const unsigned t1 = mt_now();
        unsigned g1 = rt_now();
        g1 += rt_now();
        g1 = (g1 + rt_now()) * 8u;
        const unsigned kq = (unsigned)((float)(t1 - o[0]) * __builtin_amdgcn_rcpf((float)(g1 - o[1])) * 65536.f);
        *st = u32x2{s == CA0 ? t1 : kq, g1};
        if (s == CA1 && (wg & 31) == 0 && vo == 0u) st_sc1_64(rw, OFF_REC + (64u + wg) * 16u + 8u, u32x2{kq, kq ^ launch_no(ws)[1]});
      }
      if (s == CA2) {
        unsigned lv = vo;
        asm volatile("" : "+v"(lv));
        const int kq = (*st)[0];
        const u32x2 r = ld_sc1_64(rw, OFF_REC + 64u * 16u + 8u + (lv & 48u) * 32u);
        unsigned mn = (r[0] ^ r[1]) == launch_no(ws)[1] ? r[0] : (unsigned)kq;
        mn = min(mn, (unsigned)__builtin_amdgcn_update_dpp(0u, mn, 0xb1, 0xf, 0xf, true));
        mn = min(mn, (unsigned)__builtin_amdgcn_update_dpp(0u, mn, 0x4e, 0xf, 0xf, true));
        const int ex = (int)((float)(kq - (int)mn) * __builtin_amdgcn_rcpf((float)mn) * (float)DLYC(CAK) + 0.5f);
        *(unsigned*)(lds + KC_OFF + 48) = DLYC(CAB) + min(max(ex, 0), 8);
      }
    }
    if ((LOCK || SL) && w == 1) {
      unsigned t1, g1;
      clk_pair(t1, g1);
      const int err = (int)(g1 - ge) - ((int)(t1 - te) * kq >> 16);
      const int dk = (unsigned)(err + 400) < 800u ? err * 3 : 0;
      kq += dk; rq -= dk * 57 >> 6;
      if (__builtin_expect(s == 0, 0)) {
        const float r = (float)err * __builtin_amdgcn_rcpf((float)(int)(t1 - te));
        kq = __builtin_amdgcn_readfirstlane((int)(r * 65536.f)); rq = __builtin_amdgcn_readfirstlane((int)(__builtin_amdgcn_rcpf(r) * 65536.f));
      }
      te = t1; ge = g1;
      if (s == 0) { t1g = t3g = g1; *(unsigned*)(lds + TG_OFF) = t1; }
    }
    if (PRE3 && w == 0) prep3(); else none3();
    reset();
    TR(5);
    if (S0D && __builtin_expect(s == 0, 0) && w >= 2) {
      unsigned lv = vo;
      int w2 = w;
      asm volatile("" : "+v"(lv));
      asm volatile("" : "+s"(w2));
      kv_wait(rs, 1, h, budget);
      u32x4 t[8];
      do {
        req4(rs, lv ^ (((lv >> 3) & 32u) + ((lv >> 2) & 128u)), kv_src(1, h, w2, 0u), t);
        req4(rs, lv, kv_src(1, h, w2 & 1, 0u), t + 4);
        asm volatile("s_waitcnt vmcnt(0)" : "+v"(t[0]), "+v"(t[1]), "+v"(t[2]), "+v"(t[3]), "+v"(t[4]), "+v"(t[5]), "+v"(t[6]), "+v"(t[7]));
      } while ((bad4(t) || bad4(t + 4)) && --budget > 0);
#pragma unroll
      for (int i = 0; i < 8; ++i) *(u32x4*)(lds + KV_OFF + 16384 + (i < 4 ? w2 : w2 & 1) * 4096 + (i & 3) * 1024 + lv) = t[i];
      if (lv == 0u) asm volatile("ds_add_u32 %0, %1" :: "v"((unsigned)(size_t)LDS3 + KC_OFF + 32u), "v"(1u) : "memory");
    }
    if (w == 3) {
      unsigned now = 1u;
      if (__builtin_expect(jj == 0 && s >= FS_, 0)) now = rt_now() | 1u;
      if (l == 0 && s > 1) st_sc1_32(rw, cooff(h, hb) + (unsigned)((s - 2) * 16 + jj * 4), s < XS_ + 2 ? 2u : now);
      if (__builtin_expect(s < XS_, 0)) {
        unsigned lv = vo;
        asm volatile("" : "+v"(lv));
        const float* p = (const float*)(lds + PQK_OFF + 2560 + ((lv >> 8) & 1u) * 640u + ((lv >> 1) & 96u) + (lv >> 9) * 16u + ((lv >> 2) & 12u));
        float v[4];
#pragma unroll
        for (int r = 0; r < 4; ++r) v[r] = (p[r * 40] + p[5120 + r * 40]) + (p[10240 + r * 40] + p[15360 + r * 40]);
        st_sc1s_64(rw, (lv & 511u) + (lv >> 9) * 8u, kmoff(s, h, hb) + 2048u + vq * 512u, u32x2{cvt2(v[0], v[1]), cvt2(v[2], v[3])});
      }
      if (__builtin_expect(s == FS_ - 1, 0)) {
        while (__builtin_amdgcn_readfirstlane(ld_sc1_32(rw, OFF_CF + 256u)) < 32u && --budget > 0) __builtin_amdgcn_s_sleep(8);
      }
    } else if (w == 1) {
      if (__builtin_expect(known < min(s + 4 + TC1, NSEG_), 0)) {
        unsigned lv = vo;
        asm volatile("" : "+v"(lv));
        const unsigned c = ld_sc1_32(rw, OFF_CKV + (unsigned)h * 256u + (lv >> 2)), want = lv < XS_ * 16u ? 0x404u : 0x40404u;
        const unsigned long long miss = __builtin_amdgcn_ballot_w64((c & want) != want);
        known = miss ? __builtin_ctzll(miss) : NSEG_;
        *(unsigned*)(lds + KC_OFF + 8) = known > s + 2 ? 0x404u : 0u;
      }
      if (SL && __builtin_expect(s > 0, 1)) {
        const int lt = min(max(late, 0), 20000);
        t1g += dly[KG1] + (unsigned)((lt > 500 ? lt : lt * GAM >> 2) * kq >> 16);
        unsigned t1, g1;
        clk_pair(t1, g1);
        *(unsigned*)(lds + TG_OFF) = local_of(t1g, rq, t1, g1);
      }
      if (LOCK && __builtin_expect(s > 0, 1)) {
        u32x4 gp;
        unsigned lv = vo;
        asm volatile("" : "+v"(lv));
#define MX(r) "v_max_i32_dpp %1, %1, %1 row_ror:" #r " row_mask:0xf bank_mask:0xf\n v_max_i32_dpp %2, %2, %2 row_ror:" #r " row_mask:0xf bank_mask:0xf\n v_max_i32_dpp %3, %3, %3 row_ror:" #r " row_mask:0xf bank_mask:0xf\n"
        asm volatile("s_waitcnt vmcnt(1)\n ds_read_b128 %0, %1\n s_waitcnt lgkmcnt(0)" : "=&v"(gp) : "v"((unsigned)(size_t)LDS3 + GP_OFF + lv) : "memory");
        asm volatile("v_subrev_u32 %1, %4, %1\n v_subrev_u32 %2, %4, %2\n v_subrev_u32 %3, %4, %3\n" MX(8) MX(4) MX(2) MX(1) "s_nop 0"
                     : "+v"(gp) : "v"(gp[0]), "v"(gp[1]), "v"(gp[2]), "s"(ge) : "memory");
        t1g = ge + __builtin_amdgcn_readfirstlane(gp[0]); t3g = ge + __builtin_amdgcn_readfirstlane(gp[2]);
        unsigned t1, g1;
        clk_pair(t1, g1);
        typedef unsigned u32x3 __attribute__((ext_vector_type(3)));
        *(u32x3*)(lds + TG_OFF) = u32x3{local_of(t1g, rq, t1, g1), ge + gp[1], (unsigned)rq};
      }
    }
    }
    int lf3 = 0;
    float gv;
    u32x2 bw;
    unsigned oa;
    asm volatile("" : "=v"(gv), "=v"(bw), "=v"(oa));
    if (__builtin_expect(last, 0)) {
      unsigned lv = vo;
      asm volatile("" : "+v"(lv));
      const unsigned tk = (NSEG_ - 1) * SEG_ + 8 * w + 4 * (h & 1);
      gv = __uint_as_float(__builtin_amdgcn_raw_buffer_load_b32(rw, (lv >> 2) & 12u, OFF_G + tk * 4u, 16));
      bw = __builtin_amdgcn_raw_buffer_load_b64(rw, (lv & 64u) * 1024u + (lv & 48u) + ((lv >> 5) & 8u), bfoff(jj * 32 + (h & ~1), tk, hb), 16);
      sc = w ? (2 + w) * SET_ : 0u;
      const float omg = rbf(1.0f - gv);
#pragma unroll
      for (int i = 0; i < 2; ++i) bw[i] = cvt2(omg * __uint_as_float(bw[i] << 16), omg * __uint_as_float(bw[i] & 0xffff0000u));
      oa = (lv & 48u) * (D_ / 8) + ((lv >> 2) & 16u) + ((lv >> 5) & 8u);
      asm volatile("" : "+v"(gv), "+v"(bw), "+v"(oa));
      pause(dly[DLYL]);
    }
    if (w == 0 || (W7 && w == 3) || last) {
      prep3();
      if (S3) __builtin_amdgcn_s_sleep(S3);
      pause(dl3);
      __builtin_amdgcn_s_setprio(3);
      f32x4 d;
      u32x2 c;
      unsigned t0, t1, r3s = r3r + sc, dlb = dlb0;
      b[0] = oc[0]; b[1] = oc[1];
      asm volatile("" : "=v"(d), "=v"(b[2]), "=v"(b[3]), "=v"(c), "=v"(m), "=v"(t0), "=v"(t1), "=v"(a[0]), "=v"(a[1]), "=v"(a[2]), "=v"(a[3]));
      asm volatile("" : "+s"(r3s), "+s"(dlb), "=s"(thr));
#define RO8 " row_ror:8 row_mask:0xf bank_mask:0xf\n"
#define RQ4 "buffer_load_dwordx4 %0, off, %4, %5 sc1\n buffer_load_dwordx4 %1, off, %4, %5 offset:1024 sc1\n buffer_load_dwordx4 %2, off, %4, %5 offset:2048 sc1\n buffer_load_dwordx4 %3, off, %4, %5 offset:3072 sc1\n"
#define BUD "s_add_i32 %6, %6, -1\n s_cmp_gt_i32 %6, 0\n s_cselect_b32 %7, -2, -1\n v_mov_b64 %8, 0\n v_mov_b64 %9, 0\n"
#define OST "buffer_store_dwordx4 %10, %12, %13, %14 offen sc1\n buffer_store_dwordx4 %11, %12, %13, %14 offen offset:2048 sc1\n"
#define RQO(x) "v"(x[0]), "v"(x[1]), "v"(x[2]), "v"(x[3]), "s"(rt), "s"(r3s), "s"(budget), "s"(thr), "v"(LO2(d)), "v"(HI2(d))
#define BT(d, i, j) "v_bitop3_b32 " d ", %" #i ", %" #j ", %9 bitop3:0x7e\n"
#define SUM(x, n, to) asm volatile(A4 "s_waitcnt vmcnt(3 + " #n ")\n" MF "%4, %8, %0, %4\n" BT("%5", 10, 11) BT("%6", 12, 13) "s_waitcnt vmcnt(2 + " #n ")\n" MF "%4, %8, %1, %4\n" BT("%7", 14, 15) "v_or3_b32 %5, %5, %6, %7\n" \
                     "s_waitcnt vmcnt(1 + " #n ")\n" MF "%4, %8, %2, %4\n" BT("%6", 16, 17) BT("%7", 18, 19) "s_waitcnt vmcnt(" #n ")\n" MF "%4, %8, %3, %4\n" \
                     "v_or3_b32 %5, %5, %6, %7\n" BT("%6", 20, 21) BT("%7", 22, 23) "v_or3_b32 %5, %5, %6, %7\n" BT("%6", 24, 25) "v_or3_b32 %5, %5, %6, -2\n v_cmp_lt_u32 vcc, %26, %5\n s_cbranch_vccnz " to \
                     :: "v"(x[0]), "v"(x[1]), "v"(x[2]), "v"(x[3]), "v"(d), "v"(m), "v"(t0), "v"(t1), "v"(sel), "v"(tv_), \
                        "v"(x[0][0]), "v"(x[0][1]), "v"(x[0][2]), "v"(x[0][3]), "v"(x[1][0]), "v"(x[1][1]), "v"(x[1][2]), "v"(x[1][3]), \
                        "v"(x[2][0]), "v"(x[2][1]), "v"(x[2][2]), "v"(x[2][3]), "v"(x[3][0]), "v"(x[3][1]), "v"(x[3][2]), "v"(x[3][3]), "s"(thr) : "vcc")
#define PUBM(end, ...) asm volatile("v_add_f32_dpp %1, %1, %1" RO8 "v_add_f32_dpp %2, %2, %2" RO8 "v_add_f32_dpp %3, %3, %3" RO8 "v_add_f32_dpp %4, %4, %4" RO8 \
                   "v_cvt_pk_bf16_f32 %1, %1, %2\n v_cvt_pk_bf16_f32 %2, %3, %4\n v_min_u32 %1, -2, %1\n v_min_u32 %2, -2, %2\n buffer_store_dwordx2 %5, %6, %7, %8 offen sc1\n v_mov_b64 %0, %5\n" end \
                   :: "v"(c), "v"(d[0]), "v"(d[1]), "v"(d[2]), "v"(d[3]), "v"(LO2(d)), "v"(mvo), "s"(rs), "s"(pm) __VA_ARGS__ : "memory")
      SCHED();
      if (W7 && w == 3 && !last) {
        asm volatile(RQ4 ".p2align 6\n 1: s_sub_u32 %10, %10, 1\n s_cbranch_scc0 1b\n s_mov_b32 %7, -2\n v_mov_b64 %8, 0\n v_mov_b64 %9, 0" :: RQO(a), "s"(dlb) : "memory", "scc");
        SCHED();
        asm volatile(RQ4 :: RQO(b) : "memory");
        SCHED();
        SUM(a, 4, "7f");
        SCHED();
        PUBM("s_branch 8f\n 7: s_nop 4\n v_mov_b64 %9, 0\n v_mov_b64 %10, 0", , "v"(LO2(d)), "v"(HI2(d)));
        SCHED();
        SUM(b, 0, "8f");
        SCHED();
        PUBM("8: s_waitcnt vmcnt(1)", , "v"(a[0]), "v"(a[1]), "v"(a[2]), "v"(a[3]), "v"(b[0]), "v"(b[1]), "v"(b[2]), "v"(b[3]), "v"(sel), "v"(tv_), "v"(m), "v"(t0), "v"(t1), "s"(thr));
      } else {
      asm volatile(RQ4 OST "s_nop 1\n v_bfrev_b32 %12, 1\n .p2align 6\n 1: s_sub_u32 %15, %15, 1\n s_cbranch_scc0 1b\n s_branch 4f\n"
                   "5:" RQ4 OST BUD :: RQO(a), "v"(b[0]), "v"(b[1]), "v"(ov), "s"(rs), "s"(os), "s"(dlb) : "memory", "scc");
      SCHED();
      SUM(b, 6, "4f");
      SCHED();
      PUBM("s_branch 6f");
      SCHED();
      asm volatile("4:" RQ4 BUD :: RQO(b) : "memory", "scc");
      SCHED();
      SUM(a, 6, "5b");
      SCHED();
      PUBM("6:\n .if " STR(EARLY) " == 0\n s_waitcnt vmcnt(1)\n .endif", , "v"(a[0]), "v"(a[1]), "v"(a[2]), "v"(a[3]), "v"(b[0]), "v"(b[1]), "v"(b[2]), "v"(b[3]), "v"(ov), "s"(os), "s"(r3s), "v"(sel), "v"(tv_), "v"(m), "v"(t0), "v"(t1), "s"(thr));
      }
      asm volatile("" : "+s"(budget));
      asm volatile("" : "+v"(c));
      TR(6);
      T0(3);
      if (__builtin_expect(last, CIN)) {
        if (EARLY) asm volatile("" :: "v"(a[0]), "v"(a[1]), "v"(a[2]), "v"(a[3]), "v"(b[0]), "v"(b[1]), "v"(b[2]), "v"(b[3]));
        u32x2 r;
#pragma unroll
        for (int i = 0; i < 2; ++i) {
          const unsigned x = cvt2(gv * __uint_as_float(c[i] << 16), gv * __uint_as_float(c[i] & 0xffff0000u));
          r[i] = cvt2(__uint_as_float(x << 16) + __uint_as_float(bw[i] << 16), __uint_as_float(x & 0xffff0000u) + __uint_as_float(bw[i] & 0xffff0000u));
        }
        __builtin_amdgcn_raw_buffer_store_b64(r, __builtin_amdgcn_make_buffer_rsrc(out, 0, S_ * D_ * 2, 0x00020000), oa,
                                              (unsigned)(((NSEG_ - 1) * SEG_ + 8 * w + 4 * (h & 1)) * (D_ * 2) + (jj * 32 + (h & ~1)) * 16), OAUX);
        asm volatile("s_waitcnt vmcnt(0)" :: "v"(a[0]), "v"(a[1]), "v"(a[2]), "v"(a[3]), "v"(b[0]), "v"(b[1]), "v"(b[2]), "v"(b[3]) : "memory");
        break;
      }
      __builtin_amdgcn_s_setprio(0);
      TR(7);
      SLP(1);
    }
    else asm volatile("" : "=v"(a[0]), "=v"(a[1]), "=v"(a[2]), "=v"(a[3]), "=v"(b[0]), "=v"(b[1]), "=v"(b[2]), "=v"(b[3]));
    if (w == 2) qs = s + 1 >= XS_ && s + 1 < NSEG_ - 1 ? OFF_Q + tile8(s + 1, h, hb) + (jj >> 1) * HALF_ + (jj & 1) * 128u : 0u;
    asm volatile("" : "+s"(qs));
    if (CAD && __builtin_expect(s == CA2, 0)) dly[DLY1] = __builtin_amdgcn_readfirstlane(*(const unsigned*)(lds + KC_OFF + 48));
    bar();
    TRW();
    if (!EARLY) asm volatile("" : "=v"(a[0]), "=v"(a[1]), "=v"(a[2]), "=v"(a[3]));
    tg ^= sc >> 23;
    sc = sn; sn = sn == 2u * SET_ ? 0u : sn + SET_;
  }
  TRE(4 + w);
}

DEVI void recur_at(char* lds, rsrc_t rw, const u32x4 rs, const u16* __restrict__ Wo, const char* ws, int wg TRARGD DLYARG) {
  DLYI(LOCK ? 0x8486 : 0x2c87);
  const int l = threadIdx.x & 63, w = __builtin_amdgcn_readfirstlane(threadIdx.x >> 6), g = l >> 4, lo = l & 15;
  const int h = wg >> 2, jj = wg & 3;
  int budget = SPIN_CAP;
  unsigned mz = 0u;
  if (ALG) asm volatile("" : "+s"(mz));
  u32x8 w3[6][2];
  const int er = (lo >> 2 & 1) + 4 * (lo >> 3);
  const unsigned vo = (unsigned)l * 16u;
  const unsigned dvo = vo ^ (unsigned)((w >> 1) * ((g & 1) * 32 + (g & 2) * 64));
  u32x2 no;
  {
    u32x4 p4 = u32x4{POISON, POISON, POISON, POISON};
    asm volatile("" : "+v"(p4));
#pragma unroll
    for (int i = 0; i < 2; ++i) st_sc1s(rw, vo + i * 1024u, ((w & 1) ? OFF_VX : OFF_K) + tile8(w >> 1, h, 0u) + (jj >> 1) * HALF_ + (jj & 1) * 2048u, p4);
    st_sc1s(rw, w < 2 ? vo : 0x80000000u, NearMap{0u}(0u, h >> 3, NB2 + (h & 7) * 2 + (w & 1)) + jj * 1024u, p4);
    const u16* gw = Wo + (size_t)(h * DH_ + (l >> 3)) * D_ + jj * 256 + 64 * w + ((l & 7) ^ (l >> 5)) * 8;
#pragma unroll
    for (int i = 0; i < 16; ++i)
      __builtin_amdgcn_global_load_lds((const unsigned*)(gw + (size_t)i * 8 * D_), (unsigned*)(lds + W3_OFF + w * 16384 + i * 1024 + l * 16), 16, 0, NTO);
    no = launch_no(ws);
    drain_vm();
    asm volatile("" :: "v"(p4));
    TP(0);
    __builtin_amdgcn_s_barrier();
    asm volatile("" ::: "memory");
    if (threadIdx.x == 0) st_sc1_64(rw, OFF_REC + (64u + wg) * 16u, no);
#pragma unroll
    for (int T = 0; T < 6; ++T) {
      const int Tc = T < 4 ? T : (T & 1) + (w & 2);
      const char* base = lds + W3_OFF + (T < 4 ? w * 16384 : 0) + (16 * (g & 1) + 8 * (g >> 1) + er) * 128 + (((lo & 3) * 2 + (T & 1)) ^ (lo >> 3)) * 16 + (Tc >> 1) * 8;
#pragma unroll
      for (int q = 0; q < 8; ++q) {
        const u32x2 v = tr_quad(base + (32 * (q >> 1) + 2 * (q & 1)) * 128);
        w3[T][q >> 2][2 * (q & 3)] = v[0];
        w3[T][q >> 2][2 * (q & 3) + 1] = v[1];
      }
      asm volatile("" : "+v"(w3[T][0]), "+v"(w3[T][1]));
    }
    bar();
    TP(1);
    *(u32x4*)(lds + PS_OFF + threadIdx.x * 16) = u32x4{0u, 0u, 0u, 0u};
    *(unsigned*)(lds + PZ_OFF + threadIdx.x * 4) = 0u;
    *(unsigned*)(lds + KC_OFF + 12) = 0u;
    *(unsigned*)(lds + KC_OFF + 32) = 0u;
    rec_wait(rw, no, 3u, 64u + 4u * h, 31u);
    if (w == 1) {
      const NearMap nm{near_kind(rw, no)};
      *(unsigned*)(lds + KC_OFF + 4) = nm.k;
      const unsigned pv = (l & 8) ? 0u : POISON;
      u32x4 p3 = u32x4{pv, pv, pv, pv};
      asm volatile("" : "+v"(p3));
#pragma unroll
      for (int i = 0; i < 12; ++i) st_sc1s(rw, vo + (i & 3) * 1024u, nm(i >> 2, h >> 3, NB3 + jj * 8 + (h & 7)), p3);
#pragma unroll
      for (int i = 0; i < 2; ++i) st_sc1s(rw, vo, nm(1u, h >> 3, NB2 + (h & 7) * 2 + i) + jj * 1024u, p4);
      st_sc1s(rw, l < 32 ? vo : 0x80000000u, nm(1u, h >> 3, NB1 + jj) + (h & 7) * 512u, p4);
      drain_vm();
      asm volatile("" :: "v"(p3), "v"(p4));
      if (l == 0) st_sc1_64(rw, OFF_REC + (192u + wg) * 16u, no);
    } else kv_wait(rs, 0, h, budget);
    TP(2);
    if (!S0A || w != 1) kv_first(lds, rs, 0, h, w, vo, budget);
    if (S0A && w == 0) kv_first(lds, rs, 0, h, 1, vo, budget);
    TP(3);
    if (!S0C) bar();
    TP(4);
  }
  const int idx = lo < 8 ? 0x4444 : 0xeeee;
  const int xg = 2 * (w & 1) + (l >> 5), xm = w >> 1;
  const f32x2* pq = (const f32x2*)((const float*)(lds + PQK_OFF) + xm * 320 + ((l >> 2) & 7) * 40 + 8 * xg + 2 * (l & 3));
  const unsigned xvo = (unsigned)((2 * (jj & 1) + xm) * 1024 + xg * 256 + (jj >> 1) * 128 + (l & 31) * 4), kvo = (unsigned)(xg * 128 + (l & 31) * 4);
  unsigned r2b;
  unsigned sc = 0u, sn = SET_;
  unsigned tg = h & 1;
  unsigned dlyd = LOCK || SL ? dly[DLYD] * 28 : dly[DLY1] + dly[DLYD];
  const unsigned dlyv = (w == 0 ? dly[DLY2] : dly[DLYV] + (w == 1 ? dly[DLYW] : 0u)) * (LOCK && L2 ? 28 : 1), kgv = !LOCK || w == 0 ? 0u : dly[KGV] + (w == 1 ? dly[DLYW] * 28 : 0u);
  unsigned t2 = LOCK ? mt_now() : 0u;
  unsigned xo = vo, xt = POISON - 1u;
  const unsigned vo2 = vo * 2u;
  const unsigned tch = w == TCW && l < 32 ? lo * 128u + (l >> 4) * (OFF_VX - OFF_K) + (jj & 1) * 2048u + (jj >> 1) * HALF_ : 0x80000000u;
  unsigned tcs = 0x80000000u;
  f32x4 ma = zero4(), mb = zero4();
  const int vr = (lo & 3) * 16 + ((8 * (g >> 1) + er) ^ ((lo & 1) * 2 + (lo & 2) * 4));
  unsigned vb = KV_OFF + 8192 + (g & 1) * 1024 + vr * 16, kb = KV_OFF + l * 16;
  const unsigned ls = (unsigned)(size_t)LDS3;
  const unsigned pw1 = ls + PS_OFF + w * 1024 + (g * 8 + (lo & 7)) * 16 + (lo & 8);
  const unsigned pw2 = ls + (g >= 2 ? PS_OFF + w * 1024 + 512 : UM_OFF + 2048) + ((g & 1) * 8 + (lo & 7)) * 32 + (lo >> 3) * 4;
  const unsigned pr0 = ls + PS_OFF + (g * 8 + (lo & 7)) * 16, pr3 = ls + PS_OFF + 512 + ((g & 1) * 8 + (lo & 7)) * 32 + (lo >> 3) * 16;
  const unsigned pr1 = lo < 8 ? pr0 : ls + PZ_OFF, pr2 = lo < 8 ? ls + PZ_OFF : pr0;
  const unsigned oa1 = lo < 8 ? pr0 + 1024u : pr1, oa2 = lo < 8 ? pr2 : pr0 + 1024u, oa3 = g < 2 ? pr3 + 1024u : ls + PZ_OFF;
  const unsigned oam = ls + XV_OFF + (lo & 3) * 128 + ((l >> 2) & 7) * 16;
  const unsigned uma = ls + UM_OFF + max(w - 1, 0) * 1024 + vo;
  unsigned long long w0m;
  asm("s_cmp_eq_u32 %1, 0\n s_cselect_b64 %0, -1, 0" : "=s"(w0m) : "s"(w) : "scc");
  unsigned long long xwm;
  asm("s_cmp_eq_u32 %1, 1\n s_cselect_b64 %0, -1, 0" : "=s"(xwm) : "s"(w) : "scc");
  xwm |= w0m & 0xff00ff00ff00ff00ull;
  const unsigned xw = ls + (w ? XV_OFF + vo : XK_OFF + g * 128 + (lo & 7) * 16);
  const float ninf = g < 2 ? -INFINITY : 0.f;
  u32x4 one = u32x4{0x3f803f80u, 0x3f803f80u, 0x3f803f80u, 0x3f803f80u}, half = u32x4{0x3f003f00u, 0x3f003f00u, 0x3f003f00u, 0x3f003f00u};
  f32x4 nin4 = f32x4{ninf, ninf, ninf, ninf};
  asm volatile("" : "+v"(one), "+v"(half), "+v"(nin4));
  u32x4 kx[8], us[4];
  auto lo4 = [](u32x8 b) { return u32x4{b[0], b[1], b[2], b[3]}; };
  auto hi4 = [](u32x8 b) { return u32x4{b[4], b[5], b[6], b[7]}; };
  u32x4 va[2][4];
  auto useg_a = [&]() {
#pragma unroll
    for (int i = 0; i < 8; ++i) {
      const u32x2 x0 = tr_quad(lds + vb + (i & 3) * 2048 + (i >> 2) * 8), x1 = tr_quad(lds + (vb ^ 32u) + (i & 3) * 2048 + (i >> 2) * 8);
      va[i >> 2][i & 3] = u32x4{x0[0], x0[1], x1[0], x1[1]};
    }
#pragma unroll
    for (int i = 0; i < 8; ++i) kx[i] = *(const u32x4*)(lds + kb + i * 1024);
    vb ^= 16384u; kb ^= 16384u;
  };
  auto useg_b = [&](int T0, int T1) {
#pragma unroll
    for (int T = 0; T < 4; ++T) {
      if (T < T0 || T >= T1) continue;
      f32x4 d[2];
#pragma unroll
      for (int ks = 0; ks < 4; ++ks)
#pragma unroll
        for (int n = 0; n < 2; ++n) d[n] = mfu(va[n][ks], (ks & 1) ? hi4(w3[T][ks >> 1]) : lo4(w3[T][ks >> 1]), ks ? d[n] : f32x4{0.f, 0.f, 0.f, 0.f});
      us[T] = cvt8(d[0], d[1]);
      asm volatile("" : "+v"(us[T]));
    }
    asm volatile("" : "+v"(kx[0]), "+v"(kx[1]), "+v"(kx[2]), "+v"(kx[3]), "+v"(kx[4]), "+v"(kx[5]), "+v"(kx[6]), "+v"(kx[7]));
  };
#define USP 4
  u32x4 rt = rs_tid(rs);
  if (TID) asm volatile("" : "+s"(rt));
  const unsigned qsu = jj < 2 ? __builtin_bit_cast(unsigned, CS) : 0x3f800000u;
  u32x2 qs2 = u32x2{qsu, qsu};
  if (NMX) asm volatile("" : "+s"(qs2));
  unsigned mlo = lo < 8 ? ~0u : 0u, mhi = ~mlo;
  if (PAN) asm volatile("" : "+v"(mlo), "+v"(mhi));
  unsigned c, cnt = 256u, zr = 0u;
  asm volatile("" : "+v"(zr));
  auto pub = [&]() {
    SCHED();
    if (PBA) asm volatile(".p2align %0\n .rept %1\n s_nop 0\n .endr" :: "n"(PBA), "n"(PBN));
    unsigned so1 = r2b + sc, so2 = r2b + BLK_ + (2 * (~jj & 1) + (jj >> 1)) * 512u + sc;
    f32x2 t0, t1, t2, t3;
    asm volatile("" : "+s"(so1), "+s"(so2));
    unsigned t4;
    asm volatile("" : "=v"(t0), "=v"(t1), "=v"(t2), "=v"(t3), "=v"(c), "=v"(t4));
#define PSUM(o0, o1, o2, o3, d, mul) "ds_read_b64 %0, %7 offset:" #o0 "\n ds_read_b64 %1, %7 offset:" #o1 "\n ds_read_b64 %2, %7 offset:" #o2 "\n ds_read_b64 %3, %7 offset:" #o3 "\n" \
      "s_waitcnt lgkmcnt(2)\n v_pk_add_f32 %0, %0, %1\n s_waitcnt lgkmcnt(0)\n v_pk_add_f32 %2, %2, %3\n v_pk_add_f32 %0, %0, %2\n" mul CV d ", %4, %5\n .if " STR(NOMIN) " == 0\n v_min_u32 " d ", -2, " d "\n .endif\n"
    asm volatile("s_waitcnt lgkmcnt(0)\n s_barrier\n" PSUM(0, 20480, 40960, 61440, "%6", ".if " STR(NMX) "\n v_pk_mul_f32 %0, %14, %0\n .endif\n") "buffer_store_dword %6, %8, %10, %11 offen sc1\n s_cmp_eq_u32 %13, 0\n s_cbranch_scc0 2f\n s_barrier\n s_branch 3f\n"
                 "2: s_barrier\n"
                 PSUM(1280, 21760, 42240, 62720, "%4", "")
                 "buffer_store_dword %4, %9, %10, %12 offen sc1\n 3:"
                 :: "v"(t0), "v"(t1), "v"(t2), "v"(t3), "v"(t0[0]), "v"(t0[1]), "v"(c), "v"((unsigned)(size_t)pq), "v"(xvo), "v"(kvo), "s"(rs), "s"(so1), "s"(so2), "s"(xm), "s"(qs2), "v"(t4), "v"(zr), "s"(cnt) : "memory", "scc", "vcc");
    cnt += 256u;
    asm volatile("" : "+v"(c));
    SCHED();
  };
  r2b = NearMap{0u}(0u, h >> 3, NB2 + (h & 7) * 2);
  if (S0C) { pub(); TP(7); }
  const NearMap nm{(unsigned)__builtin_amdgcn_readfirstlane(*(const unsigned*)(lds + KC_OFF + 4))};
  const unsigned hb = far_bit(nm.k);
  const unsigned r3o = nm(0u, w, NB3 + jj * 8 + 2 * g + ((lo >> 2) & 1)) + h * 128u + (unsigned)((lo >> 3) * 4 + (lo & 3)) * 16u;
  asm volatile("" ::: "memory");
  *(unsigned*)(lds + CNT_OFF) = 0u;
  useg_a(); useg_b(0, 4);
  if (w == 1) kb = KV_OFF + 8192 + (l ^ ((g & 1) * 2 + (g & 2) * 4)) * 16;
  TP(5);
  if (!S0C) { pub(); TP(7); }
  unsigned dlc = S0C ? 0u : dlyv;
  TRD;
  for (int s = 0;;) {
    const bool last = s == NSEG_ - 1;
    const unsigned r2c = r2b + sc;
    unsigned m;
    f32x4 d[6], sa, sb, sm;
    u32x4 x[4];
    asm volatile("" : "=v"(d[0]), "=v"(d[1]), "=v"(d[2]), "=v"(d[3]), "=v"(x[0]), "=v"(x[1]), "=v"(x[2]), "=v"(x[3]), "=v"(sa), "=v"(sb), "=v"(sm));
    unsigned dox = w == 0 || last ? 2u : 1u, lim = last ? 5u * SET_ : sc, rlim = last || !NMX ? 0xffffffffu : 0x28000000u;
    asm volatile("" : "+s"(dox), "+s"(lim), "+s"(rlim));
    int lf2 = 0;
    if (LOCK && L2) lf2 = until(t2, dlyv + (s > 1 ? 0u : 84u)) - (int)dlyv; else pause(dlc);
    dlc = dlyv;
#define SH8 " row_shr:8 row_mask:0xf bank_mask:0xc\n"
#define SOFT(e4, e5, a1, a2, cs, lo2) "v_max3_f32 %4, %0, %1, %2\n v_max3_f32 %4, %4, " e4 ", " e5 "\n v_max_f32 %4, %4, %3\n s_nop 1\n" \
                   "v_max_f32_dpp %5, %4, %4" RO8 "v_max_f32_dpp %6, %4, %4" RO8 "s_nop 1\n" \
                   "v_permlane16_swap_b32 %5, %6\n v_max_f32 %4, %5, %6\n v_max_f32 %5, %5, %6\n s_nop 1\n" \
                   "v_permlane32_swap_b32 %4, %5\n v_max_f32 %4, %4, %5\n" \
                   "v_sub_f32 %0, %0, %4\n v_sub_f32 %1, %1, %4\n v_sub_f32 %2, %2, %4\n v_sub_f32 %3, %3, %4\n v_sub_f32 " e4 ", " e4 ", %4\n v_sub_f32 " e5 ", " e5 ", %4\n" \
                   "v_mul_f32 %0, " cs ", %0\n v_mul_f32 %1, " cs ", %1\n v_mul_f32 %2, " cs ", %2\n v_exp_f32 %0, %0\n v_exp_f32 %1, %1\n v_mul_f32 %3, " cs ", %3\n" \
                   "v_mul_f32 " e4 ", " cs ", " e4 "\n v_exp_f32 %2, %2\n v_exp_f32 %3, %3\n v_mul_f32 " e5 ", " cs ", " e5 "\n v_cvt_pk_bf16_f32 %0, %0, %1\n v_exp_f32 " e4 ", " e4 "\n" \
                   "v_exp_f32 " e5 ", " e5 "\n v_cvt_pk_bf16_f32 %1, %2, %3\n ds_write_b64 " a1 ", " lo2 "\n v_cvt_pk_bf16_f32 " e4 ", " e4 ", " e5 "\n ds_write2_b32 " a2 ", " e4 ", " e4 " offset1:6"
    if (dox == 2u) {
      TR(0);
      do {
        asm volatile("s_cmp_eq_u32 %11, %12\n s_cbranch_scc0 6f\n buffer_load_dwordx4 %0, off, %4, %5 sc1\n buffer_load_dwordx4 %1, off, %4, %5 offset:1024 sc1\n"
                     "buffer_load_dwordx4 %2, off, %4, %5 offset:2048 sc1\n buffer_load_dwordx4 %3, off, %4, %5 offset:3072 sc1\n s_branch 7f\n"
                     "6: buffer_load_dwordx4 %0, %6, %7, %5 offen sc1\n buffer_load_dwordx4 %1, %6, %7, %5 offen offset:1024 sc1\n"
                     "buffer_load_dwordx4 %2, %6, %7, %5 offen offset:2048 sc1\n buffer_load_dwordx4 %3, %6, %7, %5 offen offset:3072 sc1\n"
                     "s_sleep %13\n buffer_load_dword %8, %9, %7, %10 offen sc1\n 7:"
                     : "=&v"(x[0]), "=&v"(x[1]), "=&v"(x[2]), "=&v"(x[3])
                     : "s"(rt), "s"(r2c), "v"(xo), "s"(rs), "v"(d[0][0]), "v"(vo2), "s"(r2c + BLK_), "s"(lim), "s"(sc), "n"(LVS) : "memory", "scc");
        if (--budget <= 0) xt = POISON;
        SCHED();
#define MX3(a, b, c) "v_max3_u32 %0, %" #a ", %" #b ", %" #c "\n"
        asm volatile(A4 "s_waitcnt vmcnt(3)\n" MF "%2, %5, %1, %7\n" MX3(10, 11, 12) MF "%3, %6, %1, %8\n" MX3(13, 13, 0) MF "%4, %1, %1, %9"
                     : "=&v"(m), "+v"(x[0]), "=&v"(sa), "=&v"(sb), "=&v"(sm) : "v"(kx[0]), "v"(kx[4]), "v"(ma), "v"(mb), "v"(nin4), "v"(x[0][0]), "v"(x[0][1]), "v"(x[0][2]), "v"(x[0][3]));
        SCHED();
        asm volatile("s_waitcnt vmcnt(0)\n" MF "%4, %7, %1, %4\n" MX3(13, 14, 0) MF "%5, %10, %1, %5\n" MX3(15, 16, 0) MF "%6, %1, %1, %6\n" MX3(17, 18, 0)
                     MF "%4, %8, %2, %4\n" MX3(19, 20, 0) MF "%5, %11, %2, %5\n" MX3(21, 22, 0) MF "%6, %2, %2, %6\n" MX3(23, 24, 0)
                     MF "%4, %9, %3, %4\n" MF "%5, %12, %3, %5\n" MF "%6, %3, %3, %6"
                     : "+v"(m), "+v"(x[1]), "+v"(x[2]), "+v"(x[3]), "+v"(sa), "+v"(sb), "+v"(sm)
                     : "v"(kx[1]), "v"(kx[2]), "v"(kx[3]), "v"(kx[5]), "v"(kx[6]), "v"(kx[7]), "v"(x[1][0]), "v"(x[1][1]), "v"(x[1][2]), "v"(x[1][3]),
                       "v"(x[2][0]), "v"(x[2][1]), "v"(x[2][2]), "v"(x[2][3]), "v"(x[3][0]), "v"(x[3][1]), "v"(x[3][2]), "v"(x[3][3]));
        SCHED();
      } while (over(m, xt));
      TR(3);
#define MOV6 A8 "v_mov_b32_dpp %0, %4" SH8 "v_mov_b32_dpp %1, %5" SH8 "v_mov_b32_dpp %2, %6" SH8 "v_mov_b32_dpp %8, %10" SH8 "v_mov_b32_dpp %9, %11" SH8 "v_mov_b32_dpp %3, %7" SH8
#define SC12 "v"(sa[0]), "v"(sa[1]), "v"(sa[2]), "v"(sa[3]), "v"(sb[0]), "v"(sb[1]), "v"(sb[2]), "v"(sb[3]), "v"(sm[0]), "v"(sm[1]), "v"(sm[2]), "v"(sm[3])
      asm volatile("s_cmp_eq_u32 %17, -1\n s_cbranch_scc1 3f\n s_nop " STR(XIL * 2) "\n" MOV6
                   "v_exp_f32 %4, %0\n v_exp_f32 %5, %1\n v_exp_f32 %6, %2\n v_exp_f32 %7, %3\n" CV "%4, %4, %5\n v_exp_f32 %10, %8\n v_exp_f32 %11, %9\n" CV "%5, %6, %7\n"
                   "ds_write_b64 %12, %14\n" CV "%10, %10, %11\n ds_write2_b32 %13, %10, %10 offset1:6\n 9:\n"
                   ".subsection 1\n 3: s_nop 2\n" MOV6 SOFT("%8", "%9", "%12", "%13", "%16", "%15") "\n s_branch 9b\n .subsection 2\n code_to: .fill 64, 4, 0xbf800000\n .subsection 0"
                   :: SC12, "v"(pw1), "v"(pw2), "v"(LO2(sb)), "v"(LO2(sa)), "s"(CS), "s"(rlim) : "memory", "scc");
    }
    if (w != 0) {
      u32x4* vv = x;
      unsigned thr, r2v = r2c + BLK_;
      asm volatile("" : "=s"(thr), "+s"(r2v));
      TR(0);
      asm volatile("5:\n .if " STR(TID) "\n buffer_load_dwordx4 %0, off, %6, %7 sc1\n buffer_load_dwordx4 %1, off, %6, %7 offset:1024 sc1\n .else\n"
                   "buffer_load_dwordx4 %0, %5, %6, %7 offen sc1\n buffer_load_dwordx4 %1, %5, %6, %7 offen offset:1024 sc1\n .endif\n s_add_i32 %8, %8, -1\n s_cmp_gt_i32 %8, 0\n s_cselect_b32 %9, -2, -1"
                   : "=&v"(vv[0]), "=&v"(vv[1]), "=&v"(d[4]), "=&v"(d[5]), "=&v"(m) : "v"(vo), "s"(TID ? rt : rs), "s"(r2v), "s"(budget), "s"(thr) : "memory", "scc");
      if (TCH) asm volatile("s_mov_b32 m0, %0\n s_nop 0\n buffer_load_dword %1, %2, %3 offen sc1 lds" :: "n"(UM_OFF + 2560), "v"(tch), "s"(rs), "s"(tcs) : "memory");
      SCHED();
      asm volatile("s_nop 3\n v_mov_b64 %0, 0\n v_mov_b64 %1, 0\n v_mov_b64 %2, 0\n v_mov_b64 %3, 0\n v_mov_b64 %4, 0\n v_mov_b64 %5, 0\n"
                   "v_mov_b64 %6, 0\n v_mov_b64 %7, 0\n v_mov_b64 %8, 0\n v_mov_b64 %9, 0\n v_mov_b64 %10, 0\n v_mov_b64 %11, 0"
                   :: "v"(LO2(d[0])), "v"(HI2(d[0])), "v"(LO2(d[1])), "v"(HI2(d[1])), "v"(LO2(d[2])), "v"(HI2(d[2])),
                      "v"(LO2(d[3])), "v"(HI2(d[3])), "v"(LO2(d[4])), "v"(HI2(d[4])), "v"(LO2(d[5])), "v"(HI2(d[5])));
      SCHED();
      __builtin_amdgcn_s_setprio(0);
      SCHED();
      asm volatile(A4 "s_waitcnt vmcnt(1 + " STR(TC1) ")\n" SM "%5, %0, %8, %15\n v_max3_u32 %7, %16, %17, %18\n" SM "%6, %0, %9, %15\n s_nop 0\n s_waitcnt vmcnt(" STR(TC1) ")\n"
                   SM "%5, %1, %10, %15\n v_max3_u32 %7, %19, %20, %7\n" SM "%6, %1, %11, %15\n v_max3_u32 %7, %21, %22, %7\n"
                   SM "%2, %0, %12, %15\n v_max3_u32 %7, %23, %23, %7\n" SM "%3, %0, %13, %15\n v_cmp_lt_u32 vcc, %24, %7\n" SM "%4, %0, %14, %15\n s_cbranch_vccnz 5b"
                   :: "v"(vv[0]), "v"(vv[1]), "v"(d[0]), "v"(d[1]), "v"(d[2]), "v"(d[4]), "v"(d[5]), "v"(m),
                      "v"(w3[4][0]), "v"(w3[5][0]), "v"(w3[4][1]), "v"(w3[5][1]), "v"(w3[0][0]), "v"(w3[1][0]), "v"(w3[2][0]), "v"(idx),
                      "v"(vv[0][0]), "v"(vv[0][1]), "v"(vv[0][2]), "v"(vv[0][3]), "v"(vv[1][0]), "v"(vv[1][1]), "v"(vv[1][2]), "v"(vv[1][3]), "s"(thr), "s"(r2v), "v"(d[3]) : "vcc");
      SCHED();
      asm volatile(CV "%8, %8, %9\n" CV "%9, %10, %11\n" CV "%10, %12, %13\n" CV "%11, %14, %15\n ds_write_b128 %6, %7\n"
                   SM "%5, %0, %16, %21\n" SM "%2, %1, %17, %21\n" SM "%3, %1, %18, %21\n" SM "%4, %1, %19, %21\n" SM "%5, %1, %20, %21"
                   :: "v"(vv[0]), "v"(vv[1]), "v"(d[0]), "v"(d[1]), "v"(d[2]), "v"(d[3]), "v"(uma), "v"(d[4]),
                      "v"(d[4][0]), "v"(d[4][1]), "v"(d[4][2]), "v"(d[4][3]), "v"(d[5][0]), "v"(d[5][1]), "v"(d[5][2]), "v"(d[5][3]),
                      "v"(w3[3][0]), "v"(w3[0][1]), "v"(w3[1][1]), "v"(w3[2][1]), "v"(w3[3][1]), "v"(idx) : "memory");
      SCHED();
    }
    TR(4);
    T0(8);
    f32x4 su, c0, c1;
    u32x4 p0, p1, p2, p3;
    SCHED();
    asm volatile("s_waitcnt lgkmcnt(0)" : "=v"(su), "=v"(c0), "=v"(c1), "=v"(p0), "=v"(p1), "=v"(p2), "=v"(p3) :: "memory");
    __builtin_amdgcn_s_barrier();
    TR(5);
#define RDP "ds_read_b128 %0, %4\n ds_read_b128 %1, %5\n ds_read_b128 %2, %6\n ds_read_b128 %3, %7\n"
    asm volatile("ds_read_b128 %0, %2\n ds_read_b128 %1, %3\n s_nop 0\n s_nop 0\n" CV "%4, %4, %5\n" CV "%5, %6, %7\n" CV "%6, %8, %9\n" CV "%7, %10, %11\n" CV "%12, %12, %13\n" CV "%13, %14, %15\n" CV "%14, %16, %17\n" CV "%15, %18, %19\n"
                 "s_mov_b64 exec, %23\n ds_read_b128 %20, %22\n ds_read_b128 %21, %22 offset:1024\n s_mov_b64 exec, -1"
                 :: "v"(p0), "v"(p3), "v"(pr0), "v"(pr3),
                   "v"(d[0][0]), "v"(d[0][1]), "v"(d[0][2]), "v"(d[0][3]), "v"(d[1][0]), "v"(d[1][1]), "v"(d[1][2]), "v"(d[1][3]),
                   "v"(d[2][0]), "v"(d[2][1]), "v"(d[2][2]), "v"(d[2][3]), "v"(d[3][0]), "v"(d[3][1]), "v"(d[3][2]), "v"(d[3][3]), "v"(d[0]), "v"(d[2]), "v"(uma), "s"(w0m) : "memory");
    SCHED();
#define AN(d, s, m) "v_and_b32 %" #d ", %" #m ", %" #s "\n"
    asm volatile(A8 "s_waitcnt lgkmcnt(2)\n" AN(17, 13, 25) AN(18, 14, 25) AN(19, 15, 25) MF "%0, %8, %3, 0\n" AN(20, 16, 25) AN(21, 13, 26) MF "%0, %9, %6, %0\n"
                 AN(22, 14, 26) AN(23, 15, 26) MF "%1, %10, %4, 0\n" AN(24, 16, 26) "s_waitcnt lgkmcnt(1)\n" MF "%2, %12, %4, 0\n"
                 MF "%1, %11, %5, %1\n" MF "%1, %7, %6, %1\n" MF "%2, %27, %5, %2\n"
                 :: "v"(su), "v"(c0), "v"(c1), "v"(p0), "v"(p1), "v"(p2), "v"(p3), "v"(d[0]), "v"(one), "v"(half), "v"(us[0]), "v"(us[1]), "v"(us[2]),
                    "v"(p0[0]), "v"(p0[1]), "v"(p0[2]), "v"(p0[3]), "v"(p1[0]), "v"(p1[1]), "v"(p1[2]), "v"(p1[3]), "v"(p2[0]), "v"(p2[1]), "v"(p2[2]), "v"(p2[3]),
                    "v"(mlo), "v"(mhi), "v"(us[3]) : "memory");
    SCHED();
    unsigned so;
#define TAIL "v_pk_mul_f32 %2, %2, %17 op_sel_hi:[1,0]\n v_pk_mul_f32 %3, %3, %17 op_sel_hi:[1,0]\n v_cvt_pk_bf16_f32 %6, %6, %7\n v_cvt_pk_bf16_f32 %7, %8, %9\n" \
             "v_pk_mul_f32 %4, %4, %17 op_sel_hi:[1,0]\n v_pk_mul_f32 %5, %5, %17 op_sel_hi:[1,0]\n v_and_or_b32 %6, %6, -2, %14\n v_cvt_pk_bf16_f32 %8, %10, %11\n" \
             "v_cvt_pk_bf16_f32 %9, %12, %13\n v_and_or_b32 %7, %7, -2, %14\n v_and_or_b32 %8, %8, -2, %14\n v_and_or_b32 %9, %9, -2, %14\n" \
             "buffer_store_dwordx4 %1, %15, %16, %0 offen sc1\n s_nop 1"
#define TOPS "v"(c0), "v"(LO2(c0)), "v"(HI2(c0)), "v"(LO2(c1)), "v"(HI2(c1)), "v"(c0[0]), "v"(c0[1]), "v"(c0[2]), "v"(c0[3]), "v"(c1[0]), "v"(c1[1]), "v"(c1[2]), "v"(c1[3]), \
             "s"(tg), "v"(r3o), "s"(rs), "v"(LO2(su))
    asm volatile(".if " STR(PAN) "\n s_waitcnt lgkmcnt(0)\n v_rcp_f32 %21, %21\n .else\n v_rcp_f32_e64 %21, %21\n .endif\n" MF "%18, %19, %20, %18\n"
                 "v_add_u32 %22, 0xd4800000, %21\n .if " STR(PAN) "\n v_cmp_lt_u32 vcc, %23, %22\n .else\n v_cmp_lt_u32_e64 vcc, %23, %22\n .endif\n s_cmp_lg_u64 vcc, 0\n s_cselect_b32 %0, 0x80000000, %24\n .if " STR(PAN) " == 0\n s_nop 0\n .endif\n" TAIL
                 : "=&s"(so) : TOPS, "v"(c1), "v"(d[2]), "v"(p3), "v"(su[0]), "v"(su[2]), "s"(rlim), "s"(sc) : "memory", "vcc", "scc");
    SCHED();
    asm volatile("" : "+s"(budget));
    if (__builtin_expect(so != lim, CIN)) {
      if (so == 0x80000000u) {
        if (w == 0) {
          asm volatile(SOFT("%7", "%8", "%9", "%10", "1.0", "%11") "\n ds_write_b32 %12, %13"
                       :: "v"(sa[0]), "v"(sa[1]), "v"(sa[2]), "v"(sa[3]), "v"(sb[0]), "v"(sb[1]), "v"(sb[2]), "v"(sm[0]), "v"(sm[1]),
                          "v"(pw1 + 2048u), "v"(g >= 2 ? pw2 + 2048u : pw2), "v"(LO2(sa)), "v"(ls + KC_OFF + 12u), "v"(s + 1) : "memory");
        }
        unsigned fv;
        do asm volatile("ds_read_b32 %0, %1\n s_waitcnt lgkmcnt(0)" : "=&v"(fv) : "v"(ls + KC_OFF + 12u) : "memory");
        while (__builtin_amdgcn_readfirstlane(fv) != (unsigned)(s + 1) && --budget > 0);
      }
      asm volatile("" : "+v"(d[0]), "+v"(d[2]));
      unsigned lv = vo;
      asm volatile("" : "+v"(lv));
      const unsigned z1 = (lv & 128u) ? PZ_OFF : pr0 - ls, z2 = (lv & 128u) ? pr0 - ls : PZ_OFF, g1 = (lv & 128u) ? 0u : 1024u;
      u32x4 r[3];
#pragma unroll
      for (int j = 1; j < 4; ++j) {
        const u32x4 q0 = *(const u32x4*)(lds + (pr0 - ls) + j * 1024), q3 = *(const u32x4*)(lds + (pr3 - ls) + j * 1024);
        const u32x4 q1 = *(const u32x4*)(lds + z1 + j * g1), q2 = *(const u32x4*)(lds + z2 + j * (1024u - g1));
        const f32x4 z = f32x4{0.f, 0.f, 0.f, 0.f};
        const float ir = __builtin_amdgcn_rcpf(mfu(half, q3, mfu(one, q0, z))[0]);
        const f32x4 e0 = mfu(__builtin_bit_cast(u32x4, d[0]), q3, mfu(us[1], q2, mfu(us[0], q1, z))) * ir, e1 = mfu(__builtin_bit_cast(u32x4, d[2]), q3, mfu(us[3], q2, mfu(us[2], q1, z))) * ir;
        r[j - 1] = (cvt8(e0, e1) & ~1u) | tg;
        asm volatile("" : "+v"(r[j - 1]));
        st_sc1s(rw, last || j == 2 ? r3o : 0x80000000u, last ? (2 + j) * SET_ : sc, r[j - 1]);
      }
      asm volatile("s_nop 1" :: "v"(r[0]), "v"(r[1]), "v"(r[2]));
    }
    TR(6);
    T0(9);
    SLP(0);
    asm volatile("" : "=v"(kx[0]), "=v"(kx[1]), "=v"(kx[2]), "=v"(kx[3]), "=v"(kx[4]), "=v"(kx[5]), "=v"(kx[6]), "=v"(kx[7]));
    if (last) break;
    asm volatile("s_mov_b64 exec, %5\n ds_write_b128 %4, %0\n ds_write_b128 %4, %1 offset:1024\n s_and_b64 exec, exec, %6\n"
                 "ds_write_b128 %4, %2 offset:512\n ds_write_b128 %4, %3 offset:1536\n ds_write_b32 %7, %8\n s_mov_b64 exec, -1"
                 :: "v"(x[0]), "v"(x[1]), "v"(x[2]), "v"(x[3]), "v"(xw), "s"(xwm), "s"(w0m), "v"(ls + XK_OFF + 2048u), "v"(s) : "memory", "scc");
    if (__builtin_expect(s < XS_, 0))
      if (jj & 2) st_sc1s_32(rw, kvo, kmoff(s, h, hb) + (2 * (jj & 1) + xm) * 512u, c);
    if (S0D && __builtin_expect(s == 0, 0)) {
      unsigned fv;
      do asm volatile("ds_read_b32 %0, %1\n s_waitcnt lgkmcnt(0)" : "=&v"(fv) : "v"(ls + KC_OFF + 32u) : "memory");
      while (__builtin_amdgcn_readfirstlane(fv) != 2u);
    }
    const u32x2 kc2 = *(const u32x2*)(lds + KC_OFF + 40);
    const unsigned kc = kc2[0];
    useg_a(); useg_b(0, USP);
    if (USP < 4) asm volatile("" : "+v"(va[0][0]), "+v"(va[0][1]), "+v"(va[0][2]), "+v"(va[0][3]), "+v"(va[1][0]), "+v"(va[1][1]), "+v"(va[1][2]), "+v"(va[1][3]));
    TRX(0);
    const bool have = __builtin_amdgcn_readfirstlane(kc) == 0x404u;
    unsigned ks = kv_src(s + 2, h, w, hb), kd = kv_dst(s, w), kv = have && s < NSEG_ - 2 ? dvo : 0x80000000u;
    if (TCH) {
      tcs = __builtin_amdgcn_readfirstlane(kc2[1]) && s < NSEG_ - 3 ? OFF_K + tile8(s + 3, h, hb) : 0x80000000u;
      asm volatile("" : "+s"(tcs));
    }
    asm volatile("" : "+s"(ks), "+s"(kd), "+v"(kv));
    if (CAD && __builtin_expect(s == CA2, 0)) dlyd = dly[DLYD] + __builtin_amdgcn_readfirstlane(*(const unsigned*)(lds + KC_OFF + 48));
    if (D4) pause(dly[DLY4]);
    bar();
#define KVE 2
    if (w == KVE || KVE == 8) kv_dma(rs, kv, ks, kd);
    r2b = nm(0u, h >> 3, NB2 + (h & 7) * 2);
    tg ^= sc >> 23;
    sc = sn; sn = sn == 2u * SET_ ? 0u : sn + SET_;
    TRW();
    useg_b(USP, 4);
    if (w == 1 && s >= XS_) {
      u32x4 p1, p2, p3;
      u32x2 vm[8];
      asm volatile("ds_read_b128 %0, %11\n ds_read_b128 %1, %12\n ds_read_b128 %2, %13\n"
                   "ds_read_b64_tr_b16 %3, %14\n ds_read_b64_tr_b16 %4, %14 offset:512\n ds_read_b64_tr_b16 %5, %14 offset:8\n ds_read_b64_tr_b16 %6, %14 offset:520\n"
                   "ds_read_b64_tr_b16 %7, %14 offset:1024\n ds_read_b64_tr_b16 %8, %14 offset:1536\n ds_read_b64_tr_b16 %9, %14 offset:1032\n ds_read_b64_tr_b16 %10, %14 offset:1544\n"
                   "s_waitcnt lgkmcnt(0)"
                   : "=&v"(p1), "=&v"(p2), "=&v"(p3), "=&v"(vm[0]), "=&v"(vm[1]), "=&v"(vm[2]), "=&v"(vm[3]), "=&v"(vm[4]), "=&v"(vm[5]), "=&v"(vm[6]), "=&v"(vm[7])
                   : "v"(oa1), "v"(oa2), "v"(oa3), "v"(oam) : "memory");
      const f32x4 su = mfu(one, p3, mfu(one, p2, mfu(one, p1, f32x4{0.f, 0.f, 0.f, 0.f})));
      f32x4 o[4];
#pragma unroll
      for (int i = 0; i < 4; ++i) o[i] = mfu(kx[4 * (i >> 1) + (i & 1)], p1, f32x4{0.f, 0.f, 0.f, 0.f});
#pragma unroll
      for (int i = 0; i < 4; ++i) o[i] = mfu(kx[4 * (i >> 1) + 2 + (i & 1)], p2, o[i]);
#pragma unroll
      for (int i = 0; i < 4; ++i) o[i] = mfu(u32x4{vm[2 * i][0], vm[2 * i][1], vm[2 * i + 1][0], vm[2 * i + 1][1]}, p3, o[i]);
      SCHED();
      const float ir = __builtin_amdgcn_rcpf(su[0]);
      u32x4 oc[2];
#pragma unroll
      for (int i = 0; i < 8; ++i) oc[i >> 2][i & 3] = cvt2x(o[i >> 1][2 * (i & 1)] * ir, o[i >> 1][2 * (i & 1) + 1] * ir);
      *(u32x4*)(lds + OB_OFF + vo) = oc[0];
      *(u32x4*)(lds + OB_OFF + 1024 + vo) = oc[1];
      if (__builtin_expect(s == NL_ - 1, 0)) {
        unsigned lv = vo;
        asm volatile("" : "+v"(lv));
        ostore(rs, oc, ovoff(lv), osoff(s, h, jj, hb));
      }
      TRX(6);
    }
    if (B5) {
      __builtin_amdgcn_s_barrier();
      if (B5 > 1) asm volatile("s_nop %0" :: "n"(B5 - 1));
      if (w != KVE) kv_dma(rs, kv, ks, kd);
    } else if (w != KVE && KVE != 8) {
      pause(max((int)dlyd - (w == 1 && s >= XS_ ? 19 : 0) - 5 * (4 - USP), 0));
      kv_dma(rs, kv, ks, kd);
    }
    ++s;
    if (__builtin_expect(!have && s < NSEG_ - 1, 0))
      if (__builtin_amdgcn_readfirstlane(*(const unsigned*)(lds + KC_OFF + 8)) == 0x404u) kv_dma(rs, dvo, ks, kd);
    if (__builtin_expect(s == NSEG_ - 1, 0)) {
      static_assert((NSEG_ - 1) % 3 == 0, "set 0");
      asm volatile("" : "+v"(xo));
      const int qi = 8 * w + (int)((xo >> 4) & 7u) - 4 * (int)(xo >> 8);
#pragma unroll
      for (int r = 0; r < 4; ++r) {
        ma[r] = r <= qi ? 0.f : rbf(-1000000000.0f) / SCALE_;
        mb[r] = 16 + r <= qi ? 0.f : rbf(-1000000000.0f) / SCALE_;
      }
      if (w == 1) {
#pragma unroll
        for (int i = 0; i < 8; ++i) kx[i] = *(const u32x4*)(lds + KV_OFF + 16384 + xo + i * 1024);
        asm volatile("" : "+v"(kx[0]), "+v"(kx[1]), "+v"(kx[2]), "+v"(kx[3]), "+v"(kx[4]), "+v"(kx[5]), "+v"(kx[6]), "+v"(kx[7]));
      }
      xt = (xo & 128u) ? xt : POISON;
      xo += (xo & 128u) ? 0u : OFF_Q + tile8(NSEG_ - 1, h, hb) + (w >> 1) * HALF_ + (w & 1) * 128u - r2b;
    }
    pub();
    TR(2);
  }
  asm volatile("" : "+s"(no));
  if (wg == 0 && w == 0 && vo == 0u) st_sc1_64(rw, OFF_EP, no);
  TRE(w);
}

DEVI void recur_role(char* lds, char* ws, rsrc_t rw, const u16* __restrict__ mem0, const u16* __restrict__ Wq, const u16* __restrict__ Wk,
                     const u16* __restrict__ Wv, const u16* __restrict__ Wo, u16* __restrict__ out, int wg TRARGD) {
  const u32x4 rs = u32x4{(unsigned)(size_t)ws, (unsigned)((size_t)ws >> 32) & 0xffffu, WS_USED, 0x00020000u};
  if (__builtin_amdgcn_readfirstlane(threadIdx.x) >= 256) recur_p1(lds, rw, rs, mem0, Wq, Wk, Wv, out, ws, wg TRARG DLYA);
  else recur_at(lds, rw, rs, Wo, ws, wg TRARG DLYA);
}

#define HTR(i)
#define XSLP 16
#define PD1 8
#define ODS 3
#define OB0 17
#define OB1 20
#define OB2 14
#define OPR 2
#define OLIM 16
#define ON0 6
#define CPF 1
#define OD0 1
#define OP0 19
#define OP1 22
#define OP2 25
#define OWV 5
#define OPA 18
#define OPB 2
#define OVT 15
#define OFL 2
#define OX 0
#define OFA 11
#define FSLP 16
template <int SLP>
DEVI unsigned poll_flags(rsrc_t rw, unsigned off, unsigned f) {
#pragma nounroll
  for (int it = 0; it < HSPIN_CAP; ++it) {
    if (__all(f != 0u)) break;
    __builtin_amdgcn_s_sleep(SLP);
    f = ld_sc1_32(rw, off);
  }
  return f;
}
DEVI void after(unsigned f, int d) {
  const unsigned t = __builtin_amdgcn_readfirstlane(f) + (unsigned)d;
#pragma nounroll
  for (int k = 0; k < 4 && t != 1u + (unsigned)d; ++k) {
    const int r = min((int)(t - rt_now()), 4096);
    if (r < 4) break;
#pragma nounroll
    for (int i = r * 9 >> 5; i > 0; --i) __builtin_amdgcn_s_sleep(1);
    if (r < 40) break;
  }
}
DEVI void xattn_wave(rsrc_t rw, int s, int hq, unsigned hb) {
  int tx = threadIdx.x;
  asm volatile("" : "+v"(tx));
  const int l = tx & 63, g = l >> 4, lo = l & 15, h = hq * 8 + __builtin_amdgcn_readfirstlane(tx >> 6);
#pragma nounroll
  for (int it = 0; it < HSPIN_CAP; ++it) {
    const unsigned f = ld_sc1_32(rw, l < 32 ? OFF_CKV + (unsigned)(h * 64 + s) * 4u : cooff(h, hb) + (unsigned)(s * 16 + (l & 3) * 4));
    if (__all((f & 0xffffffu) == (l < 32 ? 0x40404u : 2u))) break;
    __builtin_amdgcn_s_sleep(XSLP);
  }
  asm volatile("" ::: "memory");
  const unsigned lv = (unsigned)l * 16u, so = tile8(s, h, hb);
  u32x4 bq[4][2], kx[8], vx[8];
#pragma unroll
  for (int ks = 0; ks < 4; ++ks) {
#pragma unroll
    for (int n = 0; n < 2; ++n) bq[ks][n] = ld_sc1s(rw, lv + ks * 1024u, OFF_Q + so + n * HALF_);
    kx[ks] = ld_sc1s(rw, lv + ks * 1024u, OFF_K + so);
    kx[4 + ks] = ld_sc1s(rw, lv + ks * 1024u, OFF_K + so + HALF_);
  }
#pragma unroll
  for (int te = 0; te < 8; ++te) vx[te] = ld_sc1s(rw, lv + (te & 3) * 1024u, OFF_VX + so + (te >> 2) * HALF_);
  constexpr float L2E = 1.4426950408889634f;
  float m1[2], s1[2];
  f32x4 os[8][2];
  auto seg_part = [&]() {
    f32x4 st[2][2];
#pragma unroll
    for (int i = 0; i < 4; ++i) st[i >> 1][i & 1] = f32x4{0.f, 0.f, 0.f, 0.f};
#pragma unroll
    for (int ks = 0; ks < 4; ++ks) {
      st[0][0] = mfu(kx[ks], bq[ks][0], st[0][0]);
      st[0][1] = mfu(kx[ks], bq[ks][1], st[0][1]);
      st[1][1] = mfu(kx[4 + ks], bq[ks][1], st[1][1]);
    }
#pragma unroll
    for (int n = 0; n < 2; ++n) {
      float sv[8], mx = -3.0e38f, sum = 0.f;
#pragma unroll
      for (int i = 0; i < 8; ++i) {
        sv[i] = 16 * (i >> 2) + 4 * g + (i & 3) <= 16 * n + lo ? rbf(rbf(st[i >> 2][n][i & 3]) * SCALE_) : rbf(-1000000000.0f);
        mx = fmaxf(mx, sv[i]);
      }
      mx = rows_max(mx);
#pragma unroll
      for (int i = 0; i < 8; ++i) { sv[i] = __builtin_amdgcn_exp2f((sv[i] - mx) * L2E); sum += sv[i]; }
      m1[n] = mx;
      s1[n] = rows_sum(sum);
      const u32x4 pb = u32x4{cvt2(sv[0], sv[1]), cvt2(sv[2], sv[3]), cvt2(sv[4], sv[5]), cvt2(sv[6], sv[7])};
#pragma unroll
      for (int te = 0; te < 8; ++te) os[te][n] = mfu(vx[te], pb, f32x4{0.f, 0.f, 0.f, 0.f});
    }
  };
  u32x4 km[4], vm[4];
#pragma unroll
  for (int i = 0; i < 4; ++i) km[i] = ld_sc1(rw, kmoff(s, h, hb) + (unsigned)(i * 512 + g * 128 + (lo & 7) * 16));
#pragma unroll
  for (int i = 0; i < 4; ++i) vm[i] = ld_sc1(rw, kmoff(s, h, hb) + 2048u + (unsigned)(i * 512 + (l & 31) * 16));
  seg_part();
  f32x4 sm[2];
#pragma unroll
  for (int ks = 0; ks < 4; ++ks)
#pragma unroll
    for (int n = 0; n < 2; ++n) sm[n] = mfu(km[ks], bq[ks][n], ks ? sm[n] : f32x4{0.f, 0.f, 0.f, 0.f});
  s16x4 pm[2];
  float cs[2];
#pragma unroll
  for (int n = 0; n < 2; ++n) {
    float sv[4], mx = m1[n], sum = 0.f;
#pragma unroll
    for (int r = 0; r < 4; ++r) {
      sv[r] = g < 2 ? rbf(rbf(sm[n][r]) * SCALE_) : -INFINITY;
      mx = fmaxf(mx, sv[r]);
    }
    mx = rows_max(mx);
#pragma unroll
    for (int r = 0; r < 4; ++r) { sv[r] = __builtin_amdgcn_exp2f((sv[r] - mx) * L2E); sum += sv[r]; }
    const float cr = __builtin_amdgcn_exp2f((m1[n] - mx) * L2E);
    const float inv = __builtin_amdgcn_rcpf(__builtin_fmaf(s1[n], cr, rows_sum(sum)));
    cs[n] = cr * inv;
    pm[n] = __builtin_bit_cast(s16x4, u32x2{cvt2(sv[0] * inv, sv[1] * inv), cvt2(sv[2] * inv, sv[3] * inv)});
  }
#pragma unroll
  for (int n = 0; n < 2; ++n)
#pragma unroll
    for (int a = 0; a < 4; ++a) {
      f32x4 o[2];
#pragma unroll
      for (int i = 0; i < 2; ++i)
        o[i] = __builtin_amdgcn_mfma_f32_16x16x16bf16_1k(__builtin_bit_cast(s16x4, u32x2{vm[a][2 * i], vm[a][2 * i + 1]}), pm[n], os[2 * a + i][n] * cs[n], 0, 0, 0);
      st_sc1(rw, ooff(s, h, hb) + n * HALF_ + (unsigned)(a * 1024 + l * 16),
             u32x4{cvt2x(o[0][0], o[0][1]), cvt2x(o[0][2], o[0][3]), cvt2x(o[1][0], o[1][1]), cvt2x(o[1][2], o[1][3])});
    }
  drain_vm();
  if (l < 4) st_sc1_32(rw, cooff(h, hb) + (unsigned)(s * 16 + l * 4), 1u);
}

typedef f32x2 cf;
DEVI cf cmul(cf a, cf b) { return __builtin_elementwise_fma(cf{a.y, a.y}, cf{-b.y, b.x}, cf{a.x, a.x} * b); }
DEVI cf mni(cf a) { return cf{a.y, -a.x}; }
DEVI cf wexp(float r) {
  float sn, cs;
  sincospif(r, &sn, &cs);
  return cf{cs, -sn};
}
DEVI void dft4(cf& a0, cf& a1, cf& a2, cf& a3) {
  const cf t0 = a0 + a2, t1 = a0 - a2, t2 = a1 + a3, t3 = mni(a1 - a3);
  a0 = t0 + t2; a1 = t1 + t3; a2 = t0 - t2; a3 = t1 - t3;
}
constexpr float RH_ = 0.70710678118654752f, C8_ = 0.92387953251128674f, S8_ = 0.38268343236508977f;
DEVI cf w8_1(cf a) { return (a + mni(a)) * RH_; }
DEVI cf w8_3(cf a) { return (mni(a) - a) * RH_; }
template <int ST>
DEVI void dft8(cf* v) {
  dft4(v[0], v[2 * ST], v[4 * ST], v[6 * ST]);
  dft4(v[ST], v[3 * ST], v[5 * ST], v[7 * ST]);
  const cf e[4] = {v[0], v[2 * ST], v[4 * ST], v[6 * ST]}, o[4] = {v[ST], w8_1(v[3 * ST]), mni(v[5 * ST]), w8_3(v[7 * ST])};
#pragma unroll
  for (int c = 0; c < 4; ++c) { v[c * ST] = e[c] + o[c]; v[(c + 4) * ST] = e[c] - o[c]; }
}
DEVI void dft16(cf* v) {
#pragma unroll
  for (int b = 0; b < 4; ++b) dft4(v[b], v[4 + b], v[8 + b], v[12 + b]);
  v[5] = cmul(v[5], cf{C8_, -S8_}); v[6] = w8_1(v[6]); v[7] = cmul(v[7], cf{S8_, -C8_});
  v[9] = w8_1(v[9]); v[10] = mni(v[10]); v[11] = w8_3(v[11]);
  v[13] = cmul(v[13], cf{S8_, -C8_}); v[14] = w8_3(v[14]); v[15] = cmul(v[15], cf{-C8_, S8_});
  cf y[16];
#pragma unroll
  for (int c = 0; c < 4; ++c) {
    dft4(v[4 * c], v[4 * c + 1], v[4 * c + 2], v[4 * c + 3]);
#pragma unroll
    for (int d = 0; d < 4; ++d) y[c + 4 * d] = v[4 * c + d];
  }
#pragma unroll
  for (int i = 0; i < 16; ++i) v[i] = y[i];
}
template <int N, int ST>
DEVI void twiddle(cf* v, const cf* b) {
  cf w[8];
  w[1] = b[0]; w[2] = b[1]; w[4] = b[2];
  w[3] = cmul(w[1], w[2]); w[5] = cmul(w[1], w[4]); w[6] = cmul(w[2], w[4]); w[7] = cmul(w[3], w[4]);
#pragma unroll
  for (int i = 1; i < 8; ++i) v[i * ST] = cmul(v[i * ST], w[i]);
  if (N == 16) {
    v[8 * ST] = cmul(v[8 * ST], b[3]);
#pragma unroll
    for (int i = 1; i < 8; ++i) v[(8 + i) * ST] = cmul(v[(8 + i) * ST], cmul(w[i], b[3]));
  }
}
template <int N>
DEVI void wpow(cf* b, float r) {
#pragma unroll
  for (int i = 0; i < N; ++i) b[i] = wexp(r * (float)(1 << i));
}
DEVI u32x4 cc2u(cf a, cf b) { return u32x4{__float_as_uint(a.x), __float_as_uint(a.y), __float_as_uint(b.x), __float_as_uint(b.y)}; }

DEVI void fft2048(int t, char* lds, cf* v, const cf* b1, const cf* b2) {
  const int p = t & 3, j = t >> 2;
  dft16(v);
  twiddle<16, 1>(v, b1);
#pragma unroll
  for (int i = 0; i < 16; ++i) *(cf*)(lds + i * 4096 + t * 8) = v[i];
  bar();
#pragma unroll
  for (int i = 0; i < 16; ++i) v[i] = *(const cf*)(lds + (j >> 3) * 4096 + i * 256 + (t & 31) * 8);
  dft16(v);
  twiddle<16, 1>(v, b2);
  char* l2 = lds + 65536;
#pragma unroll
  for (int i = 0; i < 16; ++i) *(cf*)(l2 + (j & 7) * 8224 + i * 512 + (j >> 3) * 32 + p * 8) = v[i];
  bar();
#pragma unroll
  for (int i = 0; i < 16; ++i) v[i] = *(const cf*)(l2 + (i >> 1) * 8224 + (i & 1) * 4096 + t * 8);
  dft8<2>(v);
  dft8<2>(v + 1);
}
DEVI void fft_cols_fwd(char* lds, rsrc_t rw, int blk, unsigned hb) {
  int t = threadIdx.x;
  asm volatile("" : "+v"(t));
  const int p = t & 3, j = t >> 2;
  u32x4 xv[16];
#pragma unroll
  for (int i = 0; i < 16; ++i) xv[i] = ld_sc1s(rw, (j >> 4) * 65536 + p * 256 + (j & 15) * 16, OFF_XF + far8(32 * i + (blk >> 3), hb) + (blk & 7) * 1024u);
  cf b1[4], b2[4];
  wpow<4>(b1, (float)j * (1.f / 1024.f));
  wpow<4>(b2, (float)(j & 7) * (1.f / 64.f));
#pragma nounroll
  for (int it = 0; it < 4; ++it) {
    asm volatile("" : "+v"(t), "+v"(b1[0]), "+v"(b1[1]), "+v"(b1[2]), "+v"(b1[3]), "+v"(b2[0]), "+v"(b2[1]), "+v"(b2[2]), "+v"(b2[3]));
    cf v[16];
#pragma unroll
    for (int i = 0; i < 16; ++i) {
      v[i] = cf{__uint_as_float(xv[i][0] << 16), __uint_as_float(xv[i][0] & 0xffff0000u)};
      xv[i] = u32x4{xv[i][1], xv[i][2], xv[i][3], 0u};
    }
    bar();
    fft2048(t, lds, v, b1, b2);
#pragma unroll
    for (int i = 8; i < 16; ++i) *(cf*)(lds + (i - 8) * 4096 + t * 8) = v[i];
    bar();
#pragma unroll
    for (int i = 0; i < 8; ++i) {
      const int k = (t >> 2) + 128 * i;
      const cf z = v[i], c = *(const cf*)(lds + ((1024 - k) & 1023) * 32 + (t & 3) * 8);
      cf A = cf{z.x + c.x, z.y - c.y}, B = cf{z.y + c.y, c.x - z.x};
      if (k == 0) { A = cf{2.f * z.x, 2.f * c.x}; B = cf{2.f * z.y, 2.f * c.y}; }
      st_sc1(rw, OFF_T + far8a(128 * i, hb) + (unsigned)((t >> 2) * 16384 + blk * 256 + it * 64 + (t & 3) * 16), cc2u(A, B));
    }
  }
}
#define RPF 1
#define NTF 1
DEVI unsigned wpair(const u16* __restrict__ wr, const u16* __restrict__ wi, int o) {
  return NTF ? pack2(__builtin_nontemporal_load(wr + o), __builtin_nontemporal_load(wi + o)) : pack2(wr[o], wi[o]);
}
typedef unsigned u32x4u __attribute__((ext_vector_type(4), aligned(2)));
DEVI void fft_rows_ask(const u16* __restrict__ wr, const u16* __restrict__ wi, int row, int l, u32x4* raw, unsigned* wv) {
  const int rm = (2048 - row) & 2047;
#pragma unroll
  for (int i = 0; i < 4; ++i) raw[i] = __builtin_nontemporal_load((const u32x4u*)(((i & 1) ? wi : wr) + ((i & 2) ? rm * FB_ + 1 : row * FB_) + 8 * l));
  wv[16] = wpair(wr, wi, (l & 1) ? row * FB_ + 512 : rm * FB_);
}
DEVI void fft_rows_w(char* wl, int l, const u32x4* raw, unsigned* wv) {
#pragma unroll
  for (int i = 0; i < 4; ++i) {
    const u32x4 a = raw[i & 2], b = raw[(i & 2) + 1];
    const int k = 2 * (i & 1);
    *(u32x4*)(wl + (i >> 1) * 2048 + l * 32 + (i & 1) * 16) = u32x4{__builtin_amdgcn_perm(b[k], a[k], 0x05040100u), __builtin_amdgcn_perm(b[k], a[k], 0x07060302u),
                                                                   __builtin_amdgcn_perm(b[k + 1], a[k + 1], 0x05040100u), __builtin_amdgcn_perm(b[k + 1], a[k + 1], 0x07060302u)};
  }
#pragma unroll
  for (int i = 0; i < 16; ++i) wv[i] = *(const unsigned*)(wl + (i < 8 ? (l + 64 * i) * 4 : 2048 + (1023 - l - 64 * i) * 4));
  wv[17] = __builtin_amdgcn_update_dpp(0u, wv[16], 0x101, 0xf, 0xf, true);
}
DEVI cf wval(const unsigned* wv, int i, int l) {
  cf w = cf{__uint_as_float(wv[i] << 16), __uint_as_float(wv[i] & 0xffff0000u)};
  if (i >= 8) w.y = -w.y;
  if ((i & 7) == 0) {
    const unsigned e = wv[16 + (i >> 3)];
    const cf m = (w + cf{__uint_as_float(e << 16), __uint_as_float((e & 0xffff0000u) ^ (i ? 0u : 0x80000000u))}) * 0.5f;
    if (l == 0) w = m;
  }
  return w;
}
DEVI void fft_rows(char* lds, rsrc_t rw, const u16* __restrict__ wr, const u16* __restrict__ wi, int blk, unsigned hb) {
  int t = threadIdx.x;
  asm volatile("" : "+v"(t));
  int l = t & 63, row = blk * 8 + __builtin_amdgcn_readfirstlane(t >> 6);
  char* wl = lds + __builtin_amdgcn_readfirstlane(t >> 6) * 8704;
  unsigned wv[18];
  u32x4 in[8], raw[4];
  auto get = [&](int r) {
#pragma unroll
    for (int i = 0; i < 8; ++i) in[i] = ld_sc1s(rw, (l & 48) * 16 + (l & 3) * 64 + (l & 12) * 4 + (i & 3) * 1024, OFF_T + far8a(r, hb) + (i >> 2) * 4096u);
    fft_rows_ask(wr, wi, r, l, raw, wv);
  };
  get(row);
  cf b1[2][3], b2[4];
  wpow<3>(b1[0], (float)(2 * l) * (1.f / 512.f));
  wpow<3>(b1[1], (float)(2 * l + 1) * (1.f / 512.f));
  wpow<4>(b2, (float)(l & 7) * (1.f / 64.f));
#pragma nounroll
  for (int it = 0; it < 4; ++it, row += 256) {
  auto fresh = [&]() {
    asm volatile("" : "+v"(l), "+v"(b1[0][0]), "+v"(b1[0][1]), "+v"(b1[0][2]), "+v"(b1[1][0]), "+v"(b1[1][1]), "+v"(b1[1][2]), "+v"(b2[0]), "+v"(b2[1]), "+v"(b2[2]), "+v"(b2[3]));
  };
  fresh();
  const unsigned go = far8a(row, hb) + (unsigned)(l * 16);
  cf v[16];
#pragma unroll
  for (int i = 0; i < 8; ++i) {
    v[2 * i] = cf{__uint_as_float(in[i][0]), __uint_as_float(in[i][1])};
    v[2 * i + 1] = cf{__uint_as_float(in[i][2]), __uint_as_float(in[i][3])};
  }
  dft8<2>(v); dft8<2>(v + 1);
  twiddle<8, 2>(v, b1[0]); twiddle<8, 2>(v + 1, b1[1]);
#pragma unroll
  for (int i = 0; i < 8; ++i) *(u32x4*)(wl + i * 1088 + l * 16) = cc2u(v[2 * i], v[2 * i + 1]);
#pragma unroll
  for (int i = 0; i < 16; ++i) v[i] = *(const cf*)(wl + (l >> 3) * 1088 + i * 64 + (l & 7) * 8);
  dft16(v);
  twiddle<16, 1>(v, b2);
#pragma unroll
  for (int i = 0; i < 16; ++i) *(cf*)(wl + (l & 7) * 1040 + i * 64 + (l >> 3) * 8) = v[i];
#pragma unroll
  for (int i = 0; i < 16; ++i) v[i] = *(const cf*)(wl + (i >> 1) * 1040 + (i & 1) * 512 + l * 8);
  dft8<2>(v); dft8<2>(v + 1);
  fft_rows_w(wl, l, raw, wv);
  if (row == 0) {
    unsigned w1[18];
    fft_rows_ask(wr, wi, 1024, l, raw, w1);
    fft_rows_w(wl, l, raw, w1);
#pragma unroll
    for (int i = 0; i < 16; ++i) *(cf*)(wl + i * 512 + l * 8) = v[i];
#pragma unroll
    for (int i = 0; i < 16; ++i) {
      const cf c = *(const cf*)(wl + ((1024 - l - 64 * i) & 1023) * 8), wa = wval(wv, i, l), wb = wval(w1, i, l);
      v[i] = cmul(v[i], (wa + wb) * 0.5f) + cmul(cf{c.x, -c.y}, (wa - wb) * 0.5f);
    }
  } else {
#pragma unroll
    for (int i = 0; i < 16; ++i) v[i] = cmul(v[i], wval(wv, i, l));
  }
#pragma unroll
  for (int i = 0; i < 16; ++i) v[i] = cf{v[i].y, v[i].x};
  asm volatile("" : "+v"(v[0]), "+v"(v[8]), "+v"(v[15]) :: "memory");
  if (it < 3) get(row + 256);
  asm volatile("" ::: "memory");
  fresh();
  dft8<2>(v); dft8<2>(v + 1);
#pragma unroll
  for (int i = 0; i < 16; ++i) *(cf*)(wl + (i >> 1) * 1056 + (i & 1) * 512 + l * 8) = v[i];
#pragma unroll
  for (int i = 0; i < 16; ++i) v[i] = *(const cf*)(wl + (l & 7) * 1056 + i * 64 + (l >> 3) * 8);
  twiddle<16, 1>(v, b2);
  dft16(v);
#pragma unroll
  for (int i = 0; i < 16; ++i) *(cf*)(wl + (l >> 3) * 1088 + i * 64 + (l & 7) * 8) = v[i];
#pragma unroll
  for (int i = 0; i < 8; ++i) {
    const f32x4 a = *(const f32x4*)(wl + i * 1088 + l * 16);
    v[2 * i] = cf{a[0], a[1]}; v[2 * i + 1] = cf{a[2], a[3]};
  }
  twiddle<8, 2>(v, b1[0]); twiddle<8, 2>(v + 1, b1[1]);
  dft8<2>(v); dft8<2>(v + 1);
#pragma unroll
  for (int i = 0; i < 8; ++i) st_sc1(rw, OFF_T + go + i * 1024u, cc2u(cf{v[2 * i].y, v[2 * i].x}, cf{v[2 * i + 1].y, v[2 * i + 1].x}));
  }
}
DEVI void fft_cols_inv(char* lds, rsrc_t rw, int blk, unsigned hb) {
  int t = threadIdx.x;
  asm volatile("" : "+v"(t));
  const int j = t >> 2;
  u32x4 in[8];
  auto get = [&](int b) {
#pragma unroll
    for (int i = 0; i < 8; ++i) in[i] = ld_sc1s(rw, (unsigned)((t >> 2) * 16384 + (t & 3) * 16), OFF_T + far8a(128 * i, hb) + (unsigned)b * 64u);
  };
  get(blk);
  cf b1[4], b2[4];
  wpow<4>(b1, (float)j * (1.f / 1024.f));
  wpow<4>(b2, (float)(j & 7) * (1.f / 64.f));
#pragma nounroll
  for (int it = 0; it < 4; ++it, blk += 8) {
    asm volatile("" : "+v"(t), "+v"(b1[0]), "+v"(b1[1]), "+v"(b1[2]), "+v"(b1[3]), "+v"(b2[0]), "+v"(b2[1]), "+v"(b2[2]), "+v"(b2[3]));
    cf v[16];
#pragma unroll
    for (int i = 0; i < 8; ++i) {
      const f32x4 a = __builtin_bit_cast(f32x4, in[i]);
      cf m = cf{a[2] - a[1], a[0] + a[3]};
      v[i] = cf{a[1] + a[2], a[0] - a[3]};
      if (i == 0 && t < 4) { v[0] = cf{a[2], a[0]}; m = cf{a[3], a[1]}; }
      *(cf*)(lds + 65536 + i * 4096 + t * 8) = m;
    }
    asm volatile("" : "+v"(v[0]), "+v"(v[1]), "+v"(v[2]), "+v"(v[3]), "+v"(v[4]), "+v"(v[5]), "+v"(v[6]), "+v"(v[7]) :: "memory");
    if (it < 3) get(blk + 8);
    bar();
#pragma unroll
    for (int i = 8; i < 16; ++i) v[i] = *(const cf*)(lds + 65536 + ((2048 - 128 * i - (t >> 2)) & 1023) * 32 + (t & 3) * 8);
    fft2048(t, lds, v, b1, b2);
    const float sc = 0.5f / ((float)S_ * (float)D_);
#pragma unroll
    for (int i = 0; i < 16; ++i) *(unsigned*)(lds + i * 2048 + t * 4) = cvt2(v[i].y * sc, v[i].x * sc);
    bar();
#pragma unroll
    for (int i = 0; i < 4; ++i) st_sc1(rw, bfoff(blk, i * 512, hb) + (unsigned)t * 16u, *(const u32x4*)(lds + i * 8192 + t * 16));
  }
}

DEVI void oproj_worker(char* lds, rsrc_t rw, const u16* __restrict__ Wo, int set, int c, int q, u16* __restrict__ out, unsigned hb DLYARG
) {
  DLYI(0x300);
  int t = threadIdx.x;
  asm volatile("" : "+v"(t));
  const int l = t & 63, w = __builtin_amdgcn_readfirstlane(t >> 6), g = l >> 4, lo = l & 15, hh = 4 * q + (w >> 1);
  constexpr int sstep = 4;
  const int s0 = set < 3 ? set - ON0 : 3 * ON0 + 3;
  auto seg = [&](int s) { return s >= 3 * ON0 ? s : (s < s0 ? 0 : ((s - s0) >> 2) * 3 + set); };
  u32x4 a[4][4][2];
  __syncthreads();
  {
    const u16* gp = Wo + (size_t)(hh * DH_ + g) * D_ + c * 256 + (w & 1) * 128 + (lo ^ (4 * g)) * 8;
    auto ask = [&](int p) {
#pragma unroll
      for (int i = 0; i < 4; ++i)
        __builtin_amdgcn_global_load_lds((const unsigned*)(gp + (size_t)(16 * p + 4 * i) * D_), (unsigned*)(lds + p % 5 * 32768 + w * 4096 + i * 1024 + l * 16), 16, 0, NTO);
      SCHED();
    };
#pragma unroll
    for (int p = 0; p < 5; ++p) ask(p);
    const char* pp = lds + (w >> 2) * 4096 + (4 * g + (lo >> 2)) * 256 + ((4 * (w & 3)) ^ lo) * 16;
#pragma unroll
    for (int n = 0; n < 8; ++n) {
      asm volatile("s_waitcnt vmcnt(%0) lgkmcnt(0)\n s_barrier" :: "n"(4 * (n == 0 ? 4 : (n < 5 ? 3 : 7 - n))) : "memory");
      if (n >= 1 && n < 4) ask(n + 4);
#pragma unroll
      for (int i = 0; i < 8; ++i) {
        const u32x2 x0 = tr_quad(pp + n % 5 * 32768 + (i >> 1) * 8192 + (i & 1) * 8);
        a[i >> 1][n >> 1][i & 1][2 * (n & 1)] = x0[0];
        a[i >> 1][n >> 1][i & 1][2 * (n & 1) + 1] = x0[1];
      }
      SCHED();
    }
    bar();
  }
  const unsigned vo = (unsigned)l * 16u;
  const unsigned fo = cooff(4 * q, hb) + (unsigned)(((l >> 2) & 3) * 1024 + (l & 3) * 4);
  auto flags = [&](int s) { return ld_sc1_32(rw, fo + (unsigned)((l & 16) ? seg(s + sstep) : seg(s)) * 16u); };
  auto fbits = [&](int s, unsigned f) {
    const unsigned m = (unsigned)__builtin_amdgcn_ballot_w64((f & 1u) != 0u);
    return ((m & 0xffffu) == 0xffffu && s < NL_ ? 1u : 0u) | (m == 0xffffffffu && s + sstep < NL_ ? 2u : 0u);
  };
  auto wait_b = [&](int s) {
#pragma nounroll
    for (int it = 0; it < HSPIN_CAP; ++it) {
      if (__all((ld_sc1_32(rw, cooff(hh, hb) + (unsigned)(seg(s) * 16 + (l & 3) * 4)) & 1u) != 0u)) break;
      __builtin_amdgcn_s_sleep(XSLP);
    }
    asm volatile("" ::: "memory");
  };
  auto load_b = [&](int s, int par, int i, unsigned vd) {
    auto* dst = (__attribute__((address_space(3))) void*)(LDS3 + par * 32768 + w * 4096);
    const unsigned so = ooff(seg(s), hh, hb) + (w & 1) * HALF_;
    if (i == 0) __builtin_amdgcn_raw_ptr_buffer_load_lds(rw, dst, 16, vd, so, 0, 16);
    if (i == 1) __builtin_amdgcn_raw_ptr_buffer_load_lds(rw, dst, 16, vd, so, 1024, 16);
    if (i == 2) __builtin_amdgcn_raw_ptr_buffer_load_lds(rw, dst, 16, vd, so, 2048, 16);
    if (i == 3) __builtin_amdgcn_raw_ptr_buffer_load_lds(rw, dst, 16, vd, so, 3072, 16);
  };
  auto pxs = [&](int slot) { return OFF_PX + far8a((slot * 4 + c) * 16 + (w >> 2), hb) + (unsigned)(w & 3) * 2048u; };
  auto pxo = [&](int s) { return pxs(s < FS_ ? s & 31 : s - FS_ + 32); };
  for (int s = FS_ + (set + sstep - FS_ % sstep) % sstep; s < NL_; s += sstep) {
    st_sc1s(rw, vo, pxo(s) + q * 32768u, u32x4{POISON, POISON, POISON, POISON});
    st_sc1s(rw, vo + 1024u, pxo(s) + q * 32768u, u32x4{POISON, POISON, POISON, POISON});
    SCHED();
    asm volatile("s_nop 1");
    SCHED();
  }
  const unsigned sv = (unsigned)((lo >> 2) * 256 + g * 64 + (lo & 3) * 16);
  auto flag = [&](int s) { if (t == 64) st_sc1_32(rw, OFF_CPX + (unsigned)(seg(s) * 4 + c) * 128u + q * 4u, 1u); };
  float* gl = (float*)(lds + 132096);
  u32x4 pv[2];
  float gv;
  unsigned bb;
  auto pf = [&](int s) { return ld_sc1_32(rw, OFF_CPX + (unsigned)(seg(s) * 4 + c) * 128u + (l & 7) * 4u); };
  auto fin_a = [&](int s, bool ok) {
    if (!ok && s < FS_) {
#pragma nounroll
      for (int it = 0; it < HSPIN_CAP; ++it) {
        if (__all(pf(s) == 1u)) break;
        __builtin_amdgcn_s_sleep(2);
      }
    }
    int ll = l;
    asm volatile("" : "+v"(ll));
    gv = gl[seg(s) * 4 + (ll & 3)];
    bb = __builtin_amdgcn_raw_buffer_load_b32(rw, (ll & 12) * 16384 + (ll & 3) * 16 + (ll >> 4) * 4, bfoff(32 * c + 4 * w, seg(s) * SEG_ + 4 * q, hb), 16);
    if (s >= FS_) __builtin_amdgcn_s_sleep(PD1);
#pragma nounroll
    for (int it = 0; it < HSPIN_CAP; ++it) {
      asm volatile("" ::: "memory");
#pragma unroll
      for (int k = 0; k < 2; ++k) pv[k] = ld_sc1s(rw, (ll >> 4) * 32768 + (ll & 15) * 16, pxo(s) + k * 131072u + q * 256u);
      if (s < FS_ || !bad(umax5(pv[1], umax5(pv[0], 0u)))) break;
      __builtin_amdgcn_s_sleep(1);
    }
  };
  auto fin_b = [&](int s) {
    int ll = l;
    asm volatile("" : "+v"(ll));
    const unsigned s1 = (ll & 2) ? 0u : 0x3f80u << (16 * (ll & 1));
    const u32x4 sel = u32x4{(ll & 12) == 0 ? s1 : 0u, (ll & 12) == 4 ? s1 : 0u, (ll & 12) == 8 ? s1 : 0u, (ll & 12) == 12 ? s1 : 0u};
    const f32x4 v = mfu(sel, pv[1], mfu(sel, pv[0], f32x4{0.f, 0.f, 0.f, 0.f}));
    const float omg = rbf(1.0f - gv);
    const unsigned y = cvt2(omg * __uint_as_float(bb << 16), omg * __uint_as_float(bb & 0xffff0000u));
    const unsigned av = cvt2(v[0], v[1]);
    const unsigned x = cvt2(gv * __uint_as_float(av << 16), gv * __uint_as_float(av & 0xffff0000u));
    const unsigned r = cvt2(__uint_as_float(x << 16) + __uint_as_float(y << 16), __uint_as_float(x & 0xffff0000u) + __uint_as_float(y & 0xffff0000u));
    __builtin_amdgcn_raw_buffer_store_b32(r, __builtin_amdgcn_make_buffer_rsrc(out, 0, S_ * D_ * 2, 0x00020000), (ll & 3) * (D_ * 2) + (ll & 12) * 4 + (ll >> 4) * 4,
                                          (unsigned)((seg(s) * SEG_ + 4 * q) * (D_ * 2) + (256 * c + 32 * w) * 2), OAUX);
  };
  f32x4 acc[2][2];
  auto send = [&](unsigned po) {
    u32x4 u[2];
#pragma unroll
    for (int i = 0; i < 8; ++i) u[i >> 2][i & 3] = cvt2x(acc[i >> 2][(i >> 1) & 1][2 * (i & 1)], acc[i >> 2][(i >> 1) & 1][2 * (i & 1) + 1]);
    asm volatile("" : "+v"(u[0]), "+v"(u[1]));
    st_sc1s(rw, sv, po + q * 32768u, u[0]);
    st_sc1s(rw, sv + 1024u, po + q * 32768u, u[1]);
  };
  auto touch = [&](int s, bool on) {
    if (w == 5)
      __builtin_amdgcn_raw_ptr_buffer_load_lds(rw, (__attribute__((address_space(3))) void*)(LDS3 + 131328), 4, on ? (l >> 5) * HALF_ + (l & 31) * 128u : 0x80000000u,
                                               ooff(s, 4 * q + c, hb), 0, 16);
  };
  auto rdo = [](int j) { return (j >> 3) * 8192 + ((j & 1) * 4 + ((j >> 1) & 3)) * 1024; };
#pragma unroll
  for (int k = 0; k < 2; ++k) {
    wait_b(s0 + k * sstep);
#pragma unroll
    for (int i = 0; i < 4; ++i) load_b(s0 + k * sstep, k, i, vo);
  }
  unsigned* vote = (unsigned*)(lds + 131072);
  unsigned fl = flags(s0 + 2 * sstep);
  if (w == 0) {
#pragma nounroll
    for (int it = 0; it < HSPIN_CAP && !(fbits(s0 + 2 * sstep, fl) & 1u); ++it) {
      __builtin_amdgcn_s_sleep(8);
      fl = flags(s0 + 2 * sstep);
    }
    fl = flags(s0 + 3 * sstep);
  }
  drain_vm();
  wg_wait_s(rw, OFF_CF + 256u, 32u);
  if (w == 0) *(u32x4*)(gl + 4 * l) = ld_sc1(rw, OFF_G + (unsigned)(l * SEG_ + 4 * q) * 4u);
  HTR(10);
  int par = 0, s1 = s0;
  {
    constexpr unsigned NOW = 0x80000000u;
    const unsigned s1_ = (l & 2) ? 0u : 0x3f80u << (16 * (l & 1));
    const u32x4 sel = u32x4{(l & 12) == 0 ? s1_ : 0u, (l & 12) == 4 ? s1_ : 0u, (l & 12) == 8 ? s1_ : 0u, (l & 12) == 12 ? s1_ : 0u};
    const unsigned ob = (l & 12) * 16384 + (l & 3) * 16 + g * 4, op = g * 32768 + lo * 16, oo = (l & 3) * (D_ * 2) + (l & 12) * 4 + g * 4;
    const unsigned ofx = l < 32 ? fo : OFF_CPX + (unsigned)(c * 128 + (l & 7) * 4);
    const rsrc_t ro = __builtin_amdgcn_make_buffer_rsrc(out, 0, S_ * D_ * 2, 0x00020000);
    u32x4 b[8];
#pragma unroll
    for (int j = 0; j < 8; ++j) b[j] = *(const u32x4*)(lds + l * 16 + rdo(j));
    unsigned vt = 1u, vp, px[4] = {0u, 0u, 0u, 0u};
    pv[0] = pv[1] = u32x4{0u, 0u, 0u, 0u};
    gv = 0.f; bb = 0u;
    unsigned vd = vo, so = ooff(seg(s0 + 2 * sstep), hh, hb) + (w & 1) * HALF_;
    int rn = 0, sg[9];
#pragma unroll
    for (int i = 0; i < 9; ++i) sg[i] = __builtin_amdgcn_readfirstlane(seg(s0 + (i - 4) * sstep));
    if (OPR == 1 && w >= 4) __builtin_amdgcn_s_setprio(1);
    if (OPR == 2 && w < 4) __builtin_amdgcn_s_setprio(1);
#pragma nounroll
    do {
      const int s = s1, s_ = s1;
      const char* bp = lds + par * 32768 + l * 16;
      const char* bn = lds + ((par + 1) & 3) * 32768 + l * 16;
      unsigned x, y, bo, oso, r3, r4, glo, p1;
      vp = vt;
      int rn1;
      f32x4 v;
#define OTM(i)
      SCHED();
      OTM(1);
      sfor<32>([&](auto J) __attribute__((always_inline)) {
        constexpr int j = J;
        if (!(OX & 4) && j >= OD0 && j < OD0 + 4 * ODS && (j - OD0) % ODS == 0) {
          auto* dst = (__attribute__((address_space(3))) void*)(LDS3 + (par ^ 2) * 32768 + w * 4096);
          __builtin_amdgcn_raw_ptr_buffer_load_lds(rw, dst, 16, vd, so, (j - OD0) / ODS * 1024, 16);
        }
        if (j < 2) asm volatile(MF "%0, %2, %4, 0\n" MF "%1, %3, %4, 0" : "=&v"(acc[j & 1][0]), "=&v"(acc[j & 1][1]) : "v"(a[0][0][0]), "v"(a[0][0][1]), "v"(b[j & 7]));
        else asm volatile(MF "%0, %2, %4, %0\n" MF "%1, %3, %4, %1" : "+v"(acc[j & 1][0]), "+v"(acc[j & 1][1]) : "v"(a[j >> 3][(j >> 1) & 3][0]), "v"(a[j >> 3][(j >> 1) & 3][1]), "v"(b[j & 7]));
        SCHED();
        if (!(OX & 2)) b[j & 7] = *(const u32x4*)((j < 24 ? bp : bn) + rdo((j + 8) & 31));
        auto here = [](int v) { asm volatile("" : "+s"(v)); return v; };
        if (j == 0) { p1 = here(s); p1 = (p1 < FS_ ? p1 & 31 : p1 - FS_ + 32) * 64 + c * 16 + (w >> 2); asm volatile("" : "+s"(p1)); }
        if (j == 1) { px[3] = px[2]; px[2] = px[1]; px[1] = px[0]; px[0] = OFF_PX + far8a(here(p1), hb) + (unsigned)(w & 3) * 2048u; asm volatile("" : "+s"(px[0])); }
        if (j == 22) { p1 = here(sg[7]); so = (p1 < FS_ ? OFF_Q : OFF_OT - (NSEG_ - 8) * H_ * 16384u) + (unsigned)(p1 * 8 + (hh & 7)) * (2u * BLK_) + (w & 1) * HALF_; asm volatile("" : "+s"(so)); }
        if (j == 23) { p1 = here(sg[7]); p1 = (p1 < 2 ? 0u : hb >> 1) ^ (unsigned)(hh >> 3) ^ (unsigned)p1; asm volatile("" : "+s"(p1)); }
        if (j == 24) { so += NearMap::pg(here(p1)); asm volatile("" : "+s"(so)); }
        if (j == 5) { bo = bfoff(32 * c + 4 * w, here(sg[1]) * SEG_ + 4 * q, hb); asm volatile("" : "+s"(bo)); }
        if (j == 6) { oso = (unsigned)((here(sg[0]) * SEG_ + 4 * q) * (D_ * 2) + (256 * c + 32 * w) * 2); r4 = oo | (here(rn) >= 4 ? 0u : NOW); asm volatile("" : "+s"(oso)); asm volatile("" : "+v"(r4)); }
        if (j == 13) { r3 = op | (here(rn) >= 3 && !(OX & 8) ? 0u : NOW); glo = here(sg[1]) * 4 + (l & 3); asm volatile("" : "+v"(r3), "+v"(glo)); }
        if (j == 14) {
          s1 = here(s1) + sstep; rn1 = here(rn) + 1;
          asm volatile("" : "+s"(s1), "+s"(rn1));
        }
        if (!(OX & 8) && j == OB2 - 5) {
          v = mfu(sel, pv[1], mfu(sel, pv[0], f32x4{0.f, 0.f, 0.f, 0.f}));
          const float omg = rbf(1.0f - gv);
          y = cvt2(omg * __uint_as_float(bb << 16), omg * __uint_as_float(bb & 0xffff0000u));
          asm volatile("" : "+v"(y));
        }
        if (!(OX & 8) && j == OB2 - 3) {
          const unsigned av = cvt2(v[0], v[1]);
          x = cvt2(gv * __uint_as_float(av << 16), gv * __uint_as_float(av & 0xffff0000u));
          asm volatile("" : "+v"(x));
        }
        if (!(OX & 8) && j == OB2 - 2)
          __builtin_amdgcn_raw_buffer_store_b32(cvt2(__uint_as_float(x << 16) + __uint_as_float(y << 16), __uint_as_float(x & 0xffff0000u) + __uint_as_float(y & 0xffff0000u)),
                                                ro, r4, oso, OAUX);
        SCHED();
        if (OPR == 3 && j == OPA && w >= 4) __builtin_amdgcn_s_setprio(1);
        if (OPR == 3 && j == OPB && w >= 4) __builtin_amdgcn_s_setprio(0);
        if (j == 7) OTM(2);
        if (j == OB0 - 8) OTM(5);
        if (j == OVT && w == 0) {
          const int s = here(s_);
          const unsigned long long fm = __builtin_amdgcn_ballot_w64((fl & 1u) != 0u);
          unsigned fb = (unsigned)fm == 0xffffffffu && s + 3 * sstep < NL_ ? 1u : 0u;
          if (__builtin_expect(fm != ~0ull, 0)) {
            if (!fb && s + sstep < OLIM) {
#pragma nounroll
              for (int it = 0; it < HSPIN_CAP && !fb; ++it) {
                __builtin_amdgcn_s_sleep(8);
                fb = fbits(s + 3 * sstep, flags(s + 3 * sstep)) & 1u;
              }
            }
            if ((fm >> 32) != 0xffffffffu && rn >= 3 && !OX) {
#pragma nounroll
              for (int it = 0; it < HSPIN_CAP; ++it) {
                if (__all(pf(s - 3 * sstep) == 1u)) break;
                __builtin_amdgcn_s_sleep(2);
              }
            }
          }
          vote[par] = fb;
        }
        if (!(OX & 8) && j == OP0) pv[0] = ld_sc1s(rw, r3, px[3] + q * 256u);
        if (!(OX & 8) && j == OP1) pv[1] = ld_sc1s(rw, r3, px[3] + 131072u + q * 256u);
        if (!(OX & 8) && j == OP2) {
          gv = gl[glo];
          bb = __builtin_amdgcn_raw_buffer_load_b32(rw, ob, bo, 16);
        }
        if (j == OB0) {
          OTM(3);
          if (!OX) wait_vm<OWV>();
          __builtin_amdgcn_s_barrier();
          OTM(4);
          asm volatile("" ::: "memory");
          vt = vote[par];
          if (rn >= 1 && t == 64) st_sc1_32(rw, OFF_CPX + (unsigned)(sg[3] * 4 + c) * 128u + q * 4u, 1u);
        }
        if (j == OFL && w == 0) fl = ld_sc1_32(rw, ofx + (l < 32 ? (unsigned)sg[OFL < OB0 ? 7 : 8] * 16u : (unsigned)sg[OFL < OB0 ? 1 : 2] * 512u));
        if (j == 26) {
          vt = __builtin_amdgcn_readfirstlane(vt) & vp;
          vd = (vt & 1u) ? vo : NOW;
          asm volatile("" : "+v"(vd));
        }
      });
      OTM(6);
      asm volatile("s_nop 5" : "+v"(acc[0][0]), "+v"(acc[0][1]), "+v"(acc[1][0]), "+v"(acc[1][1]));
      if (!(OX & 16)) send(px[0]);
      OTM(7);
#pragma unroll
      for (int i = 0; i < 8; ++i) sg[i] = sg[i + 1];
      sg[8] += sg[8] < 3 * ON0 ? 3 : 4;
      par = (par + 1) & 3;
      rn = rn1;
    } while (vp & 1u);
    if (w == 0) vote[par] = fbits(s1 + sstep, flags(s1 + sstep));
    asm volatile("" ::: "memory");
    __builtin_amdgcn_s_waitcnt(0x0f72);
    bar();
  }
  if (OX) return;
  int np = min((s1 - s0) / sstep, 4);
#pragma nounroll
  for (int s = s1; s < NL_; s += sstep, par ^= 1) {
    const int nxt = s + sstep;
    const unsigned vt = __builtin_amdgcn_readfirstlane(vote[par]);
    const bool pre = (vt & 1u) != 0u;
    const unsigned vd = pre ? vo : 0x80000000u;
    const char* bp = lds + par * 32768 + l * 16;
    auto rd = [&](int j) { return *(const u32x4*)(bp + rdo(j)); };
    unsigned f;
    if (np >= 3) f = pf(s - 3 * sstep);
    u32x4 b[8];
#pragma unroll
    for (int j = 0; j < 8; ++j) b[j] = rd(j);
    if (np >= 2) flag(s - 2 * sstep);
    if (np >= 4) fin_b(s - 4 * sstep);
    SCHED();
#pragma unroll
    for (int j = 0; j < 32; ++j) {
      if (j < 4 * ODS && j % ODS == 0) load_b(nxt, par ^ 1, j / ODS, vd);
#pragma unroll
      for (int ct = 0; ct < 2; ++ct) acc[j & 1][ct] = mfu(a[j >> 3][(j >> 1) & 3][ct], b[j & 7], j >= 2 ? acc[j & 1][ct] : f32x4{0.f, 0.f, 0.f, 0.f});
      if (j + 8 < 32) b[j & 7] = rd(j + 8);
      SCHED();
      if (j == OFA) {
        if (np >= 3) fin_a(s - 3 * sstep, __all(f == 1u));
        if (w == 0) fl = flags(nxt + sstep);
        touch(seg(nxt + sstep), vt & 2u);
        SCHED();
      }
    }
    send(pxo(s));
    asm volatile("" ::: "memory");
    __builtin_amdgcn_s_waitcnt(0x0f72);
    asm volatile("" ::: "memory");
    if (w == 0) vote[par ^ 1] = fbits(nxt + sstep, fl);
    np = min(np + 1, 4);
    if (!pre) {
      if (np >= 4) fin_b(s - 3 * sstep);
      if (s - sstep < FS_) {
        if (s < FS_) drain_vm();
        bar();
        if (np >= 2) flag(s - sstep);
        if (s < FS_) flag(s);
      }
#pragma nounroll
      for (int k = min(np, 3) - 1; k >= 0; --k) { fin_a(s - k * sstep, false); fin_b(s - k * sstep); }
      np = 0;
      if (nxt >= NL_) break;
      if (nxt < FS_) {
        wait_b(nxt);
#pragma unroll
        for (int i = 0; i < 4; ++i) load_b(nxt, par ^ 1, i, vo);
        drain_vm();
      } else {
        const unsigned so = ooff(nxt, hh, hb) + (w & 1) * HALF_;
        const unsigned cm = cooff(hh, hb) + (unsigned)((nxt - 2) * 16 + (l & 3) * 4);
        after(poll_flags<FSLP>(rw, cm, ld_sc1_32(rw, cm)), (int)(nxt == NL_ - 1 ? dly[XTL] : dly[XT1]));
#pragma nounroll
        for (int it = 0; it < HSPIN_CAP; ++it) {
          asm volatile("" ::: "memory");
#pragma unroll
          for (int j = 0; j < 4; ++j) b[j] = ld_sc1s(rw, vo + j * 1024u, so);
          if (!bad4(b)) break;
          __builtin_amdgcn_s_sleep(1);
        }
#pragma unroll
        for (int j = 0; j < 4; ++j) *(u32x4*)(lds + (par ^ 1) * 32768 + w * 4096 + j * 1024 + l * 16) = b[j];
      }
    }
    bar();
  }
}

DEVI void helper_role(char* lds, char* ws, rsrc_t rw, const u16* __restrict__ x, const u16* __restrict__ Wq,
                      const u16* __restrict__ Wk, const u16* __restrict__ Wv, const u16* __restrict__ Wo,
                      const u16* __restrict__ wr, const u16* __restrict__ wi, const u16* __restrict__ gw, u16* __restrict__ out, int hw
                      ) {
  const int xg = hw & 7, loc = hw >> 3;
  const int q = xg >> 1, m = loc * 2 + (hw & 1);
  DLYI(0x30);
  HTR(0);
  const int xi = (m & 7) * 4 + q, l = threadIdx.x & 63, w = __builtin_amdgcn_readfirstlane(threadIdx.x >> 6);
  auto xld = [&](int i) {
    return __builtin_amdgcn_raw_buffer_load_b128(__builtin_amdgcn_make_buffer_rsrc((void*)x, 0, S_ * D_ * 2, 0x00020000),
                                                 (l & 7) * 2048 + w * 256 + (l >> 3) * 16 + (i & 2) * 64, (unsigned)(32 * (i >> 2) + xi) * 32768u + (i & 1) * 16384u, NTX);
  };
  auto xst = [&](int i, unsigned hb, u32x4 v) { st_sc1s(rw, (l >> 3) * 256 + (l & 7) * 16 + (i & 2) * 1024 + (i & 1) * 128, OFF_XF + far8((32 * (i >> 2) + xi) * 4 + (w >> 1), hb) + (w & 1) * 4096u, v); };
  unsigned hb;
  if (m < 24) {
    if (m >= 16) {
      static_assert((OFF_REC - OFF_CKV) % 512u == 0 && (OFF_REC - OFF_CKV) / 512u <= 512u, "shares");
      if (threadIdx.x < (OFF_REC - OFF_CKV) / 512u) st_sc1(rw, OFF_CKV + xi * ((OFF_REC - OFF_CKV) / 32u) + threadIdx.x * 16u, u32x4{0u, 0u, 0u, 0u});
      u32x4 xs[4];
#pragma unroll
      for (int i = 0; i < 4; ++i) xs[i] = xld(i);
      const u32x2 no = launch_no(ws);
#pragma unroll
      for (int i = 0; i < 4; ++i) xst(i, 0u, xs[i]);
      rec_put(rw, xi, no);
      asm volatile("" :: "v"(xs[3]));
      HTR(4);
      for (unsigned i = 0; i < (RSE ? 7u : dly[QDL]); ++i) __builtin_amdgcn_s_sleep(32);
    }
    if ((m >> 3) == 1) hb = proj_reg<false>(lds, ws, rw, Wv, 8 * q + (m & 7), 2, hw);
    else hb = proj_reg<true>(lds, ws, rw, m < 8 ? Wk : Wq, 8 * q + (m & 7), m < 8 ? 1 : 0, hw);
    HTR(2);
  } else {
    probe(ws, rw, 8 * q + m - 24);
    const u32x2 no = launch_no(ws);
    rec_put(rw, 32 + 8 * q + m - 24, no);
    HTR(4);
    for (unsigned i = 0; i < dly[XDL]; ++i) __builtin_amdgcn_s_sleep(32);
    NearMap nm;
    {
      u32x4 wv[2], xs[16];
#pragma unroll
      for (int i = 0; i < 16; ++i)
        xs[(i + 4) & 15] = __builtin_amdgcn_raw_buffer_load_b128(__builtin_amdgcn_make_buffer_rsrc((void*)x, 0, S_ * D_ * 2, 0x00020000), l * 16 + (i & 3) * 1024,
                                                                 (unsigned)(32 * (((i + 4) & 15) >> 2) + xi) * 32768u + w * 4096u, NTX);
#pragma unroll
      for (int i = 0; i < 2; ++i) wv[i] = *(const u32x4*)(gw + i * 512 + l * 8);
      nm.k = near_kind(rw, no);
      hb = far_bit(nm.k);
#pragma unroll
      for (int i = 4; i < 16; ++i) *(u32x4*)(lds + (i >> 2) * 32768 + (i & 1) * 16384 + l * 256 + ((2 * w + ((i >> 1) & 1)) ^ (l & 15)) * 16) = xs[i];
      __syncthreads();
      u32x4 xf[12];
#pragma unroll
      for (int i = 0; i < 12; ++i) xf[i] = *(const u32x4*)(lds + ((i >> 2) + 1) * 32768 + w * 4096 + (i & 3) * 1024 + (l >> 4) * 256 + ((l & 15) ^ ((i & 3) * 4 + (l >> 4))) * 16);
#pragma unroll
      for (int i = 0; i < 12; ++i) st_sc1s(rw, l * 16 + (i & 3) * 1024, OFF_XF + far8((32 * ((i >> 2) + 1) + xi) * 4 + (w >> 1), hb) + (w & 1) * 4096u, xf[i]);
      float gs = 0.f;
#pragma unroll
      for (int n = 0; n < 8; ++n) {
        float acc = 0.f;
#pragma unroll
        for (int i = 0; i < 8; ++i) {
          const unsigned a = xs[2 * n + (i >> 2)][i & 3], b = wv[i >> 2][i & 3];
          const unsigned pr = cvt2(__uint_as_float(a << 16) * __uint_as_float(b << 16), __uint_as_float(a & 0xffff0000u) * __uint_as_float(b & 0xffff0000u));
          acc += __uint_as_float(pr << 16) + __uint_as_float(pr & 0xffff0000u);
        }
#pragma unroll
        for (int o = 1; o < 8; o *= 2) acc += __shfl_xor(acc, o);
        acc = rows_sum(fold8(acc));
        if (l == n) gs = acc;
      }
      asm volatile("" :: "v"(xf[11]));
      wg_signal(ws, OFF_CF + 384u);
      if (l < 8) st_sc1_32(rw, OFF_G + (unsigned)((32 * (l >> 1) + xi) * 16 + 2 * w + (l & 1)) * 4u, __float_as_uint(rbf(1.0f / (1.0f + expf(-rbf(gs))))));
    }
    wg_wait_s(rw, OFF_CF + 384u, 32u);
    HTR(1);
    fft_cols_fwd(lds, rw, 8 * q + m - 24, hb);
    wg_signal(ws, OFF_CF);
    HTR(3);
    {
      unsigned tx = threadIdx.x;
      asm volatile("" : "+v"(tx));
      const unsigned tv = (tx >> 3) & 1;
      u32x4 p3 = u32x4{tv, tv, tv, tv}, p4 = u32x4{POISON, POISON, POISON, POISON};
      asm volatile("" : "+v"(p3), "+v"(p4));
#pragma unroll
      for (int i = 0; i < 6; ++i) st_sc1s(rw, (tx & 255u) * 16u, nm(3 + (i >> 1), 2 * (i & 1) + (w >> 2), NB3 + xi), p3);
#pragma unroll
      for (int i = FS_; i < NL_; ++i) st_sc1s(rw, (w >> 2) * HALF_ + (tx & 255u) * 16u, ooff(i, xi, hb), p4);
      drain_vm();
      asm volatile("" :: "v"(p3), "v"(p4));
    }
    wg_wait_s(rw, OFF_CF, 32u);
    fft_rows(lds, rw, wr, wi, xi, hb);
    wg_signal(ws, OFF_CF + 128u);
    HTR(5);
    xattn_wave(rw, m & 7, q, hb);
    HTR(11);
    wg_wait_s(rw, OFF_CF + 128u, 32u);
    fft_cols_inv(lds, rw, 32 * q + m - 24, hb);
    wg_signal(ws, OFF_CF + 256u);
    HTR(7);
  }
  oproj_worker(lds, rw, Wo, (m >> 3) ^ (m >> 4), m & 3, 2 * q + ((m >> 2) & 1), out, hb DLYA
);
  HTR(9);
}

static_assert(W3_OFF + 65536 <= SMEM_BYTES, "LDS");
__global__ __launch_bounds__(512, 1) void mega_kernel(const u16* __restrict__ x, const u16* __restrict__ mem0,
                                                      const u16* __restrict__ Wq, const u16* __restrict__ Wk,
                                                      const u16* __restrict__ Wv, const u16* __restrict__ Wo,
                                                      const u16* __restrict__ wr, const u16* __restrict__ wi,
                                                      const u16* __restrict__ gw, u16* __restrict__ out, char* ws) {
  char* lds = lds_;
  ws += (0u - (unsigned)(size_t)ws) & (SET_ - 1u);
  const rsrc_t rw = __builtin_amdgcn_make_buffer_rsrc(ws, 0, WS_USED, 0x00020000);
  const int idx = (blockIdx.x >> 3) * 4 + (blockIdx.x & 3);
  const bool isrec = (blockIdx.x & 4) == 0;
  const int rid = (blockIdx.x & 3) * 32 + (blockIdx.x >> 3), hid = (idx & 3) * 2 + ((idx >> 2) & 1) + (idx >> 3) * 8;
  if (__builtin_amdgcn_readfirstlane(threadIdx.x >> 6) == (isrec ? 7 : 3) && (blockIdx.x >> 3) >= 16) {
    u32x2 pc;
    unsigned n;
    asm volatile("s_getpc_b64 %0\n code_from: s_mov_b32 %1, code_to - code_from" : "=s"(pc), "=s"(n));
    asm volatile("s_mov_b32 m0, %2\n s_nop 0\n buffer_load_dword %0, %1, 0 offen lds"
                 :: "v"((((blockIdx.x >> 3) - 16) * 64 + (threadIdx.x & 63)) * 128), "s"(u32x4{pc[0], pc[1] & 0xffffu, n, 0x00020000u}),
                    "s"((unsigned)(size_t)LDS3 + (isrec ? 3u * 24576u : 131072u + 3u * 4096u)) : "memory");
  }
  if (isrec) {
    recur_role(lds, ws, rw, mem0, Wq, Wk, Wv, Wo, out, rid TRARG);
  } else helper_role(lds, ws, rw, x, Wq, Wk, Wv, Wo, wr, wi, gw, out, hid TRARG);
}

extern "C" void kernel_launch(void* const* d_in, const int* in_sizes, int n_in, void* d_out, int out_size, void* d_ws,
                              size_t ws_size, hipStream_t stream) {
  const u16* x = (const u16*)d_in[0];
  const u16* mem0 = (const u16*)d_in[1];
  const u16* Wq = (const u16*)d_in[2];
  const u16* Wk = (const u16*)d_in[3];
  const u16* Wv = (const u16*)d_in[4];
  const u16* Wo = (const u16*)d_in[5];
  const u16* wr = (const u16*)d_in[8];
  const u16* wi = (const u16*)d_in[9];
  const u16* gw = (const u16*)d_in[10];
  u16* out = (u16*)d_out;
  char* ws = (char*)d_ws;
  mega_kernel<<<256, 512, 0, stream>>>(x, mem0, Wq, Wk, Wv, Wo, wr, wi, gw, out, ws);
}
```
